```python
import torch

def _softmax(x, axis=-1):
    return torch.softmax(x, dim=axis)
def _gelu(x):
    return torch.nn.functional.gelu(x, approximate="tanh")
def _silu(x):
    return torch.nn.functional.silu(x)
def _sigmoid(x):
    return torch.sigmoid(x)
def _layernorm(x, axis=-1, eps=1e-5):
    m = x.mean(dim=axis, keepdim=True)
    v = x.var(dim=axis, keepdim=True, unbiased=False)
    return (x - m) / torch.sqrt(v + eps)
def _scatter_add(tgt, idx, src, axis=0):
    return tgt.clone().index_add_(axis, idx, src)
def _segment_sum(src, seg, n):
    out = torch.zeros((n,) + src.shape[1:], dtype=src.dtype)
    return out.index_add_(0, seg, src)
def _pad(x, pw):
    flat = tuple(v for pair in reversed(pw) for v in pair)
    return torch.nn.functional.pad(x, flat)

kernel_name = 'concat_heads__ssm_conv_s4d__spectral_gfnet__prompt'

def setup_inputs():

    _w = (lambda a: (lambda t: t.to(torch.bfloat16) if t.is_floating_point() else t)(torch.as_tensor(a)))
    def _A_setup():
        Bsz, L, D, N = 1, 2048, 576, 128
        return {
            "u": _w((lambda *s: torch.randn(*s, dtype=torch.bfloat16) * 0.05)(Bsz, L, D)),
            "log_A_real": _w((lambda *s: torch.randn(*s, dtype=torch.bfloat16) * 6.4)(D, N) * 0.5),
            "A_imag": _w((lambda *s: torch.randn(*s, dtype=torch.bfloat16) * 0.05)(D, N) * 3.14),
            "C_real": _w((lambda *s: torch.randn(*s, dtype=torch.bfloat16) * 0.05)(D, N) / N ** 0.5),
            "C_imag": _w((lambda *s: torch.randn(*s, dtype=torch.bfloat16) * 0.05)(D, N) / N ** 0.5),
            "log_dt": _w((lambda *s: torch.randn(*s, dtype=torch.bfloat16) * 0.05)(D) * 0.5 - 4.0),
            "D_skip": _w((lambda *s: torch.randn(*s, dtype=torch.bfloat16) * 0.05)(D)),
        }
    _A_in = _A_setup()

    def _B_setup():
        B, S, D = 1, 2048, 576
        Df = D // 2 + 1
        return {
            "x": _w((lambda *s: torch.randn(*s, dtype=torch.bfloat16) * 0.05)(B, S, D)),
            "w_r": _w((lambda *s: torch.randn(*s, dtype=torch.bfloat16) * 0.8)(S, Df) * 0.02),
            "w_i": _w((lambda *s: torch.randn(*s, dtype=torch.bfloat16) * 0.8)(S, Df) * 0.02),
        }
    _B_in = _B_setup()

    return {
        "A_u": _A_in["u"],
        "A_log_A_real": _A_in["log_A_real"],
        "A_A_imag": _A_in["A_imag"],
        "A_C_real": _A_in["C_real"],
        "A_C_imag": _A_in["C_imag"],
        "A_log_dt": _A_in["log_dt"],
        "A_D_skip": _A_in["D_skip"],
        "B_x": _B_in["x"],
        "B_w_r": _B_in["w_r"],
        "B_w_i": _B_in["w_i"],
    }

def reference(A_u, A_log_A_real, A_A_imag, A_C_real, A_C_imag, A_log_dt, A_D_skip, B_x, B_w_r, B_w_i):
    def _A_ref(u, log_A_real, A_imag, C_real, C_imag, log_dt, D_skip):
        L = u.shape[1]
        dt = torch.exp(log_dt)
        A = -torch.exp(log_A_real) + 1j * A_imag
        C = (C_real + 1j * C_imag) * dt[:, None]
        pos = (lambda x, dt: x.to(dt))(torch.arange(L), log_dt.dtype)
        P = torch.exp((A * dt[:, None])[:, :, None] * pos[None, None, :])
        K = 2.0 * torch.einsum("dn,dnl->dl", C, P).real
        L2 = 2 * L
        u_t = (lambda x, *d: x.permute(*d))(u, 0, 2, 1)
        Kf = (lambda x, *a, **k: torch.fft.rfft(x.float(), *a, **k))(K, L2)
        Uf = (lambda x, *a, **k: torch.fft.rfft(x.float(), *a, **k))(u_t, L2)
        y = (lambda x, *a, **k: torch.fft.irfft(x, *a, **k).to(torch.bfloat16))(Uf * Kf[None, :, :], L2)[:, :, :L]
        y = (lambda x, *d: x.permute(*d))(y, 0, 2, 1)
        return y + u * D_skip[None, None, :]

    def _B_ref(x, w_r, w_i):
        B, S, D = x.shape
        X = (lambda x, axes=(-2,-1): torch.fft.rfft2(x.float(), dim=axes))(x, axes=(-2, -1))
        W = w_r + 1j * w_i
        X = X * W
        y = (lambda x, s, axes=(-2,-1): torch.fft.irfft2(x, s=s, dim=axes).to(torch.bfloat16))(X, (S, D), axes=(-2, -1))
        return y

    a = _A_ref(A_u, A_log_A_real, A_A_imag, A_C_real, A_C_imag, A_log_dt, A_D_skip)
    b = _B_ref(B_x, B_w_r, B_w_i)
    return torch.concatenate([a, b], axis=-1)
```

```cpp
#include <hip/hip_runtime.h>
#include <hip/hip_bf16.h>

typedef unsigned short u16;
__device__ __forceinline__ float bf2f(u16 v){ return __uint_as_float(((unsigned)v)<<16); }
__device__ __forceinline__ u16 f2bf(float f){ __hip_bfloat16 b = __float2bfloat16(f); return *reinterpret_cast<u16*>(&b); }
__device__ __forceinline__ float rbf(float f){ return bf2f(f2bf(f)); }

constexpr int L=2048, D=576, N=128, DF=289;

__global__ void kgen(const u16* lar,const u16* aim,const u16* cr,const u16* ci,const u16* ldt,float* K){
  int l = blockIdx.x*256+threadIdx.x; int d = blockIdx.y;
  float dt = rbf(expf(bf2f(ldt[d])));
  float pos = rbf((float)l);
  float acc=0;
  for(int n=0;n<N;n++){
    float ar = -rbf(expf(bf2f(lar[d*N+n])))*dt;
    float ai = bf2f(aim[d*N+n])*dt;
    float c_r = bf2f(cr[d*N+n])*dt, c_i = bf2f(ci[d*N+n])*dt;
    float e = expf(ar*pos); float s,c; sincosf(ai*pos,&s,&c);
    acc += c_r*e*c - c_i*e*s;
  }
  K[d*L+l]=2.f*acc;
}
__global__ void conv(const u16* u,const float* K,const u16* dsk,u16* out){
  int d = blockIdx.x*64+ (threadIdx.x&63); int l = blockIdx.y*4+(threadIdx.x>>6);
  float acc=0;
  for(int m=0;m<=l;m++) acc += K[d*L+m]*bf2f(u[(l-m)*D+d]);
  float y = rbf(acc);
  float sk = rbf(bf2f(u[l*D+d])*bf2f(dsk[d]));
  out[l*(2*D)+d]=f2bf(y+sk);
}
__global__ void b1(const u16* x,float2* X1){
  int s = blockIdx.x*256+threadIdx.x; int k=blockIdx.y;
  __shared__ float2 tw[D];
  for(int i=threadIdx.x;i<D;i+=256){ float sn,cs; sincospif(-2.f*i/D,&sn,&cs); tw[i]=make_float2(cs,sn);}
  __syncthreads();
  float re=0,im=0; int idx=0;
  for(int d=0;d<D;d++){ float v=bf2f(x[s*D+d]); re+=v*tw[idx].x; im+=v*tw[idx].y; idx+=k; if(idx>=D) idx-=D; }
  X1[k*L+s]=make_float2(re,im);
}
template<int INV>
__global__ void b2(const float2* in,float2* o,const u16* wr,const u16* wi){
  int s = blockIdx.x*256+threadIdx.x; int k=blockIdx.y;
  __shared__ float2 tw[L];
  for(int i=threadIdx.x;i<L;i+=256){ float sn,cs; sincospif((INV?2.f:-2.f)*i/L,&sn,&cs); tw[i]=make_float2(cs,sn);}
  __syncthreads();
  float re=0,im=0; int idx=0;
  for(int t=0;t<L;t++){ float2 v=in[k*L+t]; float2 w=tw[idx]; re+=v.x*w.x-v.y*w.y; im+=v.x*w.y+v.y*w.x; idx=(idx+s)&(L-1);}
  if(!INV){ float a=bf2f(wr[s*DF+k]), b=bf2f(wi[s*DF+k]); float r2=re*a-im*b, i2=re*b+im*a; re=r2; im=i2; }
  o[k*L+s]=make_float2(re,im);
}
__global__ void b3(const float2* Y,u16* out){
  int s = blockIdx.x*256+threadIdx.x; int d=blockIdx.y;
  __shared__ float2 tw[D];
  for(int i=threadIdx.x;i<D;i+=256){ float sn,cs; sincospif(2.f*i/D,&sn,&cs); tw[i]=make_float2(cs,sn);}
  __syncthreads();
  float acc=Y[s].x + ((d&1)?-1.f:1.f)*Y[288*L+s].x; int idx=d;
  for(int k=1;k<288;k++){ float2 v=Y[k*L+s]; acc+=2.f*(v.x*tw[idx].x-v.y*tw[idx].y); idx+=d; if(idx>=D) idx-=D; }
  out[s*(2*D)+D+d]=f2bf(acc*(1.f/(float)(L*D)));
}

extern "C" void kernel_launch(void* const* d_in, const int* in_sizes, int n_in,
                              void* d_out, int out_size, void* d_ws, size_t ws_size,
                              hipStream_t stream) {
  const u16* u=(const u16*)d_in[0]; const u16* lar=(const u16*)d_in[1]; const u16* aim=(const u16*)d_in[2];
  const u16* cr=(const u16*)d_in[3]; const u16* ci=(const u16*)d_in[4]; const u16* ldt=(const u16*)d_in[5];
  const u16* dsk=(const u16*)d_in[6]; const u16* x=(const u16*)d_in[7]; const u16* wr=(const u16*)d_in[8]; const u16* wi=(const u16*)d_in[9];
  u16* out=(u16*)d_out;
  float* K=(float*)d_ws;
  float2* X1=(float2*)((char*)d_ws+ (8<<20)); float2* X2=(float2*)((char*)d_ws+(16<<20)); float2* X3=(float2*)((char*)d_ws+(24<<20));
  kgen<<<dim3(L/256,D),256,0,stream>>>(lar,aim,cr,ci,ldt,K);
  conv<<<dim3(D/64,L/4),256,0,stream>>>(u,K,dsk,out);
  b1<<<dim3(L/256,DF),256,0,stream>>>(x,X1);
  b2<0><<<dim3(L/256,DF),256,0,stream>>>(X1,X2,wr,wi);
  b2<1><<<dim3(L/256,DF),256,0,stream>>>(X2,X3,wr,wi);
  b3<<<dim3(L/256,D),256,0,stream>>>(X3,out);
}
```

```cpp
#include <hip/hip_runtime.h>

typedef unsigned short u16;
typedef unsigned int u32;
typedef float f2 __attribute__((ext_vector_type(2)));
typedef float f4 __attribute__((ext_vector_type(4)));
typedef u32 u2 __attribute__((ext_vector_type(2)));
typedef u32 u4 __attribute__((ext_vector_type(4)));
typedef int i8v __attribute__((ext_vector_type(8)));
typedef short s4 __attribute__((ext_vector_type(4)));
typedef short s8 __attribute__((ext_vector_type(8)));
typedef __bf16 bf2 __attribute__((ext_vector_type(2)));
#define DI __device__ __forceinline__
#define LAS __attribute__((address_space(3)))
#define GAS __attribute__((address_space(1)))
#define CAS __attribute__((address_space(4)))
#pragma clang diagnostic ignored "-Wint-to-pointer-cast"
struct u3 { u32 v[3]; DI u32& operator[](int i) { return v[i]; } DI const u32& operator[](int i) const { return v[i]; } };

constexpr int SEQ = 2048, DM = 576, NS = 128, DF = 289;
constexpr int NB1 = 128;
constexpr int NB2 = 145;
constexpr int A0 = 152;
constexpr int NA = 96;
constexpr int GRID = A0 + NA;
constexpr int NT = 1024;

constexpr u32 WS_EPOCH = 0, WS_F1 = 4096, WS_F2 = WS_F1 + 32768, WS_X1 = 1u << 20, WS_Y1 = 4u << 20, WS_USED = 8u << 20;
#define STAMP(i)
#define STAMP_DECL
#define STAMP_OUT(T)

constexpr int F_SA = 0, F_EO = 4;
constexpr int F_ARE = 9, F_AIM = 10, F_BRE = 11, F_BIM = 12, F_G = 13;
constexpr int F_F16 = 21, F_GI = 23, F_AI = 31, F_M1C = 33;
constexpr int F_VAR = 37;
constexpr int F_END = F_VAR + 40;
constexpr int ET = 452, ER = 28, E2V = 580, E2U = 36, OP = 1168;
constexpr int LB_X = 0, LB_I = 0, LB_Y = 0;
constexpr int LB_E = 16 * OP;
constexpr int LB_O = LB_E, LB_E2 = LB_E;
constexpr int LB_S = LB_E + 16 * ET * 4;
constexpr int LB_W = LB_S + 1200 * 16;
constexpr int LB_T = LB_W + 2 * 256 * 36;
constexpr int LB_END = LB_T + (F_VAR + 16) * 1024;
struct CosTab { double c[2048]; double c9[9], s9[9]; };
constexpr double tay_cos(double x) { double t = 1, s = 1; for (int k = 1; k <= 14; k++) { t *= -x * x / ((2 * k - 1) * (2 * k)); s += t; } return s; }
constexpr double tay_sin(double x) { double t = x, s = x; for (int k = 1; k <= 14; k++) { t *= -x * x / ((2 * k + 1) * (2 * k)); s += t; } return s; }
constexpr CosTab make_cos() {
    CosTab t = {};
    const double PI2 = 6.283185307179586476925;
    for (int k = 0; k <= 512; k++) {
        const double v = k == 512 ? 0.0 : (k <= 256 ? tay_cos(PI2 * k / 2048) : tay_sin(PI2 * (512 - k) / 2048));
        t.c[k] = v; t.c[(2048 - k) & 2047] = v; t.c[1024 - k] = -v; t.c[(1024 + k) & 2047] = -v;
    }
    t.c[512] = 0.0; t.c[1536] = 0.0;
    for (int k = 0; k < 9; k++) { const double a = PI2 * (k <= 4 ? k : k - 9) / 9; t.c9[k] = tay_cos(a); t.s9[k] = tay_sin(a); }
    return t;
}
constexpr CosTab CT = make_cos();
constexpr double cs(int n) { return CT.c[n & 2047]; }
constexpr double sn(int n) { return CT.c[(n - 512) & 2047]; }
constexpr u32 bfr(double x) { const u32 b = __builtin_bit_cast(u32, (float)x); return (b + 0x7fffu + ((b >> 16) & 1u)) >> 16; }
constexpr u32 pkc(double lo, double hi) { return bfr(lo) | (bfr(hi) << 16); }
constexpr u32 f32c(double x) { return __builtin_bit_cast(u32, (float)x); }
template <int N> struct Frags { u32 w[N * 256]; };
constexpr u32 w_re(double amp, int n) { return pkc(amp * cs(n), amp * sn(n)); }
constexpr u32 w_im(double amp, int n) { return pkc(-amp * sn(n), amp * cs(n)); }
constexpr Frags<F_VAR> make_t0() {
    Frags<F_VAR> t = {};
    const double R2 = 1.4142135623730951;
    for (int l = 0; l < 64; l++)
        for (int i = 0; i < 4; i++) {
            const int c = l & 15, g = l >> 4, r = 4 * g + i, o = l * 4 + i;
            {
                const int b = c >> 2, tq = c & 3;
                const double amp = b ? 1.0 : R2;
                const int n0 = 512 * tq * g + (b ? 256 * b * (2 * i) : -256), n1 = 512 * tq * g + (b ? 256 * b * (2 * i + 1) : 256);
                t.w[F_ARE * 256 + o] = pkc(amp * cs(n0), amp * cs(n1));
                t.w[F_AIM * 256 + o] = pkc(-amp * sn(n0), -amp * sn(n1));
            }
            for (int w = 0; w < 9; w++) {
                const int sh = 16 * (i & 1), oo = w * 128 + l * 2 + (i >> 1);
                t.w[F_EO * 256 + oo] |= (u32)((c * ET + r * ER + 2 * ((7 * w + r) % 9)) * 4) << sh;
                if (w < 8) {
                    const int k = (9 * (w & 3) + 36 * r + 64 * (c % 9)) % DM, slot = w < 4 ? (k <= 288 ? 2 * k : 2 * (DM - k) + 1) : k;
                    t.w[F_SA * 256 + oo] |= (u32)(slot * 8 + (slot >> 6) * 16) << sh;
                }
            }
            if (r < 9) { const int k = (r * c) % 9; t.w[F_BRE * 256 + o] = pkc(CT.c9[k], CT.s9[k]); t.w[F_BIM * 256 + o] = pkc(-CT.s9[k], CT.c9[k]); }
            for (int q = 0; q < 4; q++) {
                t.w[(F_G + 2 * q) * 256 + o] = w_re(1.0, 32 * r * (q + 4 * c)); t.w[(F_G + 2 * q + 1) * 256 + o] = w_im(1.0, 32 * r * (q + 4 * c));
                t.w[(F_GI + 2 * q) * 256 + o] = w_re(1.0, 32 * c * (q + 4 * r)); t.w[(F_GI + 2 * q + 1) * 256 + o] = w_im(1.0, 32 * c * (q + 4 * r));
            }
            t.w[F_F16 * 256 + o] = w_re(1.0, 128 * r * c); t.w[(F_F16 + 1) * 256 + o] = w_im(1.0, 128 * r * c);
            for (int h = 0; h < 2; h++) {
                const int j = 4 * h + (c >> 2), n = 512 * i * (c & 3) + (g ? 256 * g * j : ((j & 1) ? 256 : -256));
                t.w[(F_AI + h) * 256 + o] = w_re(g ? 2.0 : R2, n);
                const int m = 16 * r * (4 * h + 8 * c) - 512 * h;
                t.w[(F_M1C + 2 * h) * 256 + o] = pkc(0.5 * cs(m), -0.5 * sn(m));
                t.w[(F_M1C + 2 * h + 1) * 256 + o] = pkc(-0.5 * sn(m), -0.5 * cs(m));
            }
        }
    return t;
}
constexpr Frags<40> make_t1() {
    Frags<40> t = {};
    for (int v = 0; v < 5; v++)
        for (int l = 0; l < 64; l++)
            for (int i = 0; i < 4; i++) {
                const int be = v < 3 ? v + 1 : 4 * (v - 3), c = l & 15, r = 4 * (l >> 4) + i, o = v * 2048 + l * 4 + i;
                const double amp = v < 3 ? 1.0 : 0.5;
                const int n1 = 16 * r * (be + 8 * c) + (v == 4 ? 512 : 0), n4 = 16 * c * (be + 8 * r);
                t.w[o] = w_re(amp, n1); t.w[o + 256] = w_im(amp, n1);
                t.w[o + 512] = w_re(1.0, n4); t.w[o + 768] = w_im(1.0, n4);
                t.w[o + 1024] = f32c(cs(r * (be + 8 * c))); t.w[o + 1280] = f32c(-sn(r * (be + 8 * c)));
                t.w[o + 1536] = f32c(cs(c * (be + 8 * r))); t.w[o + 1792] = f32c(-sn(c * (be + 8 * r)));
            }
    return t;
}
struct WgPar { int unit, w0, wm, wh, tvar, kind, kbase, bq; };
struct WgTab { WgPar p[NB2]; };
constexpr WgTab make_wg() {
    WgTab t = {};
    for (int id = 0; id < NB2; id++) {
        const int u = (id & 7) * 18 + ((id & 7) ? 1 : 0) + (id >> 3);
        const bool star = u >= 108;
        const int b = star ? 0 : 1 + u / 36, kg = star ? u - 108 : u % 36;
        const bool mir = !star && kg >= 18;
        WgPar& p = t.p[id];
        p.unit = u; p.bq = b; p.kind = star ? 2 : (mir ? 1 : 0);
        p.w0 = 2 * (star ? (kg < 36 ? 8 * kg : 281) : (mir ? (2048 - b) * DF + DM - 16 * kg - 15 : b * DF + 16 * kg));
        p.wm = 2 * (mir ? -8 * DF : 8 * DF); p.wh = 2 * (star ? 4 * DF : 8);
        p.tvar = (F_VAR + 8 * (star ? 3 : b - 1)) * 1024; p.kbase = star ? 8 * kg : 16 * kg;
    }
    return t;
}
struct Tables { Frags<F_VAR> a; Frags<40> b; WgTab w; };
constexpr Frags<F_VAR> T0 = make_t0();
constexpr Frags<40> T1 = make_t1();
constexpr WgTab T3 = make_wg();
static __device__ const __attribute__((aligned(128))) Tables TAB = {T0, T1, T3};
constexpr int LA_K = 0;
constexpr int LA_U = LA_K + 6 * 16640;
constexpr int LA_Q = LA_U + 6 * 4608;
constexpr int LA_F = LA_Q + 12 * 644 * 4;
constexpr int LA_END = LA_F + 16;
constexpr int LDS_BYTES = (LA_END > LB_END ? LA_END : LB_END);
static_assert(LDS_BYTES <= 160 * 1024 && LDS_BYTES > 80 * 1024, "one workgroup per CU");

DI u32 pk(float a, float b) { f2 v = {a, b}; return __builtin_bit_cast(u32, __builtin_convertvector(v, bf2)); }
DI float blo(u32 v) { return __uint_as_float(v << 16); }
DI float bhi(u32 v) { return __uint_as_float(v & 0xffff0000u); }
DI float bf2f(u16 v) { return __uint_as_float((u32)v << 16); }
DI float rbf(float x) { return blo(pk(x, x)); }
DI f2 cmul(f2 a, f2 b) { f2 t = a.xx * b; f2 s = {-b.y, b.x}; return __builtin_elementwise_fma(a.yy, s, t); }
DI f4 mfma(u4 a, u4 b, f4 c) { return __builtin_amdgcn_mfma_f32_16x16x32_bf16(__builtin_bit_cast(s8, a), __builtin_bit_cast(s8, b), c, 0, 0, 0); }
DI u4 pack4(f4 re, f4 im) { u4 r = {pk(re[0], im[0]), pk(re[1], im[1]), pk(re[2], im[2]), pk(re[3], im[3])}; return r; }
DI u4 twid(f4 re, f4 im, f4 wr, f4 wi) { return pack4(__builtin_elementwise_fma(re, wr, -(im * wi)), __builtin_elementwise_fma(re, wi, im * wr)); }
DI int mod9(int x) { return x - 9 * ((x * 57) >> 9); }
template <class T> DI void pin(T& v) { asm volatile("" : "+v"(v)); }

template <class T> DI T lds_ld(int off) { return *(const LAS T*)(u32)off; }
template <class T> DI void lds_st(int off, T v) { *(LAS T*)(u32)off = v; }
DI void bar() { __syncthreads(); }
DI void glds16(const void* g, u32 off, int lds) {
    u32 keep;
    asm volatile("s_mov_b32 %0, m0\n\ts_mov_b32 m0, %3\n\ts_nop 4\n\tglobal_load_lds_dwordx4 %1, %2\n\ts_mov_b32 m0, %0" : "=&s"(keep) : "v"(off), "s"(g), "s"(lds) : "memory");
}

template <int N> DI bool wait_flags(__amdgpu_buffer_rsrc_t r, u32 base, u32 tag, int lane) {
    const unsigned long long t0 = __builtin_amdgcn_s_memrealtime();
    const int o2 = (lane + 128 < N ? lane + 128 : lane) * 128;
    for (;;) {
        const u32 a = __builtin_amdgcn_raw_buffer_load_b32(r, base + lane * 128, 0, 16);
        const u32 b = __builtin_amdgcn_raw_buffer_load_b32(r, base + (lane + 64) * 128, 0, 16);
        const u32 c = N > 128 ? __builtin_amdgcn_raw_buffer_load_b32(r, base + o2, 0, 16) : tag;
        if (__all((a == tag) & (b == tag) & (c == tag))) return true;
        if (__builtin_amdgcn_s_memrealtime() - t0 > 5000000ull) return false;
        asm volatile("" ::: "memory");
    }
}

DI void head_b(const u16* __restrict__ x, const u16* __restrict__ wr, const u16* __restrict__ wi,
               u16* __restrict__ out, char* ws) {
    const int tid = threadIdx.x, lane = tid & 63, c16 = lane & 15, g = lane >> 4;
    const int wv = __builtin_amdgcn_readfirstlane(tid >> 6);
    const int id = blockIdx.x;
    STAMP_DECL;
    const bool rows = id < NB1;
    const int s00 = 32 * (id >> 4) + (id & 15);
    const char* tab = (const char*)&TAB;
    const int R = (tid * 911) >> 16;
    const u32 xoff = (s00 + 16 * (R >> 3) + 256 * (R & 7)) * (2 * DM) + (tid - R * 72) * 16;
    asm volatile("" :: "s"(x), "s"(wr), "s"(wi), "s"(out), "s"(ws), "v"(xoff));
    const __amdgpu_buffer_rsrc_t rws = __builtin_amdgcn_make_buffer_rsrc(ws, 0, WS_USED, 0x00020000);
    u32 tag = 0;
    if (wv == 0) asm volatile("global_load_dword %0, %0, %1 sc1" : "+v"(tag) : "s"(ws) : "memory");
    if (rows) {
        glds16(x, xoff, LB_X + wv * 1024);
        if (wv < 2) glds16(x, (s00 + 16 + 1536) * (2 * DM) + (tid < 56 ? (tid + 16) * 16 : 256 * 2 * DM + (tid - 56) * 16), LB_X + 16384 + wv * 1024);
        else glds16(tab, tid * 16 - 2048, LB_T + (wv - 2) * 1024);
        if (wv >= 9) glds16(tab, tid * 16 + 5 * 1024, LB_T + (wv + 5) * 1024);
    }
    asm volatile("" : "+s"(tab));
    const WgPar par = __builtin_bit_cast(WgPar, *(const CAS i8v*)(tab + F_END * 1024 + id * 32));
    u4 wreg;
    __builtin_memcpy(&wreg, (const char*)((wv >> 3) ? wi : wr) + (u32)(par.w0 + ((tid >> 1) & 255) * par.wm + (tid & 1) * par.wh), 16);
    __builtin_amdgcn_sched_barrier(0);
    const f4 Z = {0.f, 0.f, 0.f, 0.f};
    const int u2i = par.unit, bq = par.bq;
    const bool star = par.kind == 2, mir = par.kind == 1;
    const int tau = wv & 3, bw = wv >> 2;
    int tl = LB_T + lane * 16;
    int lin = LB_S + (tid + (tid >> 5)) * 16;
    pin(tl); pin(lin);
    int sadr[4];

    if (rows) {
        if (tid < 256) {
            const int o = LB_E + 72 + (tid & 15) * (ER * 4) + (tid >> 4) * (ET * 4);
            lds_st<u2>(o, u2{0u, 0u}); lds_st<u4>(o + 8, u4{0u, 0u, 0u, 0u});
        }
        int xo = LB_X + (c16 >> 2) * 1152 + 2 * (144 * ((g - wv) & 3) + 16 * wv + 4 * (c16 & 3));
        int bo = LB_E + (wv * ET + c16 * ER + min(8 * g, 16)) * 4;
        int go = WS_X1 + (tid >> 3) * 16384 + id * 128 + (tid & 7) * 16;
        int ta = LB_T + lane * 8;
        pin(xo); pin(bo); pin(go); pin(ta);
        STAMP(1);
        asm volatile("s_waitcnt vmcnt(1) lgkmcnt(0)\n\ts_barrier" ::: "memory");
        STAMP(2);
        if (wv < 9) {
            u4 d[2];
#pragma unroll
            for (int s = 0; s < 2; s++) {
                const s4 lo = __builtin_amdgcn_ds_read_tr16_b64_v4i16((LAS s4*)(u32)(xo + s * 9216));
                const s4 hi = __builtin_amdgcn_ds_read_tr16_b64_v4i16((LAS s4*)(u32)(xo + s * 9216 + 4608));
                d[s] = __builtin_bit_cast(u4, __builtin_shufflevector(lo, hi, 0, 1, 2, 3, 4, 5, 6, 7));
            }
            const u4 are = lds_ld<u4>(tl + F_ARE * 1024), aim = lds_ld<u4>(tl + F_AIM * 1024);
            const u2 eo = lds_ld<u2>(ta + F_EO * 1024 + wv * 512);
#pragma unroll
            for (int s = 0; s < 2; s++) d[s] = pack4(mfma(d[s], are, Z), mfma(d[s], aim, Z));
#pragma unroll
            for (int i = 0; i < 4; i++) lds_st<u2>(LB_E + ((i & 1) ? eo[i >> 1] >> 16 : eo[i >> 1] & 0xffff), u2{d[0][i], d[1][i]});
        }
        u4 bre = lds_ld<u4>(tl + F_BRE * 1024), bim = lds_ld<u4>(tl + F_BIM * 1024);
        u4 gre = lds_ld<u4>(tl + (F_G + 2 * tau) * 1024), gim = lds_ld<u4>(tl + (F_G + 2 * tau + 1) * 1024);
        {
            const u2 sa = lds_ld<u2>(ta + F_SA * 1024 + (tau + (bw ? 4 : 0)) * 512);
            const int base = LB_S + ((bw + 3) & 3) * (DM * 8 + 144);
#pragma unroll
            for (int i = 0; i < 4; i++) { sadr[i] = base + ((i & 1) ? sa[i >> 1] >> 16 : sa[i >> 1] & 0xffff); pin(sadr[i]); }
        }
        pin(bre); pin(bim); pin(gre); pin(gim);
        STAMP(12);
        bar();
        STAMP(13);
        {
            const u4 p0 = lds_ld<u4>(bo), p1 = lds_ld<u4>(bo + 16);
            u4 d[2];
#pragma unroll
            for (int s = 0; s < 2; s++) {
                const u4 a = {p0[s], p0[2 + s], p1[s], p1[2 + s]};
                const u4 t = pack4(mfma(a, bre, Z), mfma(a, bim, Z));
                d[s] = pack4(mfma(gre, t, Z), mfma(gim, t, Z));
            }
            if (c16 < 9) {
#pragma unroll
                for (int i = 0; i < 4; i++) lds_st<u2>(sadr[i], u2{d[0][i], d[1][i]});
                if (wv == 0 && c16 == 0 && !(g & 1)) lds_st<u2>(sadr[0] + 8, u2{d[0][0], d[1][0]});
            }
        }
        STAMP(14);
        bar();
        STAMP(3);
        __builtin_amdgcn_raw_buffer_store_b128(lds_ld<u4>(lin), rws, go, 0, 16);
        if (tid <= 128) __builtin_amdgcn_raw_buffer_store_b128(lds_ld<u4>(lin + 1056 * 16), rws, go, 128 * 16384, 16);
    } else {
        if (wv < 7) { u32 t = *(const GAS u32*)(tab + F_F16 * 1024 + (u32)(tid * 128)); asm volatile("" :: "v"(t)); }
    }
    {
        const int o = LB_W + (tid >> 1) * 36 + (tid & 1) * 16;
        lds_st<u32>(o, wreg[0]); lds_st<u32>(o + 4, wreg[1]); lds_st<u32>(o + 8, wreg[2]); lds_st<u32>(o + 12, wreg[3]);
    }
    asm volatile("s_waitcnt vmcnt(0)" : "+v"(tag) :: "memory");
    tag += 0x9E3779B1u;
    bar();
    STAMP(4);
    if (rows && tid == 0) __builtin_amdgcn_raw_buffer_store_b32(tag, rws, WS_F1 + id * 128, 0, 16);
    glds16(tab, F_F16 * 1024 + tid * 16, LB_T + F_F16 * 1024 + wv * 1024);
    if (star || wv < 8) glds16(tab, par.tvar + tid * 16, LB_T + F_VAR * 1024 + wv * 1024);
    asm volatile("s_waitcnt vmcnt(0)\n\ts_barrier" ::: "memory");

    const int typ = star ? (wv & 1) : 0;
    const int tv = tl + (F_VAR + 8 * typ) * 1024;
    u4 fre = lds_ld<u4>(tl + F_F16 * 1024), fim = lds_ld<u4>(tl + (F_F16 + 1) * 1024);
    u4 m1r = lds_ld<u4>(tv), m1i = lds_ld<u4>(tv + 1024), m4r = lds_ld<u4>(tv + 2048), m4i = lds_ld<u4>(tv + 3072);
    f4 twr = lds_ld<f4>(tv + 4096), twi = lds_ld<f4>(tv + 5120), tur = lds_ld<f4>(tv + 6144), tui = lds_ld<f4>(tv + 7168);
    u4 mcr = lds_ld<u4>(tl + (F_M1C + 2 * typ) * 1024), mci = lds_ld<u4>(tl + (F_M1C + 2 * typ + 1) * 1024);
    f4 wfr, wfi;
    {
        const int kcol = par.kbase + (star ? (wv >> 1) : wv);
        const int cc = star ? 8 * typ + (u2i < NB2 - 1 ? (wv >> 1) : 7) : (mir ? 15 - wv : wv);
        const int wo = LB_W + (c16 + 64 * g) * 36 + cc * 2;
#pragma unroll
        for (int i = 0; i < 4; i++) {
            wfr[i] = bf2f(lds_ld<u16>(wo + i * 576));
            wfi[i] = bf2f(lds_ld<u16>(wo + i * 576 + 9216));
        }
        float sc = 1.f / (float)(SEQ * DM);
        if (kcol == 0 || kcol == 288) {
            sc *= 0.5f;
#pragma unroll
            for (int i = 0; i < 4; i++) {
                const int m = c16 + 64 * g + 16 * i;
                const int orow = (mir ? bq + 8 * m : 2048 - (star ? 4 * typ : bq) - 8 * m) & 2047;
                wfr[i] += bf2f(wr[orow * DF + kcol]);
                wfi[i] -= bf2f(wi[orow * DF + kcol]);
            }
        }
        wfr *= sc; wfi *= (mir ? -sc : sc);
    }
    f4 wnr = -wfr, wni = -wfi;
    int io = (32 * g + c16) * 128 + (((wv >> 1) ^ (c16 >> 1)) * 16);
    int so = (tid >> 3) * 128 + (((tid & 7) ^ ((tid >> 4) & 7)) * 16);
    pin(fre); pin(fim); pin(m1r); pin(m1i); pin(m4r); pin(m4i); pin(twr); pin(twi); pin(tur); pin(tui); pin(mcr); pin(mci);
    int gi = WS_X1 + u2i * 16384 + tid * 16, gq = WS_Y1 + (tid >> 3) * (NB2 * 128) + u2i * 128 + (tid & 7) * 16;
    int i1 = LB_I + io + (star ? 0 : (wv & 1) * 8);
    io += LB_O + (wv & 1) * 8;
    pin(wfr); pin(wnr); pin(wni); pin(io); pin(so); pin(gi); pin(gq); pin(i1);
    if (wv == 0) wait_flags<NB1>(rws, WS_F1, tag, lane);
    bar();
    STAMP(5);
    lds_st<u4>(LB_I + so, __builtin_amdgcn_raw_buffer_load_b128(rws, gi, 0, 16));
    bar();
    STAMP(6);
    {
        const int o1 = i1;
        const u2 a0 = lds_ld<u2>(o1), a1 = lds_ld<u2>(o1 + 2048);
        const u4 a = {a0[0], a0[1], a1[0], a1[1]};
        f4 re = mfma(a, m1r, Z), im = mfma(a, m1i, Z);
        if (star) {
            const u2 b0 = lds_ld<u2>(o1 + 8), b1 = lds_ld<u2>(o1 + 8 + 2048);
            const u4 b = {b0[0], b0[1], b1[0], b1[1]};
            re = mfma(b, mcr, re); im = mfma(b, mci, im);
        }
        u4 t = twid(re, im, twr, twi);
        re = mfma(fre, t, Z); im = mfma(fim, t, Z);
        t = pack4(__builtin_elementwise_fma(re, wfr, im * wni), __builtin_elementwise_fma(re, wni, im * wnr));
        re = mfma(t, fre, Z); im = mfma(t, fim, Z);
        t = twid(re, im, tur, tui);
        t = pack4(mfma(m4r, t, Z), mfma(m4i, t, Z));
        lds_st<u2>(io, u2{t[0], t[1]}); lds_st<u2>(io + 2048, u2{t[2], t[3]});
    }
    bar();
    STAMP(7);
    {
        u4 v = lds_ld<u4>(LB_O + so);
        if (star) {
            u4 w;
#pragma unroll
            for (int s = 0; s < 2; s++) {
                const float ar = blo(v[s]), ai = bhi(v[s]), cr = blo(v[2 + s]), ci = bhi(v[2 + s]);
                w[s] = pk(ar + ci, ai - cr); w[2 + s] = pk(ar - ci, -ai - cr);
            }
            v = w;
        }
        if (u2i < NB2 - 1 || (tid & 7) == 0)
            __builtin_amdgcn_raw_buffer_store_b128(v, rws, gq, 0, 16);
    }
    asm volatile("s_waitcnt vmcnt(0)" ::: "memory");
    bar();
    STAMP(8);
    if (tid == 0) __builtin_amdgcn_raw_buffer_store_b32(tag, rws, WS_F2 + id * 128, 0, 16);
    if (!rows) { STAMP_OUT(0); return; }

    u4 gre = lds_ld<u4>(tl + (F_GI + 2 * tau) * 1024), gim = lds_ld<u4>(tl + (F_GI + 2 * tau + 1) * 1024);
    u4 bre = lds_ld<u4>(tl + F_BRE * 1024), bim = lds_ld<u4>(tl + F_BIM * 1024);
    u4 ai0 = lds_ld<u4>(tl + F_AI * 1024), ai1 = lds_ld<u4>(tl + (F_AI + 1) * 1024);
    int eadr[4];
#pragma unroll
    for (int i = 0; i < 4; i++) {
        const int r = 4 * g + i;
        eadr[i] = LB_E2 + (mod9(4 * (c16 + 18 - r)) * E2V + r * E2U + 2 * wv) * 4;
        pin(eadr[i]);
    }
    int ao = LB_E2 + (wv * E2V + c16 * E2U + 8 * g) * 4;
    int gy = WS_Y1 + id * (NB2 * 128) + tid * 16;
    int yo = LB_Y + (c16 >> 2) * OP + 2 * (144 * (((c16 & 3) - wv) & 3) + 16 * wv + 4 * g);
    int yl = LB_Y + tid * 16 + R * (OP - 1152);
    u32 oo = 2 * xoff - (tid - R * 72) * 16 + 2 * DM;
    pin(gre); pin(gim); pin(bre); pin(bim); pin(ai0); pin(ai1); pin(ao); pin(gy); pin(yo); pin(yl); pin(oo);
    if (wv == 0) {
        const bool ok = wait_flags<NB2>(rws, WS_F2, tag, lane);
        if (id == 0 && lane == 0 && ok) __builtin_amdgcn_raw_buffer_store_b32(tag, rws, WS_EPOCH, 0, 16);
    }
    bar();
    STAMP(9);
    {
        const u4 v = __builtin_amdgcn_raw_buffer_load_b128(rws, gy, 0, 16);
        if (tid <= 128) lds_st<u4>(lin + 1056 * 16, __builtin_amdgcn_raw_buffer_load_b128(rws, gy, 16384, 16));
        lds_st<u4>(lin, v);
    }
    bar();
    STAMP(10);
    {
        u4 d[2];
        u2 p[4];
#pragma unroll
        for (int i = 0; i < 4; i++) p[i] = lds_ld<u2>(sadr[i]);
#pragma unroll
        for (int s = 0; s < 2; s++) {
            const u4 a = {p[0][s], p[1][s], p[2][s], p[3][s]};
            const u4 t = pack4(mfma(a, gre, Z), mfma(a, gim, Z));
            d[s] = pack4(mfma(t, bre, Z), mfma(t, bim, Z));
        }
        if (c16 < 9) {
#pragma unroll
            for (int i = 0; i < 4; i++) lds_st<u2>(eadr[i], u2{d[0][i], d[1][i]});
        }
    }
    STAMP(15);
    bar();
    if (wv < 9) {
        const u4 p0 = lds_ld<u4>(ao), p1 = lds_ld<u4>(ao + 16);
#pragma unroll
        for (int s = 0; s < 2; s++) {
            const u4 a = {p0[s], p0[2 + s], p1[s], p1[2 + s]};
            const f4 y0 = mfma(a, ai0, Z), y1 = mfma(a, ai1, Z);
            lds_st<u2>(yo + s * (8 * OP), u2{pk(y0[0], y0[1]), pk(y0[2], y0[3])});
            lds_st<u2>(yo + s * (8 * OP) + 4 * OP, u2{pk(y1[0], y1[1]), pk(y1[2], y1[3])});
        }
    }
    bar();
    *(GAS u4*)((char*)out + oo) = lds_ld<u4>(yl);
    if (wv < 2) {
        const int r2 = tid < 56 ? 14 : 15, c2 = tid + 1024 - 72 * r2;
        *(GAS u4*)((char*)out + (u32)((s00 + 16 + 256 * (r2 - 8)) * (4 * DM) + 2 * DM + c2 * 16)) = lds_ld<u4>(LB_Y + r2 * OP + c2 * 16);
    }
    STAMP(11);
    STAMP_OUT(0);
}

DI f2 cexpa(float ar2, float air, float p) {
    const float e = __builtin_amdgcn_exp2f(ar2 * p), t = __builtin_amdgcn_fractf(air * p);
    return f2{e * __builtin_amdgcn_cosf(t), e * __builtin_amdgcn_sinf(t)};
}
DI u4 ld_k(int o) {
    const u2 a = *(const volatile LAS u2*)(u32)o, b = *(const volatile LAS u2*)(u32)(o + 8);
    return u4{a[0], a[1], b[0], b[1]};
}
DI void put8(int o, u4 a, u4 b) {
    lds_st<u4>(o, a); lds_st<u4>(o + 16, b);
    lds_st<u32>(o + 8316, a[0]); lds_st<u4>(o + 8320, u4{a[1], a[2], a[3], b[0]});
    lds_st<u2>(o + 8336, u2{b[1], b[2]}); lds_st<u32>(o + 8344, b[3]);
}
DI void put4(int o, u4 a) {
    lds_st<u4>(o, a);
    lds_st<u32>(o + 8316, a[0]); lds_st<u2>(o + 8320, u2{a[1], a[2]}); lds_st<u32>(o + 8328, a[3]);
}
DI void put2(int o, u32 a, u32 b) { lds_st<u2>(o, u2{a, b}); lds_st<u32>(o + 8316, a); lds_st<u32>(o + 8320, b); }

DI void a_rows_out(int yb, int ystr, const u3* ur, u3 dk, char* o) {
    u2 y[6];
#pragma unroll
    for (int c = 0; c < 6; c++) y[c] = lds_ld<u2>(yb + c * ystr);
#pragma unroll
    for (int r = 0; r < 4; r++) {
        u3 v;
#pragma unroll
        for (int c = 0; c < 3; c++) {
            const u32 s = pk(blo(ur[r][c]) * blo(dk[c]), bhi(ur[r][c]) * bhi(dk[c]));
            const u32 ya = y[2 * c][r >> 1], yc = y[2 * c + 1][r >> 1];
            v[c] = pk(((r & 1) ? bhi(ya) : blo(ya)) + blo(s), ((r & 1) ? bhi(yc) : blo(yc)) + bhi(s));
        }
        *(u3*)(o + r * (4 * DM)) = v;
    }
}

DI void head_a(const u16* __restrict__ u, const u16* __restrict__ lar, const u16* __restrict__ aimg,
               const u16* __restrict__ cre, const u16* __restrict__ cim, const u16* __restrict__ ldt,
               const u16* __restrict__ dsk, u16* __restrict__ out, char* ws) {
    const int tid = threadIdx.x, lane = tid & 63, c16 = lane & 15, g = lane >> 4;
    const int wv = __builtin_amdgcn_readfirstlane(tid >> 6);
    STAMP_DECL;
    const int ai_ = blockIdx.x - A0;
    const int d0 = 6 * ((ai_ & 7) * 12 + (ai_ >> 3));
    const f4 Z = {0.f, 0.f, 0.f, 0.f};

    if (wv >= 12) {
        const int t = tid - 768;
        u3 ua[4], ub[4];
#pragma unroll
        for (int r = 0; r < 4; r++) ua[r] = *(const u3*)(u + (4 * t + r) * DM + d0);
        const u3 dk = *(const u3*)(dsk + d0);
        if (t < 180) lds_st<u4>(LA_U + (t / 30) * 4608 + (t % 30) * 16, u4{0u, 0u, 0u, 0u});
        if (t == 0) lds_st<u32>(LA_F, 0u);
#pragma unroll
        for (int c = 0; c < 6; c++) {
            const u32 sel = (c & 1) ? 0x07060302u : 0x05040100u;
            lds_st<u2>(LA_U + c * 4608 + 480 + t * 8, u2{__builtin_amdgcn_perm(ua[1][c >> 1], ua[0][c >> 1], sel), __builtin_amdgcn_perm(ua[3][c >> 1], ua[2][c >> 1], sel)});
        }
        asm volatile("" ::: "memory");
#pragma unroll
        for (int r = 0; r < 4; r++) ub[r] = *(const u3*)(u + (1024 + 4 * t + r) * DM + d0);
        STAMP(7);
        bar();
        bar();
        bar();
        bar();
#pragma unroll
        for (int c = 0; c < 6; c++) {
            const u32 sel = (c & 1) ? 0x07060302u : 0x05040100u;
            lds_st<u2>(LA_U + c * 4608 + 480 + 2048 + t * 8, u2{__builtin_amdgcn_perm(ub[1][c >> 1], ub[0][c >> 1], sel), __builtin_amdgcn_perm(ub[3][c >> 1], ub[2][c >> 1], sel)});
        }
        asm volatile("s_waitcnt lgkmcnt(0)" ::: "memory");
        if (lane == 0) __hip_atomic_fetch_add((LAS u32*)(u32)LA_F, 1u, __ATOMIC_RELAXED, __HIP_MEMORY_SCOPE_WORKGROUP);
        STAMP(8);
        const int tile = wv - 12;
        char* o = (char*)(out + 4 * t * (2 * DM) + d0);
        a_rows_out(LA_Q + ((tile >> 1) ^ 1) * 2048 + (tile & 1) * 1024 + lane * 8, 4096, ua, dk, o);
        STAMP(9);
        bar();
        bar();
        a_rows_out(LA_K + (2 * ((tile >> 1) ^ 1) + (tile & 1)) * 4160 + lane * 8, 16640, ub, dk, o + 1024 * (4 * DM));
        STAMP(10);
        STAMP_OUT(768);
        return;
    }
    const int ch = wv >> 1, hf = wv & 1;
    {
        const int po = (d0 + ch) * NS + 64 * hf + lane;
        const u16 p_l = lar[po], p_a = aimg[po], p_r = cre[po], p_i = cim[po], p_t = ldt[d0 + ch];
        const float dt = rbf(expf(bf2f(p_t)));
        const float ar2 = -rbf(expf(bf2f(p_l))) * dt * 1.44269504f, air = bf2f(p_a) * dt * 0.159154943f;
        const f2 C = {2.f * dt * bf2f(p_r), 2.f * dt * bf2f(p_i)};
        STAMP(1);
        const f2 z1 = cexpa(ar2, air, 1.f), z8c = cexpa(ar2, -air, 8.f), z128 = cexpa(ar2, air, 128.f);
        const int gw = LA_K + wv * 4608 + lane * 4, gr = LA_K + wv * 4608 + c16 * 288 + g * 16;
        u4 bfr[4];
        {
            const f2 z16c = cmul(z8c, z8c);
            f2 w0 = {1.f, 0.f}, w1 = z8c;
#pragma unroll
            for (int r = 0; r < 16; r += 2) {
                lds_st<u32>(gw + r * 288, pk(w0.x, w0.y)); lds_st<u32>(gw + (r + 1) * 288, pk(w1.x, w1.y));
                if (r < 14) { w0 = r ? cmul(w0, z16c) : z16c; w1 = cmul(w1, z16c); }
            }
#pragma unroll
            for (int s = 0; s < 4; s++) bfr[s] = lds_ld<u4>(gr + 64 * s);
        }
        f2 e[8];
        e[0] = C;
#pragma unroll
        for (int j = 1; j < 8; j++) e[j] = cmul(e[j - 1], z1);
        const f2 z256 = cmul(z128, z128), z512 = cmul(z256, z256), z1024 = cmul(z512, z512);
        const int q = LA_Q + wv * 2576;
#pragma unroll
        for (int t = 0; t < 3; t++) {
            if (t == 0) {
#pragma unroll
                for (int j = 0; j < 8; j++) {
                    lds_st<u32>(gw + j * 288, pk(e[j].x, e[j].y));
                    const f2 f = cmul(e[j], z128);
                    lds_st<u32>(gw + (8 + j) * 288, pk(f.x, f.y));
                }
            } else if (t == 1) {
#pragma unroll
                for (int j = 0; j < 4; j++) {
                    f2 f = cmul(e[2 * j], z256);
                    lds_st<u32>(gw + j * 288, pk(f.x, f.y));
                    f = cmul(f, z128);
                    lds_st<u32>(gw + (4 + j) * 288, pk(f.x, f.y));
                }
#pragma unroll
                for (int j = 0; j < 2; j++) {
                    f2 f = cmul(e[4 * j], z512);
#pragma unroll
                    for (int k = 0; k < 4; k++) {
                        lds_st<u32>(gw + (8 + j + 2 * k) * 288, pk(f.x, f.y));
                        if (k < 3) f = cmul(f, z128);
                    }
                }
            } else {
                f2 f0 = cmul(C, z1024), f1 = cmul(f0, z128);
#pragma unroll
                for (int k = 0; k < 9; k += 2) {
                    lds_st<u32>(gw + k * 288, pk(f0.x, f0.y));
                    if (k < 8) { lds_st<u32>(gw + (k + 1) * 288, pk(f1.x, f1.y)); f0 = cmul(f0, z256); }
                    if (k < 6) f1 = cmul(f1, z256);
                }
            }
            f4 acc = Z;
#pragma unroll
            for (int s = 0; s < 4; s++) acc = mfma(lds_ld<u4>(gr + 64 * s), bfr[s], acc);
            if (t == 0) lds_st<f4>(q + (4 * (g & 1) + 128 * (g >> 1) + 8 * c16) * 4, acc);
            else if (t == 1) {
                if (g < 2) lds_st<f4>(q + (256 + 64 * g + 4 * c16) * 4, acc);
                else {
                    const int o = q + (384 + 64 * (g - 2) + 2 * c16) * 4;
                    lds_st<f2>(o, f2{acc[0], acc[1]}); lds_st<f2>(o + 128, f2{acc[2], acc[3]});
                }
            } else {
                if (g < 2) {
#pragma unroll
                    for (int i = 0; i < 4; i++) lds_st<float>(q + (512 + 64 * g + 16 * i + c16) * 4, acc[i]);
                } else if (lane == 32) lds_st<float>(q + 640 * 4, acc[0]);
            }
        }
    }
    STAMP(2);
    bar();
    STAMP(11);
    {
        const LAS float* qa = (const LAS float*)(u32)(LA_Q + ch * 5152);
        const LAS float* qb = qa + 644;
        const int kE = LA_K + ch * 16640, kO = kE + 4160;
        if (hf == 0) {
            const int j = 512 + 2 * lane;
            const float q0 = qa[j] + qb[j], q1 = qa[j + 1] + qb[j + 1], q2 = qa[j + 2] + qb[j + 2];
            const u32 a = pk(q2, q2), b = pk(q1, q1), c = pk(q0, q0);
            const int y = (504 - 8 * lane) * 4;
            put8(kE + y, u4{a, a, b, b}, u4{b, pk(q1, q0), c, c});
            put8(kO + y, u4{a, pk(q2, q1), b, b}, u4{b, c, c, c});
            if (lane < 36) {
                const int cp = lane / 9;
                if (cp >= 2 || lane != 9 * cp) lds_st<u32>(kE + cp * 4160 + (1023 + lane - 9 * cp) * 4, 0u);
            }
        } else {
            {
                const int j = 384 + 2 * lane;
                const float q0 = qa[j] + qb[j], q1 = qa[j + 1] + qb[j + 1], q2 = qa[j + 2] + qb[j + 2];
                const u32 b = pk(q1, q1), c = pk(q0, q0);
                const int y = (764 - 4 * lane) * 4;
                put4(kE + y, u4{pk(q2, q2), b, pk(q1, q0), c});
                put4(kO + y, u4{pk(q2, q1), b, c, c});
            }
            {
                const int j = 256 + 2 * lane;
                const float q0 = qa[j] + qb[j], q1 = qa[j + 1] + qb[j + 1], q2 = qa[j + 2] + qb[j + 2];
                const float qm = lane ? q0 : qa[255] + qb[255];
                const int y = (894 - 2 * lane) * 4;
                put2(kE + y, pk(q2, q1), pk(q0, q0));
                put2(kO + y, pk(q1, q0), pk(q0, qm));
            }
            {
                const f4 k = *(const LAS f4*)(qa + 4 * lane) + *(const LAS f4*)(qb + 4 * lane);
                const float km = lane ? qa[4 * lane - 1] + qb[4 * lane - 1] : 0.f;
                const int y = (1022 - 2 * lane) * 4;
                put2(kE + y, pk(k[3], k[2]), pk(k[1], k[0]));
                put2(kO + y, pk(k[2], k[1]), pk(k[0], km));
            }
        }
    }
    STAMP(12);
    bar();
    STAMP(3);
    const int x0 = 2031 - c16 + 8 * g;
    int ka = LA_K + ch * 16640 + (x0 & 3) * 4160 + (x0 >> 2) * 8 - 256 * hf - 192 - 3584;
    int ubs = LA_U + ch * 4608 + 480 + (16 * c16 + 8 * g - 16) * 2 - 256 * hf - 192;
    pin(ka); pin(ubs);
    f4 acc[4];
#define KFR(rho, p) ld_k(ka + 192 - 64 * (rho) + 3584 - 512 * (p))
#define UFR(rho, q) lds_ld<u4>(ubs + 192 - 64 * (rho) + 512 * (q))
#define SB __builtin_amdgcn_sched_barrier(0)
#define TRIANGLE(q0)                                                                          \
    {                                                                                         \
        u4 A[2][4], B[2][4];                                                                  \
        _Pragma("unroll") for (int p = 0; p < 4; p++) { B[0][p] = UFR(0, q0 + p); A[0][p] = KFR(0, p); } \
        _Pragma("unroll") for (int rho = 0; rho < 4; rho++)                                   \
            _Pragma("unroll") for (int p = 0; p < 4; p++) {                                   \
                SB;                                                                           \
                if (rho < 3) { B[(rho + 1) & 1][p] = UFR(rho + 1, q0 + p); A[(rho + 1) & 1][p] = KFR(rho + 1, p); } \
                _Pragma("unroll") for (int q = 0; p + q < 4; q++) acc[p + q] = mfma(A[rho & 1][p], B[rho & 1][q], acc[p + q]); \
            }                                                                                 \
        SB;                                                                                   \
    }
#define EXCHANGE(mine, other, str, S1)                                                        \
    {                                                                                         \
        if (hf) { lds_st<f4>((mine) + lane * 16, acc[0]); lds_st<f4>((mine) + (str) + lane * 16, acc[1]); acc[0] = acc[2]; acc[1] = acc[3]; } \
        else { lds_st<f4>((mine) + lane * 16, acc[2]); lds_st<f4>((mine) + (str) + lane * 16, acc[3]); } \
        STAMP(S1);                                                                            \
        bar();                                                                                \
        const f4 y0 = lds_ld<f4>((other) + lane * 16) + acc[0], y1 = lds_ld<f4>((other) + (str) + lane * 16) + acc[1]; \
        const int w = (other) + (16 * c16 + 4 * g) * 2;                                       \
        lds_st<u2>(w, u2{pk(y0[0], y0[1]), pk(y0[2], y0[3])});                                \
        lds_st<u2>(w + (str), u2{pk(y1[0], y1[1]), pk(y1[2], y1[3])});                        \
    }
#pragma unroll
    for (int i = 0; i < 4; i++) acc[i] = Z;
    TRIANGLE(0);
    EXCHANGE(LA_Q + wv * 2048, LA_Q + (wv ^ 1) * 2048, 1024, 4);
#pragma unroll
    for (int i = 0; i < 4; i++) acc[i] = Z;
    {
        u4 A[8], B[2][4];
#pragma unroll
        for (int p = 0; p < 4; p++) B[0][p] = UFR(0, p);
#pragma unroll
        for (int p = 1; p < 8; p++) A[p] = KFR(0, p);
        bar();
        STAMP(13);
#pragma unroll
        for (int rho = 0; rho < 4; rho++)
#pragma unroll
            for (int p = 1; p < 8; p++) {
                SB;
                if (rho < 3 && p < 5) B[(rho + 1) & 1][p - 1] = UFR(rho + 1, p - 1);
#pragma unroll
                for (int q = 0; q < 4; q++)
                    if (p + q >= 4 && p + q < 8) acc[p + q - 4] = mfma(A[p], B[rho & 1][q], acc[p + q - 4]);
                SB;
                if (rho < 3) A[p] = KFR(rho + 1, p);
            }
    }
    STAMP(14);
    pin(acc[0]); pin(acc[1]); pin(acc[2]); pin(acc[3]);
    while (__builtin_amdgcn_readfirstlane(*(const volatile LAS u32*)(u32)LA_F) < 4) __builtin_amdgcn_s_sleep(1);
    asm volatile("" ::: "memory");
    TRIANGLE(4);
    EXCHANGE(LA_K + ch * 16640 + 2 * hf * 4160, LA_K + ch * 16640 + 2 * (hf ^ 1) * 4160, 4160, 5);
    bar();
    STAMP(6);
    STAMP_OUT(0);
}

__global__ void __launch_bounds__(NT) fused_kernel(const u16* __restrict__ u, const u16* __restrict__ lar, const u16* __restrict__ aimg,
                                                   const u16* __restrict__ cre, const u16* __restrict__ cim, const u16* __restrict__ ldt,
                                                   const u16* __restrict__ dsk, const u16* __restrict__ x, const u16* __restrict__ wr,
                                                   const u16* __restrict__ wi, u16* __restrict__ out, char* ws) {
    if (__builtin_expect(blockIdx.x < NB2, 1)) head_b(x, wr, wi, out, ws);
    else if (blockIdx.x >= A0) head_a(u, lar, aimg, cre, cim, ldt, dsk, out, ws);
    else if (threadIdx.x < 448) { u32 t = *(const u32*)((const char*)&TAB + F_F16 * 1024 + threadIdx.x * 128); asm volatile("" :: "v"(t)); }
}

extern "C" void kernel_launch(void* const* d_in, const int* in_sizes, int n_in, void* d_out, int out_size, void* d_ws,
                              size_t ws_size, hipStream_t stream) {
    (void)hipFuncSetAttribute((const void*)fused_kernel, hipFuncAttributeMaxDynamicSharedMemorySize, LDS_BYTES);
    fused_kernel<<<GRID, NT, LDS_BYTES, stream>>>((const u16*)d_in[0], (const u16*)d_in[1], (const u16*)d_in[2], (const u16*)d_in[3],
                                                  (const u16*)d_in[4], (const u16*)d_in[5], (const u16*)d_in[6], (const u16*)d_in[7],
                                                  (const u16*)d_in[8], (const u16*)d_in[9], (u16*)d_out, (char*)d_ws);
}
```

```cpp
#include <hip/hip_runtime.h>

typedef unsigned short u16;
typedef unsigned int u32;
typedef float f2 __attribute__((ext_vector_type(2)));
typedef float f4 __attribute__((ext_vector_type(4)));
typedef u32 u2 __attribute__((ext_vector_type(2)));
typedef u32 u4 __attribute__((ext_vector_type(4)));
typedef int i8v __attribute__((ext_vector_type(8)));
typedef short s4 __attribute__((ext_vector_type(4)));
typedef short s8 __attribute__((ext_vector_type(8)));
typedef __bf16 bf2 __attribute__((ext_vector_type(2)));
#define DI __device__ __forceinline__
#define LAS __attribute__((address_space(3)))
#define GAS __attribute__((address_space(1)))
#define CAS __attribute__((address_space(4)))
#pragma clang diagnostic ignored "-Wint-to-pointer-cast"
struct u3 { u32 v[3]; DI u32& operator[](int i) { return v[i]; } DI const u32& operator[](int i) const { return v[i]; } };

constexpr int SEQ = 2048, DM = 576, NS = 128, DF = 289;
constexpr int NB1 = 128;
constexpr int NB2 = 145;
constexpr int A0 = 152;
constexpr int NA = 96;
constexpr int GRID = A0 + NA;
constexpr int NT = 1024;

constexpr u32 WS_EPOCH = 0, WS_F1 = 4096, WS_F2 = WS_F1 + 32768, WS_X1 = 1u << 20, WS_Y1 = 4u << 20, WS_USED = 8u << 20;
#define STAMP(i)
#define STAMP_DECL
#define STAMP_OUT(T)

constexpr int F_SA = 0, F_EO = 4;
constexpr int F_ARE = 9, F_AIM = 10, F_BRE = 11, F_BIM = 12, F_G = 13;
constexpr int F_F16 = 21, F_GI = 23, F_AI = 31, F_M1C = 33;
constexpr int F_VAR = 37;
constexpr int F_END = F_VAR + 40;
constexpr int ET = 452, ER = 28, E2V = 580, E2U = 36, OP = 1168;
constexpr int LB_X = 0, LB_I = 0, LB_Y = 0;
constexpr int LB_E = 16 * OP;
constexpr int LB_O = LB_E, LB_E2 = LB_E;
constexpr int LB_S = LB_E + 16 * ET * 4;
constexpr int LB_W = LB_S + 1200 * 16;
constexpr int LB_T = LB_W + 2 * 256 * 36;
constexpr int LB_END = LB_T + (F_VAR + 16) * 1024;
struct CosTab { double c[2048]; double c9[9], s9[9]; };
constexpr double tay_cos(double x) { double t = 1, s = 1; for (int k = 1; k <= 14; k++) { t *= -x * x / ((2 * k - 1) * (2 * k)); s += t; } return s; }
constexpr double tay_sin(double x) { double t = x, s = x; for (int k = 1; k <= 14; k++) { t *= -x * x / ((2 * k + 1) * (2 * k)); s += t; } return s; }
constexpr CosTab make_cos() {
    CosTab t = {};
    const double PI2 = 6.283185307179586476925;
    for (int k = 0; k <= 512; k++) {
        const double v = k == 512 ? 0.0 : (k <= 256 ? tay_cos(PI2 * k / 2048) : tay_sin(PI2 * (512 - k) / 2048));
        t.c[k] = v; t.c[(2048 - k) & 2047] = v; t.c[1024 - k] = -v; t.c[(1024 + k) & 2047] = -v;
    }
    t.c[512] = 0.0; t.c[1536] = 0.0;
    for (int k = 0; k < 9; k++) { const double a = PI2 * (k <= 4 ? k : k - 9) / 9; t.c9[k] = tay_cos(a); t.s9[k] = tay_sin(a); }
    return t;
}
constexpr CosTab CT = make_cos();
constexpr double cs(int n) { return CT.c[n & 2047]; }
constexpr double sn(int n) { return CT.c[(n - 512) & 2047]; }
constexpr u32 bfr(double x) { const u32 b = __builtin_bit_cast(u32, (float)x); return (b + 0x7fffu + ((b >> 16) & 1u)) >> 16; }
constexpr u32 pkc(double lo, double hi) { return bfr(lo) | (bfr(hi) << 16); }
constexpr u32 f32c(double x) { return __builtin_bit_cast(u32, (float)x); }
template <int N> struct Frags { u32 w[N * 256]; };
constexpr u32 w_re(double amp, int n) { return pkc(amp * cs(n), amp * sn(n)); }
constexpr u32 w_im(double amp, int n) { return pkc(-amp * sn(n), amp * cs(n)); }
constexpr Frags<F_VAR> make_t0() {
    Frags<F_VAR> t = {};
    const double R2 = 1.4142135623730951;
    for (int l = 0; l < 64; l++)
        for (int i = 0; i < 4; i++) {
            const int c = l & 15, g = l >> 4, r = 4 * g + i, o = l * 4 + i;
            {
                const int b = c >> 2, tq = c & 3;
                const double amp = b ? 1.0 : R2;
                const int n0 = 512 * tq * g + (b ? 256 * b * (2 * i) : -256), n1 = 512 * tq * g + (b ? 256 * b * (2 * i + 1) : 256);
                t.w[F_ARE * 256 + o] = pkc(amp * cs(n0), amp * cs(n1));
                t.w[F_AIM * 256 + o] = pkc(-amp * sn(n0), -amp * sn(n1));
            }
            for (int w = 0; w < 9; w++) {
                const int sh = 16 * (i & 1), oo = w * 128 + l * 2 + (i >> 1);
                t.w[F_EO * 256 + oo] |= (u32)((c * ET + r * ER + 2 * ((7 * w + r) % 9)) * 4) << sh;
                if (w < 8) {
                    const int k = (9 * (w & 3) + 36 * r + 64 * (c % 9)) % DM, slot = w < 4 ? (k <= 288 ? 2 * k : 2 * (DM - k) + 1) : k;
                    t.w[F_SA * 256 + oo] |= (u32)(slot * 8 + (slot >> 6) * 16) << sh;
                }
            }
            if (r < 9) { const int k = (r * c) % 9; t.w[F_BRE * 256 + o] = pkc(CT.c9[k], CT.s9[k]); t.w[F_BIM * 256 + o] = pkc(-CT.s9[k], CT.c9[k]); }
            for (int q = 0; q < 4; q++) {
                t.w[(F_G + 2 * q) * 256 + o] = w_re(1.0, 32 * r * (q + 4 * c)); t.w[(F_G + 2 * q + 1) * 256 + o] = w_im(1.0, 32 * r * (q + 4 * c));
                t.w[(F_GI + 2 * q) * 256 + o] = w_re(1.0, 32 * c * (q + 4 * r)); t.w[(F_GI + 2 * q + 1) * 256 + o] = w_im(1.0, 32 * c * (q + 4 * r));
            }
            t.w[F_F16 * 256 + o] = w_re(1.0, 128 * r * c); t.w[(F_F16 + 1) * 256 + o] = w_im(1.0, 128 * r * c);
            for (int h = 0; h < 2; h++) {
                const int j = 4 * h + (c >> 2), n = 512 * i * (c & 3) + (g ? 256 * g * j : ((j & 1) ? 256 : -256));
                t.w[(F_AI + h) * 256 + o] = w_re(g ? 2.0 : R2, n);
                const int m = 16 * r * (4 * h + 8 * c) - 512 * h;
                t.w[(F_M1C + 2 * h) * 256 + o] = pkc(0.5 * cs(m), -0.5 * sn(m));
                t.w[(F_M1C + 2 * h + 1) * 256 + o] = pkc(-0.5 * sn(m), -0.5 * cs(m));
            }
        }
    return t;
}
constexpr Frags<40> make_t1() {
    Frags<40> t = {};
    for (int v = 0; v < 5; v++)
        for (int l = 0; l < 64; l++)
            for (int i = 0; i < 4; i++) {
                const int be = v < 3 ? v + 1 : 4 * (v - 3), c = l & 15, r = 4 * (l >> 4) + i, o = v * 2048 + l * 4 + i;
                const double amp = v < 3 ? 1.0 : 0.5;
                const int n1 = 16 * r * (be + 8 * c) + (v == 4 ? 512 : 0), n4 = 16 * c * (be + 8 * r);
                t.w[o] = w_re(amp, n1); t.w[o + 256] = w_im(amp, n1);
                t.w[o + 512] = w_re(1.0, n4); t.w[o + 768] = w_im(1.0, n4);
                t.w[o + 1024] = f32c(cs(r * (be + 8 * c))); t.w[o + 1280] = f32c(-sn(r * (be + 8 * c)));
                t.w[o + 1536] = f32c(cs(c * (be + 8 * r))); t.w[o + 1792] = f32c(-sn(c * (be + 8 * r)));
            }
    return t;
}
struct WgPar { int unit, w0, wm, wh, tvar, kind, kbase, bq; };
struct WgTab { WgPar p[NB2]; };
constexpr WgTab make_wg() {
    WgTab t = {};
    for (int id = 0; id < NB2; id++) {
        const int u = (id & 7) * 18 + ((id & 7) ? 1 : 0) + (id >> 3);
        const bool star = u >= 108;
        const int b = star ? 0 : 1 + u / 36, kg = star ? u - 108 : u % 36;
        const bool mir = !star && kg >= 18;
        WgPar& p = t.p[id];
        p.unit = u; p.bq = b; p.kind = star ? 2 : (mir ? 1 : 0);
        p.w0 = 2 * (star ? (kg < 36 ? 8 * kg : 281) : (mir ? (2048 - b) * DF + DM - 16 * kg - 15 : b * DF + 16 * kg));
        p.wm = 2 * (mir ? -8 * DF : 8 * DF); p.wh = 2 * (star ? 4 * DF : 8);
        p.tvar = (F_VAR + 8 * (star ? 3 : b - 1)) * 1024; p.kbase = star ? 8 * kg : 16 * kg;
    }
    return t;
}
struct Tables { Frags<F_VAR> a; Frags<40> b; WgTab w; };
constexpr Frags<F_VAR> T0 = make_t0();
constexpr Frags<40> T1 = make_t1();
constexpr WgTab T3 = make_wg();
static __device__ const __attribute__((aligned(128))) Tables TAB = {T0, T1, T3};
constexpr int LA_K = 0;
constexpr int LA_U = LA_K + 6 * 16640;
constexpr int LA_Q = LA_U + 6 * 4608;
constexpr int LA_F = LA_Q + 12 * 644 * 4;
constexpr int LA_END = LA_F + 16;
constexpr int LDS_BYTES = (LA_END > LB_END ? LA_END : LB_END);
static_assert(LDS_BYTES <= 160 * 1024 && LDS_BYTES > 80 * 1024, "one workgroup per CU");

DI u32 pk(float a, float b) { f2 v = {a, b}; return __builtin_bit_cast(u32, __builtin_convertvector(v, bf2)); }
DI float blo(u32 v) { return __uint_as_float(v << 16); }
DI float bhi(u32 v) { return __uint_as_float(v & 0xffff0000u); }
DI float bf2f(u16 v) { return __uint_as_float((u32)v << 16); }
DI float rbf(float x) { return blo(pk(x, x)); }
DI f2 cmul(f2 a, f2 b) { f2 t = a.xx * b; f2 s = {-b.y, b.x}; return __builtin_elementwise_fma(a.yy, s, t); }
DI f4 mfma(u4 a, u4 b, f4 c) { return __builtin_amdgcn_mfma_f32_16x16x32_bf16(__builtin_bit_cast(s8, a), __builtin_bit_cast(s8, b), c, 0, 0, 0); }
DI u4 pack4(f4 re, f4 im) { u4 r = {pk(re[0], im[0]), pk(re[1], im[1]), pk(re[2], im[2]), pk(re[3], im[3])}; return r; }
DI u4 twid(f4 re, f4 im, f4 wr, f4 wi) { return pack4(__builtin_elementwise_fma(re, wr, -(im * wi)), __builtin_elementwise_fma(re, wi, im * wr)); }
DI int mod9(int x) { return x - 9 * ((x * 57) >> 9); }
template <class T> DI void pin(T& v) { asm volatile("" : "+v"(v)); }

template <class T> DI T lds_ld(int off) { return *(const LAS T*)(u32)off; }
template <class T> DI void lds_st(int off, T v) { *(LAS T*)(u32)off = v; }
DI void bar() { __syncthreads(); }
DI void glds16(const void* g, u32 off, int lds) {
    u32 keep;
    asm volatile("s_mov_b32 %0, m0\n\ts_mov_b32 m0, %3\n\ts_nop 4\n\tglobal_load_lds_dwordx4 %1, %2\n\ts_mov_b32 m0, %0" : "=&s"(keep) : "v"(off), "s"(g), "s"(lds) : "memory");
}

template <int N> DI bool wait_flags(__amdgpu_buffer_rsrc_t r, u32 base, u32 tag, int lane) {
    const unsigned long long t0 = __builtin_amdgcn_s_memrealtime();
    const int o2 = (lane + 128 < N ? lane + 128 : lane) * 128;
    for (;;) {
        const u32 a = __builtin_amdgcn_raw_buffer_load_b32(r, base + lane * 128, 0, 16);
        const u32 b = __builtin_amdgcn_raw_buffer_load_b32(r, base + (lane + 64) * 128, 0, 16);
        const u32 c = N > 128 ? __builtin_amdgcn_raw_buffer_load_b32(r, base + o2, 0, 16) : tag;
        if (__all((a == tag) & (b == tag) & (c == tag))) return true;
        if (__builtin_amdgcn_s_memrealtime() - t0 > 5000000ull) return false;
        asm volatile("" ::: "memory");
    }
}

DI void head_b(const u16* __restrict__ x, const u16* __restrict__ wr, const u16* __restrict__ wi,
               u16* __restrict__ out, char* ws) {
    const int tid = threadIdx.x, lane = tid & 63, c16 = lane & 15, g = lane >> 4;
    const int wv = __builtin_amdgcn_readfirstlane(tid >> 6);
    const int id = blockIdx.x;
    STAMP_DECL;
    const bool rows = id < NB1;
    const int s00 = 32 * (id >> 4) + (id & 15);
    const char* tab = (const char*)&TAB;
    const int R = (tid * 911) >> 16;
    const u32 xoff = (s00 + 16 * (R >> 3) + 256 * (R & 7)) * (2 * DM) + (tid - R * 72) * 16;
    asm volatile("" :: "s"(x), "s"(wr), "s"(wi), "s"(out), "s"(ws), "v"(xoff));
    const __amdgpu_buffer_rsrc_t rws = __builtin_amdgcn_make_buffer_rsrc(ws, 0, WS_USED, 0x00020000);
    u32 tag = 0;
    if (wv == 0) asm volatile("global_load_dword %0, %0, %1 sc1" : "+v"(tag) : "s"(ws) : "memory");
    if (rows) {
        glds16(x, xoff, LB_X + wv * 1024);
        if (wv < 2) glds16(x, (s00 + 16 + 1536) * (2 * DM) + (tid < 56 ? (tid + 16) * 16 : 256 * 2 * DM + (tid - 56) * 16), LB_X + 16384 + wv * 1024);
        else glds16(tab, tid * 16 - 2048, LB_T + (wv - 2) * 1024);
        if (wv >= 9) glds16(tab, tid * 16 + 5 * 1024, LB_T + (wv + 5) * 1024);
    }
    asm volatile("" : "+s"(tab));
    const WgPar par = __builtin_bit_cast(WgPar, *(const CAS i8v*)(tab + F_END * 1024 + id * 32));
    u4 wreg;
    __builtin_memcpy(&wreg, (const char*)((wv >> 3) ? wi : wr) + (u32)(par.w0 + ((tid >> 1) & 255) * par.wm + (tid & 1) * par.wh), 16);
    __builtin_amdgcn_sched_barrier(0);
    const f4 Z = {0.f, 0.f, 0.f, 0.f};
    const int u2i = par.unit, bq = par.bq;
    const bool star = par.kind == 2, mir = par.kind == 1;
    const int tau = wv & 3, bw = wv >> 2;
    int tl = LB_T + lane * 16;
    int lin = LB_S + (tid + (tid >> 5)) * 16;
    pin(tl); pin(lin);
    int sadr[4];

    if (rows) {
        if (tid < 256) {
            const int o = LB_E + 72 + (tid & 15) * (ER * 4) + (tid >> 4) * (ET * 4);
            lds_st<u2>(o, u2{0u, 0u}); lds_st<u4>(o + 8, u4{0u, 0u, 0u, 0u});
        }
        int xo = LB_X + (c16 >> 2) * 1152 + 2 * (144 * ((g - wv) & 3) + 16 * wv + 4 * (c16 & 3));
        int bo = LB_E + (wv * ET + c16 * ER + min(8 * g, 16)) * 4;
        int go = WS_X1 + (tid >> 3) * 16384 + id * 128 + (tid & 7) * 16;
        int ta = LB_T + lane * 8;
        pin(xo); pin(bo); pin(go); pin(ta);
        STAMP(1);
        asm volatile("s_waitcnt vmcnt(1) lgkmcnt(0)\n\ts_barrier" ::: "memory");
        STAMP(2);
        if (wv < 9) {
            u4 d[2];
#pragma unroll
            for (int s = 0; s < 2; s++) {
                const s4 lo = __builtin_amdgcn_ds_read_tr16_b64_v4i16((LAS s4*)(u32)(xo + s * 9216));
                const s4 hi = __builtin_amdgcn_ds_read_tr16_b64_v4i16((LAS s4*)(u32)(xo + s * 9216 + 4608));
                d[s] = __builtin_bit_cast(u4, __builtin_shufflevector(lo, hi, 0, 1, 2, 3, 4, 5, 6, 7));
            }
            const u4 are = lds_ld<u4>(tl + F_ARE * 1024), aim = lds_ld<u4>(tl + F_AIM * 1024);
            const u2 eo = lds_ld<u2>(ta + F_EO * 1024 + wv * 512);
#pragma unroll
            for (int s = 0; s < 2; s++) d[s] = pack4(mfma(d[s], are, Z), mfma(d[s], aim, Z));
#pragma unroll
            for (int i = 0; i < 4; i++) lds_st<u2>(LB_E + ((i & 1) ? eo[i >> 1] >> 16 : eo[i >> 1] & 0xffff), u2{d[0][i], d[1][i]});
        }
        u4 bre = lds_ld<u4>(tl + F_BRE * 1024), bim = lds_ld<u4>(tl + F_BIM * 1024);
        u4 gre = lds_ld<u4>(tl + (F_G + 2 * tau) * 1024), gim = lds_ld<u4>(tl + (F_G + 2 * tau + 1) * 1024);
        {
            const u2 sa = lds_ld<u2>(ta + F_SA * 1024 + (tau + (bw ? 4 : 0)) * 512);
            const int base = LB_S + ((bw + 3) & 3) * (DM * 8 + 144);
#pragma unroll
            for (int i = 0; i < 4; i++) { sadr[i] = base + ((i & 1) ? sa[i >> 1] >> 16 : sa[i >> 1] & 0xffff); pin(sadr[i]); }
        }
        pin(bre); pin(bim); pin(gre); pin(gim);
        STAMP(12);
        bar();
        STAMP(13);
        {
            const u4 p0 = lds_ld<u4>(bo), p1 = lds_ld<u4>(bo + 16);
            u4 d[2];
#pragma unroll
            for (int s = 0; s < 2; s++) {
                const u4 a = {p0[s], p0[2 + s], p1[s], p1[2 + s]};
                const u4 t = pack4(mfma(a, bre, Z), mfma(a, bim, Z));
                d[s] = pack4(mfma(gre, t, Z), mfma(gim, t, Z));
            }
            if (c16 < 9) {
#pragma unroll
                for (int i = 0; i < 4; i++) lds_st<u2>(sadr[i], u2{d[0][i], d[1][i]});
                if (wv == 0 && c16 == 0 && !(g & 1)) lds_st<u2>(sadr[0] + 8, u2{d[0][0], d[1][0]});
            }
        }
        STAMP(14);
        bar();
        STAMP(3);
        __builtin_amdgcn_raw_buffer_store_b128(lds_ld<u4>(lin), rws, go, 0, 16);
        if (tid <= 128) __builtin_amdgcn_raw_buffer_store_b128(lds_ld<u4>(lin + 1056 * 16), rws, go, 128 * 16384, 16);
    } else {
        if (wv < 7) { u32 t = *(const GAS u32*)(tab + F_F16 * 1024 + (u32)(tid * 128)); asm volatile("" :: "v"(t)); }
    }
    {
        const int o = LB_W + (tid >> 1) * 36 + (tid & 1) * 16;
        lds_st<u32>(o, wreg[0]); lds_st<u32>(o + 4, wreg[1]); lds_st<u32>(o + 8, wreg[2]); lds_st<u32>(o + 12, wreg[3]);
    }
    asm volatile("s_waitcnt vmcnt(0)" : "+v"(tag) :: "memory");
    tag += 0x9E3779B1u;
    bar();
    STAMP(4);
    if (rows && tid == 0) __builtin_amdgcn_raw_buffer_store_b32(tag, rws, WS_F1 + id * 128, 0, 16);
    glds16(tab, F_F16 * 1024 + tid * 16, LB_T + F_F16 * 1024 + wv * 1024);
    if (star || wv < 8) glds16(tab, par.tvar + tid * 16, LB_T + F_VAR * 1024 + wv * 1024);
    asm volatile("s_waitcnt vmcnt(0)\n\ts_barrier" ::: "memory");

    const int typ = star ? (wv & 1) : 0;
    const int tv = tl + (F_VAR + 8 * typ) * 1024;
    u4 fre = lds_ld<u4>(tl + F_F16 * 1024), fim = lds_ld<u4>(tl + (F_F16 + 1) * 1024);
    u4 m1r = lds_ld<u4>(tv), m1i = lds_ld<u4>(tv + 1024), m4r = lds_ld<u4>(tv + 2048), m4i = lds_ld<u4>(tv + 3072);
    f4 twr = lds_ld<f4>(tv + 4096), twi = lds_ld<f4>(tv + 5120), tur = lds_ld<f4>(tv + 6144), tui = lds_ld<f4>(tv + 7168);
    u4 mcr = lds_ld<u4>(tl + (F_M1C + 2 * typ) * 1024), mci = lds_ld<u4>(tl + (F_M1C + 2 * typ + 1) * 1024);
    f4 wfr, wfi;
    {
        const int kcol = par.kbase + (star ? (wv >> 1) : wv);
        const int cc = star ? 8 * typ + (u2i < NB2 - 1 ? (wv >> 1) : 7) : (mir ? 15 - wv : wv);
        const int wo = LB_W + (c16 + 64 * g) * 36 + cc * 2;
#pragma unroll
        for (int i = 0; i < 4; i++) {
            wfr[i] = bf2f(lds_ld<u16>(wo + i * 576));
            wfi[i] = bf2f(lds_ld<u16>(wo + i * 576 + 9216));
        }
        float sc = 1.f / (float)(SEQ * DM);
        if (kcol == 0 || kcol == 288) {
            sc *= 0.5f;
#pragma unroll
            for (int i = 0; i < 4; i++) {
                const int m = c16 + 64 * g + 16 * i;
                const int orow = (mir ? bq + 8 * m : 2048 - (star ? 4 * typ : bq) - 8 * m) & 2047;
                wfr[i] += bf2f(wr[orow * DF + kcol]);
                wfi[i] -= bf2f(wi[orow * DF + kcol]);
            }
        }
        wfr *= sc; wfi *= (mir ? -sc : sc);
    }
    f4 wnr = -wfr, wni = -wfi;
    int io = (32 * g + c16) * 128 + (((wv >> 1) ^ (c16 >> 1)) * 16);
    int so = (tid >> 3) * 128 + (((tid & 7) ^ ((tid >> 4) & 7)) * 16);
    pin(fre); pin(fim); pin(m1r); pin(m1i); pin(m4r); pin(m4i); pin(twr); pin(twi); pin(tur); pin(tui); pin(mcr); pin(mci);
    int gi = WS_X1 + u2i * 16384 + tid * 16, gq = WS_Y1 + (tid >> 3) * (NB2 * 128) + u2i * 128 + (tid & 7) * 16;
    int i1 = LB_I + io + (star ? 0 : (wv & 1) * 8);
    io += LB_O + (wv & 1) * 8;
    pin(wfr); pin(wnr); pin(wni); pin(io); pin(so); pin(gi); pin(gq); pin(i1);
    if (wv == 0) wait_flags<NB1>(rws, WS_F1, tag, lane);
    bar();
    STAMP(5);
    lds_st<u4>(LB_I + so, __builtin_amdgcn_raw_buffer_load_b128(rws, gi, 0, 16));
    bar();
    STAMP(6);
    {
        const int o1 = i1;
        const u2 a0 = lds_ld<u2>(o1), a1 = lds_ld<u2>(o1 + 2048);
        const u4 a = {a0[0], a0[1], a1[0], a1[1]};
        f4 re = mfma(a, m1r, Z), im = mfma(a, m1i, Z);
        if (star) {
            const u2 b0 = lds_ld<u2>(o1 + 8), b1 = lds_ld<u2>(o1 + 8 + 2048);
            const u4 b = {b0[0], b0[1], b1[0], b1[1]};
            re = mfma(b, mcr, re); im = mfma(b, mci, im);
        }
        u4 t = twid(re, im, twr, twi);
        re = mfma(fre, t, Z); im = mfma(fim, t, Z);
        t = pack4(__builtin_elementwise_fma(re, wfr, im * wni), __builtin_elementwise_fma(re, wni, im * wnr));
        re = mfma(t, fre, Z); im = mfma(t, fim, Z);
        t = twid(re, im, tur, tui);
        t = pack4(mfma(m4r, t, Z), mfma(m4i, t, Z));
        lds_st<u2>(io, u2{t[0], t[1]}); lds_st<u2>(io + 2048, u2{t[2], t[3]});
    }
    bar();
    STAMP(7);
    {
        u4 v = lds_ld<u4>(LB_O + so);
        if (star) {
            u4 w;
#pragma unroll
            for (int s = 0; s < 2; s++) {
                const float ar = blo(v[s]), ai = bhi(v[s]), cr = blo(v[2 + s]), ci = bhi(v[2 + s]);
                w[s] = pk(ar + ci, ai - cr); w[2 + s] = pk(ar - ci, -ai - cr);
            }
            v = w;
        }
        if (u2i < NB2 - 1 || (tid & 7) == 0)
            __builtin_amdgcn_raw_buffer_store_b128(v, rws, gq, 0, 16);
    }
    asm volatile("s_waitcnt vmcnt(0)" ::: "memory");
    bar();
    STAMP(8);
    if (tid == 0) __builtin_amdgcn_raw_buffer_store_b32(tag, rws, WS_F2 + id * 128, 0, 16);
    if (!rows) { STAMP_OUT(0); return; }

    u4 gre = lds_ld<u4>(tl + (F_GI + 2 * tau) * 1024), gim = lds_ld<u4>(tl + (F_GI + 2 * tau + 1) * 1024);
    u4 bre = lds_ld<u4>(tl + F_BRE * 1024), bim = lds_ld<u4>(tl + F_BIM * 1024);
    u4 ai0 = lds_ld<u4>(tl + F_AI * 1024), ai1 = lds_ld<u4>(tl + (F_AI + 1) * 1024);
    int eadr[4];
#pragma unroll
    for (int i = 0; i < 4; i++) {
        const int r = 4 * g + i;
        eadr[i] = LB_E2 + (mod9(4 * (c16 + 18 - r)) * E2V + r * E2U + 2 * wv) * 4;
        pin(eadr[i]);
    }
    int ao = LB_E2 + (wv * E2V + c16 * E2U + 8 * g) * 4;
    int gy = WS_Y1 + id * (NB2 * 128) + tid * 16;
    int yo = LB_Y + (c16 >> 2) * OP + 2 * (144 * (((c16 & 3) - wv) & 3) + 16 * wv + 4 * g);
    int yl = LB_Y + tid * 16 + R * (OP - 1152);
    u32 oo = 2 * xoff - (tid - R * 72) * 16 + 2 * DM;
    pin(gre); pin(gim); pin(bre); pin(bim); pin(ai0); pin(ai1); pin(ao); pin(gy); pin(yo); pin(yl); pin(oo);
    if (wv == 0) {
        const bool ok = wait_flags<NB2>(rws, WS_F2, tag, lane);
        if (id == 0 && lane == 0 && ok) __builtin_amdgcn_raw_buffer_store_b32(tag, rws, WS_EPOCH, 0, 16);
    }
    bar();
    STAMP(9);
    {
        const u4 v = __builtin_amdgcn_raw_buffer_load_b128(rws, gy, 0, 16);
        if (tid <= 128) lds_st<u4>(lin + 1056 * 16, __builtin_amdgcn_raw_buffer_load_b128(rws, gy, 16384, 16));
        lds_st<u4>(lin, v);
    }
    bar();
    STAMP(10);
    {
        u4 d[2];
        u2 p[4];
#pragma unroll
        for (int i = 0; i < 4; i++) p[i] = lds_ld<u2>(sadr[i]);
#pragma unroll
        for (int s = 0; s < 2; s++) {
            const u4 a = {p[0][s], p[1][s], p[2][s], p[3][s]};
            const u4 t = pack4(mfma(a, gre, Z), mfma(a, gim, Z));
            d[s] = pack4(mfma(t, bre, Z), mfma(t, bim, Z));
        }
        if (c16 < 9) {
#pragma unroll
            for (int i = 0; i < 4; i++) lds_st<u2>(eadr[i], u2{d[0][i], d[1][i]});
        }
    }
    STAMP(15);
    bar();
    if (wv < 9) {
        const u4 p0 = lds_ld<u4>(ao), p1 = lds_ld<u4>(ao + 16);
#pragma unroll
        for (int s = 0; s < 2; s++) {
            const u4 a = {p0[s], p0[2 + s], p1[s], p1[2 + s]};
            const f4 y0 = mfma(a, ai0, Z), y1 = mfma(a, ai1, Z);
            lds_st<u2>(yo + s * (8 * OP), u2{pk(y0[0], y0[1]), pk(y0[2], y0[3])});
            lds_st<u2>(yo + s * (8 * OP) + 4 * OP, u2{pk(y1[0], y1[1]), pk(y1[2], y1[3])});
        }
    }
    bar();
    const __amdgpu_buffer_rsrc_t rout = __builtin_amdgcn_make_buffer_rsrc(out, 0, SEQ * DM * 4, 0x00020000);
    __builtin_amdgcn_raw_buffer_store_b128(lds_ld<u4>(yl), rout, oo, 0, 16);
    if (wv < 2) {
        const int r2 = tid < 56 ? 14 : 15, c2 = tid + 1024 - 72 * r2;
        __builtin_amdgcn_raw_buffer_store_b128(lds_ld<u4>(LB_Y + r2 * OP + c2 * 16), rout, (s00 + 16 + 256 * (r2 - 8)) * (4 * DM) + 2 * DM + c2 * 16, 0, 16);
    }
    STAMP(11);
    STAMP_OUT(0);
}

DI f2 cexpa(float ar2, float air, float p) {
    const float e = __builtin_amdgcn_exp2f(ar2 * p), t = __builtin_amdgcn_fractf(air * p);
    return f2{e * __builtin_amdgcn_cosf(t), e * __builtin_amdgcn_sinf(t)};
}
DI u4 ld_k(int o) {
    const u2 a = *(const volatile LAS u2*)(u32)o, b = *(const volatile LAS u2*)(u32)(o + 8);
    return u4{a[0], a[1], b[0], b[1]};
}
DI void put8(int o, u4 a, u4 b) {
    lds_st<u4>(o, a); lds_st<u4>(o + 16, b);
    lds_st<u32>(o + 8316, a[0]); lds_st<u4>(o + 8320, u4{a[1], a[2], a[3], b[0]});
    lds_st<u2>(o + 8336, u2{b[1], b[2]}); lds_st<u32>(o + 8344, b[3]);
}
DI void put4(int o, u4 a) {
    lds_st<u4>(o, a);
    lds_st<u32>(o + 8316, a[0]); lds_st<u2>(o + 8320, u2{a[1], a[2]}); lds_st<u32>(o + 8328, a[3]);
}
DI void put2(int o, u32 a, u32 b) { lds_st<u2>(o, u2{a, b}); lds_st<u32>(o + 8316, a); lds_st<u32>(o + 8320, b); }

DI void a_rows_out(int yb, int ystr, const u3* ur, u3 dk, char* o) {
    u2 y[6];
#pragma unroll
    for (int c = 0; c < 6; c++) y[c] = lds_ld<u2>(yb + c * ystr);
#pragma unroll
    for (int r = 0; r < 4; r++) {
        u3 v;
#pragma unroll
        for (int c = 0; c < 3; c++) {
            const u32 s = pk(blo(ur[r][c]) * blo(dk[c]), bhi(ur[r][c]) * bhi(dk[c]));
            const u32 ya = y[2 * c][r >> 1], yc = y[2 * c + 1][r >> 1];
            v[c] = pk(((r & 1) ? bhi(ya) : blo(ya)) + blo(s), ((r & 1) ? bhi(yc) : blo(yc)) + bhi(s));
        }
        { GAS u32* p_ = (GAS u32*)(o + r * (4 * DM)); for (int c_ = 0; c_ < 3; c_++) __hip_atomic_store(p_ + c_, v[c_], __ATOMIC_RELAXED, __HIP_MEMORY_SCOPE_AGENT); }
    }
}

DI void head_a(const u16* __restrict__ u, const u16* __restrict__ lar, const u16* __restrict__ aimg,
               const u16* __restrict__ cre, const u16* __restrict__ cim, const u16* __restrict__ ldt,
               const u16* __restrict__ dsk, u16* __restrict__ out, char* ws) {
    const int tid = threadIdx.x, lane = tid & 63, c16 = lane & 15, g = lane >> 4;
    const int wv = __builtin_amdgcn_readfirstlane(tid >> 6);
    STAMP_DECL;
    const int ai_ = blockIdx.x - A0;
    const int d0 = 6 * ((ai_ & 7) * 12 + (ai_ >> 3));
    const f4 Z = {0.f, 0.f, 0.f, 0.f};

    if (wv >= 12) {
        const int t = tid - 768;
        u3 ua[4], ub[4];
#pragma unroll
        for (int r = 0; r < 4; r++) ua[r] = *(const u3*)(u + (4 * t + r) * DM + d0);
        const u3 dk = *(const u3*)(dsk + d0);
        if (t < 180) lds_st<u4>(LA_U + (t / 30) * 4608 + (t % 30) * 16, u4{0u, 0u, 0u, 0u});
        if (t == 0) lds_st<u32>(LA_F, 0u);
#pragma unroll
        for (int c = 0; c < 6; c++) {
            const u32 sel = (c & 1) ? 0x07060302u : 0x05040100u;
            lds_st<u2>(LA_U + c * 4608 + 480 + t * 8, u2{__builtin_amdgcn_perm(ua[1][c >> 1], ua[0][c >> 1], sel), __builtin_amdgcn_perm(ua[3][c >> 1], ua[2][c >> 1], sel)});
        }
        asm volatile("" ::: "memory");
#pragma unroll
        for (int r = 0; r < 4; r++) ub[r] = *(const u3*)(u + (1024 + 4 * t + r) * DM + d0);
        STAMP(7);
        bar();
        bar();
        bar();
        bar();
#pragma unroll
        for (int c = 0; c < 6; c++) {
            const u32 sel = (c & 1) ? 0x07060302u : 0x05040100u;
            lds_st<u2>(LA_U + c * 4608 + 480 + 2048 + t * 8, u2{__builtin_amdgcn_perm(ub[1][c >> 1], ub[0][c >> 1], sel), __builtin_amdgcn_perm(ub[3][c >> 1], ub[2][c >> 1], sel)});
        }
        asm volatile("s_waitcnt lgkmcnt(0)" ::: "memory");
        if (lane == 0) __hip_atomic_fetch_add((LAS u32*)(u32)LA_F, 1u, __ATOMIC_RELAXED, __HIP_MEMORY_SCOPE_WORKGROUP);
        STAMP(8);
        const int tile = wv - 12;
        char* o = (char*)(out + 4 * t * (2 * DM) + d0);
        a_rows_out(LA_Q + ((tile >> 1) ^ 1) * 2048 + (tile & 1) * 1024 + lane * 8, 4096, ua, dk, o);
        STAMP(9);
        bar();
        bar();
        a_rows_out(LA_K + (2 * ((tile >> 1) ^ 1) + (tile & 1)) * 4160 + lane * 8, 16640, ub, dk, o + 1024 * (4 * DM));
        STAMP(10);
        STAMP_OUT(768);
        return;
    }
    const int ch = wv >> 1, hf = wv & 1;
    {
        const int po = (d0 + ch) * NS + 64 * hf + lane;
        const u16 p_l = lar[po], p_a = aimg[po], p_r = cre[po], p_i = cim[po], p_t = ldt[d0 + ch];
        const float dt = rbf(expf(bf2f(p_t)));
        const float ar2 = -rbf(expf(bf2f(p_l))) * dt * 1.44269504f, air = bf2f(p_a) * dt * 0.159154943f;
        const f2 C = {2.f * dt * bf2f(p_r), 2.f * dt * bf2f(p_i)};
        STAMP(1);
        const f2 z1 = cexpa(ar2, air, 1.f), z8c = cexpa(ar2, -air, 8.f), z128 = cexpa(ar2, air, 128.f);
        const int gw = LA_K + wv * 4608 + lane * 4, gr = LA_K + wv * 4608 + c16 * 288 + g * 16;
        u4 bfr[4];
        {
            const f2 z16c = cmul(z8c, z8c);
            f2 w0 = {1.f, 0.f}, w1 = z8c;
#pragma unroll
            for (int r = 0; r < 16; r += 2) {
                lds_st<u32>(gw + r * 288, pk(w0.x, w0.y)); lds_st<u32>(gw + (r + 1) * 288, pk(w1.x, w1.y));
                if (r < 14) { w0 = r ? cmul(w0, z16c) : z16c; w1 = cmul(w1, z16c); }
            }
#pragma unroll
            for (int s = 0; s < 4; s++) bfr[s] = lds_ld<u4>(gr + 64 * s);
        }
        f2 e[8];
        e[0] = C;
#pragma unroll
        for (int j = 1; j < 8; j++) e[j] = cmul(e[j - 1], z1);
        const f2 z256 = cmul(z128, z128), z512 = cmul(z256, z256), z1024 = cmul(z512, z512);
        const int q = LA_Q + wv * 2576;
#pragma unroll
        for (int t = 0; t < 3; t++) {
            if (t == 0) {
#pragma unroll
                for (int j = 0; j < 8; j++) {
                    lds_st<u32>(gw + j * 288, pk(e[j].x, e[j].y));
                    const f2 f = cmul(e[j], z128);
                    lds_st<u32>(gw + (8 + j) * 288, pk(f.x, f.y));
                }
            } else if (t == 1) {
#pragma unroll
                for (int j = 0; j < 4; j++) {
                    f2 f = cmul(e[2 * j], z256);
                    lds_st<u32>(gw + j * 288, pk(f.x, f.y));
                    f = cmul(f, z128);
                    lds_st<u32>(gw + (4 + j) * 288, pk(f.x, f.y));
                }
#pragma unroll
                for (int j = 0; j < 2; j++) {
                    f2 f = cmul(e[4 * j], z512);
#pragma unroll
                    for (int k = 0; k < 4; k++) {
                        lds_st<u32>(gw + (8 + j + 2 * k) * 288, pk(f.x, f.y));
                        if (k < 3) f = cmul(f, z128);
                    }
                }
            } else {
                f2 f0 = cmul(C, z1024), f1 = cmul(f0, z128);
#pragma unroll
                for (int k = 0; k < 9; k += 2) {
                    lds_st<u32>(gw + k * 288, pk(f0.x, f0.y));
                    if (k < 8) { lds_st<u32>(gw + (k + 1) * 288, pk(f1.x, f1.y)); f0 = cmul(f0, z256); }
                    if (k < 6) f1 = cmul(f1, z256);
                }
            }
            f4 acc = Z;
#pragma unroll
            for (int s = 0; s < 4; s++) acc = mfma(lds_ld<u4>(gr + 64 * s), bfr[s], acc);
            if (t == 0) lds_st<f4>(q + (4 * (g & 1) + 128 * (g >> 1) + 8 * c16) * 4, acc);
            else if (t == 1) {
                if (g < 2) lds_st<f4>(q + (256 + 64 * g + 4 * c16) * 4, acc);
                else {
                    const int o = q + (384 + 64 * (g - 2) + 2 * c16) * 4;
                    lds_st<f2>(o, f2{acc[0], acc[1]}); lds_st<f2>(o + 128, f2{acc[2], acc[3]});
                }
            } else {
                if (g < 2) {
#pragma unroll
                    for (int i = 0; i < 4; i++) lds_st<float>(q + (512 + 64 * g + 16 * i + c16) * 4, acc[i]);
                } else if (lane == 32) lds_st<float>(q + 640 * 4, acc[0]);
            }
        }
    }
    STAMP(2);
    bar();
    STAMP(11);
    {
        const LAS float* qa = (const LAS float*)(u32)(LA_Q + ch * 5152);
        const LAS float* qb = qa + 644;
        const int kE = LA_K + ch * 16640, kO = kE + 4160;
        if (hf == 0) {
            const int j = 512 + 2 * lane;
            const float q0 = qa[j] + qb[j], q1 = qa[j + 1] + qb[j + 1], q2 = qa[j + 2] + qb[j + 2];
            const u32 a = pk(q2, q2), b = pk(q1, q1), c = pk(q0, q0);
            const int y = (504 - 8 * lane) * 4;
            put8(kE + y, u4{a, a, b, b}, u4{b, pk(q1, q0), c, c});
            put8(kO + y, u4{a, pk(q2, q1), b, b}, u4{b, c, c, c});
            if (lane < 36) {
                const int cp = lane / 9;
                if (cp >= 2 || lane != 9 * cp) lds_st<u32>(kE + cp * 4160 + (1023 + lane - 9 * cp) * 4, 0u);
            }
        } else {
            {
                const int j = 384 + 2 * lane;
                const float q0 = qa[j] + qb[j], q1 = qa[j + 1] + qb[j + 1], q2 = qa[j + 2] + qb[j + 2];
                const u32 b = pk(q1, q1), c = pk(q0, q0);
                const int y = (764 - 4 * lane) * 4;
                put4(kE + y, u4{pk(q2, q2), b, pk(q1, q0), c});
                put4(kO + y, u4{pk(q2, q1), b, c, c});
            }
            {
                const int j = 256 + 2 * lane;
                const float q0 = qa[j] + qb[j], q1 = qa[j + 1] + qb[j + 1], q2 = qa[j + 2] + qb[j + 2];
                const float qm = lane ? q0 : qa[255] + qb[255];
                const int y = (894 - 2 * lane) * 4;
                put2(kE + y, pk(q2, q1), pk(q0, q0));
                put2(kO + y, pk(q1, q0), pk(q0, qm));
            }
            {
                const f4 k = *(const LAS f4*)(qa + 4 * lane) + *(const LAS f4*)(qb + 4 * lane);
                const float km = lane ? qa[4 * lane - 1] + qb[4 * lane - 1] : 0.f;
                const int y = (1022 - 2 * lane) * 4;
                put2(kE + y, pk(k[3], k[2]), pk(k[1], k[0]));
                put2(kO + y, pk(k[2], k[1]), pk(k[0], km));
            }
        }
    }
    STAMP(12);
    bar();
    STAMP(3);
    const int x0 = 2031 - c16 + 8 * g;
    int ka = LA_K + ch * 16640 + (x0 & 3) * 4160 + (x0 >> 2) * 8 - 256 * hf - 192 - 3584;
    int ubs = LA_U + ch * 4608 + 480 + (16 * c16 + 8 * g - 16) * 2 - 256 * hf - 192;
    pin(ka); pin(ubs);
    f4 acc[4];
#define KFR(rho, p) ld_k(ka + 192 - 64 * (rho) + 3584 - 512 * (p))
#define UFR(rho, q) lds_ld<u4>(ubs + 192 - 64 * (rho) + 512 * (q))
#define SB __builtin_amdgcn_sched_barrier(0)
#define TRIANGLE(q0)                                                                          \
    {                                                                                         \
        u4 A[2][4], B[2][4];                                                                  \
        _Pragma("unroll") for (int p = 0; p < 4; p++) { B[0][p] = UFR(0, q0 + p); A[0][p] = KFR(0, p); } \
        _Pragma("unroll") for (int rho = 0; rho < 4; rho++)                                   \
            _Pragma("unroll") for (int p = 0; p < 4; p++) {                                   \
                SB;                                                                           \
                if (rho < 3) { B[(rho + 1) & 1][p] = UFR(rho + 1, q0 + p); A[(rho + 1) & 1][p] = KFR(rho + 1, p); } \
                _Pragma("unroll") for (int q = 0; p + q < 4; q++) acc[p + q] = mfma(A[rho & 1][p], B[rho & 1][q], acc[p + q]); \
            }                                                                                 \
        SB;                                                                                   \
    }
#define EXCHANGE(mine, other, str, S1)                                                        \
    {                                                                                         \
        if (hf) { lds_st<f4>((mine) + lane * 16, acc[0]); lds_st<f4>((mine) + (str) + lane * 16, acc[1]); acc[0] = acc[2]; acc[1] = acc[3]; } \
        else { lds_st<f4>((mine) + lane * 16, acc[2]); lds_st<f4>((mine) + (str) + lane * 16, acc[3]); } \
        STAMP(S1);                                                                            \
        bar();                                                                                \
        const f4 y0 = lds_ld<f4>((other) + lane * 16) + acc[0], y1 = lds_ld<f4>((other) + (str) + lane * 16) + acc[1]; \
        const int w = (other) + (16 * c16 + 4 * g) * 2;                                       \
        lds_st<u2>(w, u2{pk(y0[0], y0[1]), pk(y0[2], y0[3])});                                \
        lds_st<u2>(w + (str), u2{pk(y1[0], y1[1]), pk(y1[2], y1[3])});                        \
    }
#pragma unroll
    for (int i = 0; i < 4; i++) acc[i] = Z;
    TRIANGLE(0);
    EXCHANGE(LA_Q + wv * 2048, LA_Q + (wv ^ 1) * 2048, 1024, 4);
#pragma unroll
    for (int i = 0; i < 4; i++) acc[i] = Z;
    {
        u4 A[8], B[2][4];
#pragma unroll
        for (int p = 0; p < 4; p++) B[0][p] = UFR(0, p);
#pragma unroll
        for (int p = 1; p < 8; p++) A[p] = KFR(0, p);
        bar();
        STAMP(13);
#pragma unroll
        for (int rho = 0; rho < 4; rho++)
#pragma unroll
            for (int p = 1; p < 8; p++) {
                SB;
                if (rho < 3 && p < 5) B[(rho + 1) & 1][p - 1] = UFR(rho + 1, p - 1);
#pragma unroll
                for (int q = 0; q < 4; q++)
                    if (p + q >= 4 && p + q < 8) acc[p + q - 4] = mfma(A[p], B[rho & 1][q], acc[p + q - 4]);
                SB;
                if (rho < 3) A[p] = KFR(rho + 1, p);
            }
    }
    STAMP(14);
    pin(acc[0]); pin(acc[1]); pin(acc[2]); pin(acc[3]);
    while (__builtin_amdgcn_readfirstlane(*(const volatile LAS u32*)(u32)LA_F) < 4) __builtin_amdgcn_s_sleep(1);
    asm volatile("" ::: "memory");
    TRIANGLE(4);
    EXCHANGE(LA_K + ch * 16640 + 2 * hf * 4160, LA_K + ch * 16640 + 2 * (hf ^ 1) * 4160, 4160, 5);
    bar();
    STAMP(6);
    STAMP_OUT(0);
}

__global__ void __launch_bounds__(NT) fused_kernel(const u16* __restrict__ u, const u16* __restrict__ lar, const u16* __restrict__ aimg,
                                                   const u16* __restrict__ cre, const u16* __restrict__ cim, const u16* __restrict__ ldt,
                                                   const u16* __restrict__ dsk, const u16* __restrict__ x, const u16* __restrict__ wr,
                                                   const u16* __restrict__ wi, u16* __restrict__ out, char* ws) {
    if (blockIdx.x == 150) { const unsigned long long t0 = __builtin_amdgcn_s_memrealtime(); while (__builtin_amdgcn_s_memrealtime() - t0 < 5000ull) __builtin_amdgcn_s_sleep(1); return; }
    if (__builtin_expect(blockIdx.x < NB2, 1)) head_b(x, wr, wi, out, ws);
    else if (blockIdx.x >= A0) head_a(u, lar, aimg, cre, cim, ldt, dsk, out, ws);
    else if (threadIdx.x < 448) { u32 t = *(const u32*)((const char*)&TAB + F_F16 * 1024 + threadIdx.x * 128); asm volatile("" :: "v"(t)); }
}

extern "C" void kernel_launch(void* const* d_in, const int* in_sizes, int n_in, void* d_out, int out_size, void* d_ws,
                              size_t ws_size, hipStream_t stream) {
    (void)hipFuncSetAttribute((const void*)fused_kernel, hipFuncAttributeMaxDynamicSharedMemorySize, LDS_BYTES);
    fused_kernel<<<GRID, NT, LDS_BYTES, stream>>>((const u16*)d_in[0], (const u16*)d_in[1], (const u16*)d_in[2], (const u16*)d_in[3],
                                                  (const u16*)d_in[4], (const u16*)d_in[5], (const u16*)d_in[6], (const u16*)d_in[7],
                                                  (const u16*)d_in[8], (const u16*)d_in[9], (u16*)d_out, (char*)d_ws);
}
```

```cpp
#include <hip/hip_runtime.h>

typedef unsigned short u16;
typedef unsigned int u32;
typedef float f2 __attribute__((ext_vector_type(2)));
typedef float f4 __attribute__((ext_vector_type(4)));
typedef u32 u2 __attribute__((ext_vector_type(2)));
typedef u32 u4 __attribute__((ext_vector_type(4)));
typedef int i8v __attribute__((ext_vector_type(8)));
typedef short s4 __attribute__((ext_vector_type(4)));
typedef short s8 __attribute__((ext_vector_type(8)));
typedef __bf16 bf2 __attribute__((ext_vector_type(2)));
#define DI __device__ __forceinline__
#define LAS __attribute__((address_space(3)))
#define GAS __attribute__((address_space(1)))
#define CAS __attribute__((address_space(4)))
#pragma clang diagnostic ignored "-Wint-to-pointer-cast"
struct u3 { u32 v[3]; DI u32& operator[](int i) { return v[i]; } DI const u32& operator[](int i) const { return v[i]; } };

constexpr int SEQ = 2048, DM = 576, NS = 128, DF = 289;
constexpr int NB1 = 128;
constexpr int NB2 = 145;
constexpr int A0 = 152;
constexpr int NA = 96;
constexpr int GRID = A0 + NA;
constexpr int NT = 1024;

constexpr u32 WS_EPOCH = 0, WS_F1 = 4096, WS_F2 = WS_F1 + 32768, WS_X1 = 1u << 20, WS_Y1 = 4u << 20, WS_USED = 8u << 20;
#define STAMP(i)
#define STAMP_DECL
#define STAMP_OUT(T)

constexpr int F_SA = 0, F_EO = 4;
constexpr int F_ARE = 9, F_AIM = 10, F_BRE = 11, F_BIM = 12, F_G = 13;
constexpr int F_F16 = 21, F_GI = 23, F_AI = 31, F_M1C = 33;
constexpr int F_VAR = 37;
constexpr int F_END = F_VAR + 40;
constexpr int ET = 452, ER = 28, E2V = 580, E2U = 36, OP = 1168;
constexpr int LB_X = 0, LB_I = 0, LB_Y = 0;
constexpr int LB_E = 16 * OP;
constexpr int LB_O = LB_E, LB_E2 = LB_E;
constexpr int LB_S = LB_E + 16 * ET * 4;
constexpr int LB_W = LB_S + 1200 * 16;
constexpr int LB_T = LB_W + 2 * 256 * 36;
constexpr int LB_END = LB_T + (F_VAR + 16) * 1024;
struct CosTab { double c[2048]; double c9[9], s9[9]; };
constexpr double tay_cos(double x) { double t = 1, s = 1; for (int k = 1; k <= 14; k++) { t *= -x * x / ((2 * k - 1) * (2 * k)); s += t; } return s; }
constexpr double tay_sin(double x) { double t = x, s = x; for (int k = 1; k <= 14; k++) { t *= -x * x / ((2 * k + 1) * (2 * k)); s += t; } return s; }
constexpr CosTab make_cos() {
    CosTab t = {};
    const double PI2 = 6.283185307179586476925;
    for (int k = 0; k <= 512; k++) {
        const double v = k == 512 ? 0.0 : (k <= 256 ? tay_cos(PI2 * k / 2048) : tay_sin(PI2 * (512 - k) / 2048));
        t.c[k] = v; t.c[(2048 - k) & 2047] = v; t.c[1024 - k] = -v; t.c[(1024 + k) & 2047] = -v;
    }
    t.c[512] = 0.0; t.c[1536] = 0.0;
    for (int k = 0; k < 9; k++) { const double a = PI2 * (k <= 4 ? k : k - 9) / 9; t.c9[k] = tay_cos(a); t.s9[k] = tay_sin(a); }
    return t;
}
constexpr CosTab CT = make_cos();
constexpr double cs(int n) { return CT.c[n & 2047]; }
constexpr double sn(int n) { return CT.c[(n - 512) & 2047]; }
constexpr u32 bfr(double x) { const u32 b = __builtin_bit_cast(u32, (float)x); return (b + 0x7fffu + ((b >> 16) & 1u)) >> 16; }
constexpr u32 pkc(double lo, double hi) { return bfr(lo) | (bfr(hi) << 16); }
constexpr u32 f32c(double x) { return __builtin_bit_cast(u32, (float)x); }
template <int N> struct Frags { u32 w[N * 256]; };
constexpr u32 w_re(double amp, int n) { return pkc(amp * cs(n), amp * sn(n)); }
constexpr u32 w_im(double amp, int n) { return pkc(-amp * sn(n), amp * cs(n)); }
constexpr Frags<F_VAR> make_t0() {
    Frags<F_VAR> t = {};
    const double R2 = 1.4142135623730951;
    for (int l = 0; l < 64; l++)
        for (int i = 0; i < 4; i++) {
            const int c = l & 15, g = l >> 4, r = 4 * g + i, o = l * 4 + i;
            {
                const int b = c >> 2, tq = c & 3;
                const double amp = b ? 1.0 : R2;
                const int n0 = 512 * tq * g + (b ? 256 * b * (2 * i) : -256), n1 = 512 * tq * g + (b ? 256 * b * (2 * i + 1) : 256);
                t.w[F_ARE * 256 + o] = pkc(amp * cs(n0), amp * cs(n1));
                t.w[F_AIM * 256 + o] = pkc(-amp * sn(n0), -amp * sn(n1));
            }
            for (int w = 0; w < 9; w++) {
                const int sh = 16 * (i & 1), oo = w * 128 + l * 2 + (i >> 1);
                t.w[F_EO * 256 + oo] |= (u32)((c * ET + r * ER + 2 * ((7 * w + r) % 9)) * 4) << sh;
                if (w < 8) {
                    const int k = (9 * (w & 3) + 36 * r + 64 * (c % 9)) % DM, slot = w < 4 ? (k <= 288 ? 2 * k : 2 * (DM - k) + 1) : k;
                    t.w[F_SA * 256 + oo] |= (u32)(slot * 8 + (slot >> 6) * 16) << sh;
                }
            }
            if (r < 9) { const int k = (r * c) % 9; t.w[F_BRE * 256 + o] = pkc(CT.c9[k], CT.s9[k]); t.w[F_BIM * 256 + o] = pkc(-CT.s9[k], CT.c9[k]); }
            for (int q = 0; q < 4; q++) {
                t.w[(F_G + 2 * q) * 256 + o] = w_re(1.0, 32 * r * (q + 4 * c)); t.w[(F_G + 2 * q + 1) * 256 + o] = w_im(1.0, 32 * r * (q + 4 * c));
                t.w[(F_GI + 2 * q) * 256 + o] = w_re(1.0, 32 * c * (q + 4 * r)); t.w[(F_GI + 2 * q + 1) * 256 + o] = w_im(1.0, 32 * c * (q + 4 * r));
            }
            t.w[F_F16 * 256 + o] = w_re(1.0, 128 * r * c); t.w[(F_F16 + 1) * 256 + o] = w_im(1.0, 128 * r * c);
            for (int h = 0; h < 2; h++) {
                const int j = 4 * h + (c >> 2), n = 512 * i * (c & 3) + (g ? 256 * g * j : ((j & 1) ? 256 : -256));
                t.w[(F_AI + h) * 256 + o] = w_re(g ? 2.0 : R2, n);
                const int m = 16 * r * (4 * h + 8 * c) - 512 * h;
                t.w[(F_M1C + 2 * h) * 256 + o] = pkc(0.5 * cs(m), -0.5 * sn(m));
                t.w[(F_M1C + 2 * h + 1) * 256 + o] = pkc(-0.5 * sn(m), -0.5 * cs(m));
            }
        }
    return t;
}
constexpr Frags<40> make_t1() {
    Frags<40> t = {};
    for (int v = 0; v < 5; v++)
        for (int l = 0; l < 64; l++)
            for (int i = 0; i < 4; i++) {
                const int be = v < 3 ? v + 1 : 4 * (v - 3), c = l & 15, r = 4 * (l >> 4) + i, o = v * 2048 + l * 4 + i;
                const double amp = v < 3 ? 1.0 : 0.5;
                const int n1 = 16 * r * (be + 8 * c) + (v == 4 ? 512 : 0), n4 = 16 * c * (be + 8 * r);
                t.w[o] = w_re(amp, n1); t.w[o + 256] = w_im(amp, n1);
                t.w[o + 512] = w_re(1.0, n4); t.w[o + 768] = w_im(1.0, n4);
                t.w[o + 1024] = f32c(cs(r * (be + 8 * c))); t.w[o + 1280] = f32c(-sn(r * (be + 8 * c)));
                t.w[o + 1536] = f32c(cs(c * (be + 8 * r))); t.w[o + 1792] = f32c(-sn(c * (be + 8 * r)));
            }
    return t;
}
struct WgPar { int unit, w0, wm, wh, tvar, kind, kbase, bq; };
struct WgTab { WgPar p[NB2]; };
constexpr WgTab make_wg() {
    WgTab t = {};
    for (int id = 0; id < NB2; id++) {
        const int u = (id & 7) * 18 + ((id & 7) ? 1 : 0) + (id >> 3);
        const bool star = u >= 108;
        const int b = star ? 0 : 1 + u / 36, kg = star ? u - 108 : u % 36;
        const bool mir = !star && kg >= 18;
        WgPar& p = t.p[id];
        p.unit = u; p.bq = b; p.kind = star ? 2 : (mir ? 1 : 0);
        p.w0 = 2 * (star ? (kg < 36 ? 8 * kg : 281) : (mir ? (2048 - b) * DF + DM - 16 * kg - 15 : b * DF + 16 * kg));
        p.wm = 2 * (mir ? -8 * DF : 8 * DF); p.wh = 2 * (star ? 4 * DF : 8);
        p.tvar = (F_VAR + 8 * (star ? 3 : b - 1)) * 1024; p.kbase = star ? 8 * kg : 16 * kg;
    }
    return t;
}
struct Tables { Frags<F_VAR> a; Frags<40> b; WgTab w; };
constexpr Frags<F_VAR> T0 = make_t0();
constexpr Frags<40> T1 = make_t1();
constexpr WgTab T3 = make_wg();
static __device__ const __attribute__((aligned(128))) Tables TAB = {T0, T1, T3};
constexpr int LA_K = 0;
constexpr int LA_U = LA_K + 6 * 16640;
constexpr int LA_Q = LA_U + 6 * 4608;
constexpr int LA_F = LA_Q + 12 * 644 * 4;
constexpr int LA_END = LA_F + 16;
constexpr int LDS_BYTES = (LA_END > LB_END ? LA_END : LB_END);
static_assert(LDS_BYTES <= 160 * 1024 && LDS_BYTES > 80 * 1024, "one workgroup per CU");

DI u32 pk(float a, float b) { f2 v = {a, b}; return __builtin_bit_cast(u32, __builtin_convertvector(v, bf2)); }
DI float blo(u32 v) { return __uint_as_float(v << 16); }
DI float bhi(u32 v) { return __uint_as_float(v & 0xffff0000u); }
DI float bf2f(u16 v) { return __uint_as_float((u32)v << 16); }
DI float rbf(float x) { return blo(pk(x, x)); }
DI f2 cmul(f2 a, f2 b) { f2 t = a.xx * b; f2 s = {-b.y, b.x}; return __builtin_elementwise_fma(a.yy, s, t); }
DI f4 mfma(u4 a, u4 b, f4 c) { return __builtin_amdgcn_mfma_f32_16x16x32_bf16(__builtin_bit_cast(s8, a), __builtin_bit_cast(s8, b), c, 0, 0, 0); }
DI u4 pack4(f4 re, f4 im) { u4 r = {pk(re[0], im[0]), pk(re[1], im[1]), pk(re[2], im[2]), pk(re[3], im[3])}; return r; }
DI u4 twid(f4 re, f4 im, f4 wr, f4 wi) { return pack4(__builtin_elementwise_fma(re, wr, -(im * wi)), __builtin_elementwise_fma(re, wi, im * wr)); }
DI int mod9(int x) { return x - 9 * ((x * 57) >> 9); }
template <class T> DI void pin(T& v) { asm volatile("" : "+v"(v)); }

template <class T> DI T lds_ld(int off) { return *(const LAS T*)(u32)off; }
template <class T> DI void lds_st(int off, T v) { *(LAS T*)(u32)off = v; }
DI void bar() { __syncthreads(); }
DI void glds16(const void* g, u32 off, int lds) {
    u32 keep;
    asm volatile("s_mov_b32 %0, m0\n\ts_mov_b32 m0, %3\n\ts_nop 4\n\tglobal_load_lds_dwordx4 %1, %2\n\ts_mov_b32 m0, %0" : "=&s"(keep) : "v"(off), "s"(g), "s"(lds) : "memory");
}

template <int N> DI bool wait_flags(__amdgpu_buffer_rsrc_t r, u32 base, u32 tag, int lane) {
    const unsigned long long t0 = __builtin_amdgcn_s_memrealtime();
    const int o2 = (lane + 128 < N ? lane + 128 : lane) * 128;
    for (;;) {
        const u32 a = __builtin_amdgcn_raw_buffer_load_b32(r, base + lane * 128, 0, 16);
        const u32 b = __builtin_amdgcn_raw_buffer_load_b32(r, base + (lane + 64) * 128, 0, 16);
        const u32 c = N > 128 ? __builtin_amdgcn_raw_buffer_load_b32(r, base + o2, 0, 16) : tag;
        if (__all((a == tag) & (b == tag) & (c == tag))) return true;
        if (__builtin_amdgcn_s_memrealtime() - t0 > 5000000ull) return false;
        asm volatile("" ::: "memory");
    }
}

DI void head_b(const u16* __restrict__ x, const u16* __restrict__ wr, const u16* __restrict__ wi,
               u16* __restrict__ out, char* ws) {
    const int tid = threadIdx.x, lane = tid & 63, c16 = lane & 15, g = lane >> 4;
    const int wv = __builtin_amdgcn_readfirstlane(tid >> 6);
    const int id = blockIdx.x;
    STAMP_DECL;
    const bool rows = id < NB1;
    const int s00 = 32 * (id >> 4) + (id & 15);
    const char* tab = (const char*)&TAB;
    const int R = (tid * 911) >> 16;
    const u32 xoff = (s00 + 16 * (R >> 3) + 256 * (R & 7)) * (2 * DM) + (tid - R * 72) * 16;
    asm volatile("" :: "s"(x), "s"(wr), "s"(wi), "s"(out), "s"(ws), "v"(xoff));
    const __amdgpu_buffer_rsrc_t rws = __builtin_amdgcn_make_buffer_rsrc(ws, 0, WS_USED, 0x00020000);
    u32 tag = 0;
    if (wv == 0) asm volatile("global_load_dword %0, %0, %1 sc1" : "+v"(tag) : "s"(ws) : "memory");
    if (rows) {
        glds16(x, xoff, LB_X + wv * 1024);
        if (wv < 2) glds16(x, (s00 + 16 + 1536) * (2 * DM) + (tid < 56 ? (tid + 16) * 16 : 256 * 2 * DM + (tid - 56) * 16), LB_X + 16384 + wv * 1024);
        else glds16(tab, tid * 16 - 2048, LB_T + (wv - 2) * 1024);
        if (wv >= 9) glds16(tab, tid * 16 + 5 * 1024, LB_T + (wv + 5) * 1024);
    }
    asm volatile("" : "+s"(tab));
    const WgPar par = __builtin_bit_cast(WgPar, *(const CAS i8v*)(tab + F_END * 1024 + id * 32));
    u4 wreg;
    __builtin_memcpy(&wreg, (const char*)((wv >> 3) ? wi : wr) + (u32)(par.w0 + ((tid >> 1) & 255) * par.wm + (tid & 1) * par.wh), 16);
    __builtin_amdgcn_sched_barrier(0);
    const f4 Z = {0.f, 0.f, 0.f, 0.f};
    const int u2i = par.unit, bq = par.bq;
    const bool star = par.kind == 2, mir = par.kind == 1;
    const int tau = wv & 3, bw = wv >> 2;
    int tl = LB_T + lane * 16;
    int lin = LB_S + (tid + (tid >> 5)) * 16;
    pin(tl); pin(lin);
    int sadr[4];

    if (rows) {
        if (tid < 256) {
            const int o = LB_E + 72 + (tid & 15) * (ER * 4) + (tid >> 4) * (ET * 4);
            lds_st<u2>(o, u2{0u, 0u}); lds_st<u4>(o + 8, u4{0u, 0u, 0u, 0u});
        }
        int xo = LB_X + (c16 >> 2) * 1152 + 2 * (144 * ((g - wv) & 3) + 16 * wv + 4 * (c16 & 3));
        int bo = LB_E + (wv * ET + c16 * ER + min(8 * g, 16)) * 4;
        int go = WS_X1 + (tid >> 3) * 16384 + id * 128 + (tid & 7) * 16;
        int ta = LB_T + lane * 8;
        pin(xo); pin(bo); pin(go); pin(ta);
        STAMP(1);
        asm volatile("s_waitcnt vmcnt(1) lgkmcnt(0)\n\ts_barrier" ::: "memory");
        STAMP(2);
        if (wv < 9) {
            u4 d[2];
#pragma unroll
            for (int s = 0; s < 2; s++) {
                const s4 lo = __builtin_amdgcn_ds_read_tr16_b64_v4i16((LAS s4*)(u32)(xo + s * 9216));
                const s4 hi = __builtin_amdgcn_ds_read_tr16_b64_v4i16((LAS s4*)(u32)(xo + s * 9216 + 4608));
                d[s] = __builtin_bit_cast(u4, __builtin_shufflevector(lo, hi, 0, 1, 2, 3, 4, 5, 6, 7));
            }
            const u4 are = lds_ld<u4>(tl + F_ARE * 1024), aim = lds_ld<u4>(tl + F_AIM * 1024);
            const u2 eo = lds_ld<u2>(ta + F_EO * 1024 + wv * 512);
#pragma unroll
            for (int s = 0; s < 2; s++) d[s] = pack4(mfma(d[s], are, Z), mfma(d[s], aim, Z));
#pragma unroll
            for (int i = 0; i < 4; i++) lds_st<u2>(LB_E + ((i & 1) ? eo[i >> 1] >> 16 : eo[i >> 1] & 0xffff), u2{d[0][i], d[1][i]});
        }
        u4 bre = lds_ld<u4>(tl + F_BRE * 1024), bim = lds_ld<u4>(tl + F_BIM * 1024);
        u4 gre = lds_ld<u4>(tl + (F_G + 2 * tau) * 1024), gim = lds_ld<u4>(tl + (F_G + 2 * tau + 1) * 1024);
        {
            const u2 sa = lds_ld<u2>(ta + F_SA * 1024 + (tau + (bw ? 4 : 0)) * 512);
            const int base = LB_S + ((bw + 3) & 3) * (DM * 8 + 144);
#pragma unroll
            for (int i = 0; i < 4; i++) { sadr[i] = base + ((i & 1) ? sa[i >> 1] >> 16 : sa[i >> 1] & 0xffff); pin(sadr[i]); }
        }
        pin(bre); pin(bim); pin(gre); pin(gim);
        STAMP(12);
        bar();
        STAMP(13);
        {
            const u4 p0 = lds_ld<u4>(bo), p1 = lds_ld<u4>(bo + 16);
            u4 d[2];
#pragma unroll
            for (int s = 0; s < 2; s++) {
                const u4 a = {p0[s], p0[2 + s], p1[s], p1[2 + s]};
                const u4 t = pack4(mfma(a, bre, Z), mfma(a, bim, Z));
                d[s] = pack4(mfma(gre, t, Z), mfma(gim, t, Z));
            }
            if (c16 < 9) {
#pragma unroll
                for (int i = 0; i < 4; i++) lds_st<u2>(sadr[i], u2{d[0][i], d[1][i]});
                if (wv == 0 && c16 == 0 && !(g & 1)) lds_st<u2>(sadr[0] + 8, u2{d[0][0], d[1][0]});
            }
        }
        STAMP(14);
        bar();
        STAMP(3);
        __builtin_amdgcn_raw_buffer_store_b128(lds_ld<u4>(lin), rws, go, 0, 16);
        if (tid <= 128) __builtin_amdgcn_raw_buffer_store_b128(lds_ld<u4>(lin + 1056 * 16), rws, go, 128 * 16384, 16);
    } else {
        if (wv < 7) { u32 t = *(const GAS u32*)(tab + F_F16 * 1024 + (u32)(tid * 128)); asm volatile("" :: "v"(t)); }
    }
    {
        const int o = LB_W + (tid >> 1) * 36 + (tid & 1) * 16;
        lds_st<u32>(o, wreg[0]); lds_st<u32>(o + 4, wreg[1]); lds_st<u32>(o + 8, wreg[2]); lds_st<u32>(o + 12, wreg[3]);
    }
    asm volatile("s_waitcnt vmcnt(0)" : "+v"(tag) :: "memory");
    tag += 0x9E3779B1u;
    bar();
    STAMP(4);
    if (rows && tid == 0) __builtin_amdgcn_raw_buffer_store_b32(tag, rws, WS_F1 + id * 128, 0, 16);
    glds16(tab, F_F16 * 1024 + tid * 16, LB_T + F_F16 * 1024 + wv * 1024);
    if (star || wv < 8) glds16(tab, par.tvar + tid * 16, LB_T + F_VAR * 1024 + wv * 1024);
    asm volatile("s_waitcnt vmcnt(0)\n\ts_barrier" ::: "memory");

    const int typ = star ? (wv & 1) : 0;
    const int tv = tl + (F_VAR + 8 * typ) * 1024;
    u4 fre = lds_ld<u4>(tl + F_F16 * 1024), fim = lds_ld<u4>(tl + (F_F16 + 1) * 1024);
    u4 m1r = lds_ld<u4>(tv), m1i = lds_ld<u4>(tv + 1024), m4r = lds_ld<u4>(tv + 2048), m4i = lds_ld<u4>(tv + 3072);
    f4 twr = lds_ld<f4>(tv + 4096), twi = lds_ld<f4>(tv + 5120), tur = lds_ld<f4>(tv + 6144), tui = lds_ld<f4>(tv + 7168);
    u4 mcr = lds_ld<u4>(tl + (F_M1C + 2 * typ) * 1024), mci = lds_ld<u4>(tl + (F_M1C + 2 * typ + 1) * 1024);
    f4 wfr, wfi;
    {
        const int kcol = par.kbase + (star ? (wv >> 1) : wv);
        const int cc = star ? 8 * typ + (u2i < NB2 - 1 ? (wv >> 1) : 7) : (mir ? 15 - wv : wv);
        const int wo = LB_W + (c16 + 64 * g) * 36 + cc * 2;
#pragma unroll
        for (int i = 0; i < 4; i++) {
            wfr[i] = bf2f(lds_ld<u16>(wo + i * 576));
            wfi[i] = bf2f(lds_ld<u16>(wo + i * 576 + 9216));
        }
        float sc = 1.f / (float)(SEQ * DM);
        if (kcol == 0 || kcol == 288) {
            sc *= 0.5f;
#pragma unroll
            for (int i = 0; i < 4; i++) {
                const int m = c16 + 64 * g + 16 * i;
                const int orow = (mir ? bq + 8 * m : 2048 - (star ? 4 * typ : bq) - 8 * m) & 2047;
                wfr[i] += bf2f(wr[orow * DF + kcol]);
                wfi[i] -= bf2f(wi[orow * DF + kcol]);
            }
        }
        wfr *= sc; wfi *= (mir ? -sc : sc);
    }
    f4 wnr = -wfr, wni = -wfi;
    int io = (32 * g + c16) * 128 + (((wv >> 1) ^ (c16 >> 1)) * 16);
    int so = (tid >> 3) * 128 + (((tid & 7) ^ ((tid >> 4) & 7)) * 16);
    pin(fre); pin(fim); pin(m1r); pin(m1i); pin(m4r); pin(m4i); pin(twr); pin(twi); pin(tur); pin(tui); pin(mcr); pin(mci);
    int gi = WS_X1 + u2i * 16384 + tid * 16, gq = WS_Y1 + (tid >> 3) * (NB2 * 128) + u2i * 128 + (tid & 7) * 16;
    int i1 = LB_I + io + (star ? 0 : (wv & 1) * 8);
    io += LB_O + (wv & 1) * 8;
    pin(wfr); pin(wnr); pin(wni); pin(io); pin(so); pin(gi); pin(gq); pin(i1);
    if (wv == 0) wait_flags<NB1>(rws, WS_F1, tag, lane);
    bar();
    STAMP(5);
    lds_st<u4>(LB_I + so, __builtin_amdgcn_raw_buffer_load_b128(rws, gi, 0, 16));
    bar();
    STAMP(6);
    {
        const int o1 = i1;
        const u2 a0 = lds_ld<u2>(o1), a1 = lds_ld<u2>(o1 + 2048);
        const u4 a = {a0[0], a0[1], a1[0], a1[1]};
        f4 re = mfma(a, m1r, Z), im = mfma(a, m1i, Z);
        if (star) {
            const u2 b0 = lds_ld<u2>(o1 + 8), b1 = lds_ld<u2>(o1 + 8 + 2048);
            const u4 b = {b0[0], b0[1], b1[0], b1[1]};
            re = mfma(b, mcr, re); im = mfma(b, mci, im);
        }
        u4 t = twid(re, im, twr, twi);
        re = mfma(fre, t, Z); im = mfma(fim, t, Z);
        t = pack4(__builtin_elementwise_fma(re, wfr, im * wni), __builtin_elementwise_fma(re, wni, im * wnr));
        re = mfma(t, fre, Z); im = mfma(t, fim, Z);
        t = twid(re, im, tur, tui);
        t = pack4(mfma(m4r, t, Z), mfma(m4i, t, Z));
        lds_st<u2>(io, u2{t[0], t[1]}); lds_st<u2>(io + 2048, u2{t[2], t[3]});
    }
    bar();
    STAMP(7);
    {
        u4 v = lds_ld<u4>(LB_O + so);
        if (star) {
            u4 w;
#pragma unroll
            for (int s = 0; s < 2; s++) {
                const float ar = blo(v[s]), ai = bhi(v[s]), cr = blo(v[2 + s]), ci = bhi(v[2 + s]);
                w[s] = pk(ar + ci, ai - cr); w[2 + s] = pk(ar - ci, -ai - cr);
            }
            v = w;
        }
        if (u2i < NB2 - 1 || (tid & 7) == 0)
            __builtin_amdgcn_raw_buffer_store_b128(v, rws, gq, 0, 16);
    }
    asm volatile("s_waitcnt vmcnt(0)" ::: "memory");
    bar();
    STAMP(8);
    if (tid == 0) __builtin_amdgcn_raw_buffer_store_b32(tag, rws, WS_F2 + id * 128, 0, 16);
    if (!rows) { STAMP_OUT(0); return; }

    u4 gre = lds_ld<u4>(tl + (F_GI + 2 * tau) * 1024), gim = lds_ld<u4>(tl + (F_GI + 2 * tau + 1) * 1024);
    u4 bre = lds_ld<u4>(tl + F_BRE * 1024), bim = lds_ld<u4>(tl + F_BIM * 1024);
    u4 ai0 = lds_ld<u4>(tl + F_AI * 1024), ai1 = lds_ld<u4>(tl + (F_AI + 1) * 1024);
    int eadr[4];
#pragma unroll
    for (int i = 0; i < 4; i++) {
        const int r = 4 * g + i;
        eadr[i] = LB_E2 + (mod9(4 * (c16 + 18 - r)) * E2V + r * E2U + 2 * wv) * 4;
        pin(eadr[i]);
    }
    int ao = LB_E2 + (wv * E2V + c16 * E2U + 8 * g) * 4;
    int gy = WS_Y1 + id * (NB2 * 128) + tid * 16;
    int yo = LB_Y + (c16 >> 2) * OP + 2 * (144 * (((c16 & 3) - wv) & 3) + 16 * wv + 4 * g);
    int yl = LB_Y + tid * 16 + R * (OP - 1152);
    u32 oo = 2 * xoff - (tid - R * 72) * 16 + 2 * DM;
    pin(gre); pin(gim); pin(bre); pin(bim); pin(ai0); pin(ai1); pin(ao); pin(gy); pin(yo); pin(yl); pin(oo);
    if (wv == 0) {
        const bool ok = wait_flags<NB2>(rws, WS_F2, tag, lane);
        if (id == 0 && lane == 0 && ok) __builtin_amdgcn_raw_buffer_store_b32(tag, rws, WS_EPOCH, 0, 16);
    }
    bar();
    STAMP(9);
    {
        const u4 v = __builtin_amdgcn_raw_buffer_load_b128(rws, gy, 0, 16);
        if (tid <= 128) lds_st<u4>(lin + 1056 * 16, __builtin_amdgcn_raw_buffer_load_b128(rws, gy, 16384, 16));
        lds_st<u4>(lin, v);
    }
    bar();
    STAMP(10);
    {
        u4 d[2];
        u2 p[4];
#pragma unroll
        for (int i = 0; i < 4; i++) p[i] = lds_ld<u2>(sadr[i]);
#pragma unroll
        for (int s = 0; s < 2; s++) {
            const u4 a = {p[0][s], p[1][s], p[2][s], p[3][s]};
            const u4 t = pack4(mfma(a, gre, Z), mfma(a, gim, Z));
            d[s] = pack4(mfma(t, bre, Z), mfma(t, bim, Z));
        }
        if (c16 < 9) {
#pragma unroll
            for (int i = 0; i < 4; i++) lds_st<u2>(eadr[i], u2{d[0][i], d[1][i]});
        }
    }
    STAMP(15);
    bar();
    if (wv < 9) {
        const u4 p0 = lds_ld<u4>(ao), p1 = lds_ld<u4>(ao + 16);
#pragma unroll
        for (int s = 0; s < 2; s++) {
            const u4 a = {p0[s], p0[2 + s], p1[s], p1[2 + s]};
            const f4 y0 = mfma(a, ai0, Z), y1 = mfma(a, ai1, Z);
            lds_st<u2>(yo + s * (8 * OP), u2{pk(y0[0], y0[1]), pk(y0[2], y0[3])});
            lds_st<u2>(yo + s * (8 * OP) + 4 * OP, u2{pk(y1[0], y1[1]), pk(y1[2], y1[3])});
        }
    }
    bar();
    *(GAS u4*)((char*)out + oo) = lds_ld<u4>(yl);
    if (wv < 2) {
        const int r2 = tid < 56 ? 14 : 15, c2 = tid + 1024 - 72 * r2;
        *(GAS u4*)((char*)out + (u32)((s00 + 16 + 256 * (r2 - 8)) * (4 * DM) + 2 * DM + c2 * 16)) = lds_ld<u4>(LB_Y + r2 * OP + c2 * 16);
    }
    STAMP(11);
    STAMP_OUT(0);
}

DI f2 cexpa(float ar2, float air, float p) {
    const float e = __builtin_amdgcn_exp2f(ar2 * p), t = __builtin_amdgcn_fractf(air * p);
    return f2{e * __builtin_amdgcn_cosf(t), e * __builtin_amdgcn_sinf(t)};
}
DI u4 ld_k(int o) {
    const u2 a = *(const volatile LAS u2*)(u32)o, b = *(const volatile LAS u2*)(u32)(o + 8);
    return u4{a[0], a[1], b[0], b[1]};
}
DI void put8(int o, u4 a, u4 b) {
    lds_st<u4>(o, a); lds_st<u4>(o + 16, b);
    lds_st<u32>(o + 8316, a[0]); lds_st<u4>(o + 8320, u4{a[1], a[2], a[3], b[0]});
    lds_st<u2>(o + 8336, u2{b[1], b[2]}); lds_st<u32>(o + 8344, b[3]);
}
DI void put4(int o, u4 a) {
    lds_st<u4>(o, a);
    lds_st<u32>(o + 8316, a[0]); lds_st<u2>(o + 8320, u2{a[1], a[2]}); lds_st<u32>(o + 8328, a[3]);
}
DI void put2(int o, u32 a, u32 b) { lds_st<u2>(o, u2{a, b}); lds_st<u32>(o + 8316, a); lds_st<u32>(o + 8320, b); }

DI void a_rows_out(int yb, int ystr, const u3* ur, u3 dk, char* o) {
    u2 y[6];
#pragma unroll
    for (int c = 0; c < 6; c++) y[c] = lds_ld<u2>(yb + c * ystr);
#pragma unroll
    for (int r = 0; r < 4; r++) {
        u3 v;
#pragma unroll
        for (int c = 0; c < 3; c++) {
            const u32 s = pk(blo(ur[r][c]) * blo(dk[c]), bhi(ur[r][c]) * bhi(dk[c]));
            const u32 ya = y[2 * c][r >> 1], yc = y[2 * c + 1][r >> 1];
            v[c] = pk(((r & 1) ? bhi(ya) : blo(ya)) + blo(s), ((r & 1) ? bhi(yc) : blo(yc)) + bhi(s));
        }
        *(u3*)(o + r * (4 * DM)) = v;
    }
}

DI void head_a(const u16* __restrict__ u, const u16* __restrict__ lar, const u16* __restrict__ aimg,
               const u16* __restrict__ cre, const u16* __restrict__ cim, const u16* __restrict__ ldt,
               const u16* __restrict__ dsk, u16* __restrict__ out, char* ws) {
    const int tid = threadIdx.x, lane = tid & 63, c16 = lane & 15, g = lane >> 4;
    const int wv = __builtin_amdgcn_readfirstlane(tid >> 6);
    STAMP_DECL;
    const int ai_ = blockIdx.x - A0;
    const int d0 = 6 * ((ai_ & 7) * 12 + (ai_ >> 3));
    const f4 Z = {0.f, 0.f, 0.f, 0.f};

    if (wv >= 12) {
        const int t = tid - 768;
        u3 ua[4], ub[4];
#pragma unroll
        for (int r = 0; r < 4; r++) ua[r] = *(const u3*)(u + (4 * t + r) * DM + d0);
        const u3 dk = *(const u3*)(dsk + d0);
        if (t < 180) lds_st<u4>(LA_U + (t / 30) * 4608 + (t % 30) * 16, u4{0u, 0u, 0u, 0u});
        if (t == 0) lds_st<u2>(LA_F, u2{0u, 0u});
#pragma unroll
        for (int c = 0; c < 6; c++) {
            const u32 sel = (c & 1) ? 0x07060302u : 0x05040100u;
            lds_st<u2>(LA_U + c * 4608 + 480 + t * 8, u2{__builtin_amdgcn_perm(ua[1][c >> 1], ua[0][c >> 1], sel), __builtin_amdgcn_perm(ua[3][c >> 1], ua[2][c >> 1], sel)});
        }
        asm volatile("" ::: "memory");
#pragma unroll
        for (int r = 0; r < 4; r++) ub[r] = *(const u3*)(u + (1024 + 4 * t + r) * DM + d0);
        STAMP(7);
        bar();
        bar();
        bar();
        bar();
#pragma unroll
        for (int c = 0; c < 6; c++) {
            const u32 sel = (c & 1) ? 0x07060302u : 0x05040100u;
            lds_st<u2>(LA_U + c * 4608 + 480 + 2048 + t * 8, u2{__builtin_amdgcn_perm(ub[1][c >> 1], ub[0][c >> 1], sel), __builtin_amdgcn_perm(ub[3][c >> 1], ub[2][c >> 1], sel)});
        }
        asm volatile("s_waitcnt lgkmcnt(0)" ::: "memory");
        if (lane == 0) __hip_atomic_fetch_add((LAS u32*)(u32)LA_F, 1u, __ATOMIC_RELAXED, __HIP_MEMORY_SCOPE_WORKGROUP);
        STAMP(8);
        const int tile = wv - 12;
        char* o = (char*)(out + 4 * t * (2 * DM) + d0);
        a_rows_out(LA_Q + ((tile >> 1) ^ 1) * 2048 + (tile & 1) * 1024 + lane * 8, 4096, ua, dk, o);
        STAMP(9);
        bar();
        bar();
        a_rows_out(LA_K + (2 * ((tile >> 1) ^ 1) + (tile & 1)) * 4160 + lane * 8, 16640, ub, dk, o + 1024 * (4 * DM));
        STAMP(10);
        STAMP_OUT(768);
        return;
    }
    const int ch = wv >> 1, hf = wv & 1;
    {
        const int po = (d0 + ch) * NS + 64 * hf + lane;
        const u16 p_l = lar[po], p_a = aimg[po], p_r = cre[po], p_i = cim[po], p_t = ldt[d0 + ch];
        const float dt = rbf(expf(bf2f(p_t)));
        const float ar2 = -rbf(expf(bf2f(p_l))) * dt * 1.44269504f, air = bf2f(p_a) * dt * 0.159154943f;
        const f2 C = {2.f * dt * bf2f(p_r), 2.f * dt * bf2f(p_i)};
        STAMP(1);
        const f2 z1 = cexpa(ar2, air, 1.f), z8c = cexpa(ar2, -air, 8.f), z128 = cexpa(ar2, air, 128.f);
        const int gw = LA_K + wv * 4608 + lane * 4, gr = LA_K + wv * 4608 + c16 * 288 + g * 16;
        u4 bfr[4];
        {
            const f2 z16c = cmul(z8c, z8c);
            f2 w0 = {1.f, 0.f}, w1 = z8c;
#pragma unroll
            for (int r = 0; r < 16; r += 2) {
                lds_st<u32>(gw + r * 288, pk(w0.x, w0.y)); lds_st<u32>(gw + (r + 1) * 288, pk(w1.x, w1.y));
                if (r < 14) { w0 = r ? cmul(w0, z16c) : z16c; w1 = cmul(w1, z16c); }
            }
#pragma unroll
            for (int s = 0; s < 4; s++) bfr[s] = lds_ld<u4>(gr + 64 * s);
        }
        f2 e[8];
        e[0] = C;
#pragma unroll
        for (int j = 1; j < 8; j++) e[j] = cmul(e[j - 1], z1);
        const f2 z256 = cmul(z128, z128), z512 = cmul(z256, z256), z1024 = cmul(z512, z512);
        const int q = LA_Q + wv * 2576;
#pragma unroll
        for (int t = 0; t < 3; t++) {
            if (t == 0) {
#pragma unroll
                for (int j = 0; j < 8; j++) {
                    lds_st<u32>(gw + j * 288, pk(e[j].x, e[j].y));
                    const f2 f = cmul(e[j], z128);
                    lds_st<u32>(gw + (8 + j) * 288, pk(f.x, f.y));
                }
            } else if (t == 1) {
#pragma unroll
                for (int j = 0; j < 4; j++) {
                    f2 f = cmul(e[2 * j], z256);
                    lds_st<u32>(gw + j * 288, pk(f.x, f.y));
                    f = cmul(f, z128);
                    lds_st<u32>(gw + (4 + j) * 288, pk(f.x, f.y));
                }
#pragma unroll
                for (int j = 0; j < 2; j++) {
                    f2 f = cmul(e[4 * j], z512);
#pragma unroll
                    for (int k = 0; k < 4; k++) {
                        lds_st<u32>(gw + (8 + j + 2 * k) * 288, pk(f.x, f.y));
                        if (k < 3) f = cmul(f, z128);
                    }
                }
            } else {
                f2 f0 = cmul(C, z1024), f1 = cmul(f0, z128);
#pragma unroll
                for (int k = 0; k < 9; k += 2) {
                    lds_st<u32>(gw + k * 288, pk(f0.x, f0.y));
                    if (k < 8) { lds_st<u32>(gw + (k + 1) * 288, pk(f1.x, f1.y)); f0 = cmul(f0, z256); }
                    if (k < 6) f1 = cmul(f1, z256);
                }
            }
            f4 acc = Z;
#pragma unroll
            for (int s = 0; s < 4; s++) acc = mfma(lds_ld<u4>(gr + 64 * s), bfr[s], acc);
            if (t == 0) lds_st<f4>(q + (4 * (g & 1) + 128 * (g >> 1) + 8 * c16) * 4, acc);
            else if (t == 1) {
                if (g < 2) lds_st<f4>(q + (256 + 64 * g + 4 * c16) * 4, acc);
                else {
                    const int o = q + (384 + 64 * (g - 2) + 2 * c16) * 4;
                    lds_st<f2>(o, f2{acc[0], acc[1]}); lds_st<f2>(o + 128, f2{acc[2], acc[3]});
                }
            } else {
                if (g < 2) {
#pragma unroll
                    for (int i = 0; i < 4; i++) lds_st<float>(q + (512 + 64 * g + 16 * i + c16) * 4, acc[i]);
                } else if (lane == 32) lds_st<float>(q + 640 * 4, acc[0]);
            }
        }
    }
    STAMP(2);
    bar();
    STAMP(11);
    {
        const LAS float* qa = (const LAS float*)(u32)(LA_Q + ch * 5152);
        const LAS float* qb = qa + 644;
        const int kE = LA_K + ch * 16640, kO = kE + 4160;
        if (hf == 0) {
            const int j = 512 + 2 * lane;
            const float q0 = qa[j] + qb[j], q1 = qa[j + 1] + qb[j + 1], q2 = qa[j + 2] + qb[j + 2];
            const u32 a = pk(q2, q2), b = pk(q1, q1), c = pk(q0, q0);
            const int y = (504 - 8 * lane) * 4;
            put8(kE + y, u4{a, a, b, b}, u4{b, pk(q1, q0), c, c});
            put8(kO + y, u4{a, pk(q2, q1), b, b}, u4{b, c, c, c});
            if (lane < 36) {
                const int cp = lane / 9;
                if (cp >= 2 || lane != 9 * cp) lds_st<u32>(kE + cp * 4160 + (1023 + lane - 9 * cp) * 4, 0u);
            }
        } else {
            {
                const int j = 384 + 2 * lane;
                const float q0 = qa[j] + qb[j], q1 = qa[j + 1] + qb[j + 1], q2 = qa[j + 2] + qb[j + 2];
                const u32 b = pk(q1, q1), c = pk(q0, q0);
                const int y = (764 - 4 * lane) * 4;
                put4(kE + y, u4{pk(q2, q2), b, pk(q1, q0), c});
                put4(kO + y, u4{pk(q2, q1), b, c, c});
            }
            {
                const int j = 256 + 2 * lane;
                const float q0 = qa[j] + qb[j], q1 = qa[j + 1] + qb[j + 1], q2 = qa[j + 2] + qb[j + 2];
                const float qm = lane ? q0 : qa[255] + qb[255];
                const int y = (894 - 2 * lane) * 4;
                put2(kE + y, pk(q2, q1), pk(q0, q0));
                put2(kO + y, pk(q1, q0), pk(q0, qm));
            }
            {
                const f4 k = *(const LAS f4*)(qa + 4 * lane) + *(const LAS f4*)(qb + 4 * lane);
                const float km = lane ? qa[4 * lane - 1] + qb[4 * lane - 1] : 0.f;
                const int y = (1022 - 2 * lane) * 4;
                put2(kE + y, pk(k[3], k[2]), pk(k[1], k[0]));
                put2(kO + y, pk(k[2], k[1]), pk(k[0], km));
            }
        }
    }
    STAMP(12);
    bar();
    STAMP(3);
    const int x0 = 2031 - c16 + 8 * g;
    int ka = LA_K + ch * 16640 + (x0 & 3) * 4160 + (x0 >> 2) * 8 - 256 * hf - 192 - 3584;
    int ubs = LA_U + ch * 4608 + 480 + (16 * c16 + 8 * g - 16) * 2 - 256 * hf - 192;
    pin(ka); pin(ubs);
    f4 acc[4];
#define KFR(rho, p) ld_k(ka + 192 - 64 * (rho) + 3584 - 512 * (p))
#define UFR(rho, q) lds_ld<u4>(ubs + 192 - 64 * (rho) + 512 * (q))
#define SB __builtin_amdgcn_sched_barrier(0)
#define TRIANGLE(q0)                                                                          \
    {                                                                                         \
        u4 A[2][4], B[2][4];                                                                  \
        _Pragma("unroll") for (int p = 0; p < 4; p++) { B[0][p] = UFR(0, q0 + p); A[0][p] = KFR(0, p); } \
        _Pragma("unroll") for (int rho = 0; rho < 4; rho++)                                   \
            _Pragma("unroll") for (int p = 0; p < 4; p++) {                                   \
                SB;                                                                           \
                if (rho < 3) { B[(rho + 1) & 1][p] = UFR(rho + 1, q0 + p); A[(rho + 1) & 1][p] = KFR(rho + 1, p); } \
                _Pragma("unroll") for (int q = 0; p + q < 4; q++) acc[p + q] = mfma(A[rho & 1][p], B[rho & 1][q], acc[p + q]); \
            }                                                                                 \
        SB;                                                                                   \
    }
#define EXCHANGE(mine, other, str, S1)                                                        \
    {                                                                                         \
        if (hf) { lds_st<f4>((mine) + lane * 16, acc[0]); lds_st<f4>((mine) + (str) + lane * 16, acc[1]); acc[0] = acc[2]; acc[1] = acc[3]; } \
        else { lds_st<f4>((mine) + lane * 16, acc[2]); lds_st<f4>((mine) + (str) + lane * 16, acc[3]); } \
        STAMP(S1);                                                                            \
        bar();                                                                                \
        const f4 y0 = lds_ld<f4>((other) + lane * 16) + acc[0], y1 = lds_ld<f4>((other) + (str) + lane * 16) + acc[1]; \
        const int w = (other) + (16 * c16 + 4 * g) * 2;                                       \
        lds_st<u2>(w, u2{pk(y0[0], y0[1]), pk(y0[2], y0[3])});                                \
        lds_st<u2>(w + (str), u2{pk(y1[0], y1[1]), pk(y1[2], y1[3])});                        \
    }
#pragma unroll
    for (int i = 0; i < 4; i++) acc[i] = Z;
    TRIANGLE(0);
    EXCHANGE(LA_Q + wv * 2048, LA_Q + (wv ^ 1) * 2048, 1024, 4);
#pragma unroll
    for (int i = 0; i < 4; i++) acc[i] = Z;
    {
        u4 A[8], B[2][4];
#pragma unroll
        for (int p = 0; p < 4; p++) B[0][p] = UFR(0, p);
#pragma unroll
        for (int p = 1; p < 8; p++) A[p] = KFR(0, p);
        bar();
        STAMP(13);
#pragma unroll
        for (int rho = 0; rho < 4; rho++)
#pragma unroll
            for (int p = 1; p < 8; p++) {
                SB;
                if (rho < 3 && p < 5) B[(rho + 1) & 1][p - 1] = UFR(rho + 1, p - 1);
#pragma unroll
                for (int q = 0; q < 4; q++)
                    if (p + q >= 4 && p + q < 8) acc[p + q - 4] = mfma(A[p], B[rho & 1][q], acc[p + q - 4]);
                SB;
                if (rho < 3) A[p] = KFR(rho + 1, p);
            }
    }
    STAMP(14);
    pin(acc[0]); pin(acc[1]); pin(acc[2]); pin(acc[3]);
    if (lane == 0) __hip_atomic_fetch_add((LAS u32*)(u32)(LA_F + 4), 1u, __ATOMIC_RELAXED, __HIP_MEMORY_SCOPE_WORKGROUP);
    while (__builtin_amdgcn_readfirstlane(*(const volatile LAS u32*)(u32)LA_F) < 4) __builtin_amdgcn_s_sleep(1);
    asm volatile("" ::: "memory");
    TRIANGLE(4);
    pin(acc[0]); pin(acc[1]); pin(acc[2]); pin(acc[3]);
    while (__builtin_amdgcn_readfirstlane(*(const volatile LAS u32*)(u32)(LA_F + 4)) < 12) __builtin_amdgcn_s_sleep(1);
    asm volatile("" ::: "memory");
    EXCHANGE(LA_K + ch * 16640 + 2 * hf * 4160, LA_K + ch * 16640 + 2 * (hf ^ 1) * 4160, 4160, 5);
    bar();
    STAMP(6);
    STAMP_OUT(0);
}

__global__ void __launch_bounds__(NT) fused_kernel(const u16* __restrict__ u, const u16* __restrict__ lar, const u16* __restrict__ aimg,
                                                   const u16* __restrict__ cre, const u16* __restrict__ cim, const u16* __restrict__ ldt,
                                                   const u16* __restrict__ dsk, const u16* __restrict__ x, const u16* __restrict__ wr,
                                                   const u16* __restrict__ wi, u16* __restrict__ out, char* ws) {
    if (__builtin_expect(blockIdx.x < NB2, 1)) head_b(x, wr, wi, out, ws);
    else if (blockIdx.x >= A0) head_a(u, lar, aimg, cre, cim, ldt, dsk, out, ws);
    else if (threadIdx.x < 448) { u32 t = *(const u32*)((const char*)&TAB + F_F16 * 1024 + threadIdx.x * 128); asm volatile("" :: "v"(t)); }
}

extern "C" void kernel_launch(void* const* d_in, const int* in_sizes, int n_in, void* d_out, int out_size, void* d_ws,
                              size_t ws_size, hipStream_t stream) {
    (void)hipFuncSetAttribute((const void*)fused_kernel, hipFuncAttributeMaxDynamicSharedMemorySize, LDS_BYTES);
    fused_kernel<<<GRID, NT, LDS_BYTES, stream>>>((const u16*)d_in[0], (const u16*)d_in[1], (const u16*)d_in[2], (const u16*)d_in[3],
                                                  (const u16*)d_in[4], (const u16*)d_in[5], (const u16*)d_in[6], (const u16*)d_in[7],
                                                  (const u16*)d_in[8], (const u16*)d_in[9], (u16*)d_out, (char*)d_ws);
}
```

```cpp
#include <hip/hip_runtime.h>

typedef unsigned short u16;
typedef unsigned int u32;
typedef float f2 __attribute__((ext_vector_type(2)));
typedef float f4 __attribute__((ext_vector_type(4)));
typedef u32 u2 __attribute__((ext_vector_type(2)));
typedef u32 u4 __attribute__((ext_vector_type(4)));
typedef int i8v __attribute__((ext_vector_type(8)));
typedef short s4 __attribute__((ext_vector_type(4)));
typedef short s8 __attribute__((ext_vector_type(8)));
typedef __bf16 bf2 __attribute__((ext_vector_type(2)));
#define DI __device__ __forceinline__
#define LAS __attribute__((address_space(3)))
#define GAS __attribute__((address_space(1)))
#define CAS __attribute__((address_space(4)))
#pragma clang diagnostic ignored "-Wint-to-pointer-cast"
struct u3 { u32 v[3]; DI u32& operator[](int i) { return v[i]; } DI const u32& operator[](int i) const { return v[i]; } };

constexpr int SEQ = 2048, DM = 576, NS = 128, DF = 289;
constexpr int NB1 = 128;
constexpr int NB2 = 145;
constexpr int A0 = 152;
constexpr int NA = 96;
constexpr int GRID = A0 + NA;
constexpr int NT = 1024;

constexpr u32 WS_EPOCH = 0, WS_F1 = 4096, WS_F2 = WS_F1 + 32768, WS_X1 = 1u << 20, WS_Y1 = 4u << 20, WS_USED = 8u << 20;
#define STAMP(i)
#define STAMP_DECL
#define STAMP_OUT(T)

constexpr int F_SA = 0, F_EO = 4;
constexpr int F_ARE = 9, F_AIM = 10, F_BRE = 11, F_BIM = 12, F_G = 13;
constexpr int F_F16 = 21, F_GI = 23, F_AI = 31, F_M1C = 33;
constexpr int F_VAR = 37;
constexpr int F_END = F_VAR + 40;
constexpr int ET = 452, ER = 28, E2V = 580, E2U = 36, OP = 1168;
constexpr int LB_X = 0, LB_I = 0, LB_Y = 0;
constexpr int LB_E = 16 * OP;
constexpr int LB_O = LB_E, LB_E2 = LB_E;
constexpr int LB_S = LB_E + 16 * ET * 4;
constexpr int LB_W = LB_S + 1200 * 16;
constexpr int LB_T = LB_W + 2 * 256 * 36;
constexpr int LB_END = LB_T + (F_VAR + 16) * 1024;
struct CosTab { double c[2048]; double c9[9], s9[9]; };
constexpr double tay_cos(double x) { double t = 1, s = 1; for (int k = 1; k <= 14; k++) { t *= -x * x / ((2 * k - 1) * (2 * k)); s += t; } return s; }
constexpr double tay_sin(double x) { double t = x, s = x; for (int k = 1; k <= 14; k++) { t *= -x * x / ((2 * k + 1) * (2 * k)); s += t; } return s; }
constexpr CosTab make_cos() {
    CosTab t = {};
    const double PI2 = 6.283185307179586476925;
    for (int k = 0; k <= 512; k++) {
        const double v = k == 512 ? 0.0 : (k <= 256 ? tay_cos(PI2 * k / 2048) : tay_sin(PI2 * (512 - k) / 2048));
        t.c[k] = v; t.c[(2048 - k) & 2047] = v; t.c[1024 - k] = -v; t.c[(1024 + k) & 2047] = -v;
    }
    t.c[512] = 0.0; t.c[1536] = 0.0;
    for (int k = 0; k < 9; k++) { const double a = PI2 * (k <= 4 ? k : k - 9) / 9; t.c9[k] = tay_cos(a); t.s9[k] = tay_sin(a); }
    return t;
}
constexpr CosTab CT = make_cos();
constexpr double cs(int n) { return CT.c[n & 2047]; }
constexpr double sn(int n) { return CT.c[(n - 512) & 2047]; }
constexpr u32 bfr(double x) { const u32 b = __builtin_bit_cast(u32, (float)x); return (b + 0x7fffu + ((b >> 16) & 1u)) >> 16; }
constexpr u32 pkc(double lo, double hi) { return bfr(lo) | (bfr(hi) << 16); }
constexpr u32 f32c(double x) { return __builtin_bit_cast(u32, (float)x); }
template <int N> struct Frags { u32 w[N * 256]; };
constexpr u32 w_re(double amp, int n) { return pkc(amp * cs(n), amp * sn(n)); }
constexpr u32 w_im(double amp, int n) { return pkc(-amp * sn(n), amp * cs(n)); }
constexpr Frags<F_VAR> make_t0() {
    Frags<F_VAR> t = {};
    const double R2 = 1.4142135623730951;
    for (int l = 0; l < 64; l++)
        for (int i = 0; i < 4; i++) {
            const int c = l & 15, g = l >> 4, r = 4 * g + i, o = l * 4 + i;
            {
                const int b = c >> 2, tq = c & 3;
                const double amp = b ? 1.0 : R2;
                const int n0 = 512 * tq * g + (b ? 256 * b * (2 * i) : -256), n1 = 512 * tq * g + (b ? 256 * b * (2 * i + 1) : 256);
                t.w[F_ARE * 256 + o] = pkc(amp * cs(n0), amp * cs(n1));
                t.w[F_AIM * 256 + o] = pkc(-amp * sn(n0), -amp * sn(n1));
            }
            for (int w = 0; w < 9; w++) {
                const int sh = 16 * (i & 1), oo = w * 128 + l * 2 + (i >> 1);
                t.w[F_EO * 256 + oo] |= (u32)((c * ET + r * ER + 2 * ((7 * w + r) % 9)) * 4) << sh;
                if (w < 8) {
                    const int k = (9 * (w & 3) + 36 * r + 64 * (c % 9)) % DM, slot = w < 4 ? (k <= 288 ? 2 * k : 2 * (DM - k) + 1) : k;
                    t.w[F_SA * 256 + oo] |= (u32)(slot * 8 + (slot >> 6) * 16) << sh;
                }
            }
            if (r < 9) { const int k = (r * c) % 9; t.w[F_BRE * 256 + o] = pkc(CT.c9[k], CT.s9[k]); t.w[F_BIM * 256 + o] = pkc(-CT.s9[k], CT.c9[k]); }
            for (int q = 0; q < 4; q++) {
                t.w[(F_G + 2 * q) * 256 + o] = w_re(1.0, 32 * r * (q + 4 * c)); t.w[(F_G + 2 * q + 1) * 256 + o] = w_im(1.0, 32 * r * (q + 4 * c));
                t.w[(F_GI + 2 * q) * 256 + o] = w_re(1.0, 32 * c * (q + 4 * r)); t.w[(F_GI + 2 * q + 1) * 256 + o] = w_im(1.0, 32 * c * (q + 4 * r));
            }
            t.w[F_F16 * 256 + o] = w_re(1.0, 128 * r * c); t.w[(F_F16 + 1) * 256 + o] = w_im(1.0, 128 * r * c);
            for (int h = 0; h < 2; h++) {
                const int j = 4 * h + (c >> 2), n = 512 * i * (c & 3) + (g ? 256 * g * j : ((j & 1) ? 256 : -256));
                t.w[(F_AI + h) * 256 + o] = w_re(g ? 2.0 : R2, n);
                const int m = 16 * r * (4 * h + 8 * c) - 512 * h;
                t.w[(F_M1C + 2 * h) * 256 + o] = pkc(0.5 * cs(m), -0.5 * sn(m));
                t.w[(F_M1C + 2 * h + 1) * 256 + o] = pkc(-0.5 * sn(m), -0.5 * cs(m));
            }
        }
    return t;
}
constexpr Frags<40> make_t1() {
    Frags<40> t = {};
    for (int v = 0; v < 5; v++)
        for (int l = 0; l < 64; l++)
            for (int i = 0; i < 4; i++) {
                const int be = v < 3 ? v + 1 : 4 * (v - 3), c = l & 15, r = 4 * (l >> 4) + i, o = v * 2048 + l * 4 + i;
                const double amp = v < 3 ? 1.0 : 0.5;
                const int n1 = 16 * r * (be + 8 * c) + (v == 4 ? 512 : 0), n4 = 16 * c * (be + 8 * r);
                t.w[o] = w_re(amp, n1); t.w[o + 256] = w_im(amp, n1);
                t.w[o + 512] = w_re(1.0, n4); t.w[o + 768] = w_im(1.0, n4);
                t.w[o + 1024] = f32c(cs(r * (be + 8 * c))); t.w[o + 1280] = f32c(-sn(r * (be + 8 * c)));
                t.w[o + 1536] = f32c(cs(c * (be + 8 * r))); t.w[o + 1792] = f32c(-sn(c * (be + 8 * r)));
            }
    return t;
}
struct WgPar { int unit, w0, wm, wh, tvar, kind, kbase, bq; };
struct WgTab { WgPar p[NB2]; };
constexpr WgTab make_wg() {
    WgTab t = {};
    for (int id = 0; id < NB2; id++) {
        const int u = (id & 7) * 18 + ((id & 7) ? 1 : 0) + (id >> 3);
        const bool star = u >= 108;
        const int b = star ? 0 : 1 + u / 36, kg = star ? u - 108 : u % 36;
        const bool mir = !star && kg >= 18;
        WgPar& p = t.p[id];
        p.unit = u; p.bq = b; p.kind = star ? 2 : (mir ? 1 : 0);
        p.w0 = 2 * (star ? (kg < 36 ? 8 * kg : 281) : (mir ? (2048 - b) * DF + DM - 16 * kg - 15 : b * DF + 16 * kg));
        p.wm = 2 * (mir ? -8 * DF : 8 * DF); p.wh = 2 * (star ? 4 * DF : 8);
        p.tvar = (F_VAR + 8 * (star ? 3 : b - 1)) * 1024; p.kbase = star ? 8 * kg : 16 * kg;
    }
    return t;
}
struct Tables { Frags<F_VAR> a; Frags<40> b; WgTab w; };
constexpr Frags<F_VAR> T0 = make_t0();
constexpr Frags<40> T1 = make_t1();
constexpr WgTab T3 = make_wg();
static __device__ const __attribute__((aligned(128))) Tables TAB = {T0, T1, T3};
constexpr int LA_K = 0;
constexpr int LA_U = LA_K + 6 * 16640;
constexpr int LA_Q = LA_U + 6 * 4608;
constexpr int LA_F = LA_Q + 12 * 644 * 4;
constexpr int LA_END = LA_F + 16;
constexpr int LDS_BYTES = (LA_END > LB_END ? LA_END : LB_END);
static_assert(LDS_BYTES <= 160 * 1024 && LDS_BYTES > 80 * 1024, "one workgroup per CU");

DI u32 pk(float a, float b) { f2 v = {a, b}; return __builtin_bit_cast(u32, __builtin_convertvector(v, bf2)); }
DI float blo(u32 v) { return __uint_as_float(v << 16); }
DI float bhi(u32 v) { return __uint_as_float(v & 0xffff0000u); }
DI float bf2f(u16 v) { return __uint_as_float((u32)v << 16); }
DI float rbf(float x) { return blo(pk(x, x)); }
DI f2 cmul(f2 a, f2 b) { f2 t = a.xx * b; f2 s = {-b.y, b.x}; return __builtin_elementwise_fma(a.yy, s, t); }
DI f4 mfma(u4 a, u4 b, f4 c) { return __builtin_amdgcn_mfma_f32_16x16x32_bf16(__builtin_bit_cast(s8, a), __builtin_bit_cast(s8, b), c, 0, 0, 0); }
DI u4 pack4(f4 re, f4 im) { u4 r = {pk(re[0], im[0]), pk(re[1], im[1]), pk(re[2], im[2]), pk(re[3], im[3])}; return r; }
DI u4 twid(f4 re, f4 im, f4 wr, f4 wi) { return pack4(__builtin_elementwise_fma(re, wr, -(im * wi)), __builtin_elementwise_fma(re, wi, im * wr)); }
DI int mod9(int x) { return x - 9 * ((x * 57) >> 9); }
template <class T> DI void pin(T& v) { asm volatile("" : "+v"(v)); }

template <class T> DI T lds_ld(int off) { return *(const LAS T*)(u32)off; }
template <class T> DI void lds_st(int off, T v) { *(LAS T*)(u32)off = v; }
DI void bar() { __syncthreads(); }
DI void glds16(const void* g, u32 off, int lds) {
    u32 keep;
    asm volatile("s_mov_b32 %0, m0\n\ts_mov_b32 m0, %3\n\ts_nop 4\n\tglobal_load_lds_dwordx4 %1, %2\n\ts_mov_b32 m0, %0" : "=&s"(keep) : "v"(off), "s"(g), "s"(lds) : "memory");
}

template <int N> DI bool wait_flags(__amdgpu_buffer_rsrc_t r, u32 base, u32 tag, int lane) {
    const unsigned long long t0 = __builtin_amdgcn_s_memrealtime();
    const int o2 = (lane + 128 < N ? lane + 128 : lane) * 128;
    for (;;) {
        const u32 a = __builtin_amdgcn_raw_buffer_load_b32(r, base + lane * 128, 0, 16);
        const u32 b = __builtin_amdgcn_raw_buffer_load_b32(r, base + (lane + 64) * 128, 0, 16);
        const u32 c = N > 128 ? __builtin_amdgcn_raw_buffer_load_b32(r, base + o2, 0, 16) : tag;
        if (__all((a == tag) & (b == tag) & (c == tag))) return true;
        if (__builtin_amdgcn_s_memrealtime() - t0 > 5000000ull) return false;
        asm volatile("" ::: "memory");
    }
}

DI void head_b(const u16* __restrict__ x, const u16* __restrict__ wr, const u16* __restrict__ wi,
               u16* __restrict__ out, char* ws) {
    const int tid = threadIdx.x, lane = tid & 63, c16 = lane & 15, g = lane >> 4;
    const int wv = __builtin_amdgcn_readfirstlane(tid >> 6);
    const int id = blockIdx.x;
    STAMP_DECL;
    const bool rows = id < NB1;
    const int s00 = 32 * (id >> 4) + (id & 15);
    const char* tab = (const char*)&TAB;
    const int R = (tid * 911) >> 16;
    const u32 xoff = (s00 + 16 * (R >> 3) + 256 * (R & 7)) * (2 * DM) + (tid - R * 72) * 16;
    asm volatile("" :: "s"(x), "s"(wr), "s"(wi), "s"(out), "s"(ws), "v"(xoff));
    const __amdgpu_buffer_rsrc_t rws = __builtin_amdgcn_make_buffer_rsrc(ws, 0, WS_USED, 0x00020000);
    u32 tag = 0;
    if (wv == 0) asm volatile("global_load_dword %0, %0, %1 sc1" : "+v"(tag) : "s"(ws) : "memory");
    if (rows) {
        glds16(x, xoff, LB_X + wv * 1024);
        if (wv < 2) glds16(x, (s00 + 16 + 1536) * (2 * DM) + (tid < 56 ? (tid + 16) * 16 : 256 * 2 * DM + (tid - 56) * 16), LB_X + 16384 + wv * 1024);
        else glds16(tab, tid * 16 - 2048, LB_T + (wv - 2) * 1024);
        if (wv >= 9) glds16(tab, tid * 16 + 5 * 1024, LB_T + (wv + 5) * 1024);
    }
    asm volatile("" : "+s"(tab));
    const WgPar par = __builtin_bit_cast(WgPar, *(const CAS i8v*)(tab + F_END * 1024 + id * 32));
    u4 wreg;
    __builtin_memcpy(&wreg, (const char*)((wv >> 3) ? wi : wr) + (u32)(par.w0 + ((tid >> 1) & 255) * par.wm + (tid & 1) * par.wh), 16);
    __builtin_amdgcn_sched_barrier(0);
    const f4 Z = {0.f, 0.f, 0.f, 0.f};
    const int u2i = par.unit, bq = par.bq;
    const bool star = par.kind == 2, mir = par.kind == 1;
    const int tau = wv & 3, bw = wv >> 2;
    int tl = LB_T + lane * 16;
    int lin = LB_S + (tid + (tid >> 5)) * 16;
    pin(tl); pin(lin);
    int sadr[4];

    if (rows) {
        if (tid < 256) {
            const int o = LB_E + 72 + (tid & 15) * (ER * 4) + (tid >> 4) * (ET * 4);
            lds_st<u2>(o, u2{0u, 0u}); lds_st<u4>(o + 8, u4{0u, 0u, 0u, 0u});
        }
        int xo = LB_X + (c16 >> 2) * 1152 + 2 * (144 * ((g - wv) & 3) + 16 * wv + 4 * (c16 & 3));
        int bo = LB_E + (wv * ET + c16 * ER + min(8 * g, 16)) * 4;
        int go = WS_X1 + (tid >> 3) * 16384 + id * 128 + (tid & 7) * 16;
        int ta = LB_T + lane * 8;
        pin(xo); pin(bo); pin(go); pin(ta);
        STAMP(1);
        asm volatile("s_waitcnt vmcnt(1) lgkmcnt(0)\n\ts_barrier" ::: "memory");
        STAMP(2);
        if (wv < 9) {
            u4 d[2];
#pragma unroll
            for (int s = 0; s < 2; s++) {
                const s4 lo = __builtin_amdgcn_ds_read_tr16_b64_v4i16((LAS s4*)(u32)(xo + s * 9216));
                const s4 hi = __builtin_amdgcn_ds_read_tr16_b64_v4i16((LAS s4*)(u32)(xo + s * 9216 + 4608));
                d[s] = __builtin_bit_cast(u4, __builtin_shufflevector(lo, hi, 0, 1, 2, 3, 4, 5, 6, 7));
            }
            const u4 are = lds_ld<u4>(tl + F_ARE * 1024), aim = lds_ld<u4>(tl + F_AIM * 1024);
            const u2 eo = lds_ld<u2>(ta + F_EO * 1024 + wv * 512);
#pragma unroll
            for (int s = 0; s < 2; s++) d[s] = pack4(mfma(d[s], are, Z), mfma(d[s], aim, Z));
#pragma unroll
            for (int i = 0; i < 4; i++) lds_st<u2>(LB_E + ((i & 1) ? eo[i >> 1] >> 16 : eo[i >> 1] & 0xffff), u2{d[0][i], d[1][i]});
        }
        u4 bre = lds_ld<u4>(tl + F_BRE * 1024), bim = lds_ld<u4>(tl + F_BIM * 1024);
        u4 gre = lds_ld<u4>(tl + (F_G + 2 * tau) * 1024), gim = lds_ld<u4>(tl + (F_G + 2 * tau + 1) * 1024);
        {
            const u2 sa = lds_ld<u2>(ta + F_SA * 1024 + (tau + (bw ? 4 : 0)) * 512);
            const int base = LB_S + ((bw + 3) & 3) * (DM * 8 + 144);
#pragma unroll
            for (int i = 0; i < 4; i++) { sadr[i] = base + ((i & 1) ? sa[i >> 1] >> 16 : sa[i >> 1] & 0xffff); pin(sadr[i]); }
        }
        pin(bre); pin(bim); pin(gre); pin(gim);
        STAMP(12);
        bar();
        STAMP(13);
        {
            const u4 p0 = lds_ld<u4>(bo), p1 = lds_ld<u4>(bo + 16);
            u4 d[2];
#pragma unroll
            for (int s = 0; s < 2; s++) {
                const u4 a = {p0[s], p0[2 + s], p1[s], p1[2 + s]};
                const u4 t = pack4(mfma(a, bre, Z), mfma(a, bim, Z));
                d[s] = pack4(mfma(gre, t, Z), mfma(gim, t, Z));
            }
            if (c16 < 9) {
#pragma unroll
                for (int i = 0; i < 4; i++) lds_st<u2>(sadr[i], u2{d[0][i], d[1][i]});
                if (wv == 0 && c16 == 0 && !(g & 1)) lds_st<u2>(sadr[0] + 8, u2{d[0][0], d[1][0]});
            }
        }
        STAMP(14);
        bar();
        STAMP(3);
        __builtin_amdgcn_raw_buffer_store_b128(lds_ld<u4>(lin), rws, go, 0, 16);
        if (tid <= 128) __builtin_amdgcn_raw_buffer_store_b128(lds_ld<u4>(lin + 1056 * 16), rws, go, 128 * 16384, 16);
    } else {
        if (wv < 7) { u32 t = *(const GAS u32*)(tab + F_F16 * 1024 + (u32)(tid * 128)); asm volatile("" :: "v"(t)); }
    }
    {
        const int o = LB_W + (tid >> 1) * 36 + (tid & 1) * 16;
        lds_st<u32>(o, wreg[0]); lds_st<u32>(o + 4, wreg[1]); lds_st<u32>(o + 8, wreg[2]); lds_st<u32>(o + 12, wreg[3]);
    }
    asm volatile("s_waitcnt vmcnt(0)" : "+v"(tag) :: "memory");
    tag += 0x9E3779B1u;
    bar();
    STAMP(4);
    if (rows && tid == 0) __builtin_amdgcn_raw_buffer_store_b32(tag, rws, WS_F1 + id * 128, 0, 16);
    glds16(tab, F_F16 * 1024 + tid * 16, LB_T + F_F16 * 1024 + wv * 1024);
    if (star || wv < 8) glds16(tab, par.tvar + tid * 16, LB_T + F_VAR * 1024 + wv * 1024);
    asm volatile("s_waitcnt vmcnt(0)\n\ts_barrier" ::: "memory");

    const int typ = star ? (wv & 1) : 0;
    const int tv = tl + (F_VAR + 8 * typ) * 1024;
    u4 fre = lds_ld<u4>(tl + F_F16 * 1024), fim = lds_ld<u4>(tl + (F_F16 + 1) * 1024);
    u4 m1r = lds_ld<u4>(tv), m1i = lds_ld<u4>(tv + 1024), m4r = lds_ld<u4>(tv + 2048), m4i = lds_ld<u4>(tv + 3072);
    f4 twr = lds_ld<f4>(tv + 4096), twi = lds_ld<f4>(tv + 5120), tur = lds_ld<f4>(tv + 6144), tui = lds_ld<f4>(tv + 7168);
    u4 mcr = lds_ld<u4>(tl + (F_M1C + 2 * typ) * 1024), mci = lds_ld<u4>(tl + (F_M1C + 2 * typ + 1) * 1024);
    f4 wfr, wfi;
    {
        const int kcol = par.kbase + (star ? (wv >> 1) : wv);
        const int cc = star ? 8 * typ + (u2i < NB2 - 1 ? (wv >> 1) : 7) : (mir ? 15 - wv : wv);
        const int wo = LB_W + (c16 + 64 * g) * 36 + cc * 2;
#pragma unroll
        for (int i = 0; i < 4; i++) {
            wfr[i] = bf2f(lds_ld<u16>(wo + i * 576));
            wfi[i] = bf2f(lds_ld<u16>(wo + i * 576 + 9216));
        }
        float sc = 1.f / (float)(SEQ * DM);
        if (kcol == 0 || kcol == 288) {
            sc *= 0.5f;
#pragma unroll
            for (int i = 0; i < 4; i++) {
                const int m = c16 + 64 * g + 16 * i;
                const int orow = (mir ? bq + 8 * m : 2048 - (star ? 4 * typ : bq) - 8 * m) & 2047;
                wfr[i] += bf2f(wr[orow * DF + kcol]);
                wfi[i] -= bf2f(wi[orow * DF + kcol]);
            }
        }
        wfr *= sc; wfi *= (mir ? -sc : sc);
    }
    f4 wnr = -wfr, wni = -wfi;
    int io = (32 * g + c16) * 128 + (((wv >> 1) ^ (c16 >> 1)) * 16);
    int so = (tid >> 3) * 128 + (((tid & 7) ^ ((tid >> 4) & 7)) * 16);
    pin(fre); pin(fim); pin(m1r); pin(m1i); pin(m4r); pin(m4i); pin(twr); pin(twi); pin(tur); pin(tui); pin(mcr); pin(mci);
    int gi = WS_X1 + u2i * 16384 + tid * 16, gq = WS_Y1 + (tid >> 3) * (NB2 * 128) + u2i * 128 + (tid & 7) * 16;
    int i1 = LB_I + io + (star ? 0 : (wv & 1) * 8);
    io += LB_O + (wv & 1) * 8;
    pin(wfr); pin(wnr); pin(wni); pin(io); pin(so); pin(gi); pin(gq); pin(i1);
    if (wv == 0) wait_flags<NB1>(rws, WS_F1, tag, lane);
    bar();
    STAMP(5);
    lds_st<u4>(LB_I + so, __builtin_amdgcn_raw_buffer_load_b128(rws, gi, 0, 16));
    bar();
    STAMP(6);
    {
        const int o1 = i1;
        const u2 a0 = lds_ld<u2>(o1), a1 = lds_ld<u2>(o1 + 2048);
        const u4 a = {a0[0], a0[1], a1[0], a1[1]};
        f4 re = mfma(a, m1r, Z), im = mfma(a, m1i, Z);
        if (star) {
            const u2 b0 = lds_ld<u2>(o1 + 8), b1 = lds_ld<u2>(o1 + 8 + 2048);
            const u4 b = {b0[0], b0[1], b1[0], b1[1]};
            re = mfma(b, mcr, re); im = mfma(b, mci, im);
        }
        u4 t = twid(re, im, twr, twi);
        re = mfma(fre, t, Z); im = mfma(fim, t, Z);
        t = pack4(__builtin_elementwise_fma(re, wfr, im * wni), __builtin_elementwise_fma(re, wni, im * wnr));
        re = mfma(t, fre, Z); im = mfma(t, fim, Z);
        t = twid(re, im, tur, tui);
        t = pack4(mfma(m4r, t, Z), mfma(m4i, t, Z));
        lds_st<u2>(io, u2{t[0], t[1]}); lds_st<u2>(io + 2048, u2{t[2], t[3]});
    }
    bar();
    STAMP(7);
    {
        u4 v = lds_ld<u4>(LB_O + so);
        if (star) {
            u4 w;
#pragma unroll
            for (int s = 0; s < 2; s++) {
                const float ar = blo(v[s]), ai = bhi(v[s]), cr = blo(v[2 + s]), ci = bhi(v[2 + s]);
                w[s] = pk(ar + ci, ai - cr); w[2 + s] = pk(ar - ci, -ai - cr);
            }
            v = w;
        }
        if (u2i < NB2 - 1 || (tid & 7) == 0)
            __builtin_amdgcn_raw_buffer_store_b128(v, rws, gq, 0, 16);
    }
    asm volatile("s_waitcnt vmcnt(0)" ::: "memory");
    bar();
    STAMP(8);
    if (tid == 0) __builtin_amdgcn_raw_buffer_store_b32(tag, rws, WS_F2 + id * 128, 0, 16);
    if (!rows) { STAMP_OUT(0); return; }

    u4 gre = lds_ld<u4>(tl + (F_GI + 2 * tau) * 1024), gim = lds_ld<u4>(tl + (F_GI + 2 * tau + 1) * 1024);
    u4 bre = lds_ld<u4>(tl + F_BRE * 1024), bim = lds_ld<u4>(tl + F_BIM * 1024);
    u4 ai0 = lds_ld<u4>(tl + F_AI * 1024), ai1 = lds_ld<u4>(tl + (F_AI + 1) * 1024);
    int eadr[4];
#pragma unroll
    for (int i = 0; i < 4; i++) {
        const int r = 4 * g + i;
        eadr[i] = LB_E2 + (mod9(4 * (c16 + 18 - r)) * E2V + r * E2U + 2 * wv) * 4;
        pin(eadr[i]);
    }
    int ao = LB_E2 + (wv * E2V + c16 * E2U + 8 * g) * 4;
    int gy = WS_Y1 + id * (NB2 * 128) + tid * 16;
    int yo = LB_Y + (c16 >> 2) * OP + 2 * (144 * (((c16 & 3) - wv) & 3) + 16 * wv + 4 * g);
    int yl = LB_Y + tid * 16 + R * (OP - 1152);
    u32 oo = 2 * xoff - (tid - R * 72) * 16 + 2 * DM;
    pin(gre); pin(gim); pin(bre); pin(bim); pin(ai0); pin(ai1); pin(ao); pin(gy); pin(yo); pin(yl); pin(oo);
    if (wv == 0) {
        const bool ok = wait_flags<NB2>(rws, WS_F2, tag, lane);
        if (id == 0 && lane == 0 && ok) __builtin_amdgcn_raw_buffer_store_b32(tag, rws, WS_EPOCH, 0, 16);
    }
    bar();
    STAMP(9);
    {
        const u4 v = __builtin_amdgcn_raw_buffer_load_b128(rws, gy, 0, 16);
        if (tid <= 128) lds_st<u4>(lin + 1056 * 16, __builtin_amdgcn_raw_buffer_load_b128(rws, gy, 16384, 16));
        lds_st<u4>(lin, v);
    }
    bar();
    STAMP(10);
    {
        u4 d[2];
        u2 p[4];
#pragma unroll
        for (int i = 0; i < 4; i++) p[i] = lds_ld<u2>(sadr[i]);
#pragma unroll
        for (int s = 0; s < 2; s++) {
            const u4 a = {p[0][s], p[1][s], p[2][s], p[3][s]};
            const u4 t = pack4(mfma(a, gre, Z), mfma(a, gim, Z));
            d[s] = pack4(mfma(t, bre, Z), mfma(t, bim, Z));
        }
        if (c16 < 9) {
#pragma unroll
            for (int i = 0; i < 4; i++) lds_st<u2>(eadr[i], u2{d[0][i], d[1][i]});
        }
    }
    STAMP(15);
    bar();
    if (wv < 9) {
        const u4 p0 = lds_ld<u4>(ao), p1 = lds_ld<u4>(ao + 16);
#pragma unroll
        for (int s = 0; s < 2; s++) {
            const u4 a = {p0[s], p0[2 + s], p1[s], p1[2 + s]};
            const f4 y0 = mfma(a, ai0, Z), y1 = mfma(a, ai1, Z);
            lds_st<u2>(yo + s * (8 * OP), u2{pk(y0[0], y0[1]), pk(y0[2], y0[3])});
            lds_st<u2>(yo + s * (8 * OP) + 4 * OP, u2{pk(y1[0], y1[1]), pk(y1[2], y1[3])});
        }
    }
    bar();
    *(GAS u4*)((char*)out + oo) = lds_ld<u4>(yl);
    if (wv < 2) {
        const int r2 = tid < 56 ? 14 : 15, c2 = tid + 1024 - 72 * r2;
        *(GAS u4*)((char*)out + (u32)((s00 + 16 + 256 * (r2 - 8)) * (4 * DM) + 2 * DM + c2 * 16)) = lds_ld<u4>(LB_Y + r2 * OP + c2 * 16);
    }
    STAMP(11);
    STAMP_OUT(0);
}

DI f2 cexpa(float ar2, float air, float p) {
    const float e = __builtin_amdgcn_exp2f(ar2 * p), t = __builtin_amdgcn_fractf(air * p);
    return f2{e * __builtin_amdgcn_cosf(t), e * __builtin_amdgcn_sinf(t)};
}
DI u4 ld_k(int o) {
    const u2 a = *(const volatile LAS u2*)(u32)o, b = *(const volatile LAS u2*)(u32)(o + 8);
    return u4{a[0], a[1], b[0], b[1]};
}
DI void put8(int o, u4 a, u4 b) {
    lds_st<u4>(o, a); lds_st<u4>(o + 16, b);
    lds_st<u32>(o + 8316, a[0]); lds_st<u4>(o + 8320, u4{a[1], a[2], a[3], b[0]});
    lds_st<u2>(o + 8336, u2{b[1], b[2]}); lds_st<u32>(o + 8344, b[3]);
}
DI void put4(int o, u4 a) {
    lds_st<u4>(o, a);
    lds_st<u32>(o + 8316, a[0]); lds_st<u2>(o + 8320, u2{a[1], a[2]}); lds_st<u32>(o + 8328, a[3]);
}
DI void put2(int o, u32 a, u32 b) { lds_st<u2>(o, u2{a, b}); lds_st<u32>(o + 8316, a); lds_st<u32>(o + 8320, b); }

DI void a_rows_out(int yb, int ystr, const u3* ur, u3 dk, char* o) {
    u2 y[6];
#pragma unroll
    for (int c = 0; c < 6; c++) y[c] = lds_ld<u2>(yb + c * ystr);
#pragma unroll
    for (int r = 0; r < 4; r++) {
        u3 v;
#pragma unroll
        for (int c = 0; c < 3; c++) {
            const u32 s = pk(blo(ur[r][c]) * blo(dk[c]), bhi(ur[r][c]) * bhi(dk[c]));
            const u32 ya = y[2 * c][r >> 1], yc = y[2 * c + 1][r >> 1];
            v[c] = pk(((r & 1) ? bhi(ya) : blo(ya)) + blo(s), ((r & 1) ? bhi(yc) : blo(yc)) + bhi(s));
        }
        *(u3*)(o + r * (4 * DM)) = v;
    }
}

DI void head_a(const u16* __restrict__ u, const u16* __restrict__ lar, const u16* __restrict__ aimg,
               const u16* __restrict__ cre, const u16* __restrict__ cim, const u16* __restrict__ ldt,
               const u16* __restrict__ dsk, u16* __restrict__ out, char* ws) {
    const int tid = threadIdx.x, lane = tid & 63, c16 = lane & 15, g = lane >> 4;
    const int wv = __builtin_amdgcn_readfirstlane(tid >> 6);
    STAMP_DECL;
    const int ai_ = blockIdx.x - A0;
    const int d0 = 6 * ((ai_ & 7) * 12 + (ai_ >> 3));
    const f4 Z = {0.f, 0.f, 0.f, 0.f};

    if (wv >= 12) {
        const int t = tid - 768;
        u3 ua[4], ub[4];
#pragma unroll
        for (int r = 0; r < 4; r++) ua[r] = *(const u3*)(u + (4 * t + r) * DM + d0);
        const u3 dk = *(const u3*)(dsk + d0);
        if (t < 180) lds_st<u4>(LA_U + (t / 30) * 4608 + (t % 30) * 16, u4{0u, 0u, 0u, 0u});
        if (t == 0) lds_st<u2>(LA_F, u2{0u, 0u});
#pragma unroll
        for (int c = 0; c < 6; c++) {
            const u32 sel = (c & 1) ? 0x07060302u : 0x05040100u;
            lds_st<u2>(LA_U + c * 4608 + 480 + t * 8, u2{__builtin_amdgcn_perm(ua[1][c >> 1], ua[0][c >> 1], sel), __builtin_amdgcn_perm(ua[3][c >> 1], ua[2][c >> 1], sel)});
        }
        asm volatile("" ::: "memory");
#pragma unroll
        for (int r = 0; r < 4; r++) ub[r] = *(const u3*)(u + (1024 + 4 * t + r) * DM + d0);
        STAMP(7);
        bar();
        bar();
        bar();
        bar();
#pragma unroll
        for (int c = 0; c < 6; c++) {
            const u32 sel = (c & 1) ? 0x07060302u : 0x05040100u;
            lds_st<u2>(LA_U + c * 4608 + 480 + 2048 + t * 8, u2{__builtin_amdgcn_perm(ub[1][c >> 1], ub[0][c >> 1], sel), __builtin_amdgcn_perm(ub[3][c >> 1], ub[2][c >> 1], sel)});
        }
        asm volatile("s_waitcnt lgkmcnt(0)" ::: "memory");
        if (lane == 0) __hip_atomic_fetch_add((LAS u32*)(u32)LA_F, 1u, __ATOMIC_RELAXED, __HIP_MEMORY_SCOPE_WORKGROUP);
        STAMP(8);
        const int tile = wv - 12;
        char* o = (char*)(out + 4 * t * (2 * DM) + d0);
        a_rows_out(LA_Q + ((tile >> 1) ^ 1) * 2048 + (tile & 1) * 1024 + lane * 8, 4096, ua, dk, o);
        STAMP(9);
        bar();
        bar();
        a_rows_out(LA_K + (2 * ((tile >> 1) ^ 1) + (tile & 1)) * 4160 + lane * 8, 16640, ub, dk, o + 1024 * (4 * DM));
        STAMP(10);
        STAMP_OUT(768);
        return;
    }
    const int ch = wv >> 1, hf = wv & 1;
    {
        const int po = (d0 + ch) * NS + 64 * hf + lane;
        const u16 p_l = lar[po], p_a = aimg[po], p_r = cre[po], p_i = cim[po], p_t = ldt[d0 + ch];
        const float dt = rbf(expf(bf2f(p_t)));
        const float ar2 = -rbf(expf(bf2f(p_l))) * dt * 1.44269504f, air = bf2f(p_a) * dt * 0.159154943f;
        const f2 C = {2.f * dt * bf2f(p_r), 2.f * dt * bf2f(p_i)};
        STAMP(1);
        const f2 z1 = cexpa(ar2, air, 1.f), z8c = cexpa(ar2, -air, 8.f), z128 = cexpa(ar2, air, 128.f);
        const int gw = LA_K + wv * 4608 + lane * 4, gr = LA_K + wv * 4608 + c16 * 288 + g * 16;
        u4 bfr[4];
        {
            const f2 z16c = cmul(z8c, z8c);
            f2 w0 = {1.f, 0.f}, w1 = z8c;
#pragma unroll
            for (int r = 0; r < 16; r += 2) {
                lds_st<u32>(gw + r * 288, pk(w0.x, w0.y)); lds_st<u32>(gw + (r + 1) * 288, pk(w1.x, w1.y));
                if (r < 14) { w0 = r ? cmul(w0, z16c) : z16c; w1 = cmul(w1, z16c); }
            }
#pragma unroll
            for (int s = 0; s < 4; s++) bfr[s] = lds_ld<u4>(gr + 64 * s);
        }
        f2 e[8];
        e[0] = C;
#pragma unroll
        for (int j = 1; j < 8; j++) e[j] = cmul(e[j - 1], z1);
        const f2 z256 = cmul(z128, z128), z512 = cmul(z256, z256), z1024 = cmul(z512, z512);
        const int q = LA_Q + wv * 2576;
#pragma unroll
        for (int t = 0; t < 3; t++) {
            if (t == 0) {
#pragma unroll
                for (int j = 0; j < 8; j++) {
                    lds_st<u32>(gw + j * 288, pk(e[j].x, e[j].y));
                    const f2 f = cmul(e[j], z128);
                    lds_st<u32>(gw + (8 + j) * 288, pk(f.x, f.y));
                }
            } else if (t == 1) {
#pragma unroll
                for (int j = 0; j < 4; j++) {
                    f2 f = cmul(e[2 * j], z256);
                    lds_st<u32>(gw + j * 288, pk(f.x, f.y));
                    f = cmul(f, z128);
                    lds_st<u32>(gw + (4 + j) * 288, pk(f.x, f.y));
                }
#pragma unroll
                for (int j = 0; j < 2; j++) {
                    f2 f = cmul(e[4 * j], z512);
#pragma unroll
                    for (int k = 0; k < 4; k++) {
                        lds_st<u32>(gw + (8 + j + 2 * k) * 288, pk(f.x, f.y));
                        if (k < 3) f = cmul(f, z128);
                    }
                }
            } else {
                f2 f0 = cmul(C, z1024), f1 = cmul(f0, z128);
#pragma unroll
                for (int k = 0; k < 9; k += 2) {
                    lds_st<u32>(gw + k * 288, pk(f0.x, f0.y));
                    if (k < 8) { lds_st<u32>(gw + (k + 1) * 288, pk(f1.x, f1.y)); f0 = cmul(f0, z256); }
                    if (k < 6) f1 = cmul(f1, z256);
                }
            }
            f4 acc = Z;
#pragma unroll
            for (int s = 0; s < 4; s++) acc = mfma(lds_ld<u4>(gr + 64 * s), bfr[s], acc);
            if (t == 0) lds_st<f4>(q + (4 * (g & 1) + 128 * (g >> 1) + 8 * c16) * 4, acc);
            else if (t == 1) {
                if (g < 2) lds_st<f4>(q + (256 + 64 * g + 4 * c16) * 4, acc);
                else {
                    const int o = q + (384 + 64 * (g - 2) + 2 * c16) * 4;
                    lds_st<f2>(o, f2{acc[0], acc[1]}); lds_st<f2>(o + 128, f2{acc[2], acc[3]});
                }
            } else {
                if (g < 2) {
#pragma unroll
                    for (int i = 0; i < 4; i++) lds_st<float>(q + (512 + 64 * g + 16 * i + c16) * 4, acc[i]);
                } else if (lane == 32) lds_st<float>(q + 640 * 4, acc[0]);
            }
        }
    }
    STAMP(2);
    bar();
    STAMP(11);
    {
        const LAS float* qa = (const LAS float*)(u32)(LA_Q + ch * 5152);
        const LAS float* qb = qa + 644;
        const int kE = LA_K + ch * 16640, kO = kE + 4160;
        if (hf == 0) {
            const int j = 512 + 2 * lane;
            const float q0 = qa[j] + qb[j], q1 = qa[j + 1] + qb[j + 1], q2 = qa[j + 2] + qb[j + 2];
            const u32 a = pk(q2, q2), b = pk(q1, q1), c = pk(q0, q0);
            const int y = (504 - 8 * lane) * 4;
            put8(kE + y, u4{a, a, b, b}, u4{b, pk(q1, q0), c, c});
            put8(kO + y, u4{a, pk(q2, q1), b, b}, u4{b, c, c, c});
            if (lane < 36) {
                const int cp = lane / 9;
                if (cp >= 2 || lane != 9 * cp) lds_st<u32>(kE + cp * 4160 + (1023 + lane - 9 * cp) * 4, 0u);
            }
        } else {
            {
                const int j = 384 + 2 * lane;
                const float q0 = qa[j] + qb[j], q1 = qa[j + 1] + qb[j + 1], q2 = qa[j + 2] + qb[j + 2];
                const u32 b = pk(q1, q1), c = pk(q0, q0);
                const int y = (764 - 4 * lane) * 4;
                put4(kE + y, u4{pk(q2, q2), b, pk(q1, q0), c});
                put4(kO + y, u4{pk(q2, q1), b, c, c});
            }
            {
                const int j = 256 + 2 * lane;
                const float q0 = qa[j] + qb[j], q1 = qa[j + 1] + qb[j + 1], q2 = qa[j + 2] + qb[j + 2];
                const float qm = lane ? q0 : qa[255] + qb[255];
                const int y = (894 - 2 * lane) * 4;
                put2(kE + y, pk(q2, q1), pk(q0, q0));
                put2(kO + y, pk(q1, q0), pk(q0, qm));
            }
            {
                const f4 k = *(const LAS f4*)(qa + 4 * lane) + *(const LAS f4*)(qb + 4 * lane);
                const float km = lane ? qa[4 * lane - 1] + qb[4 * lane - 1] : 0.f;
                const int y = (1022 - 2 * lane) * 4;
                put2(kE + y, pk(k[3], k[2]), pk(k[1], k[0]));
                put2(kO + y, pk(k[2], k[1]), pk(k[0], km));
            }
        }
    }
    STAMP(12);
    bar();
    STAMP(3);
    const int x0 = 2031 - c16 + 8 * g;
    int ka = LA_K + ch * 16640 + (x0 & 3) * 4160 + (x0 >> 2) * 8 - 256 * hf - 192 - 3584;
    int ubs = LA_U + ch * 4608 + 480 + (16 * c16 + 8 * g - 16) * 2 - 256 * hf - 192;
    pin(ka); pin(ubs);
    f4 acc[4];
#define KFR(rho, p) ld_k(ka + 192 - 64 * (rho) + 3584 - 512 * (p))
#define UFR(rho, q) lds_ld<u4>(ubs + 192 - 64 * (rho) + 512 * (q))
#define SB __builtin_amdgcn_sched_barrier(0)
#define TRIANGLE(q0)                                                                          \
    {                                                                                         \
        u4 A[2][4], B[2][4];                                                                  \
        _Pragma("unroll") for (int p = 0; p < 4; p++) { B[0][p] = UFR(0, q0 + p); A[0][p] = KFR(0, p); } \
        _Pragma("unroll") for (int rho = 0; rho < 4; rho++)                                   \
            _Pragma("unroll") for (int p = 0; p < 4; p++) {                                   \
                SB;                                                                           \
                if (rho < 3) { B[(rho + 1) & 1][p] = UFR(rho + 1, q0 + p); A[(rho + 1) & 1][p] = KFR(rho + 1, p); } \
                _Pragma("unroll") for (int q = 0; p + q < 4; q++) acc[p + q] = mfma(A[rho & 1][p], B[rho & 1][q], acc[p + q]); \
            }                                                                                 \
        SB;                                                                                   \
    }
#define EXCHANGE(mine, other, str, S1)                                                        \
    {                                                                                         \
        if (hf) { lds_st<f4>((mine) + lane * 16, acc[0]); lds_st<f4>((mine) + (str) + lane * 16, acc[1]); acc[0] = acc[2]; acc[1] = acc[3]; } \
        else { lds_st<f4>((mine) + lane * 16, acc[2]); lds_st<f4>((mine) + (str) + lane * 16, acc[3]); } \
        STAMP(S1);                                                                            \
        bar();                                                                                \
        const f4 y0 = lds_ld<f4>((other) + lane * 16) + acc[0], y1 = lds_ld<f4>((other) + (str) + lane * 16) + acc[1]; \
        const int w = (other) + (16 * c16 + 4 * g) * 2;                                       \
        lds_st<u2>(w, u2{pk(y0[0], y0[1]), pk(y0[2], y0[3])});                                \
        lds_st<u2>(w + (str), u2{pk(y1[0], y1[1]), pk(y1[2], y1[3])});                        \
    }
#pragma unroll
    for (int i = 0; i < 4; i++) acc[i] = Z;
    TRIANGLE(0);
    EXCHANGE(LA_Q + wv * 2048, LA_Q + (wv ^ 1) * 2048, 1024, 4);
#pragma unroll
    for (int i = 0; i < 4; i++) acc[i] = Z;
    {
        u4 A[8], B[2][4];
#pragma unroll
        for (int p = 0; p < 4; p++) B[0][p] = UFR(0, p);
#pragma unroll
        for (int p = 1; p < 8; p++) A[p] = KFR(0, p);
        bar();
        STAMP(13);
#pragma unroll
        for (int rho = 0; rho < 4; rho++)
#pragma unroll
            for (int p = 1; p < 8; p++) {
                SB;
                if (rho < 3 && p < 5) B[(rho + 1) & 1][p - 1] = UFR(rho + 1, p - 1);
#pragma unroll
                for (int q = 0; q < 4; q++)
                    if (p + q >= 4 && p + q < 8) acc[p + q - 4] = mfma(A[p], B[rho & 1][q], acc[p + q - 4]);
                SB;
                if (rho < 3) A[p] = KFR(rho + 1, p);
            }
    }
    STAMP(14);
    pin(acc[0]); pin(acc[1]); pin(acc[2]); pin(acc[3]);
    if (lane == 0) __hip_atomic_fetch_add((LAS u32*)(u32)(LA_F + 4), 1u, __ATOMIC_RELAXED, __HIP_MEMORY_SCOPE_WORKGROUP);
    while (__builtin_amdgcn_readfirstlane(*(const volatile LAS u32*)(u32)LA_F) < 4) __builtin_amdgcn_s_sleep(1);
    asm volatile("" ::: "memory");
    TRIANGLE(4);
    pin(acc[0]); pin(acc[1]); pin(acc[2]); pin(acc[3]);
    while (__builtin_amdgcn_readfirstlane(*(const volatile LAS u32*)(u32)(LA_F + 4)) < 12) __builtin_amdgcn_s_sleep(1);
    asm volatile("" ::: "memory");
    EXCHANGE(LA_K + ch * 16640 + 2 * hf * 4160, LA_K + ch * 16640 + 2 * (hf ^ 1) * 4160, 4160, 5);
    bar();
    STAMP(6);
    STAMP_OUT(0);
}

__global__ void __launch_bounds__(NT) fused_kernel(const u16* __restrict__ u, const u16* __restrict__ lar, const u16* __restrict__ aimg,
                                                   const u16* __restrict__ cre, const u16* __restrict__ cim, const u16* __restrict__ ldt,
                                                   const u16* __restrict__ dsk, const u16* __restrict__ x, const u16* __restrict__ wr,
                                                   const u16* __restrict__ wi, u16* __restrict__ out, char* ws) {
    if (__builtin_expect(blockIdx.x < NB2, 1)) head_b(x, wr, wi, out, ws);
    else if (blockIdx.x >= A0) head_a(u, lar, aimg, cre, cim, ldt, dsk, out, ws);
    else if (threadIdx.x < 448) { u32 t = *(const u32*)((const char*)&TAB + F_F16 * 1024 + threadIdx.x * 128); asm volatile("" :: "v"(t)); }
}

__global__ void __launch_bounds__(64) stamp_kernel(unsigned long long* p) { if (threadIdx.x == 0) *p = __builtin_amdgcn_s_memrealtime(); }
__global__ void __launch_bounds__(64) until_kernel(const unsigned long long* p) {
    const unsigned long long t0 = *p; const unsigned long long s0 = __builtin_amdgcn_s_memrealtime();
    while (__builtin_amdgcn_s_memrealtime() - t0 < 10000ull && __builtin_amdgcn_s_memrealtime() - s0 < 20000ull) __builtin_amdgcn_s_sleep(1);
}
extern "C" void kernel_launch(void* const* d_in, const int* in_sizes, int n_in, void* d_out, int out_size, void* d_ws,
                              size_t ws_size, hipStream_t stream) {
    stamp_kernel<<<1, 64, 0, stream>>>((unsigned long long*)((char*)d_ws + (32u << 20)));
    (void)hipFuncSetAttribute((const void*)fused_kernel, hipFuncAttributeMaxDynamicSharedMemorySize, LDS_BYTES);
    fused_kernel<<<GRID, NT, LDS_BYTES, stream>>>((const u16*)d_in[0], (const u16*)d_in[1], (const u16*)d_in[2], (const u16*)d_in[3],
                                                  (const u16*)d_in[4], (const u16*)d_in[5], (const u16*)d_in[6], (const u16*)d_in[7],
                                                  (const u16*)d_in[8], (const u16*)d_in[9], (u16*)d_out, (char*)d_ws);
    until_kernel<<<1, 64, 0, stream>>>((unsigned long long*)((char*)d_ws + (32u << 20)));

}
```

```cpp
#include <hip/hip_runtime.h>

typedef unsigned short u16;
typedef unsigned int u32;
typedef float f2 __attribute__((ext_vector_type(2)));
typedef float f4 __attribute__((ext_vector_type(4)));
typedef u32 u2 __attribute__((ext_vector_type(2)));
typedef u32 u4 __attribute__((ext_vector_type(4)));
typedef int i8v __attribute__((ext_vector_type(8)));
typedef short s4 __attribute__((ext_vector_type(4)));
typedef short s8 __attribute__((ext_vector_type(8)));
typedef __bf16 bf2 __attribute__((ext_vector_type(2)));
#define DI __device__ __forceinline__
#define LAS __attribute__((address_space(3)))
#define GAS __attribute__((address_space(1)))
#define CAS __attribute__((address_space(4)))
#pragma clang diagnostic ignored "-Wint-to-pointer-cast"
struct u3 { u32 v[3]; DI u32& operator[](int i) { return v[i]; } DI const u32& operator[](int i) const { return v[i]; } };

constexpr int SEQ = 2048, DM = 576, NS = 128, DF = 289;
constexpr int NB1 = 128;
constexpr int NB2 = 145;
constexpr int A0 = 152;
constexpr int NA = 96;
constexpr int GRID = A0 + NA;
constexpr int NT = 1024;

constexpr u32 WS_EPOCH = 0, WS_F1 = 4096, WS_F2 = WS_F1 + 16 * 16 * 128, WS_X1 = 1u << 20, WS_Y1 = 4u << 20, WS_USED = 8u << 20;
#define STAMP(i)
#define STAMP_DECL
#define STAMP_OUT(T)

constexpr int F_SA = 0, F_EO = 4;
constexpr int F_ARE = 9, F_AIM = 10, F_BRE = 11, F_BIM = 12, F_G = 13;
constexpr int F_F16 = 21, F_GI = 23, F_AI = 31, F_M1C = 33;
constexpr int F_VAR = 37;
constexpr int F_END = F_VAR + 40;
constexpr int ET = 452, ER = 28, E2V = 580, E2U = 36, OP = 1168;
constexpr int LB_X = 0, LB_I = 0, LB_Y = 0;
constexpr int LB_E = 16 * OP;
constexpr int LB_O = LB_E, LB_E2 = LB_E;
constexpr int LB_S = LB_E + 16 * ET * 4;
constexpr int LB_W = LB_S + 1200 * 16;
constexpr int LB_T = LB_W + 2 * 256 * 36;
constexpr int LB_G = LB_T + (F_VAR + 16) * 1024;
constexpr int LB_END = LB_G + 16;
struct CosTab { double c[2048]; double c9[9], s9[9]; };
constexpr double tay_cos(double x) { double t = 1, s = 1; for (int k = 1; k <= 14; k++) { t *= -x * x / ((2 * k - 1) * (2 * k)); s += t; } return s; }
constexpr double tay_sin(double x) { double t = x, s = x; for (int k = 1; k <= 14; k++) { t *= -x * x / ((2 * k + 1) * (2 * k)); s += t; } return s; }
constexpr CosTab make_cos() {
    CosTab t = {};
    const double PI2 = 6.283185307179586476925;
    for (int k = 0; k <= 512; k++) {
        const double v = k == 512 ? 0.0 : (k <= 256 ? tay_cos(PI2 * k / 2048) : tay_sin(PI2 * (512 - k) / 2048));
        t.c[k] = v; t.c[(2048 - k) & 2047] = v; t.c[1024 - k] = -v; t.c[(1024 + k) & 2047] = -v;
    }
    t.c[512] = 0.0; t.c[1536] = 0.0;
    for (int k = 0; k < 9; k++) { const double a = PI2 * (k <= 4 ? k : k - 9) / 9; t.c9[k] = tay_cos(a); t.s9[k] = tay_sin(a); }
    return t;
}
constexpr CosTab CT = make_cos();
constexpr double cs(int n) { return CT.c[n & 2047]; }
constexpr double sn(int n) { return CT.c[(n - 512) & 2047]; }
constexpr u32 bfr(double x) { const u32 b = __builtin_bit_cast(u32, (float)x); return (b + 0x7fffu + ((b >> 16) & 1u)) >> 16; }
constexpr u32 pkc(double lo, double hi) { return bfr(lo) | (bfr(hi) << 16); }
constexpr u32 f32c(double x) { return __builtin_bit_cast(u32, (float)x); }
template <int N> struct Frags { u32 w[N * 256]; };
constexpr u32 w_re(double amp, int n) { return pkc(amp * cs(n), amp * sn(n)); }
constexpr u32 w_im(double amp, int n) { return pkc(-amp * sn(n), amp * cs(n)); }
constexpr Frags<F_VAR> make_t0() {
    Frags<F_VAR> t = {};
    const double R2 = 1.4142135623730951;
    for (int l = 0; l < 64; l++)
        for (int i = 0; i < 4; i++) {
            const int c = l & 15, g = l >> 4, r = 4 * g + i, o = l * 4 + i;
            {
                const int b = c >> 2, tq = c & 3;
                const double amp = b ? 1.0 : R2;
                const int n0 = 512 * tq * g + (b ? 256 * b * (2 * i) : -256), n1 = 512 * tq * g + (b ? 256 * b * (2 * i + 1) : 256);
                t.w[F_ARE * 256 + o] = pkc(amp * cs(n0), amp * cs(n1));
                t.w[F_AIM * 256 + o] = pkc(-amp * sn(n0), -amp * sn(n1));
            }
            for (int w = 0; w < 9; w++) {
                const int sh = 16 * (i & 1), oo = w * 128 + l * 2 + (i >> 1);
                t.w[F_EO * 256 + oo] |= (u32)((c * ET + r * ER + 2 * ((7 * w + r) % 9)) * 4) << sh;
                if (w < 8) {
                    const int k = (9 * (w & 3) + 36 * r + 64 * (c % 9)) % DM, slot = w < 4 ? (k <= 288 ? 2 * k : 2 * (DM - k) + 1) : k;
                    t.w[F_SA * 256 + oo] |= (u32)(slot * 8 + (slot >> 6) * 16) << sh;
                }
            }
            if (r < 9) { const int k = (r * c) % 9; t.w[F_BRE * 256 + o] = pkc(CT.c9[k], CT.s9[k]); t.w[F_BIM * 256 + o] = pkc(-CT.s9[k], CT.c9[k]); }
            for (int q = 0; q < 4; q++) {
                t.w[(F_G + 2 * q) * 256 + o] = w_re(1.0, 32 * r * (q + 4 * c)); t.w[(F_G + 2 * q + 1) * 256 + o] = w_im(1.0, 32 * r * (q + 4 * c));
                t.w[(F_GI + 2 * q) * 256 + o] = w_re(1.0, 32 * c * (q + 4 * r)); t.w[(F_GI + 2 * q + 1) * 256 + o] = w_im(1.0, 32 * c * (q + 4 * r));
            }
            t.w[F_F16 * 256 + o] = w_re(1.0, 128 * r * c); t.w[(F_F16 + 1) * 256 + o] = w_im(1.0, 128 * r * c);
            for (int h = 0; h < 2; h++) {
                const int j = 4 * h + (c >> 2), n = 512 * i * (c & 3) + (g ? 256 * g * j : ((j & 1) ? 256 : -256));
                t.w[(F_AI + h) * 256 + o] = w_re(g ? 2.0 : R2, n);
                const int m = 16 * r * (4 * h + 8 * c) - 512 * h;
                t.w[(F_M1C + 2 * h) * 256 + o] = pkc(0.5 * cs(m), -0.5 * sn(m));
                t.w[(F_M1C + 2 * h + 1) * 256 + o] = pkc(-0.5 * sn(m), -0.5 * cs(m));
            }
        }
    return t;
}
constexpr Frags<40> make_t1() {
    Frags<40> t = {};
    for (int v = 0; v < 5; v++)
        for (int l = 0; l < 64; l++)
            for (int i = 0; i < 4; i++) {
                const int be = v < 3 ? v + 1 : 4 * (v - 3), c = l & 15, r = 4 * (l >> 4) + i, o = v * 2048 + l * 4 + i;
                const double amp = v < 3 ? 1.0 : 0.5;
                const int n1 = 16 * r * (be + 8 * c) + (v == 4 ? 512 : 0), n4 = 16 * c * (be + 8 * r);
                t.w[o] = w_re(amp, n1); t.w[o + 256] = w_im(amp, n1);
                t.w[o + 512] = w_re(1.0, n4); t.w[o + 768] = w_im(1.0, n4);
                t.w[o + 1024] = f32c(cs(r * (be + 8 * c))); t.w[o + 1280] = f32c(-sn(r * (be + 8 * c)));
                t.w[o + 1536] = f32c(cs(c * (be + 8 * r))); t.w[o + 1792] = f32c(-sn(c * (be + 8 * r)));
            }
    return t;
}
struct Tables { Frags<F_VAR> a; Frags<40> b; };
constexpr Frags<F_VAR> T0 = make_t0();
constexpr Frags<40> T1 = make_t1();
static __device__ const __attribute__((aligned(128))) Tables TAB = {T0, T1};
constexpr int LA_K = 0;
constexpr int LA_U = LA_K + 6 * 16640;
constexpr int LA_Q = LA_U + 6 * 4608;
constexpr int LA_F = LA_Q + 12 * 2576;
constexpr int LA_D = LA_F + 256;
constexpr int LA_END = LA_D + 256;
constexpr int FQ = 0, FK = 6, FX1 = 12, F2A = 18, FX2 = 24, FU1 = 30, FU2 = 31, FY1 = 32, FY2 = 33;
constexpr int LDS_BYTES = (LA_END > LB_END ? LA_END : LB_END);
static_assert(LDS_BYTES <= 160 * 1024 && LDS_BYTES > 80 * 1024, "one workgroup per CU");

DI u32 pk(float a, float b) { f2 v = {a, b}; return __builtin_bit_cast(u32, __builtin_convertvector(v, bf2)); }
DI float blo(u32 v) { return __uint_as_float(v << 16); }
DI float bhi(u32 v) { return __uint_as_float(v & 0xffff0000u); }
DI float bf2f(u16 v) { return __uint_as_float((u32)v << 16); }
DI float rbf(float x) { return blo(pk(x, x)); }
DI f2 cmul(f2 a, f2 b) { f2 t = a.xx * b; f2 s = {-b.y, b.x}; return __builtin_elementwise_fma(a.yy, s, t); }
DI f4 mfma(u4 a, u4 b, f4 c) { return __builtin_amdgcn_mfma_f32_16x16x32_bf16(__builtin_bit_cast(s8, a), __builtin_bit_cast(s8, b), c, 0, 0, 0); }
DI u4 pack4(f4 re, f4 im) { u4 r = {pk(re[0], im[0]), pk(re[1], im[1]), pk(re[2], im[2]), pk(re[3], im[3])}; return r; }
DI u4 twid(f4 re, f4 im, f4 wr, f4 wi) { return pack4(__builtin_elementwise_fma(re, wr, -(im * wi)), __builtin_elementwise_fma(re, wi, im * wr)); }
DI int mod9(int x) { return x - 9 * ((x * 57) >> 9); }
template <class T> DI void pin(T& v) { asm volatile("" : "+v"(v)); }

template <class T> DI T lds_ld(int off) { return *(const LAS T*)(u32)off; }
template <class T> DI void lds_st(int off, T v) { *(LAS T*)(u32)off = v; }
DI void bar() { __syncthreads(); }
DI void glds16(const void* g, u32 off, int lds) {
    u32 keep;
    asm volatile("s_mov_b32 %0, m0\n\ts_mov_b32 m0, %3\n\ts_nop 4\n\tglobal_load_lds_dwordx4 %1, %2\n\ts_mov_b32 m0, %0" : "=&s"(keep) : "v"(off), "s"(g), "s"(lds) : "memory");
}

DI void publish_tag(u32 epoch) {
    u32 t;
    asm volatile("v_add_u32 %0, 0x9E3779B1, %1\n\tds_write_b32 %2, %0\n\ts_waitcnt lgkmcnt(0)" : "=&v"(t) : "v"(epoch), "v"(LB_G) : "memory");
}
DI void wait_flag(__amdgpu_buffer_rsrc_t r, u32 off, u32 tag, bool on) {
    if (on) {
        const unsigned long long t0 = __builtin_amdgcn_s_memrealtime();
        while (!__all(__builtin_amdgcn_raw_buffer_load_b32(r, off, 0, 16) == tag)) {
            if (__builtin_amdgcn_s_memrealtime() - t0 > 5000000ull) break;
            asm volatile("" ::: "memory");
        }
    }
    asm volatile("" ::: "memory");
}

constexpr int CODE_LINES = 128;
DI void warm_code(const char* entry) {
    if (threadIdx.x < CODE_LINES) { const u32 t = *(const GAS u32*)(entry + threadIdx.x * 128); asm volatile("" :: "v"(t)); }
}

DI void head_b(const char* entry, const u16* __restrict__ x, const u16* __restrict__ wr, const u16* __restrict__ wi,
               u16* __restrict__ out, char* ws) {
    const int tid = threadIdx.x, lane = tid & 63, c16 = lane & 15, g = lane >> 4;
    const int wv = __builtin_amdgcn_readfirstlane(tid >> 6);
    const int id = blockIdx.x;
    STAMP_DECL;
    const bool rows = id < NB1;
    const int s00 = 32 * (id >> 4) + (id & 15);
    const char* tab = (const char*)&TAB;
    const int R = (tid * 911) >> 16;
    const u32 xoff = (s00 + 16 * (R >> 3) + 256 * (R & 7)) * (2 * DM) + (tid - R * 72) * 16;
    asm volatile("" :: "s"(x), "s"(wr), "s"(wi), "s"(out), "s"(ws), "v"(xoff));
    const __amdgpu_buffer_rsrc_t rws = __builtin_amdgcn_make_buffer_rsrc(ws, 0, WS_USED, 0x00020000);
    u32 epoch = 0;
    if (wv == 0) asm volatile("global_load_dword %0, %0, %1 sc1" : "+v"(epoch) : "s"(ws) : "memory");
    if (rows) {
        glds16(x, xoff, LB_X + wv * 1024);
        if (wv < 2) glds16(x, (s00 + 16 + 1536) * (2 * DM) + (tid < 56 ? (tid + 16) * 16 : 256 * 2 * DM + (tid - 56) * 16), LB_X + 16384 + wv * 1024);
        else glds16(tab, tid * 16 - 2048, LB_T + (wv - 2) * 1024);
        if (wv >= 9) glds16(tab, tid * 16 + 5 * 1024, LB_T + (wv + 5) * 1024);
    }
    const int u2i = (id & 7) ? (id & 7) * 18 + 1 + (id >> 3) : (id == 136 ? 0 : (id >> 3) + (id < 136 ? 1 : 0));
    const bool star = u2i >= 108;
    const int q36 = min((u2i * 57) >> 11, 3), kg = u2i - 36 * q36, bq = (q36 + 1) & 3;
    const bool mir = !star && kg >= 18;
    const int w0 = star ? min(16 * kg, 562) : (mir ? (2048 - bq) * (2 * DF) + 1122 - 32 * kg : bq * (2 * DF) + 32 * kg);
    if (wv < 8) {
        glds16(tab, F_F16 * 1024 + tid * 16, LB_T + F_F16 * 1024 + wv * 1024);
        glds16(tab, (F_F16 + 8) * 1024 + tid * 16, LB_T + (F_F16 + 8) * 1024 + wv * 1024);
        glds16(tab, (F_VAR + 8 * q36) * 1024 + tid * 16, LB_T + F_VAR * 1024 + wv * 1024);
        glds16(tab, (F_VAR + 8 * q36 + 8) * 1024 + tid * 16, LB_T + (F_VAR + 8) * 1024 + wv * 1024);
    }
    __builtin_amdgcn_s_barrier();
    const u32 woff = w0 + ((tid >> 1) & 255) * (mir ? -16 * DF : 16 * DF) + (tid & 1) * (star ? 8 * DF : 16);
    u4 wra, wrb;
    if (wv >= 8) {
        __builtin_memcpy(&wra, (const char*)wr + woff, 16);
        __builtin_memcpy(&wrb, (const char*)wi + woff, 16);
    }
    __builtin_amdgcn_sched_barrier(0);
    const f4 Z = {0.f, 0.f, 0.f, 0.f};
    const int tau = wv & 3, bw = wv >> 2;
    int tl = LB_T + lane * 16;
    int lin = LB_S + (tid + (tid >> 5)) * 16;
    pin(tl); pin(lin);
    int sadr[4];

    if (rows) {
        if (tid < 256) {
            const int o = LB_E + 72 + (tid & 15) * (ER * 4) + (tid >> 4) * (ET * 4);
            lds_st<u2>(o, u2{0u, 0u}); lds_st<u4>(o + 8, u4{0u, 0u, 0u, 0u});
        }
        int xo = LB_X + (c16 >> 2) * 1152 + 2 * (144 * ((g - wv) & 3) + 16 * wv + 4 * (c16 & 3));
        int bo = LB_E + (wv * ET + c16 * ER + min(8 * g, 16)) * 4;
        int go = WS_X1 + (tid >> 3) * 16384 + id * 128 + (tid & 7) * 16;
        int ta = LB_T + lane * 8;
        pin(xo); pin(bo); pin(go); pin(ta);
        STAMP(1);
        asm volatile("s_cmp_lt_u32 %0, 8\n\ts_cbranch_scc1 .Lw%=\n\ts_waitcnt vmcnt(2)\n.Lw%=:\n\ts_waitcnt vmcnt(4)" :: "s"(wv) : "memory", "scc");
        if (wv == 0) publish_tag(epoch);
        bar();
        STAMP(2);
        if (wv < 9) {
            u4 d[2];
#pragma unroll
            for (int s = 0; s < 2; s++) {
                const s4 lo = __builtin_amdgcn_ds_read_tr16_b64_v4i16((LAS s4*)(u32)(xo + s * 9216));
                const s4 hi = __builtin_amdgcn_ds_read_tr16_b64_v4i16((LAS s4*)(u32)(xo + s * 9216 + 4608));
                d[s] = __builtin_bit_cast(u4, __builtin_shufflevector(lo, hi, 0, 1, 2, 3, 4, 5, 6, 7));
            }
            const u4 are = lds_ld<u4>(tl + F_ARE * 1024), aim = lds_ld<u4>(tl + F_AIM * 1024);
            const u2 eo = lds_ld<u2>(ta + F_EO * 1024 + wv * 512);
#pragma unroll
            for (int s = 0; s < 2; s++) d[s] = pack4(mfma(d[s], are, Z), mfma(d[s], aim, Z));
#pragma unroll
            for (int i = 0; i < 4; i++) lds_st<u2>(LB_E + ((i & 1) ? eo[i >> 1] >> 16 : eo[i >> 1] & 0xffff), u2{d[0][i], d[1][i]});
        }
        u4 bre = lds_ld<u4>(tl + F_BRE * 1024), bim = lds_ld<u4>(tl + F_BIM * 1024);
        u4 gre = lds_ld<u4>(tl + (F_G + 2 * tau) * 1024), gim = lds_ld<u4>(tl + (F_G + 2 * tau + 1) * 1024);
        {
            const u2 sa = lds_ld<u2>(ta + F_SA * 1024 + (tau + (bw ? 4 : 0)) * 512);
            const int base = LB_S + ((bw + 3) & 3) * (DM * 8 + 144);
#pragma unroll
            for (int i = 0; i < 4; i++) { sadr[i] = base + ((i & 1) ? sa[i >> 1] >> 16 : sa[i >> 1] & 0xffff); pin(sadr[i]); }
        }
        pin(bre); pin(bim); pin(gre); pin(gim);
        bar();
        STAMP(13);
        {
            const u4 p0 = lds_ld<u4>(bo), p1 = lds_ld<u4>(bo + 16);
            u4 d[2];
#pragma unroll
            for (int s = 0; s < 2; s++) {
                const u4 a = {p0[s], p0[2 + s], p1[s], p1[2 + s]};
                const u4 t = pack4(mfma(a, bre, Z), mfma(a, bim, Z));
                d[s] = pack4(mfma(gre, t, Z), mfma(gim, t, Z));
            }
            if (c16 < 9) {
#pragma unroll
                for (int i = 0; i < 4; i++) lds_st<u2>(sadr[i], u2{d[0][i], d[1][i]});
                if (wv == 0 && c16 == 0 && !(g & 1)) lds_st<u2>(sadr[0] + 8, u2{d[0][0], d[1][0]});
            }
        }
        STAMP(14);
        bar();
        STAMP(3);
        __builtin_amdgcn_raw_buffer_store_b128(lds_ld<u4>(lin), rws, go, 0, 16);
        if (tid <= 128) __builtin_amdgcn_raw_buffer_store_b128(lds_ld<u4>(lin + 1056 * 16), rws, go, 128 * 16384, 16);
    } else {
        if (wv < 8 && (kg == 0 || kg == (star ? 36 : 18))) {
            const int m = tid & 255;
            lds_st<u16>(LB_W + tid * 36 + 32, ((wv >> 2) ? wi : wr)[((mir ? bq + 8 * m : 2048 - bq - 8 * m) & 2047) * DF + (kg ? 288 : 0)]);
        }
        warm_code(entry);
        asm volatile("s_waitcnt vmcnt(0)" ::: "memory");
        if (wv == 0) publish_tag(epoch);
        bar();
    }
    const u32 tag = lds_ld<u32>(LB_G);
    if (wv >= 8) {
        const int o = LB_W + ((tid >> 1) & 255) * 36 + (tid & 1) * 16;
#pragma unroll
        for (int i = 0; i < 4; i++) { lds_st<u32>(o + 4 * i, wra[i]); lds_st<u32>(o + 9216 + 4 * i, wrb[i]); }
    }
    asm volatile("s_waitcnt vmcnt(0)" ::: "memory");
    if (rows && lane == 0) __builtin_amdgcn_raw_buffer_store_b32(tag, rws, WS_F1 + (wv * 16 + (id >> 3)) * 128 + (id & 7) * 4, 0, 16);
    STAMP(4);
    bar();
    STAMP(12);

    const int typ = star ? (wv & 1) : 0;
    const int tv = tl + (F_VAR + 8 * typ) * 1024;
    u4 fre = lds_ld<u4>(tl + F_F16 * 1024), fim = lds_ld<u4>(tl + (F_F16 + 1) * 1024);
    u4 m1r = lds_ld<u4>(tv), m1i = lds_ld<u4>(tv + 1024), m4r = lds_ld<u4>(tv + 2048), m4i = lds_ld<u4>(tv + 3072);
    f4 twr = lds_ld<f4>(tv + 4096), twi = lds_ld<f4>(tv + 5120), tur = lds_ld<f4>(tv + 6144), tui = lds_ld<f4>(tv + 7168);
    u4 mcr = lds_ld<u4>(tl + (F_M1C + 2 * typ) * 1024), mci = lds_ld<u4>(tl + (F_M1C + 2 * typ + 1) * 1024);
    f4 wfr, wfi;
    {
        const int kcol = star ? 8 * kg + (wv >> 1) : 16 * kg + wv;
        const int cc = star ? 8 * typ + (u2i < NB2 - 1 ? (wv >> 1) : 7) : (mir ? 15 - wv : wv);
        const int wo = LB_W + (c16 + 64 * g) * 36 + cc * 2;
#pragma unroll
        for (int i = 0; i < 4; i++) {
            wfr[i] = bf2f(lds_ld<u16>(wo + i * 576));
            wfi[i] = bf2f(lds_ld<u16>(wo + i * 576 + 9216));
        }
        float sc = 1.f / (float)(SEQ * DM);
        if (kcol == 0 || kcol == 288) {
            sc *= 0.5f;
#pragma unroll
            for (int i = 0; i < 4; i++) {
                const int m = c16 + 64 * g + 16 * i;
                const int o = LB_W + (typ ? (255 - m) * 36 + cc * 2 : m * 36 + 32);
                wfr[i] += bf2f(lds_ld<u16>(o));
                wfi[i] -= bf2f(lds_ld<u16>(o + 9216));
            }
        }
        wfr *= sc; wfi *= (mir ? -sc : sc);
    }
    f4 wnr = -wfr, wni = -wfi;
    int io = (32 * g + c16) * 128 + (((wv >> 1) ^ (c16 >> 1)) * 16);
    int so = (tid >> 3) * 128 + (((tid & 7) ^ ((tid >> 4) & 7)) * 16);
    pin(fre); pin(fim); pin(m1r); pin(m1i); pin(m4r); pin(m4i); pin(twr); pin(twi); pin(tur); pin(tui); pin(mcr); pin(mci);
    int gi = WS_X1 + u2i * 16384 + tid * 16, gq = WS_Y1 + (tid >> 3) * (NB2 * 128) + u2i * 128 + (tid & 7) * 16;
    int i1 = LB_I + io + (star ? 0 : (wv & 1) * 8);
    io += LB_O + (wv & 1) * 8;
    pin(wfr); pin(wnr); pin(wni); pin(io); pin(so); pin(gi); pin(gq); pin(i1);
    wait_flag(rws, WS_F1 + (((u2i >> 3) & 15) * 16 + wv) * 128 + lane * 4, tag, lane < 8);
    STAMP(5);
    lds_st<u4>(LB_I + so, __builtin_amdgcn_raw_buffer_load_b128(rws, gi, 0, 16));
    bar();
    STAMP(6);
    {
        const int o1 = i1;
        const u2 a0 = lds_ld<u2>(o1), a1 = lds_ld<u2>(o1 + 2048);
        const u4 a = {a0[0], a0[1], a1[0], a1[1]};
        f4 re = mfma(a, m1r, Z), im = mfma(a, m1i, Z);
        if (star) {
            const u2 b0 = lds_ld<u2>(o1 + 8), b1 = lds_ld<u2>(o1 + 8 + 2048);
            const u4 b = {b0[0], b0[1], b1[0], b1[1]};
            re = mfma(b, mcr, re); im = mfma(b, mci, im);
        }
        u4 t = twid(re, im, twr, twi);
        re = mfma(fre, t, Z); im = mfma(fim, t, Z);
        t = pack4(__builtin_elementwise_fma(re, wfr, im * wni), __builtin_elementwise_fma(re, wni, im * wnr));
        re = mfma(t, fre, Z); im = mfma(t, fim, Z);
        t = twid(re, im, tur, tui);
        t = pack4(mfma(m4r, t, Z), mfma(m4i, t, Z));
        lds_st<u2>(io, u2{t[0], t[1]}); lds_st<u2>(io + 2048, u2{t[2], t[3]});
    }
    bar();
    STAMP(7);
    {
        u4 v = lds_ld<u4>(LB_O + so);
        if (star) {
            u4 w;
#pragma unroll
            for (int s = 0; s < 2; s++) {
                const float ar = blo(v[s]), ai = bhi(v[s]), cr = blo(v[2 + s]), ci = bhi(v[2 + s]);
                w[s] = pk(ar + ci, ai - cr); w[2 + s] = pk(ar - ci, -ai - cr);
            }
            v = w;
        }
        if (u2i < NB2 - 1 || (tid & 7) == 0)
            __builtin_amdgcn_raw_buffer_store_b128(v, rws, gq, 0, 16);
    }
    asm volatile("s_waitcnt vmcnt(0)" ::: "memory");
    if (lane == 0) __builtin_amdgcn_raw_buffer_store_b32(tag, rws, WS_F2 + (wv * 19 + (u2i >> 3)) * 128 + (u2i & 7) * 4, 0, 16);
    STAMP(8);
    if (!rows) { STAMP_OUT(0); return; }

    u4 gre = lds_ld<u4>(tl + (F_GI + 2 * tau) * 1024), gim = lds_ld<u4>(tl + (F_GI + 2 * tau + 1) * 1024);
    u4 bre = lds_ld<u4>(tl + F_BRE * 1024), bim = lds_ld<u4>(tl + F_BIM * 1024);
    u4 ai0 = lds_ld<u4>(tl + F_AI * 1024), ai1 = lds_ld<u4>(tl + (F_AI + 1) * 1024);
    int eadr[4];
#pragma unroll
    for (int i = 0; i < 4; i++) {
        const int r = 4 * g + i;
        eadr[i] = LB_E2 + (mod9(4 * (c16 + 18 - r)) * E2V + r * E2U + 2 * wv) * 4;
        pin(eadr[i]);
    }
    int ao = LB_E2 + (wv * E2V + c16 * E2U + 8 * g) * 4;
    int gy = WS_Y1 + id * (NB2 * 128) + tid * 16;
    int yo = LB_Y + (c16 >> 2) * OP + 2 * (144 * (((c16 & 3) - wv) & 3) + 16 * wv + 4 * g);
    int yl = LB_Y + tid * 16 + R * (OP - 1152);
    u32 oo = 2 * xoff - (tid - R * 72) * 16 + 2 * DM;
    pin(gre); pin(gim); pin(bre); pin(bim); pin(ai0); pin(ai1); pin(ao); pin(gy); pin(yo); pin(yl); pin(oo);
    {
        const int un = 8 * wv + lane + (lane >= 8 ? 120 : 0);
        wait_flag(rws, WS_F2 + ((id >> 3) * 19 + (un >> 3)) * 128 + (lane & 7) * 4, tag, lane < 16 && un < NB2);
    }
    STAMP(9);
    {
        const u4 v = __builtin_amdgcn_raw_buffer_load_b128(rws, gy, 0, 16);
        if (tid <= 128) lds_st<u4>(lin + 1056 * 16, __builtin_amdgcn_raw_buffer_load_b128(rws, gy, 16384, 16));
        lds_st<u4>(lin, v);
    }
    bar();
    if (id == 0 && tid == 0) __builtin_amdgcn_raw_buffer_store_b32(tag, rws, WS_EPOCH, 0, 16);
    STAMP(10);
    {
        u4 d[2];
        u2 p[4];
#pragma unroll
        for (int i = 0; i < 4; i++) p[i] = lds_ld<u2>(sadr[i]);
#pragma unroll
        for (int s = 0; s < 2; s++) {
            const u4 a = {p[0][s], p[1][s], p[2][s], p[3][s]};
            const u4 t = pack4(mfma(a, gre, Z), mfma(a, gim, Z));
            d[s] = pack4(mfma(t, bre, Z), mfma(t, bim, Z));
        }
        if (c16 < 9) {
#pragma unroll
            for (int i = 0; i < 4; i++) lds_st<u2>(eadr[i], u2{d[0][i], d[1][i]});
        }
    }
    STAMP(15);
    bar();
    if (wv < 9) {
        const u4 p0 = lds_ld<u4>(ao), p1 = lds_ld<u4>(ao + 16);
#pragma unroll
        for (int s = 0; s < 2; s++) {
            const u4 a = {p0[s], p0[2 + s], p1[s], p1[2 + s]};
            const f4 y0 = mfma(a, ai0, Z), y1 = mfma(a, ai1, Z);
            lds_st<u2>(yo + s * (8 * OP), u2{pk(y0[0], y0[1]), pk(y0[2], y0[3])});
            lds_st<u2>(yo + s * (8 * OP) + 4 * OP, u2{pk(y1[0], y1[1]), pk(y1[2], y1[3])});
        }
    }
    bar();
    *(GAS u4*)((char*)out + oo) = lds_ld<u4>(yl);
    if (wv < 2) {
        const int r2 = tid < 56 ? 14 : 15, c2 = tid + 1024 - 72 * r2;
        *(GAS u4*)((char*)out + (u32)((s00 + 16 + 256 * (r2 - 8)) * (4 * DM) + 2 * DM + c2 * 16)) = lds_ld<u4>(LB_Y + r2 * OP + c2 * 16);
    }
    STAMP(11);
    STAMP_OUT(0);
}

DI f2 cexpa(float ar2, float air, float p) {
    const float e = __builtin_amdgcn_exp2f(ar2 * p), t = __builtin_amdgcn_fractf(air * p);
    return f2{e * __builtin_amdgcn_cosf(t), e * __builtin_amdgcn_sinf(t)};
}
DI u4 ld_k(int o) {
    const u2 a = *(const volatile LAS u2*)(u32)o, b = *(const volatile LAS u2*)(u32)(o + 8);
    return u4{a[0], a[1], b[0], b[1]};
}
DI void put8(int o, u4 a, u4 b) {
    lds_st<u4>(o, a); lds_st<u4>(o + 16, b);
    lds_st<u32>(o + 8316, a[0]); lds_st<u4>(o + 8320, u4{a[1], a[2], a[3], b[0]});
    lds_st<u2>(o + 8336, u2{b[1], b[2]}); lds_st<u32>(o + 8344, b[3]);
}
DI void put4(int o, u4 a) {
    lds_st<u4>(o, a);
    lds_st<u32>(o + 8316, a[0]); lds_st<u2>(o + 8320, u2{a[1], a[2]}); lds_st<u32>(o + 8328, a[3]);
}
DI void put2(int o, u32 a, u32 b) { lds_st<u2>(o, u2{a, b}); lds_st<u32>(o + 8316, a); lds_st<u32>(o + 8320, b); }

DI void a_rows_out(int yb, int ystr, char* o) {
    u2 y[6];
#pragma unroll
    for (int c = 0; c < 6; c++) y[c] = lds_ld<u2>(yb + c * ystr);
#pragma unroll
    for (int r = 0; r < 4; r++) {
        u3 v;
#pragma unroll
        for (int c = 0; c < 3; c++) v[c] = __builtin_amdgcn_perm(y[2 * c + 1][r >> 1], y[2 * c][r >> 1], (r & 1) ? 0x07060302u : 0x05040100u);
        *(u3*)(o + r * (4 * DM)) = v;
    }
}

DI void sig(int f) {
    asm volatile("s_waitcnt lgkmcnt(0)" ::: "memory");
    if ((threadIdx.x & 63) == 0) __hip_atomic_fetch_add((LAS u32*)(u32)(LA_F + 4 * f), 1u, __ATOMIC_RELAXED, __HIP_MEMORY_SCOPE_WORKGROUP);
}
template <int SLEEP = 0> DI void await(int f, u32 n) {
    while ((u32)__builtin_amdgcn_readfirstlane(*(const volatile LAS u32*)(u32)(LA_F + 4 * f)) < n) if (SLEEP) __builtin_amdgcn_s_sleep(SLEEP);
    asm volatile("" ::: "memory");
}
DI float exp_bf(float x) {
    const float t = x * 1.44269504f, r = __builtin_fmaf(x, 1.92596299e-8f, __builtin_fmaf(x, 1.44269504f, -t));
    const float e = __builtin_amdgcn_exp2f(t);
    return __builtin_fmaf(e * 0.693147181f, r, e);
}

DI void head_a(const u16* __restrict__ u, const u16* __restrict__ lar, const u16* __restrict__ aimg,
               const u16* __restrict__ cre, const u16* __restrict__ cim, const u16* __restrict__ ldt,
               const u16* __restrict__ dsk, u16* __restrict__ out, char* ws) {
    const int tid = threadIdx.x, lane = tid & 63, c16 = lane & 15, g = lane >> 4;
    const int wv = __builtin_amdgcn_readfirstlane(tid >> 6);
    STAMP_DECL;
    const int ai_ = blockIdx.x - A0;
    const int d0 = 6 * ((ai_ & 7) * 12 + (ai_ >> 3));
    const f4 Z = {0.f, 0.f, 0.f, 0.f};

    if (wv >= 12) {
        __builtin_amdgcn_s_setprio(3);
        const int t = tid - 768;
        if (t < 16) lds_st<u4>(LA_F + t * 16, u4{0u, 0u, 0u, 0u});
        if (t < 180) lds_st<u4>(LA_U + (t / 30) * 4608 + (t % 30) * 16, u4{0u, 0u, 0u, 0u});
        bar();
#pragma unroll
        for (int i = 0; i < 2; i++)
            if (i == 0 || wv < 14) {
                const int r = min((ai_ >> 3) + 12 * (128 * i + (t >> 1)), SEQ - 1);
                u32 keep;
                asm volatile("s_mov_b32 %0, m0\n\ts_mov_b32 m0, %3\n\ts_nop 4\n\tglobal_load_lds_dword %1, %2\n\ts_mov_b32 m0, %0"
                             : "=&s"(keep) : "v"(r * (2 * DM) + (((ai_ & 7) * 144) & ~127) + (t & 1) * 128), "s"(u), "s"(LA_D) : "memory");
            }
        u3 ua[4], ub[4];
#pragma unroll
        for (int r = 0; r < 4; r++) ua[r] = *(const u3*)(u + (4 * t + r) * DM + d0);
#pragma unroll
        for (int c = 0; c < 6; c++) {
            const u32 sel = (c & 1) ? 0x07060302u : 0x05040100u;
            lds_st<u2>(LA_U + c * 4608 + 480 + t * 8, u2{__builtin_amdgcn_perm(ua[1][c >> 1], ua[0][c >> 1], sel), __builtin_amdgcn_perm(ua[3][c >> 1], ua[2][c >> 1], sel)});
        }
        asm volatile("" ::: "memory");
#pragma unroll
        for (int r = 0; r < 4; r++) ub[r] = *(const u3*)(u + (1024 + 4 * t + r) * DM + d0);
        sig(FU1);
        STAMP(7);
#pragma unroll
        for (int c = 0; c < 6; c++) {
            const u32 sel = (c & 1) ? 0x07060302u : 0x05040100u;
            lds_st<u2>(LA_U + c * 4608 + 480 + 2048 + t * 8, u2{__builtin_amdgcn_perm(ub[1][c >> 1], ub[0][c >> 1], sel), __builtin_amdgcn_perm(ub[3][c >> 1], ub[2][c >> 1], sel)});
        }
        sig(FU2);
        STAMP(8);
        const int tile = wv - 12;
        char* o = (char*)(out + 4 * t * (2 * DM) + d0);
        await<2>(FY1, 12);
        STAMP(11);
        a_rows_out(LA_Q + ((tile >> 1) ^ 1) * 2576 + (tile & 1) * 1024 + lane * 8, 5152, o);
        STAMP(9);
        await<1>(FY2, 12);
        STAMP(12);
        a_rows_out(LA_K + (2 * ((tile >> 1) ^ 1) + (tile & 1)) * 4160 + lane * 8, 16640, o + 1024 * (4 * DM));
        STAMP(10);
        STAMP_OUT(768);
        return;
    }
    const int ch = wv >> 1, hf = wv & 1;
    const u16 p_d = dsk[d0 + ch];
    {
        const int po = (d0 + ch) * NS + 64 * hf + lane;
        const u16 p_l = lar[po], p_a = aimg[po], p_r = cre[po], p_i = cim[po], p_t = ldt[d0 + ch];
        asm volatile("" ::: "memory");
        __builtin_amdgcn_s_barrier();
        __builtin_amdgcn_s_setprio(3);
        const float dt = rbf(exp_bf(bf2f(p_t)));
        const float ar2 = -rbf(exp_bf(bf2f(p_l))) * dt * 1.44269504f, air = bf2f(p_a) * dt * 0.159154943f;
        const f2 C = {2.f * dt * bf2f(p_r), 2.f * dt * bf2f(p_i)};
        STAMP(1);
        const f2 z1 = cexpa(ar2, air, 1.f), z8c = cexpa(ar2, -air, 8.f), z128 = cexpa(ar2, air, 128.f);
        const int gb = LA_K + ch * 16640 + hf * 4608, gw = gb + lane * 4, gr = gb + c16 * 288 + g * 16;
        u4 bfr[4];
        {
            const f2 z16c = cmul(z8c, z8c);
            f2 w0 = {1.f, 0.f}, w1 = z8c;
#pragma unroll
            for (int r = 0; r < 16; r += 2) {
                lds_st<u32>(gw + r * 288, pk(w0.x, w0.y)); lds_st<u32>(gw + (r + 1) * 288, pk(w1.x, w1.y));
                if (r < 14) { w0 = r ? cmul(w0, z16c) : z16c; w1 = cmul(w1, z16c); }
            }
#pragma unroll
            for (int s = 0; s < 4; s++) bfr[s] = lds_ld<u4>(gr + 64 * s);
        }
        f2 e[8];
        e[0] = C;
#pragma unroll
        for (int j = 1; j < 8; j++) e[j] = cmul(e[j - 1], z1);
        const f2 z256 = cmul(z128, z128), z512 = cmul(z256, z256), z1024 = cmul(z512, z512);
        const int q = LA_Q + wv * 2576;
#pragma unroll
        for (int t = 0; t < 3; t++) {
            if (t == 0) {
#pragma unroll
                for (int j = 0; j < 8; j++) {
                    lds_st<u32>(gw + j * 288, pk(e[j].x, e[j].y));
                    const f2 f = cmul(e[j], z128);
                    lds_st<u32>(gw + (8 + j) * 288, pk(f.x, f.y));
                }
            } else if (t == 1) {
                f2 f[6];
#pragma unroll
                for (int j = 0; j < 4; j++) f[j] = cmul(e[2 * j], z256);
                f[4] = cmul(e[0], z512); f[5] = cmul(e[4], z512);
#pragma unroll
                for (int j = 0; j < 4; j++) {
                    lds_st<u32>(gw + j * 288, pk(f[j].x, f[j].y));
                    const f2 h = cmul(f[j], z128);
                    lds_st<u32>(gw + (4 + j) * 288, pk(h.x, h.y));
                    lds_st<u32>(gw + (8 + 2 * j) * 288, pk(f[4].x, f[4].y));
                    lds_st<u32>(gw + (9 + 2 * j) * 288, pk(f[5].x, f[5].y));
                    if (j < 3) { f[4] = cmul(f[4], z128); f[5] = cmul(f[5], z128); }
                }
            } else {
                f2 f0 = cmul(C, z1024), f1 = cmul(f0, z128);
#pragma unroll
                for (int k = 0; k < 9; k += 2) {
                    lds_st<u32>(gw + k * 288, pk(f0.x, f0.y));
                    if (k < 8) { lds_st<u32>(gw + (k + 1) * 288, pk(f1.x, f1.y)); f0 = cmul(f0, z256); }
                    if (k < 6) f1 = cmul(f1, z256);
                }
            }
            f4 acc = Z;
#pragma unroll
            for (int s = 0; s < 4; s++) acc = mfma(lds_ld<u4>(gr + 64 * s), bfr[s], acc);
            if (t == 0) lds_st<f4>(q + (4 * (g & 1) + 128 * (g >> 1) + 8 * c16) * 4, acc);
            else if (t == 1) {
                if (g < 2) lds_st<f4>(q + (256 + 64 * g + 4 * c16) * 4, acc);
                else {
                    const int o = q + (384 + 64 * (g - 2) + 2 * c16) * 4;
                    lds_st<f2>(o, f2{acc[0], acc[1]}); lds_st<f2>(o + 128, f2{acc[2], acc[3]});
                }
            } else {
                if (g < 2) {
#pragma unroll
                    for (int i = 0; i < 4; i++) lds_st<float>(q + (512 + 64 * g + 16 * i + c16) * 4, acc[i]);
                } else if (lane == 32) lds_st<float>(q + 640 * 4, acc[0]);
            }
        }
    }
    sig(FQ + ch);
    STAMP(2);
    await(FQ + ch, 2);
    {
        const LAS float* qa = (const LAS float*)(u32)(LA_Q + ch * 5152);
        const LAS float* qb = qa + 644;
        const int kE = LA_K + ch * 16640, kO = kE + 4160;
        if (hf == 0) {
            const int j = 512 + 2 * lane;
            const float q0 = qa[j] + qb[j], q1 = qa[j + 1] + qb[j + 1], q2 = qa[j + 2] + qb[j + 2];
            const u32 a = pk(q2, q2), b = pk(q1, q1), c = pk(q0, q0);
            const int y = (504 - 8 * lane) * 4;
            put8(kE + y, u4{a, a, b, b}, u4{b, pk(q1, q0), c, c});
            put8(kO + y, u4{a, pk(q2, q1), b, b}, u4{b, c, c, c});
            if (lane < 36) {
                const int cp = lane / 9;
                if (cp >= 2 || lane != 9 * cp) lds_st<u32>(kE + cp * 4160 + (1023 + lane - 9 * cp) * 4, 0u);
            }
        } else {
            {
                const int j = 384 + 2 * lane;
                const float q0 = qa[j] + qb[j], q1 = qa[j + 1] + qb[j + 1], q2 = qa[j + 2] + qb[j + 2];
                const u32 b = pk(q1, q1), c = pk(q0, q0);
                const int y = (764 - 4 * lane) * 4;
                put4(kE + y, u4{pk(q2, q2), b, pk(q1, q0), c});
                put4(kO + y, u4{pk(q2, q1), b, c, c});
            }
            {
                const int j = 256 + 2 * lane;
                const float q0 = qa[j] + qb[j], q1 = qa[j + 1] + qb[j + 1], q2 = qa[j + 2] + qb[j + 2];
                const float qm = lane ? q0 : qa[255] + qb[255];
                const int y = (894 - 2 * lane) * 4;
                put2(kE + y, pk(q2, q1), pk(q0, q0));
                put2(kO + y, pk(q1, q0), pk(q0, qm));
            }
            {
                f4 k = *(const LAS f4*)(qa + 4 * lane) + *(const LAS f4*)(qb + 4 * lane);
                if (lane == 0) k[0] += bf2f(p_d);
                const float km = lane ? qa[4 * lane - 1] + qb[4 * lane - 1] : 0.f;
                const int y = (1022 - 2 * lane) * 4;
                put2(kE + y, pk(k[3], k[2]), pk(k[1], k[0]));
                put2(kO + y, pk(k[2], k[1]), pk(k[0], km));
            }
        }
    }
    sig(FK + ch);
    const int x0 = 2031 - c16 + 8 * g;
    int ka = LA_K + ch * 16640 + (x0 & 3) * 4160 + (x0 >> 2) * 8 - 256 * hf - 192 - 3584;
    int ubs = LA_U + ch * 4608 + 480 + (16 * c16 + 8 * g - 16) * 2 - 256 * hf - 192;
    pin(ka); pin(ubs);
    if (wv < 4) __builtin_amdgcn_s_setprio(0);
    else if (wv < 8) __builtin_amdgcn_s_setprio(1);
    else __builtin_amdgcn_s_setprio(2);
    await(FK + ch, 2);
    STAMP(13);
    await<1>(FU1, 4);
    STAMP(3);
    f4 acc[4];
#define KFR(rho, p) ld_k(ka + 192 - 64 * (rho) + 3584 - 512 * (p))
#define UFR(rho, q) lds_ld<u4>(ubs + 192 - 64 * (rho) + 512 * (q))
#define SB __builtin_amdgcn_sched_barrier(0)
#define PINACC do { pin(acc[0]); pin(acc[1]); pin(acc[2]); pin(acc[3]); } while (0)
#define TRIANGLE(q0)                                                                          \
    {                                                                                         \
        u4 A[2][4], B[2][4];                                                                  \
        _Pragma("unroll") for (int p = 0; p < 4; p++) { B[0][p] = UFR(0, q0 + p); A[0][p] = KFR(0, p); } \
        _Pragma("unroll") for (int rho = 0; rho < 4; rho++)                                   \
            _Pragma("unroll") for (int p = 0; p < 4; p++) {                                   \
                SB;                                                                           \
                if (rho < 3) { B[(rho + 1) & 1][p] = UFR(rho + 1, q0 + p); A[(rho + 1) & 1][p] = KFR(rho + 1, p); } \
                _Pragma("unroll") for (int q = 0; p + q < 4; q++) acc[p + q] = mfma(A[rho & 1][p], B[rho & 1][q], acc[p + q]); \
            }                                                                                 \
        SB;                                                                                   \
        PINACC;                                                                               \
    }
#define EXCHANGE(mine, other, str, fx, fy)                                                    \
    {                                                                                         \
        if (hf) { lds_st<f4>((mine) + lane * 16, acc[0]); lds_st<f4>((mine) + (str) + lane * 16, acc[1]); acc[0] = acc[2]; acc[1] = acc[3]; } \
        else { lds_st<f4>((mine) + lane * 16, acc[2]); lds_st<f4>((mine) + (str) + lane * 16, acc[3]); } \
        sig(fx + ch);                                                                         \
        await(fx + ch, 2);                                                                    \
        const f4 y0 = lds_ld<f4>((other) + lane * 16) + acc[0], y1 = lds_ld<f4>((other) + (str) + lane * 16) + acc[1]; \
        const int w = (other) + (16 * c16 + 4 * g) * 2;                                       \
        lds_st<u2>(w, u2{pk(y0[0], y0[1]), pk(y0[2], y0[3])});                                \
        lds_st<u2>(w + (str), u2{pk(y1[0], y1[1]), pk(y1[2], y1[3])});                        \
        sig(fy);                                                                              \
    }
#pragma unroll
    for (int i = 0; i < 4; i++) acc[i] = Z;
    TRIANGLE(0);
    STAMP(4);
    EXCHANGE(LA_Q + wv * 2576, LA_Q + (wv ^ 1) * 2576, 1024, FX1, FY1);
#pragma unroll
    for (int i = 0; i < 4; i++) acc[i] = Z;
    {
        u4 A[8], B[2][4];
#pragma unroll
        for (int p = 0; p < 4; p++) B[0][p] = UFR(0, p);
#pragma unroll
        for (int p = 1; p < 8; p++) A[p] = KFR(0, p);
#pragma unroll
        for (int rho = 0; rho < 4; rho++)
#pragma unroll
            for (int p = 1; p < 8; p++) {
                SB;
                if (rho < 3 && p < 5) B[(rho + 1) & 1][p - 1] = UFR(rho + 1, p - 1);
#pragma unroll
                for (int q = 0; q < 4; q++)
                    if (p + q >= 4 && p + q < 8) acc[p + q - 4] = mfma(A[p], B[rho & 1][q], acc[p + q - 4]);
                SB;
                if (rho < 3) A[p] = KFR(rho + 1, p);
            }
        PINACC;
    }
    sig(F2A + ch);
    STAMP(14);
    await<1>(FU2, 4);
    TRIANGLE(4);
    STAMP(5);
    await(F2A + ch, 2);
    EXCHANGE(LA_K + ch * 16640 + 2 * hf * 4160, LA_K + ch * 16640 + 2 * (hf ^ 1) * 4160, 4160, FX2, FY2);
    STAMP(6);
    STAMP_OUT(0);
}

__global__ void __launch_bounds__(NT) fused_kernel(const u16* __restrict__ u, const u16* __restrict__ lar, const u16* __restrict__ aimg,
                                                   const u16* __restrict__ cre, const u16* __restrict__ cim, const u16* __restrict__ ldt,
                                                   const u16* __restrict__ dsk, const u16* __restrict__ x, const u16* __restrict__ wr,
                                                   const u16* __restrict__ wi, u16* __restrict__ out, char* ws) {
    const char* entry;
    asm volatile("s_getpc_b64 %0" : "=s"(entry));
    if (__builtin_expect(blockIdx.x < NB2, 1)) head_b(entry, x, wr, wi, out, ws);
    else if (blockIdx.x >= A0) head_a(u, lar, aimg, cre, cim, ldt, dsk, out, ws);
    else warm_code(entry);
}

extern "C" void kernel_launch(void* const* d_in, const int* in_sizes, int n_in, void* d_out, int out_size, void* d_ws,
                              size_t ws_size, hipStream_t stream) {
    (void)hipFuncSetAttribute((const void*)fused_kernel, hipFuncAttributeMaxDynamicSharedMemorySize, LDS_BYTES);
    fused_kernel<<<GRID, NT, LDS_BYTES, stream>>>((const u16*)d_in[0], (const u16*)d_in[1], (const u16*)d_in[2], (const u16*)d_in[3],
                                                  (const u16*)d_in[4], (const u16*)d_in[5], (const u16*)d_in[6], (const u16*)d_in[7],
                                                  (const u16*)d_in[8], (const u16*)d_in[9], (u16*)d_out, (char*)d_ws);
}
```

```cpp
#include <hip/hip_runtime.h>

typedef unsigned short u16;
typedef unsigned int u32;
typedef float f2 __attribute__((ext_vector_type(2)));
typedef float f4 __attribute__((ext_vector_type(4)));
typedef u32 u2 __attribute__((ext_vector_type(2)));
typedef u32 u4 __attribute__((ext_vector_type(4)));
typedef int i8v __attribute__((ext_vector_type(8)));
typedef short s4 __attribute__((ext_vector_type(4)));
typedef short s8 __attribute__((ext_vector_type(8)));
typedef __bf16 bf2 __attribute__((ext_vector_type(2)));
#define DI __device__ __forceinline__
#define LAS __attribute__((address_space(3)))
#define GAS __attribute__((address_space(1)))
#define CAS __attribute__((address_space(4)))
#pragma clang diagnostic ignored "-Wint-to-pointer-cast"
struct u3 { u32 v[3]; DI u32& operator[](int i) { return v[i]; } DI const u32& operator[](int i) const { return v[i]; } };

constexpr int SEQ = 2048, DM = 576, NS = 128, DF = 289;
constexpr int NB1 = 128;
constexpr int NB2 = 145;
constexpr int A0 = 152;
constexpr int NA = 96;
constexpr int GRID = A0 + NA;
constexpr int NT = 1024;

constexpr u32 WS_EPOCH = 0, WS_F1 = 4096, WS_F2 = WS_F1 + 16 * 16 * 128, WS_X1 = 1u << 20, WS_Y1 = 4u << 20, WS_USED = 8u << 20;
#define STAMP(i)
#define STAMP_DECL
#define STAMP_OUT(T)

constexpr int F_SA = 0, F_EO = 4;
constexpr int F_ARE = 9, F_AIM = 10, F_BRE = 11, F_BIM = 12, F_G = 13;
constexpr int F_F16 = 21, F_GI = 23, F_AI = 31, F_M1C = 33;
constexpr int F_VAR = 37;
constexpr int F_END = F_VAR + 40;
constexpr int ET = 452, ER = 28, E2V = 580, E2U = 36, OP = 1168;
constexpr int LB_X = 0, LB_I = 0, LB_Y = 0;
constexpr int LB_E = 16 * OP;
constexpr int LB_O = LB_E, LB_E2 = LB_E;
constexpr int LB_S = LB_E + 16 * ET * 4;
constexpr int LB_W = LB_S + 1200 * 16;
constexpr int LB_T = LB_W + 2 * 256 * 36;
constexpr int LB_G = LB_T + (F_VAR + 16) * 1024;
constexpr int LB_END = LB_G + 16;
struct CosTab { double c[2048]; double c9[9], s9[9]; };
constexpr double tay_cos(double x) { double t = 1, s = 1; for (int k = 1; k <= 14; k++) { t *= -x * x / ((2 * k - 1) * (2 * k)); s += t; } return s; }
constexpr double tay_sin(double x) { double t = x, s = x; for (int k = 1; k <= 14; k++) { t *= -x * x / ((2 * k + 1) * (2 * k)); s += t; } return s; }
constexpr CosTab make_cos() {
    CosTab t = {};
    const double PI2 = 6.283185307179586476925;
    for (int k = 0; k <= 512; k++) {
        const double v = k == 512 ? 0.0 : (k <= 256 ? tay_cos(PI2 * k / 2048) : tay_sin(PI2 * (512 - k) / 2048));
        t.c[k] = v; t.c[(2048 - k) & 2047] = v; t.c[1024 - k] = -v; t.c[(1024 + k) & 2047] = -v;
    }
    t.c[512] = 0.0; t.c[1536] = 0.0;
    for (int k = 0; k < 9; k++) { const double a = PI2 * (k <= 4 ? k : k - 9) / 9; t.c9[k] = tay_cos(a); t.s9[k] = tay_sin(a); }
    return t;
}
constexpr CosTab CT = make_cos();
constexpr double cs(int n) { return CT.c[n & 2047]; }
constexpr double sn(int n) { return CT.c[(n - 512) & 2047]; }
constexpr u32 bfr(double x) { const u32 b = __builtin_bit_cast(u32, (float)x); return (b + 0x7fffu + ((b >> 16) & 1u)) >> 16; }
constexpr u32 pkc(double lo, double hi) { return bfr(lo) | (bfr(hi) << 16); }
constexpr u32 f32c(double x) { return __builtin_bit_cast(u32, (float)x); }
template <int N> struct Frags { u32 w[N * 256]; };
constexpr u32 w_re(double amp, int n) { return pkc(amp * cs(n), amp * sn(n)); }
constexpr u32 w_im(double amp, int n) { return pkc(-amp * sn(n), amp * cs(n)); }
constexpr Frags<F_VAR> make_t0() {
    Frags<F_VAR> t = {};
    const double R2 = 1.4142135623730951;
    for (int l = 0; l < 64; l++)
        for (int i = 0; i < 4; i++) {
            const int c = l & 15, g = l >> 4, r = 4 * g + i, o = l * 4 + i;
            {
                const int b = c >> 2, tq = c & 3;
                const double amp = b ? 1.0 : R2;
                const int n0 = 512 * tq * g + (b ? 256 * b * (2 * i) : -256), n1 = 512 * tq * g + (b ? 256 * b * (2 * i + 1) : 256);
                t.w[F_ARE * 256 + o] = pkc(amp * cs(n0), amp * cs(n1));
                t.w[F_AIM * 256 + o] = pkc(-amp * sn(n0), -amp * sn(n1));
            }
            for (int w = 0; w < 9; w++) {
                const int sh = 16 * (i & 1), oo = w * 128 + l * 2 + (i >> 1);
                t.w[F_EO * 256 + oo] |= (u32)((c * ET + r * ER + 2 * ((7 * w + r) % 9)) * 4) << sh;
                if (w < 8) {
                    const int k = (9 * (w & 3) + 36 * r + 64 * (c % 9)) % DM, slot = w < 4 ? (k <= 288 ? 2 * k : 2 * (DM - k) + 1) : k;
                    t.w[F_SA * 256 + oo] |= (u32)(slot * 8 + (slot >> 6) * 16) << sh;
                }
            }
            if (r < 9) { const int k = (r * c) % 9; t.w[F_BRE * 256 + o] = pkc(CT.c9[k], CT.s9[k]); t.w[F_BIM * 256 + o] = pkc(-CT.s9[k], CT.c9[k]); }
            for (int q = 0; q < 4; q++) {
                t.w[(F_G + 2 * q) * 256 + o] = w_re(1.0, 32 * r * (q + 4 * c)); t.w[(F_G + 2 * q + 1) * 256 + o] = w_im(1.0, 32 * r * (q + 4 * c));
                t.w[(F_GI + 2 * q) * 256 + o] = w_re(1.0, 32 * c * (q + 4 * r)); t.w[(F_GI + 2 * q + 1) * 256 + o] = w_im(1.0, 32 * c * (q + 4 * r));
            }
            t.w[F_F16 * 256 + o] = w_re(1.0, 128 * r * c); t.w[(F_F16 + 1) * 256 + o] = w_im(1.0, 128 * r * c);
            for (int h = 0; h < 2; h++) {
                const int j = 4 * h + (c >> 2), n = 512 * i * (c & 3) + (g ? 256 * g * j : ((j & 1) ? 256 : -256));
                t.w[(F_AI + h) * 256 + o] = w_re(g ? 2.0 : R2, n);
                const int m = 16 * r * (4 * h + 8 * c) - 512 * h;
                t.w[(F_M1C + 2 * h) * 256 + o] = pkc(0.5 * cs(m), -0.5 * sn(m));
                t.w[(F_M1C + 2 * h + 1) * 256 + o] = pkc(-0.5 * sn(m), -0.5 * cs(m));
            }
        }
    return t;
}
constexpr Frags<40> make_t1() {
    Frags<40> t = {};
    for (int v = 0; v < 5; v++)
        for (int l = 0; l < 64; l++)
            for (int i = 0; i < 4; i++) {
                const int be = v < 3 ? v + 1 : 4 * (v - 3), c = l & 15, r = 4 * (l >> 4) + i, o = v * 2048 + l * 4 + i;
                const double amp = v < 3 ? 1.0 : 0.5;
                const int n1 = 16 * r * (be + 8 * c) + (v == 4 ? 512 : 0), n4 = 16 * c * (be + 8 * r);
                t.w[o] = w_re(amp, n1); t.w[o + 256] = w_im(amp, n1);
                t.w[o + 512] = w_re(1.0, n4); t.w[o + 768] = w_im(1.0, n4);
                t.w[o + 1024] = f32c(cs(r * (be + 8 * c))); t.w[o + 1280] = f32c(-sn(r * (be + 8 * c)));
                t.w[o + 1536] = f32c(cs(c * (be + 8 * r))); t.w[o + 1792] = f32c(-sn(c * (be + 8 * r)));
            }
    return t;
}
struct Tables { Frags<F_VAR> a; Frags<40> b; };
constexpr Frags<F_VAR> T0 = make_t0();
constexpr Frags<40> T1 = make_t1();
static __device__ const __attribute__((aligned(128))) Tables TAB = {T0, T1};
constexpr int LA_K = 0;
constexpr int LA_U = LA_K + 6 * 16640;
constexpr int LA_Q = LA_U + 6 * 4608;
constexpr int LA_F = LA_Q + 12 * 2576;
constexpr int LA_D = LA_F + 256;
constexpr int LA_END = LA_D + 256;
constexpr int FQ = 0, FK = 6, FX1 = 12, F2A = 18, FX2 = 24, FU1 = 30, FU2 = 31, FY1 = 32, FY2 = 33;
constexpr int LDS_BYTES = (LA_END > LB_END ? LA_END : LB_END);
static_assert(LDS_BYTES <= 160 * 1024 && LDS_BYTES > 80 * 1024, "one workgroup per CU");

DI u32 pk(float a, float b) { f2 v = {a, b}; return __builtin_bit_cast(u32, __builtin_convertvector(v, bf2)); }
DI float blo(u32 v) { return __uint_as_float(v << 16); }
DI float bhi(u32 v) { return __uint_as_float(v & 0xffff0000u); }
DI float bf2f(u16 v) { return __uint_as_float((u32)v << 16); }
DI float rbf(float x) { return blo(pk(x, x)); }
DI f2 cmul(f2 a, f2 b) { f2 t = a.xx * b; f2 s = {-b.y, b.x}; return __builtin_elementwise_fma(a.yy, s, t); }
DI f4 mfma(u4 a, u4 b, f4 c) { return __builtin_amdgcn_mfma_f32_16x16x32_bf16(__builtin_bit_cast(s8, a), __builtin_bit_cast(s8, b), c, 0, 0, 0); }
DI u4 pack4(f4 re, f4 im) { u4 r = {pk(re[0], im[0]), pk(re[1], im[1]), pk(re[2], im[2]), pk(re[3], im[3])}; return r; }
DI u4 twid(f4 re, f4 im, f4 wr, f4 wi) { return pack4(__builtin_elementwise_fma(re, wr, -(im * wi)), __builtin_elementwise_fma(re, wi, im * wr)); }
DI int mod9(int x) { return x - 9 * ((x * 57) >> 9); }
template <class T> DI void pin(T& v) { asm volatile("" : "+v"(v)); }

template <class T> DI T lds_ld(int off) { return *(const LAS T*)(u32)off; }
template <class T> DI void lds_st(int off, T v) { *(LAS T*)(u32)off = v; }
DI void bar() { __syncthreads(); }
DI void glds16(const void* g, u32 off, int lds) {
    u32 keep;
    asm volatile("s_mov_b32 %0, m0\n\ts_mov_b32 m0, %3\n\ts_nop 4\n\tglobal_load_lds_dwordx4 %1, %2\n\ts_mov_b32 m0, %0" : "=&s"(keep) : "v"(off), "s"(g), "s"(lds) : "memory");
}

DI void publish_tag(u32 epoch) {
    u32 t;
    asm volatile("v_add_u32 %0, 0x9E3779B1, %1\n\tds_write_b32 %2, %0\n\ts_waitcnt lgkmcnt(0)" : "=&v"(t) : "v"(epoch), "v"(LB_G) : "memory");
}
DI void wait_flag(__amdgpu_buffer_rsrc_t r, u32 off, u32 tag, bool on) {
    if (on) {
        const unsigned long long t0 = __builtin_amdgcn_s_memrealtime();
        while (!__all(__builtin_amdgcn_raw_buffer_load_b32(r, off, 0, 16) == tag)) {
            if (__builtin_amdgcn_s_memrealtime() - t0 > 5000000ull) break;
            asm volatile("" ::: "memory");
        }
    }
    asm volatile("" ::: "memory");
}

constexpr int CODE_LINES = 128;
DI void warm_code(const char* entry) {
    if (threadIdx.x < CODE_LINES) { const u32 t = *(const GAS u32*)(entry + threadIdx.x * 128); asm volatile("" :: "v"(t)); }
}

DI void head_b(const char* entry, const u16* __restrict__ x, const u16* __restrict__ wr, const u16* __restrict__ wi,
               u16* __restrict__ out, char* ws) {
    const int tid = threadIdx.x, lane = tid & 63, c16 = lane & 15, g = lane >> 4;
    const int wv = __builtin_amdgcn_readfirstlane(tid >> 6);
    const int id = blockIdx.x;
    STAMP_DECL;
    const bool rows = id < NB1;
    const int s00 = 32 * (id >> 4) + (id & 15);
    const char* tab = (const char*)&TAB;
    const int R = (tid * 911) >> 16;
    const u32 xoff = (s00 + 16 * (R >> 3) + 256 * (R & 7)) * (2 * DM) + (tid - R * 72) * 16;
    asm volatile("" :: "s"(x), "s"(wr), "s"(wi), "s"(out), "s"(ws), "v"(xoff));
    const __amdgpu_buffer_rsrc_t rws = __builtin_amdgcn_make_buffer_rsrc(ws, 0, WS_USED, 0x00020000);
    u32 epoch = 0;
    if (wv == 0) asm volatile("global_load_dword %0, %0, %1 sc1" : "+v"(epoch) : "s"(ws) : "memory");
    if (rows) {
        glds16(x, xoff, LB_X + wv * 1024);
        if (wv < 2) glds16(x, (s00 + 16 + 1536) * (2 * DM) + (tid < 56 ? (tid + 16) * 16 : 256 * 2 * DM + (tid - 56) * 16), LB_X + 16384 + wv * 1024);
        else glds16(tab, tid * 16 - 2048, LB_T + (wv - 2) * 1024);
        if (wv >= 9) glds16(tab, tid * 16 + 5 * 1024, LB_T + (wv + 5) * 1024);
    }
    const int u2i = (id & 7) ? (id & 7) * 18 + 1 + (id >> 3) : (id == 136 ? 0 : (id >> 3) + (id < 136 ? 1 : 0));
    const bool star = u2i >= 108;
    const int q36 = min((u2i * 57) >> 11, 3), kg = u2i - 36 * q36, bq = (q36 + 1) & 3;
    const bool mir = !star && kg >= 18;
    const int w0 = star ? min(16 * kg, 562) : (mir ? (2048 - bq) * (2 * DF) + 1122 - 32 * kg : bq * (2 * DF) + 32 * kg);
    if (wv < 8) {
        glds16(tab, F_F16 * 1024 + tid * 16, LB_T + F_F16 * 1024 + wv * 1024);
        glds16(tab, (F_F16 + 8) * 1024 + tid * 16, LB_T + (F_F16 + 8) * 1024 + wv * 1024);
        glds16(tab, (F_VAR + 8 * q36) * 1024 + tid * 16, LB_T + F_VAR * 1024 + wv * 1024);
        glds16(tab, (F_VAR + 8 * q36 + 8) * 1024 + tid * 16, LB_T + (F_VAR + 8) * 1024 + wv * 1024);
    }
    __builtin_amdgcn_s_barrier();
    const u32 woff = w0 + ((tid >> 1) & 255) * (mir ? -16 * DF : 16 * DF) + (tid & 1) * (star ? 8 * DF : 16);
    u4 wra, wrb;
    if (wv >= 8) {
        __builtin_memcpy(&wra, (const char*)wr + woff, 16);
        __builtin_memcpy(&wrb, (const char*)wi + woff, 16);
    }
    __builtin_amdgcn_sched_barrier(0);
    const f4 Z = {0.f, 0.f, 0.f, 0.f};
    const int tau = wv & 3, bw = wv >> 2;
    int tl = LB_T + lane * 16;
    int lin = LB_S + (tid + (tid >> 5)) * 16;
    pin(tl); pin(lin);
    int sadr[4];

    if (rows) {
        if (tid < 256) {
            const int o = LB_E + 72 + (tid & 15) * (ER * 4) + (tid >> 4) * (ET * 4);
            lds_st<u2>(o, u2{0u, 0u}); lds_st<u4>(o + 8, u4{0u, 0u, 0u, 0u});
        }
        int xo = LB_X + (c16 >> 2) * 1152 + 2 * (144 * ((g - wv) & 3) + 16 * wv + 4 * (c16 & 3));
        int bo = LB_E + (wv * ET + c16 * ER + min(8 * g, 16)) * 4;
        int go = WS_X1 + (tid >> 3) * 16384 + id * 128 + (tid & 7) * 16;
        int ta = LB_T + lane * 8;
        pin(xo); pin(bo); pin(go); pin(ta);
        STAMP(1);
        asm volatile("s_cmp_lt_u32 %0, 8\n\ts_cbranch_scc1 .Lw%=\n\ts_waitcnt vmcnt(2)\n.Lw%=:\n\ts_waitcnt vmcnt(4)" :: "s"(wv) : "memory", "scc");
        if (wv == 0) publish_tag(epoch);
        bar();
        STAMP(2);
        if (wv < 9) {
            u4 d[2];
#pragma unroll
            for (int s = 0; s < 2; s++) {
                const s4 lo = __builtin_amdgcn_ds_read_tr16_b64_v4i16((LAS s4*)(u32)(xo + s * 9216));
                const s4 hi = __builtin_amdgcn_ds_read_tr16_b64_v4i16((LAS s4*)(u32)(xo + s * 9216 + 4608));
                d[s] = __builtin_bit_cast(u4, __builtin_shufflevector(lo, hi, 0, 1, 2, 3, 4, 5, 6, 7));
            }
            const u4 are = lds_ld<u4>(tl + F_ARE * 1024), aim = lds_ld<u4>(tl + F_AIM * 1024);
            const u2 eo = lds_ld<u2>(ta + F_EO * 1024 + wv * 512);
#pragma unroll
            for (int s = 0; s < 2; s++) d[s] = pack4(mfma(d[s], are, Z), mfma(d[s], aim, Z));
#pragma unroll
            for (int i = 0; i < 4; i++) lds_st<u2>(LB_E + ((i & 1) ? eo[i >> 1] >> 16 : eo[i >> 1] & 0xffff), u2{d[0][i], d[1][i]});
        }
        u4 bre = lds_ld<u4>(tl + F_BRE * 1024), bim = lds_ld<u4>(tl + F_BIM * 1024);
        u4 gre = lds_ld<u4>(tl + (F_G + 2 * tau) * 1024), gim = lds_ld<u4>(tl + (F_G + 2 * tau + 1) * 1024);
        {
            const u2 sa = lds_ld<u2>(ta + F_SA * 1024 + (tau + (bw ? 4 : 0)) * 512);
            const int base = LB_S + ((bw + 3) & 3) * (DM * 8 + 144);
#pragma unroll
            for (int i = 0; i < 4; i++) { sadr[i] = base + ((i & 1) ? sa[i >> 1] >> 16 : sa[i >> 1] & 0xffff); pin(sadr[i]); }
        }
        pin(bre); pin(bim); pin(gre); pin(gim);
        bar();
        STAMP(13);
        {
            const u4 p0 = lds_ld<u4>(bo), p1 = lds_ld<u4>(bo + 16);
            u4 d[2];
#pragma unroll
            for (int s = 0; s < 2; s++) {
                const u4 a = {p0[s], p0[2 + s], p1[s], p1[2 + s]};
                const u4 t = pack4(mfma(a, bre, Z), mfma(a, bim, Z));
                d[s] = pack4(mfma(gre, t, Z), mfma(gim, t, Z));
            }
            if (c16 < 9) {
#pragma unroll
                for (int i = 0; i < 4; i++) lds_st<u2>(sadr[i], u2{d[0][i], d[1][i]});
                if (wv == 0 && c16 == 0 && !(g & 1)) lds_st<u2>(sadr[0] + 8, u2{d[0][0], d[1][0]});
            }
        }
        STAMP(14);
        bar();
        STAMP(3);
        __builtin_amdgcn_raw_buffer_store_b128(lds_ld<u4>(lin), rws, go, 0, 16);
        if (tid <= 128) __builtin_amdgcn_raw_buffer_store_b128(lds_ld<u4>(lin + 1056 * 16), rws, go, 128 * 16384, 16);
    } else {
        if (wv < 8 && (kg == 0 || kg == (star ? 36 : 18))) {
            const int m = tid & 255;
            lds_st<u16>(LB_W + tid * 36 + 32, ((wv >> 2) ? wi : wr)[((mir ? bq + 8 * m : 2048 - bq - 8 * m) & 2047) * DF + (kg ? 288 : 0)]);
        }
        warm_code(entry);
        asm volatile("s_waitcnt vmcnt(0)" ::: "memory");
        if (wv == 0) publish_tag(epoch);
        bar();
    }
    const u32 tag = lds_ld<u32>(LB_G);
    if (wv >= 8) {
        const int o = LB_W + ((tid >> 1) & 255) * 36 + (tid & 1) * 16;
#pragma unroll
        for (int i = 0; i < 4; i++) { lds_st<u32>(o + 4 * i, wra[i]); lds_st<u32>(o + 9216 + 4 * i, wrb[i]); }
    }
    asm volatile("s_waitcnt vmcnt(0)" ::: "memory");
    if (rows && lane == 0) __builtin_amdgcn_raw_buffer_store_b32(tag, rws, WS_F1 + (wv * 16 + (id >> 3)) * 128 + (id & 7) * 4, 0, 16);
    STAMP(4);
    bar();
    STAMP(12);

    const int typ = star ? (wv & 1) : 0;
    const int tv = tl + (F_VAR + 8 * typ) * 1024;
    u4 fre = lds_ld<u4>(tl + F_F16 * 1024), fim = lds_ld<u4>(tl + (F_F16 + 1) * 1024);
    u4 m1r = lds_ld<u4>(tv), m1i = lds_ld<u4>(tv + 1024), m4r = lds_ld<u4>(tv + 2048), m4i = lds_ld<u4>(tv + 3072);
    f4 twr = lds_ld<f4>(tv + 4096), twi = lds_ld<f4>(tv + 5120), tur = lds_ld<f4>(tv + 6144), tui = lds_ld<f4>(tv + 7168);
    u4 mcr = lds_ld<u4>(tl + (F_M1C + 2 * typ) * 1024), mci = lds_ld<u4>(tl + (F_M1C + 2 * typ + 1) * 1024);
    f4 wfr, wfi;
    {
        const int kcol = star ? 8 * kg + (wv >> 1) : 16 * kg + wv;
        const int cc = star ? 8 * typ + (u2i < NB2 - 1 ? (wv >> 1) : 7) : (mir ? 15 - wv : wv);
        const int wo = LB_W + (c16 + 64 * g) * 36 + cc * 2;
#pragma unroll
        for (int i = 0; i < 4; i++) {
            wfr[i] = bf2f(lds_ld<u16>(wo + i * 576));
            wfi[i] = bf2f(lds_ld<u16>(wo + i * 576 + 9216));
        }
        float sc = 1.f / (float)(SEQ * DM);
        if (kcol == 0 || kcol == 288) {
            sc *= 0.5f;
#pragma unroll
            for (int i = 0; i < 4; i++) {
                const int m = c16 + 64 * g + 16 * i;
                const int o = LB_W + (typ ? (255 - m) * 36 + cc * 2 : m * 36 + 32);
                wfr[i] += bf2f(lds_ld<u16>(o));
                wfi[i] -= bf2f(lds_ld<u16>(o + 9216));
            }
        }
        wfr *= sc; wfi *= (mir ? -sc : sc);
    }
    f4 wnr = -wfr, wni = -wfi;
    int io = (32 * g + c16) * 128 + (((wv >> 1) ^ (c16 >> 1)) * 16);
    int so = (tid >> 3) * 128 + (((tid & 7) ^ ((tid >> 4) & 7)) * 16);
    pin(fre); pin(fim); pin(m1r); pin(m1i); pin(m4r); pin(m4i); pin(twr); pin(twi); pin(tur); pin(tui); pin(mcr); pin(mci);
    int gi = WS_X1 + u2i * 16384 + tid * 16, gq = WS_Y1 + (tid >> 3) * (NB2 * 128) + u2i * 128 + (tid & 7) * 16;
    int i1 = LB_I + io + (star ? 0 : (wv & 1) * 8);
    io += LB_O + (wv & 1) * 8;
    pin(wfr); pin(wnr); pin(wni); pin(io); pin(so); pin(gi); pin(gq); pin(i1);
    wait_flag(rws, WS_F1 + (((u2i >> 3) & 15) * 16 + wv) * 128 + lane * 4, tag, lane < 8);
    STAMP(5);
    lds_st<u4>(LB_I + so, __builtin_amdgcn_raw_buffer_load_b128(rws, gi, 0, 16));
    bar();
    STAMP(6);
    {
        const int o1 = i1;
        const u2 a0 = lds_ld<u2>(o1), a1 = lds_ld<u2>(o1 + 2048);
        const u4 a = {a0[0], a0[1], a1[0], a1[1]};
        f4 re = mfma(a, m1r, Z), im = mfma(a, m1i, Z);
        if (star) {
            const u2 b0 = lds_ld<u2>(o1 + 8), b1 = lds_ld<u2>(o1 + 8 + 2048);
            const u4 b = {b0[0], b0[1], b1[0], b1[1]};
            re = mfma(b, mcr, re); im = mfma(b, mci, im);
        }
        u4 t = twid(re, im, twr, twi);
        re = mfma(fre, t, Z); im = mfma(fim, t, Z);
        t = pack4(__builtin_elementwise_fma(re, wfr, im * wni), __builtin_elementwise_fma(re, wni, im * wnr));
        re = mfma(t, fre, Z); im = mfma(t, fim, Z);
        t = twid(re, im, tur, tui);
        t = pack4(mfma(m4r, t, Z), mfma(m4i, t, Z));
        lds_st<u2>(io, u2{t[0], t[1]}); lds_st<u2>(io + 2048, u2{t[2], t[3]});
    }
    bar();
    STAMP(7);
    {
        u4 v = lds_ld<u4>(LB_O + so);
        if (star) {
            u4 w;
#pragma unroll
            for (int s = 0; s < 2; s++) {
                const float ar = blo(v[s]), ai = bhi(v[s]), cr = blo(v[2 + s]), ci = bhi(v[2 + s]);
                w[s] = pk(ar + ci, ai - cr); w[2 + s] = pk(ar - ci, -ai - cr);
            }
            v = w;
        }
        if (u2i < NB2 - 1 || (tid & 7) == 0)
            __builtin_amdgcn_raw_buffer_store_b128(v, rws, gq, 0, 16);
    }
    asm volatile("s_waitcnt vmcnt(0)" ::: "memory");
    if (lane == 0) __builtin_amdgcn_raw_buffer_store_b32(tag, rws, WS_F2 + (wv * 19 + (u2i >> 3)) * 128 + (u2i & 7) * 4, 0, 16);
    STAMP(8);
    if (!rows) { STAMP_OUT(0); return; }

    u4 gre = lds_ld<u4>(tl + (F_GI + 2 * tau) * 1024), gim = lds_ld<u4>(tl + (F_GI + 2 * tau + 1) * 1024);
    u4 bre = lds_ld<u4>(tl + F_BRE * 1024), bim = lds_ld<u4>(tl + F_BIM * 1024);
    u4 ai0 = lds_ld<u4>(tl + F_AI * 1024), ai1 = lds_ld<u4>(tl + (F_AI + 1) * 1024);
    int eadr[4];
#pragma unroll
    for (int i = 0; i < 4; i++) {
        const int r = 4 * g + i;
        eadr[i] = LB_E2 + (mod9(4 * (c16 + 18 - r)) * E2V + r * E2U + 2 * wv) * 4;
        pin(eadr[i]);
    }
    int ao = LB_E2 + (wv * E2V + c16 * E2U + 8 * g) * 4;
    int gy = WS_Y1 + id * (NB2 * 128) + tid * 16;
    int yo = LB_Y + (c16 >> 2) * OP + 2 * (144 * (((c16 & 3) - wv) & 3) + 16 * wv + 4 * g);
    int yl = LB_Y + tid * 16 + R * (OP - 1152);
    u32 oo = 2 * xoff - (tid - R * 72) * 16 + 2 * DM;
    pin(gre); pin(gim); pin(bre); pin(bim); pin(ai0); pin(ai1); pin(ao); pin(gy); pin(yo); pin(yl); pin(oo);
    {
        const int un = 8 * wv + lane + (lane >= 8 ? 120 : 0);
        wait_flag(rws, WS_F2 + ((id >> 3) * 19 + (un >> 3)) * 128 + (lane & 7) * 4, tag, lane < 16 && un < NB2);
    }
    STAMP(9);
    {
        const u4 v = __builtin_amdgcn_raw_buffer_load_b128(rws, gy, 0, 16);
        if (tid <= 128) lds_st<u4>(lin + 1056 * 16, __builtin_amdgcn_raw_buffer_load_b128(rws, gy, 16384, 16));
        lds_st<u4>(lin, v);
    }
    bar();
    if (id == 0 && tid == 0) __builtin_amdgcn_raw_buffer_store_b32(tag, rws, WS_EPOCH, 0, 16);
    STAMP(10);
    {
        u4 d[2];
        u2 p[4];
#pragma unroll
        for (int i = 0; i < 4; i++) p[i] = lds_ld<u2>(sadr[i]);
#pragma unroll
        for (int s = 0; s < 2; s++) {
            const u4 a = {p[0][s], p[1][s], p[2][s], p[3][s]};
            const u4 t = pack4(mfma(a, gre, Z), mfma(a, gim, Z));
            d[s] = pack4(mfma(t, bre, Z), mfma(t, bim, Z));
        }
        if (c16 < 9) {
#pragma unroll
            for (int i = 0; i < 4; i++) lds_st<u2>(eadr[i], u2{d[0][i], d[1][i]});
        }
    }
    STAMP(15);
    bar();
    if (wv < 9) {
        const u4 p0 = lds_ld<u4>(ao), p1 = lds_ld<u4>(ao + 16);
#pragma unroll
        for (int s = 0; s < 2; s++) {
            const u4 a = {p0[s], p0[2 + s], p1[s], p1[2 + s]};
            const f4 y0 = mfma(a, ai0, Z), y1 = mfma(a, ai1, Z);
            lds_st<u2>(yo + s * (8 * OP), u2{pk(y0[0], y0[1]), pk(y0[2], y0[3])});
            lds_st<u2>(yo + s * (8 * OP) + 4 * OP, u2{pk(y1[0], y1[1]), pk(y1[2], y1[3])});
        }
    }
    bar();
    const __amdgpu_buffer_rsrc_t rout = __builtin_amdgcn_make_buffer_rsrc(out, 0, SEQ * DM * 4, 0x00020000);
    __builtin_amdgcn_raw_buffer_store_b128(lds_ld<u4>(yl), rout, oo, 0, 16);
    if (wv < 2) {
        const int r2 = tid < 56 ? 14 : 15, c2 = tid + 1024 - 72 * r2;
        __builtin_amdgcn_raw_buffer_store_b128(lds_ld<u4>(LB_Y + r2 * OP + c2 * 16), rout, (s00 + 16 + 256 * (r2 - 8)) * (4 * DM) + 2 * DM + c2 * 16, 0, 16);
    }
    STAMP(11);
    STAMP_OUT(0);
}

DI f2 cexpa(float ar2, float air, float p) {
    const float e = __builtin_amdgcn_exp2f(ar2 * p), t = __builtin_amdgcn_fractf(air * p);
    return f2{e * __builtin_amdgcn_cosf(t), e * __builtin_amdgcn_sinf(t)};
}
DI u4 ld_k(int o) {
    const u2 a = *(const volatile LAS u2*)(u32)o, b = *(const volatile LAS u2*)(u32)(o + 8);
    return u4{a[0], a[1], b[0], b[1]};
}
DI void put8(int o, u4 a, u4 b) {
    lds_st<u4>(o, a); lds_st<u4>(o + 16, b);
    lds_st<u32>(o + 8316, a[0]); lds_st<u4>(o + 8320, u4{a[1], a[2], a[3], b[0]});
    lds_st<u2>(o + 8336, u2{b[1], b[2]}); lds_st<u32>(o + 8344, b[3]);
}
DI void put4(int o, u4 a) {
    lds_st<u4>(o, a);
    lds_st<u32>(o + 8316, a[0]); lds_st<u2>(o + 8320, u2{a[1], a[2]}); lds_st<u32>(o + 8328, a[3]);
}
DI void put2(int o, u32 a, u32 b) { lds_st<u2>(o, u2{a, b}); lds_st<u32>(o + 8316, a); lds_st<u32>(o + 8320, b); }

DI void a_rows_out(int yb, int ystr, char* o) {
    u2 y[6];
#pragma unroll
    for (int c = 0; c < 6; c++) y[c] = lds_ld<u2>(yb + c * ystr);
#pragma unroll
    for (int r = 0; r < 4; r++) {
        u3 v;
#pragma unroll
        for (int c = 0; c < 3; c++) v[c] = __builtin_amdgcn_perm(y[2 * c + 1][r >> 1], y[2 * c][r >> 1], (r & 1) ? 0x07060302u : 0x05040100u);
        *(u3*)(o + r * (4 * DM)) = v;
    }
}

DI void sig(int f) {
    asm volatile("s_waitcnt lgkmcnt(0)" ::: "memory");
    if ((threadIdx.x & 63) == 0) __hip_atomic_fetch_add((LAS u32*)(u32)(LA_F + 4 * f), 1u, __ATOMIC_RELAXED, __HIP_MEMORY_SCOPE_WORKGROUP);
}
template <int SLEEP = 0> DI void await(int f, u32 n) {
    while ((u32)__builtin_amdgcn_readfirstlane(*(const volatile LAS u32*)(u32)(LA_F + 4 * f)) < n) if (SLEEP) __builtin_amdgcn_s_sleep(SLEEP);
    asm volatile("" ::: "memory");
}
DI float exp_bf(float x) {
    const float t = x * 1.44269504f, r = __builtin_fmaf(x, 1.92596299e-8f, __builtin_fmaf(x, 1.44269504f, -t));
    const float e = __builtin_amdgcn_exp2f(t);
    return __builtin_fmaf(e * 0.693147181f, r, e);
}

DI void head_a(const u16* __restrict__ u, const u16* __restrict__ lar, const u16* __restrict__ aimg,
               const u16* __restrict__ cre, const u16* __restrict__ cim, const u16* __restrict__ ldt,
               const u16* __restrict__ dsk, u16* __restrict__ out, char* ws) {
    const int tid = threadIdx.x, lane = tid & 63, c16 = lane & 15, g = lane >> 4;
    const int wv = __builtin_amdgcn_readfirstlane(tid >> 6);
    STAMP_DECL;
    const int ai_ = blockIdx.x - A0;
    const int d0 = 6 * ((ai_ & 7) * 12 + (ai_ >> 3));
    const f4 Z = {0.f, 0.f, 0.f, 0.f};

    if (wv >= 12) {
        __builtin_amdgcn_s_setprio(3);
        const int t = tid - 768;
        if (t < 16) lds_st<u4>(LA_F + t * 16, u4{0u, 0u, 0u, 0u});
        if (t < 180) lds_st<u4>(LA_U + (t / 30) * 4608 + (t % 30) * 16, u4{0u, 0u, 0u, 0u});
        bar();
#pragma unroll
        for (int i = 0; i < 2; i++)
            if (i == 0 || wv < 14) {
                const int r = min((ai_ >> 3) + 12 * (128 * i + (t >> 1)), SEQ - 1);
                u32 keep;
                asm volatile("s_mov_b32 %0, m0\n\ts_mov_b32 m0, %3\n\ts_nop 4\n\tglobal_load_lds_dword %1, %2\n\ts_mov_b32 m0, %0"
                             : "=&s"(keep) : "v"(r * (2 * DM) + (((ai_ & 7) * 144) & ~127) + (t & 1) * 128), "s"(u), "s"(LA_D) : "memory");
            }
        u3 ua[4], ub[4];
#pragma unroll
        for (int r = 0; r < 4; r++) ua[r] = *(const u3*)(u + (4 * t + r) * DM + d0);
#pragma unroll
        for (int c = 0; c < 6; c++) {
            const u32 sel = (c & 1) ? 0x07060302u : 0x05040100u;
            lds_st<u2>(LA_U + c * 4608 + 480 + t * 8, u2{__builtin_amdgcn_perm(ua[1][c >> 1], ua[0][c >> 1], sel), __builtin_amdgcn_perm(ua[3][c >> 1], ua[2][c >> 1], sel)});
        }
        asm volatile("" ::: "memory");
#pragma unroll
        for (int r = 0; r < 4; r++) ub[r] = *(const u3*)(u + (1024 + 4 * t + r) * DM + d0);
        sig(FU1);
        STAMP(7);
#pragma unroll
        for (int c = 0; c < 6; c++) {
            const u32 sel = (c & 1) ? 0x07060302u : 0x05040100u;
            lds_st<u2>(LA_U + c * 4608 + 480 + 2048 + t * 8, u2{__builtin_amdgcn_perm(ub[1][c >> 1], ub[0][c >> 1], sel), __builtin_amdgcn_perm(ub[3][c >> 1], ub[2][c >> 1], sel)});
        }
        sig(FU2);
        STAMP(8);
        const int tile = wv - 12;
        char* o = (char*)(out + 4 * t * (2 * DM) + d0);
        await<2>(FY1, 12);
        STAMP(11);
        a_rows_out(LA_Q + ((tile >> 1) ^ 1) * 2576 + (tile & 1) * 1024 + lane * 8, 5152, o);
        STAMP(9);
        await<1>(FY2, 12);
        STAMP(12);
        a_rows_out(LA_K + (2 * ((tile >> 1) ^ 1) + (tile & 1)) * 4160 + lane * 8, 16640, o + 1024 * (4 * DM));
        STAMP(10);
        STAMP_OUT(768);
        return;
    }
    const int ch = wv >> 1, hf = wv & 1;
    const u16 p_d = dsk[d0 + ch];
    {
        const int po = (d0 + ch) * NS + 64 * hf + lane;
        const u16 p_l = lar[po], p_a = aimg[po], p_r = cre[po], p_i = cim[po], p_t = ldt[d0 + ch];
        asm volatile("" ::: "memory");
        __builtin_amdgcn_s_barrier();
        __builtin_amdgcn_s_setprio(3);
        const float dt = rbf(exp_bf(bf2f(p_t)));
        const float ar2 = -rbf(exp_bf(bf2f(p_l))) * dt * 1.44269504f, air = bf2f(p_a) * dt * 0.159154943f;
        const f2 C = {2.f * dt * bf2f(p_r), 2.f * dt * bf2f(p_i)};
        STAMP(1);
        const f2 z1 = cexpa(ar2, air, 1.f), z8c = cexpa(ar2, -air, 8.f), z128 = cexpa(ar2, air, 128.f);
        const int gb = LA_K + ch * 16640 + hf * 4608, gw = gb + lane * 4, gr = gb + c16 * 288 + g * 16;
        u4 bfr[4];
        {
            const f2 z16c = cmul(z8c, z8c);
            f2 w0 = {1.f, 0.f}, w1 = z8c;
#pragma unroll
            for (int r = 0; r < 16; r += 2) {
                lds_st<u32>(gw + r * 288, pk(w0.x, w0.y)); lds_st<u32>(gw + (r + 1) * 288, pk(w1.x, w1.y));
                if (r < 14) { w0 = r ? cmul(w0, z16c) : z16c; w1 = cmul(w1, z16c); }
            }
#pragma unroll
            for (int s = 0; s < 4; s++) bfr[s] = lds_ld<u4>(gr + 64 * s);
        }
        f2 e[8];
        e[0] = C;
#pragma unroll
        for (int j = 1; j < 8; j++) e[j] = cmul(e[j - 1], z1);
        const f2 z256 = cmul(z128, z128), z512 = cmul(z256, z256), z1024 = cmul(z512, z512);
        const int q = LA_Q + wv * 2576;
#pragma unroll
        for (int t = 0; t < 3; t++) {
            if (t == 0) {
#pragma unroll
                for (int j = 0; j < 8; j++) {
                    lds_st<u32>(gw + j * 288, pk(e[j].x, e[j].y));
                    const f2 f = cmul(e[j], z128);
                    lds_st<u32>(gw + (8 + j) * 288, pk(f.x, f.y));
                }
            } else if (t == 1) {
                f2 f[6];
#pragma unroll
                for (int j = 0; j < 4; j++) f[j] = cmul(e[2 * j], z256);
                f[4] = cmul(e[0], z512); f[5] = cmul(e[4], z512);
#pragma unroll
                for (int j = 0; j < 4; j++) {
                    lds_st<u32>(gw + j * 288, pk(f[j].x, f[j].y));
                    const f2 h = cmul(f[j], z128);
                    lds_st<u32>(gw + (4 + j) * 288, pk(h.x, h.y));
                    lds_st<u32>(gw + (8 + 2 * j) * 288, pk(f[4].x, f[4].y));
                    lds_st<u32>(gw + (9 + 2 * j) * 288, pk(f[5].x, f[5].y));
                    if (j < 3) { f[4] = cmul(f[4], z128); f[5] = cmul(f[5], z128); }
                }
            } else {
                f2 f0 = cmul(C, z1024), f1 = cmul(f0, z128);
#pragma unroll
                for (int k = 0; k < 9; k += 2) {
                    lds_st<u32>(gw + k * 288, pk(f0.x, f0.y));
                    if (k < 8) { lds_st<u32>(gw + (k + 1) * 288, pk(f1.x, f1.y)); f0 = cmul(f0, z256); }
                    if (k < 6) f1 = cmul(f1, z256);
                }
            }
            f4 acc = Z;
#pragma unroll
            for (int s = 0; s < 4; s++) acc = mfma(lds_ld<u4>(gr + 64 * s), bfr[s], acc);
            if (t == 0) lds_st<f4>(q + (4 * (g & 1) + 128 * (g >> 1) + 8 * c16) * 4, acc);
            else if (t == 1) {
                if (g < 2) lds_st<f4>(q + (256 + 64 * g + 4 * c16) * 4, acc);
                else {
                    const int o = q + (384 + 64 * (g - 2) + 2 * c16) * 4;
                    lds_st<f2>(o, f2{acc[0], acc[1]}); lds_st<f2>(o + 128, f2{acc[2], acc[3]});
                }
            } else {
                if (g < 2) {
#pragma unroll
                    for (int i = 0; i < 4; i++) lds_st<float>(q + (512 + 64 * g + 16 * i + c16) * 4, acc[i]);
                } else if (lane == 32) lds_st<float>(q + 640 * 4, acc[0]);
            }
        }
    }
    sig(FQ + ch);
    STAMP(2);
    await(FQ + ch, 2);
    {
        const LAS float* qa = (const LAS float*)(u32)(LA_Q + ch * 5152);
        const LAS float* qb = qa + 644;
        const int kE = LA_K + ch * 16640, kO = kE + 4160;
        if (hf == 0) {
            const int j = 512 + 2 * lane;
            const float q0 = qa[j] + qb[j], q1 = qa[j + 1] + qb[j + 1], q2 = qa[j + 2] + qb[j + 2];
            const u32 a = pk(q2, q2), b = pk(q1, q1), c = pk(q0, q0);
            const int y = (504 - 8 * lane) * 4;
            put8(kE + y, u4{a, a, b, b}, u4{b, pk(q1, q0), c, c});
            put8(kO + y, u4{a, pk(q2, q1), b, b}, u4{b, c, c, c});
            if (lane < 36) {
                const int cp = lane / 9;
                if (cp >= 2 || lane != 9 * cp) lds_st<u32>(kE + cp * 4160 + (1023 + lane - 9 * cp) * 4, 0u);
            }
        } else {
            {
                const int j = 384 + 2 * lane;
                const float q0 = qa[j] + qb[j], q1 = qa[j + 1] + qb[j + 1], q2 = qa[j + 2] + qb[j + 2];
                const u32 b = pk(q1, q1), c = pk(q0, q0);
                const int y = (764 - 4 * lane) * 4;
                put4(kE + y, u4{pk(q2, q2), b, pk(q1, q0), c});
                put4(kO + y, u4{pk(q2, q1), b, c, c});
            }
            {
                const int j = 256 + 2 * lane;
                const float q0 = qa[j] + qb[j], q1 = qa[j + 1] + qb[j + 1], q2 = qa[j + 2] + qb[j + 2];
                const float qm = lane ? q0 : qa[255] + qb[255];
                const int y = (894 - 2 * lane) * 4;
                put2(kE + y, pk(q2, q1), pk(q0, q0));
                put2(kO + y, pk(q1, q0), pk(q0, qm));
            }
            {
                f4 k = *(const LAS f4*)(qa + 4 * lane) + *(const LAS f4*)(qb + 4 * lane);
                if (lane == 0) k[0] += bf2f(p_d);
                const float km = lane ? qa[4 * lane - 1] + qb[4 * lane - 1] : 0.f;
                const int y = (1022 - 2 * lane) * 4;
                put2(kE + y, pk(k[3], k[2]), pk(k[1], k[0]));
                put2(kO + y, pk(k[2], k[1]), pk(k[0], km));
            }
        }
    }
    sig(FK + ch);
    const int x0 = 2031 - c16 + 8 * g;
    int ka = LA_K + ch * 16640 + (x0 & 3) * 4160 + (x0 >> 2) * 8 - 256 * hf - 192 - 3584;
    int ubs = LA_U + ch * 4608 + 480 + (16 * c16 + 8 * g - 16) * 2 - 256 * hf - 192;
    pin(ka); pin(ubs);
    if (wv < 4) __builtin_amdgcn_s_setprio(0);
    else if (wv < 8) __builtin_amdgcn_s_setprio(1);
    else __builtin_amdgcn_s_setprio(2);
    await(FK + ch, 2);
    STAMP(13);
    await<1>(FU1, 4);
    STAMP(3);
    f4 acc[4];
#define KFR(rho, p) ld_k(ka + 192 - 64 * (rho) + 3584 - 512 * (p))
#define UFR(rho, q) lds_ld<u4>(ubs + 192 - 64 * (rho) + 512 * (q))
#define SB __builtin_amdgcn_sched_barrier(0)
#define PINACC do { pin(acc[0]); pin(acc[1]); pin(acc[2]); pin(acc[3]); } while (0)
#define TRIANGLE(q0)                                                                          \
    {                                                                                         \
        u4 A[2][4], B[2][4];                                                                  \
        _Pragma("unroll") for (int p = 0; p < 4; p++) { B[0][p] = UFR(0, q0 + p); A[0][p] = KFR(0, p); } \
        _Pragma("unroll") for (int rho = 0; rho < 4; rho++)                                   \
            _Pragma("unroll") for (int p = 0; p < 4; p++) {                                   \
                SB;                                                                           \
                if (rho < 3) { B[(rho + 1) & 1][p] = UFR(rho + 1, q0 + p); A[(rho + 1) & 1][p] = KFR(rho + 1, p); } \
                _Pragma("unroll") for (int q = 0; p + q < 4; q++) acc[p + q] = mfma(A[rho & 1][p], B[rho & 1][q], acc[p + q]); \
            }                                                                                 \
        SB;                                                                                   \
        PINACC;                                                                               \
    }
#define EXCHANGE(mine, other, str, fx, fy)                                                    \
    {                                                                                         \
        if (hf) { lds_st<f4>((mine) + lane * 16, acc[0]); lds_st<f4>((mine) + (str) + lane * 16, acc[1]); acc[0] = acc[2]; acc[1] = acc[3]; } \
        else { lds_st<f4>((mine) + lane * 16, acc[2]); lds_st<f4>((mine) + (str) + lane * 16, acc[3]); } \
        sig(fx + ch);                                                                         \
        await(fx + ch, 2);                                                                    \
        const f4 y0 = lds_ld<f4>((other) + lane * 16) + acc[0], y1 = lds_ld<f4>((other) + (str) + lane * 16) + acc[1]; \
        const int w = (other) + (16 * c16 + 4 * g) * 2;                                       \
        lds_st<u2>(w, u2{pk(y0[0], y0[1]), pk(y0[2], y0[3])});                                \
        lds_st<u2>(w + (str), u2{pk(y1[0], y1[1]), pk(y1[2], y1[3])});                        \
        sig(fy);                                                                              \
    }
#pragma unroll
    for (int i = 0; i < 4; i++) acc[i] = Z;
    TRIANGLE(0);
    STAMP(4);
    EXCHANGE(LA_Q + wv * 2576, LA_Q + (wv ^ 1) * 2576, 1024, FX1, FY1);
#pragma unroll
    for (int i = 0; i < 4; i++) acc[i] = Z;
    {
        u4 A[8], B[2][4];
#pragma unroll
        for (int p = 0; p < 4; p++) B[0][p] = UFR(0, p);
#pragma unroll
        for (int p = 1; p < 8; p++) A[p] = KFR(0, p);
#pragma unroll
        for (int rho = 0; rho < 4; rho++)
#pragma unroll
            for (int p = 1; p < 8; p++) {
                SB;
                if (rho < 3 && p < 5) B[(rho + 1) & 1][p - 1] = UFR(rho + 1, p - 1);
#pragma unroll
                for (int q = 0; q < 4; q++)
                    if (p + q >= 4 && p + q < 8) acc[p + q - 4] = mfma(A[p], B[rho & 1][q], acc[p + q - 4]);
                SB;
                if (rho < 3) A[p] = KFR(rho + 1, p);
            }
        PINACC;
    }
    sig(F2A + ch);
    STAMP(14);
    await<1>(FU2, 4);
    TRIANGLE(4);
    STAMP(5);
    await(F2A + ch, 2);
    EXCHANGE(LA_K + ch * 16640 + 2 * hf * 4160, LA_K + ch * 16640 + 2 * (hf ^ 1) * 4160, 4160, FX2, FY2);
    STAMP(6);
    STAMP_OUT(0);
}

__global__ void __launch_bounds__(NT) fused_kernel(const u16* __restrict__ u, const u16* __restrict__ lar, const u16* __restrict__ aimg,
                                                   const u16* __restrict__ cre, const u16* __restrict__ cim, const u16* __restrict__ ldt,
                                                   const u16* __restrict__ dsk, const u16* __restrict__ x, const u16* __restrict__ wr,
                                                   const u16* __restrict__ wi, u16* __restrict__ out, char* ws) {
    const char* entry;
    asm volatile("s_getpc_b64 %0" : "=s"(entry));
    if (blockIdx.x == 150) { const unsigned long long t0 = __builtin_amdgcn_s_memrealtime(); while (__builtin_amdgcn_s_memrealtime() - t0 < 5000ull) __builtin_amdgcn_s_sleep(1); return; }
    if (__builtin_expect(blockIdx.x < NB2, 1)) head_b(entry, x, wr, wi, out, ws);
    else if (blockIdx.x >= A0) head_a(u, lar, aimg, cre, cim, ldt, dsk, out, ws);
    else warm_code(entry);
}

extern "C" void kernel_launch(void* const* d_in, const int* in_sizes, int n_in, void* d_out, int out_size, void* d_ws,
                              size_t ws_size, hipStream_t stream) {
    (void)hipFuncSetAttribute((const void*)fused_kernel, hipFuncAttributeMaxDynamicSharedMemorySize, LDS_BYTES);
    fused_kernel<<<GRID, NT, LDS_BYTES, stream>>>((const u16*)d_in[0], (const u16*)d_in[1], (const u16*)d_in[2], (const u16*)d_in[3],
                                                  (const u16*)d_in[4], (const u16*)d_in[5], (const u16*)d_in[6], (const u16*)d_in[7],
                                                  (const u16*)d_in[8], (const u16*)d_in[9], (u16*)d_out, (char*)d_ws);
}
```

```cpp
#include <hip/hip_runtime.h>

typedef unsigned short u16;
typedef unsigned int u32;
typedef float f2 __attribute__((ext_vector_type(2)));
typedef float f4 __attribute__((ext_vector_type(4)));
typedef u32 u2 __attribute__((ext_vector_type(2)));
typedef u32 u4 __attribute__((ext_vector_type(4)));
typedef int i8v __attribute__((ext_vector_type(8)));
typedef short s4 __attribute__((ext_vector_type(4)));
typedef short s8 __attribute__((ext_vector_type(8)));
typedef __bf16 bf2 __attribute__((ext_vector_type(2)));
#define DI __device__ __forceinline__
#define LAS __attribute__((address_space(3)))
#define GAS __attribute__((address_space(1)))
#define CAS __attribute__((address_space(4)))
#pragma clang diagnostic ignored "-Wint-to-pointer-cast"
struct u3 { u32 v[3]; DI u32& operator[](int i) { return v[i]; } DI const u32& operator[](int i) const { return v[i]; } };

constexpr int SEQ = 2048, DM = 576, NS = 128, DF = 289;
constexpr int NB1 = 128;
constexpr int NB2 = 145;
constexpr int A0 = 152;
constexpr int NA = 96;
constexpr int GRID = A0 + NA;
constexpr int NT = 1024;

constexpr u32 WS_EPOCH = 0, WS_F1 = 4096, WS_F2 = WS_F1 + 16 * 16 * 128, WS_X1 = 1u << 20, WS_Y1 = 4u << 20, WS_USED = 8u << 20;
#define STAMP(i)
#define STAMP_DECL
#define STAMP_OUT(T)

constexpr int F_SA = 0, F_EO = 4;
constexpr int F_ARE = 9, F_AIM = 10, F_BRE = 11, F_BIM = 12, F_G = 13;
constexpr int F_F16 = 21, F_GI = 23, F_AI = 31, F_M1C = 33;
constexpr int F_VAR = 37;
constexpr int F_END = F_VAR + 40;
constexpr int ET = 452, ER = 28, E2V = 580, E2U = 36, OP = 1168;
constexpr int LB_X = 0, LB_I = 0, LB_Y = 0;
constexpr int LB_E = 16 * OP;
constexpr int LB_O = LB_E, LB_E2 = LB_E;
constexpr int LB_S = LB_E + 16 * ET * 4;
constexpr int LB_W = LB_S + 1200 * 16;
constexpr int LB_T = LB_W + 2 * 256 * 36;
constexpr int LB_G = LB_T + (F_VAR + 16) * 1024;
constexpr int LB_END = LB_G + 16;
struct CosTab { double c[2048]; double c9[9], s9[9]; };
constexpr double tay_cos(double x) { double t = 1, s = 1; for (int k = 1; k <= 14; k++) { t *= -x * x / ((2 * k - 1) * (2 * k)); s += t; } return s; }
constexpr double tay_sin(double x) { double t = x, s = x; for (int k = 1; k <= 14; k++) { t *= -x * x / ((2 * k + 1) * (2 * k)); s += t; } return s; }
constexpr CosTab make_cos() {
    CosTab t = {};
    const double PI2 = 6.283185307179586476925;
    for (int k = 0; k <= 512; k++) {
        const double v = k == 512 ? 0.0 : (k <= 256 ? tay_cos(PI2 * k / 2048) : tay_sin(PI2 * (512 - k) / 2048));
        t.c[k] = v; t.c[(2048 - k) & 2047] = v; t.c[1024 - k] = -v; t.c[(1024 + k) & 2047] = -v;
    }
    t.c[512] = 0.0; t.c[1536] = 0.0;
    for (int k = 0; k < 9; k++) { const double a = PI2 * (k <= 4 ? k : k - 9) / 9; t.c9[k] = tay_cos(a); t.s9[k] = tay_sin(a); }
    return t;
}
constexpr CosTab CT = make_cos();
constexpr double cs(int n) { return CT.c[n & 2047]; }
constexpr double sn(int n) { return CT.c[(n - 512) & 2047]; }
constexpr u32 bfr(double x) { const u32 b = __builtin_bit_cast(u32, (float)x); return (b + 0x7fffu + ((b >> 16) & 1u)) >> 16; }
constexpr u32 pkc(double lo, double hi) { return bfr(lo) | (bfr(hi) << 16); }
constexpr u32 f32c(double x) { return __builtin_bit_cast(u32, (float)x); }
template <int N> struct Frags { u32 w[N * 256]; };
constexpr u32 w_re(double amp, int n) { return pkc(amp * cs(n), amp * sn(n)); }
constexpr u32 w_im(double amp, int n) { return pkc(-amp * sn(n), amp * cs(n)); }
constexpr Frags<F_VAR> make_t0() {
    Frags<F_VAR> t = {};
    const double R2 = 1.4142135623730951;
    for (int l = 0; l < 64; l++)
        for (int i = 0; i < 4; i++) {
            const int c = l & 15, g = l >> 4, r = 4 * g + i, o = l * 4 + i;
            {
                const int b = c >> 2, tq = c & 3;
                const double amp = b ? 1.0 : R2;
                const int n0 = 512 * tq * g + (b ? 256 * b * (2 * i) : -256), n1 = 512 * tq * g + (b ? 256 * b * (2 * i + 1) : 256);
                t.w[F_ARE * 256 + o] = pkc(amp * cs(n0), amp * cs(n1));
                t.w[F_AIM * 256 + o] = pkc(-amp * sn(n0), -amp * sn(n1));
            }
            for (int w = 0; w < 9; w++) {
                const int sh = 16 * (i & 1), oo = w * 128 + l * 2 + (i >> 1);
                t.w[F_EO * 256 + oo] |= (u32)((c * ET + r * ER + 2 * ((7 * w + r) % 9)) * 4) << sh;
                if (w < 8) {
                    const int k = (9 * (w & 3) + 36 * r + 64 * (c % 9)) % DM, slot = w < 4 ? (k <= 288 ? 2 * k : 2 * (DM - k) + 1) : k;
                    t.w[F_SA * 256 + oo] |= (u32)(slot * 8 + (slot >> 6) * 16) << sh;
                }
            }
            if (r < 9) { const int k = (r * c) % 9; t.w[F_BRE * 256 + o] = pkc(CT.c9[k], CT.s9[k]); t.w[F_BIM * 256 + o] = pkc(-CT.s9[k], CT.c9[k]); }
            for (int q = 0; q < 4; q++) {
                t.w[(F_G + 2 * q) * 256 + o] = w_re(1.0, 32 * r * (q + 4 * c)); t.w[(F_G + 2 * q + 1) * 256 + o] = w_im(1.0, 32 * r * (q + 4 * c));
                t.w[(F_GI + 2 * q) * 256 + o] = w_re(1.0, 32 * c * (q + 4 * r)); t.w[(F_GI + 2 * q + 1) * 256 + o] = w_im(1.0, 32 * c * (q + 4 * r));
            }
            t.w[F_F16 * 256 + o] = w_re(1.0, 128 * r * c); t.w[(F_F16 + 1) * 256 + o] = w_im(1.0, 128 * r * c);
            for (int h = 0; h < 2; h++) {
                const int j = 4 * h + (c >> 2), n = 512 * i * (c & 3) + (g ? 256 * g * j : ((j & 1) ? 256 : -256));
                t.w[(F_AI + h) * 256 + o] = w_re(g ? 2.0 : R2, n);
                const int m = 16 * r * (4 * h + 8 * c) - 512 * h;
                t.w[(F_M1C + 2 * h) * 256 + o] = pkc(0.5 * cs(m), -0.5 * sn(m));
                t.w[(F_M1C + 2 * h + 1) * 256 + o] = pkc(-0.5 * sn(m), -0.5 * cs(m));
            }
        }
    return t;
}
constexpr Frags<40> make_t1() {
    Frags<40> t = {};
    for (int v = 0; v < 5; v++)
        for (int l = 0; l < 64; l++)
            for (int i = 0; i < 4; i++) {
                const int be = v < 3 ? v + 1 : 4 * (v - 3), c = l & 15, r = 4 * (l >> 4) + i, o = v * 2048 + l * 4 + i;
                const double amp = v < 3 ? 1.0 : 0.5;
                const int n1 = 16 * r * (be + 8 * c) + (v == 4 ? 512 : 0), n4 = 16 * c * (be + 8 * r);
                t.w[o] = w_re(amp, n1); t.w[o + 256] = w_im(amp, n1);
                t.w[o + 512] = w_re(1.0, n4); t.w[o + 768] = w_im(1.0, n4);
                t.w[o + 1024] = f32c(cs(r * (be + 8 * c))); t.w[o + 1280] = f32c(-sn(r * (be + 8 * c)));
                t.w[o + 1536] = f32c(cs(c * (be + 8 * r))); t.w[o + 1792] = f32c(-sn(c * (be + 8 * r)));
            }
    return t;
}
struct Tables { Frags<F_VAR> a; Frags<40> b; };
constexpr Frags<F_VAR> T0 = make_t0();
constexpr Frags<40> T1 = make_t1();
static __device__ const __attribute__((aligned(128))) Tables TAB = {T0, T1};
constexpr int LA_K = 0;
constexpr int LA_U = LA_K + 6 * 16640;
constexpr int LA_Q = LA_U + 6 * 4608;
constexpr int LA_F = LA_Q + 12 * 2576;
constexpr int LA_D = LA_F + 256;
constexpr int LA_END = LA_D + 256;
constexpr int FQ = 0, FK = 6, FX1 = 12, F2A = 18, FX2 = 24, FU1 = 30, FU2 = 31, FY1 = 32, FY2 = 33;
constexpr int LDS_BYTES = (LA_END > LB_END ? LA_END : LB_END);
static_assert(LDS_BYTES <= 160 * 1024 && LDS_BYTES > 80 * 1024, "one workgroup per CU");

DI u32 pk(float a, float b) { f2 v = {a, b}; return __builtin_bit_cast(u32, __builtin_convertvector(v, bf2)); }
DI float blo(u32 v) { return __uint_as_float(v << 16); }
DI float bhi(u32 v) { return __uint_as_float(v & 0xffff0000u); }
DI float bf2f(u16 v) { return __uint_as_float((u32)v << 16); }
DI float rbf(float x) { return blo(pk(x, x)); }
DI f2 cmul(f2 a, f2 b) { f2 t = a.xx * b; f2 s = {-b.y, b.x}; return __builtin_elementwise_fma(a.yy, s, t); }
DI f4 mfma(u4 a, u4 b, f4 c) { return __builtin_amdgcn_mfma_f32_16x16x32_bf16(__builtin_bit_cast(s8, a), __builtin_bit_cast(s8, b), c, 0, 0, 0); }
DI u4 pack4(f4 re, f4 im) { u4 r = {pk(re[0], im[0]), pk(re[1], im[1]), pk(re[2], im[2]), pk(re[3], im[3])}; return r; }
DI u4 twid(f4 re, f4 im, f4 wr, f4 wi) { return pack4(__builtin_elementwise_fma(re, wr, -(im * wi)), __builtin_elementwise_fma(re, wi, im * wr)); }
DI int mod9(int x) { return x - 9 * ((x * 57) >> 9); }
template <class T> DI void pin(T& v) { asm volatile("" : "+v"(v)); }

template <class T> DI T lds_ld(int off) { return *(const LAS T*)(u32)off; }
template <class T> DI void lds_st(int off, T v) { *(LAS T*)(u32)off = v; }
DI void bar() { __syncthreads(); }
DI void glds16(const void* g, u32 off, int lds) {
    u32 keep;
    asm volatile("s_mov_b32 %0, m0\n\ts_mov_b32 m0, %3\n\ts_nop 4\n\tglobal_load_lds_dwordx4 %1, %2\n\ts_mov_b32 m0, %0" : "=&s"(keep) : "v"(off), "s"(g), "s"(lds) : "memory");
}

DI void publish_tag(u32 epoch) {
    u32 t;
    asm volatile("v_add_u32 %0, 0x9E3779B1, %1\n\tds_write_b32 %2, %0\n\ts_waitcnt lgkmcnt(0)" : "=&v"(t) : "v"(epoch), "v"(LB_G) : "memory");
}
DI void wait_flag(__amdgpu_buffer_rsrc_t r, u32 off, u32 tag, bool on) {
    if (on) {
        const unsigned long long t0 = __builtin_amdgcn_s_memrealtime();
        while (!__all(__builtin_amdgcn_raw_buffer_load_b32(r, off, 0, 16) == tag)) {
            if (__builtin_amdgcn_s_memrealtime() - t0 > 5000000ull) break;
            asm volatile("" ::: "memory");
        }
    }
    asm volatile("" ::: "memory");
}

constexpr int CODE_LINES = 128;
DI void warm_code(const char* entry) {
    if (threadIdx.x < CODE_LINES) { const u32 t = *(const GAS u32*)(entry + threadIdx.x * 128); asm volatile("" :: "v"(t)); }
}

DI void head_b(const char* entry, const u16* __restrict__ x, const u16* __restrict__ wr, const u16* __restrict__ wi,
               u16* __restrict__ out, char* ws, u32 ptag_) {
    const int tid = threadIdx.x, lane = tid & 63, c16 = lane & 15, g = lane >> 4;
    const int wv = __builtin_amdgcn_readfirstlane(tid >> 6);
    const int id = blockIdx.x;
    STAMP_DECL;
    const bool rows = id < NB1;
    const int s00 = 32 * (id >> 4) + (id & 15);
    const char* tab = (const char*)&TAB;
    const int R = (tid * 911) >> 16;
    const u32 xoff = (s00 + 16 * (R >> 3) + 256 * (R & 7)) * (2 * DM) + (tid - R * 72) * 16;
    asm volatile("" :: "s"(x), "s"(wr), "s"(wi), "s"(out), "s"(ws), "v"(xoff));
    const __amdgpu_buffer_rsrc_t rws = __builtin_amdgcn_make_buffer_rsrc(ws, 0, WS_USED, 0x00020000);
    u32 epoch = 0;
    if (wv == 0) asm volatile("global_load_dword %0, %0, %1 sc1" : "+v"(epoch) : "s"(ws) : "memory");
    if (rows) {
        glds16(x, xoff, LB_X + wv * 1024);
        if (wv < 2) glds16(x, (s00 + 16 + 1536) * (2 * DM) + (tid < 56 ? (tid + 16) * 16 : 256 * 2 * DM + (tid - 56) * 16), LB_X + 16384 + wv * 1024);
        else glds16(tab, tid * 16 - 2048, LB_T + (wv - 2) * 1024);
        if (wv >= 9) glds16(tab, tid * 16 + 5 * 1024, LB_T + (wv + 5) * 1024);
    }
    const int u2i = (id & 7) ? (id & 7) * 18 + 1 + (id >> 3) : (id == 136 ? 0 : (id >> 3) + (id < 136 ? 1 : 0));
    const bool star = u2i >= 108;
    const int q36 = min((u2i * 57) >> 11, 3), kg = u2i - 36 * q36, bq = (q36 + 1) & 3;
    const bool mir = !star && kg >= 18;
    const int w0 = star ? min(16 * kg, 562) : (mir ? (2048 - bq) * (2 * DF) + 1122 - 32 * kg : bq * (2 * DF) + 32 * kg);
    if (wv < 8) {
        glds16(tab, F_F16 * 1024 + tid * 16, LB_T + F_F16 * 1024 + wv * 1024);
        glds16(tab, (F_F16 + 8) * 1024 + tid * 16, LB_T + (F_F16 + 8) * 1024 + wv * 1024);
        glds16(tab, (F_VAR + 8 * q36) * 1024 + tid * 16, LB_T + F_VAR * 1024 + wv * 1024);
        glds16(tab, (F_VAR + 8 * q36 + 8) * 1024 + tid * 16, LB_T + (F_VAR + 8) * 1024 + wv * 1024);
    }
    __builtin_amdgcn_s_barrier();
    const u32 woff = w0 + ((tid >> 1) & 255) * (mir ? -16 * DF : 16 * DF) + (tid & 1) * (star ? 8 * DF : 16);
    u4 wra, wrb;
    if (wv >= 8) {
        __builtin_memcpy(&wra, (const char*)wr + woff, 16);
        __builtin_memcpy(&wrb, (const char*)wi + woff, 16);
    }
    __builtin_amdgcn_sched_barrier(0);
    const f4 Z = {0.f, 0.f, 0.f, 0.f};
    const int tau = wv & 3, bw = wv >> 2;
    int tl = LB_T + lane * 16;
    int lin = LB_S + (tid + (tid >> 5)) * 16;
    pin(tl); pin(lin);
    int sadr[4];

    if (rows) {
        if (tid < 256) {
            const int o = LB_E + 72 + (tid & 15) * (ER * 4) + (tid >> 4) * (ET * 4);
            lds_st<u2>(o, u2{0u, 0u}); lds_st<u4>(o + 8, u4{0u, 0u, 0u, 0u});
        }
        int xo = LB_X + (c16 >> 2) * 1152 + 2 * (144 * ((g - wv) & 3) + 16 * wv + 4 * (c16 & 3));
        int bo = LB_E + (wv * ET + c16 * ER + min(8 * g, 16)) * 4;
        int go = WS_X1 + (tid >> 3) * 16384 + id * 128 + (tid & 7) * 16;
        int ta = LB_T + lane * 8;
        pin(xo); pin(bo); pin(go); pin(ta);
        STAMP(1);
        asm volatile("s_cmp_lt_u32 %0, 8\n\ts_cbranch_scc1 .Lw%=\n\ts_waitcnt vmcnt(2)\n.Lw%=:\n\ts_waitcnt vmcnt(4)" :: "s"(wv) : "memory", "scc");
        if (wv == 0) publish_tag(epoch);
        bar();
        STAMP(2);
        if (wv < 9) {
            u4 d[2];
#pragma unroll
            for (int s = 0; s < 2; s++) {
                const s4 lo = __builtin_amdgcn_ds_read_tr16_b64_v4i16((LAS s4*)(u32)(xo + s * 9216));
                const s4 hi = __builtin_amdgcn_ds_read_tr16_b64_v4i16((LAS s4*)(u32)(xo + s * 9216 + 4608));
                d[s] = __builtin_bit_cast(u4, __builtin_shufflevector(lo, hi, 0, 1, 2, 3, 4, 5, 6, 7));
            }
            const u4 are = lds_ld<u4>(tl + F_ARE * 1024), aim = lds_ld<u4>(tl + F_AIM * 1024);
            const u2 eo = lds_ld<u2>(ta + F_EO * 1024 + wv * 512);
#pragma unroll
            for (int s = 0; s < 2; s++) d[s] = pack4(mfma(d[s], are, Z), mfma(d[s], aim, Z));
#pragma unroll
            for (int i = 0; i < 4; i++) lds_st<u2>(LB_E + ((i & 1) ? eo[i >> 1] >> 16 : eo[i >> 1] & 0xffff), u2{d[0][i], d[1][i]});
        }
        u4 bre = lds_ld<u4>(tl + F_BRE * 1024), bim = lds_ld<u4>(tl + F_BIM * 1024);
        u4 gre = lds_ld<u4>(tl + (F_G + 2 * tau) * 1024), gim = lds_ld<u4>(tl + (F_G + 2 * tau + 1) * 1024);
        {
            const u2 sa = lds_ld<u2>(ta + F_SA * 1024 + (tau + (bw ? 4 : 0)) * 512);
            const int base = LB_S + ((bw + 3) & 3) * (DM * 8 + 144);
#pragma unroll
            for (int i = 0; i < 4; i++) { sadr[i] = base + ((i & 1) ? sa[i >> 1] >> 16 : sa[i >> 1] & 0xffff); pin(sadr[i]); }
        }
        pin(bre); pin(bim); pin(gre); pin(gim);
        bar();
        STAMP(13);
        {
            const u4 p0 = lds_ld<u4>(bo), p1 = lds_ld<u4>(bo + 16);
            u4 d[2];
#pragma unroll
            for (int s = 0; s < 2; s++) {
                const u4 a = {p0[s], p0[2 + s], p1[s], p1[2 + s]};
                const u4 t = pack4(mfma(a, bre, Z), mfma(a, bim, Z));
                d[s] = pack4(mfma(gre, t, Z), mfma(gim, t, Z));
            }
            if (c16 < 9) {
#pragma unroll
                for (int i = 0; i < 4; i++) lds_st<u2>(sadr[i], u2{d[0][i], d[1][i]});
                if (wv == 0 && c16 == 0 && !(g & 1)) lds_st<u2>(sadr[0] + 8, u2{d[0][0], d[1][0]});
            }
        }
        STAMP(14);
        bar();
        if (threadIdx.x == 0) __hip_atomic_store((GAS u32*)(ws + (6u << 20) + (1u << 19)) + 64 + (blockIdx.x) * 32, ptag_, __ATOMIC_RELAXED, __HIP_MEMORY_SCOPE_AGENT);
        __builtin_amdgcn_raw_buffer_store_b128(lds_ld<u4>(lin), rws, go, 0, 16);
        if (tid <= 128) __builtin_amdgcn_raw_buffer_store_b128(lds_ld<u4>(lin + 1056 * 16), rws, go, 128 * 16384, 16);
    } else {
        if (wv < 8 && (kg == 0 || kg == (star ? 36 : 18))) {
            const int m = tid & 255;
            lds_st<u16>(LB_W + tid * 36 + 32, ((wv >> 2) ? wi : wr)[((mir ? bq + 8 * m : 2048 - bq - 8 * m) & 2047) * DF + (kg ? 288 : 0)]);
        }
        warm_code(entry);
        asm volatile("s_waitcnt vmcnt(0)" ::: "memory");
        if (wv == 0) publish_tag(epoch);
        bar();
    }
    const u32 tag = lds_ld<u32>(LB_G);
    if (wv >= 8) {
        const int o = LB_W + ((tid >> 1) & 255) * 36 + (tid & 1) * 16;
#pragma unroll
        for (int i = 0; i < 4; i++) { lds_st<u32>(o + 4 * i, wra[i]); lds_st<u32>(o + 9216 + 4 * i, wrb[i]); }
    }
    asm volatile("s_waitcnt vmcnt(0)" ::: "memory");
    if (rows && lane == 0) __builtin_amdgcn_raw_buffer_store_b32(tag, rws, WS_F1 + (wv * 16 + (id >> 3)) * 128 + (id & 7) * 4, 0, 16);
    STAMP(4);
    bar();
    STAMP(12);

    const int typ = star ? (wv & 1) : 0;
    const int tv = tl + (F_VAR + 8 * typ) * 1024;
    u4 fre = lds_ld<u4>(tl + F_F16 * 1024), fim = lds_ld<u4>(tl + (F_F16 + 1) * 1024);
    u4 m1r = lds_ld<u4>(tv), m1i = lds_ld<u4>(tv + 1024), m4r = lds_ld<u4>(tv + 2048), m4i = lds_ld<u4>(tv + 3072);
    f4 twr = lds_ld<f4>(tv + 4096), twi = lds_ld<f4>(tv + 5120), tur = lds_ld<f4>(tv + 6144), tui = lds_ld<f4>(tv + 7168);
    u4 mcr = lds_ld<u4>(tl + (F_M1C + 2 * typ) * 1024), mci = lds_ld<u4>(tl + (F_M1C + 2 * typ + 1) * 1024);
    f4 wfr, wfi;
    {
        const int kcol = star ? 8 * kg + (wv >> 1) : 16 * kg + wv;
        const int cc = star ? 8 * typ + (u2i < NB2 - 1 ? (wv >> 1) : 7) : (mir ? 15 - wv : wv);
        const int wo = LB_W + (c16 + 64 * g) * 36 + cc * 2;
#pragma unroll
        for (int i = 0; i < 4; i++) {
            wfr[i] = bf2f(lds_ld<u16>(wo + i * 576));
            wfi[i] = bf2f(lds_ld<u16>(wo + i * 576 + 9216));
        }
        float sc = 1.f / (float)(SEQ * DM);
        if (kcol == 0 || kcol == 288) {
            sc *= 0.5f;
#pragma unroll
            for (int i = 0; i < 4; i++) {
                const int m = c16 + 64 * g + 16 * i;
                const int o = LB_W + (typ ? (255 - m) * 36 + cc * 2 : m * 36 + 32);
                wfr[i] += bf2f(lds_ld<u16>(o));
                wfi[i] -= bf2f(lds_ld<u16>(o + 9216));
            }
        }
        wfr *= sc; wfi *= (mir ? -sc : sc);
    }
    f4 wnr = -wfr, wni = -wfi;
    int io = (32 * g + c16) * 128 + (((wv >> 1) ^ (c16 >> 1)) * 16);
    int so = (tid >> 3) * 128 + (((tid & 7) ^ ((tid >> 4) & 7)) * 16);
    pin(fre); pin(fim); pin(m1r); pin(m1i); pin(m4r); pin(m4i); pin(twr); pin(twi); pin(tur); pin(tui); pin(mcr); pin(mci);
    int gi = WS_X1 + u2i * 16384 + tid * 16, gq = WS_Y1 + (tid >> 3) * (NB2 * 128) + u2i * 128 + (tid & 7) * 16;
    int i1 = LB_I + io + (star ? 0 : (wv & 1) * 8);
    io += LB_O + (wv & 1) * 8;
    pin(wfr); pin(wnr); pin(wni); pin(io); pin(so); pin(gi); pin(gq); pin(i1);
    wait_flag(rws, WS_F1 + (((u2i >> 3) & 15) * 16 + wv) * 128 + lane * 4, tag, lane < 8);
    STAMP(5);
    lds_st<u4>(LB_I + so, __builtin_amdgcn_raw_buffer_load_b128(rws, gi, 0, 16));
    bar();
    STAMP(6);
    {
        const int o1 = i1;
        const u2 a0 = lds_ld<u2>(o1), a1 = lds_ld<u2>(o1 + 2048);
        const u4 a = {a0[0], a0[1], a1[0], a1[1]};
        f4 re = mfma(a, m1r, Z), im = mfma(a, m1i, Z);
        if (star) {
            const u2 b0 = lds_ld<u2>(o1 + 8), b1 = lds_ld<u2>(o1 + 8 + 2048);
            const u4 b = {b0[0], b0[1], b1[0], b1[1]};
            re = mfma(b, mcr, re); im = mfma(b, mci, im);
        }
        u4 t = twid(re, im, twr, twi);
        re = mfma(fre, t, Z); im = mfma(fim, t, Z);
        t = pack4(__builtin_elementwise_fma(re, wfr, im * wni), __builtin_elementwise_fma(re, wni, im * wnr));
        re = mfma(t, fre, Z); im = mfma(t, fim, Z);
        t = twid(re, im, tur, tui);
        t = pack4(mfma(m4r, t, Z), mfma(m4i, t, Z));
        lds_st<u2>(io, u2{t[0], t[1]}); lds_st<u2>(io + 2048, u2{t[2], t[3]});
    }
    bar();
    STAMP(7);
    {
        u4 v = lds_ld<u4>(LB_O + so);
        if (star) {
            u4 w;
#pragma unroll
            for (int s = 0; s < 2; s++) {
                const float ar = blo(v[s]), ai = bhi(v[s]), cr = blo(v[2 + s]), ci = bhi(v[2 + s]);
                w[s] = pk(ar + ci, ai - cr); w[2 + s] = pk(ar - ci, -ai - cr);
            }
            v = w;
        }
        if (u2i < NB2 - 1 || (tid & 7) == 0)
            __builtin_amdgcn_raw_buffer_store_b128(v, rws, gq, 0, 16);
    }
    asm volatile("s_waitcnt vmcnt(0)" ::: "memory");
    if (lane == 0) __builtin_amdgcn_raw_buffer_store_b32(tag, rws, WS_F2 + (wv * 19 + (u2i >> 3)) * 128 + (u2i & 7) * 4, 0, 16);
    STAMP(8);
    if (!rows) { STAMP_OUT(0); return; }

    u4 gre = lds_ld<u4>(tl + (F_GI + 2 * tau) * 1024), gim = lds_ld<u4>(tl + (F_GI + 2 * tau + 1) * 1024);
    u4 bre = lds_ld<u4>(tl + F_BRE * 1024), bim = lds_ld<u4>(tl + F_BIM * 1024);
    u4 ai0 = lds_ld<u4>(tl + F_AI * 1024), ai1 = lds_ld<u4>(tl + (F_AI + 1) * 1024);
    int eadr[4];
#pragma unroll
    for (int i = 0; i < 4; i++) {
        const int r = 4 * g + i;
        eadr[i] = LB_E2 + (mod9(4 * (c16 + 18 - r)) * E2V + r * E2U + 2 * wv) * 4;
        pin(eadr[i]);
    }
    int ao = LB_E2 + (wv * E2V + c16 * E2U + 8 * g) * 4;
    int gy = WS_Y1 + id * (NB2 * 128) + tid * 16;
    int yo = LB_Y + (c16 >> 2) * OP + 2 * (144 * (((c16 & 3) - wv) & 3) + 16 * wv + 4 * g);
    int yl = LB_Y + tid * 16 + R * (OP - 1152);
    u32 oo = 2 * xoff - (tid - R * 72) * 16 + 2 * DM;
    pin(gre); pin(gim); pin(bre); pin(bim); pin(ai0); pin(ai1); pin(ao); pin(gy); pin(yo); pin(yl); pin(oo);
    {
        const int un = 8 * wv + lane + (lane >= 8 ? 120 : 0);
        wait_flag(rws, WS_F2 + ((id >> 3) * 19 + (un >> 3)) * 128 + (lane & 7) * 4, tag, lane < 16 && un < NB2);
    }
    STAMP(9);
    {
        const u4 v = __builtin_amdgcn_raw_buffer_load_b128(rws, gy, 0, 16);
        if (tid <= 128) lds_st<u4>(lin + 1056 * 16, __builtin_amdgcn_raw_buffer_load_b128(rws, gy, 16384, 16));
        lds_st<u4>(lin, v);
    }
    bar();
    if (id == 0 && tid == 0) __builtin_amdgcn_raw_buffer_store_b32(tag, rws, WS_EPOCH, 0, 16);
    STAMP(10);
    {
        u4 d[2];
        u2 p[4];
#pragma unroll
        for (int i = 0; i < 4; i++) p[i] = lds_ld<u2>(sadr[i]);
#pragma unroll
        for (int s = 0; s < 2; s++) {
            const u4 a = {p[0][s], p[1][s], p[2][s], p[3][s]};
            const u4 t = pack4(mfma(a, gre, Z), mfma(a, gim, Z));
            d[s] = pack4(mfma(t, bre, Z), mfma(t, bim, Z));
        }
        if (c16 < 9) {
#pragma unroll
            for (int i = 0; i < 4; i++) lds_st<u2>(eadr[i], u2{d[0][i], d[1][i]});
        }
    }
    STAMP(15);
    bar();
    if (wv < 9) {
        const u4 p0 = lds_ld<u4>(ao), p1 = lds_ld<u4>(ao + 16);
#pragma unroll
        for (int s = 0; s < 2; s++) {
            const u4 a = {p0[s], p0[2 + s], p1[s], p1[2 + s]};
            const f4 y0 = mfma(a, ai0, Z), y1 = mfma(a, ai1, Z);
            lds_st<u2>(yo + s * (8 * OP), u2{pk(y0[0], y0[1]), pk(y0[2], y0[3])});
            lds_st<u2>(yo + s * (8 * OP) + 4 * OP, u2{pk(y1[0], y1[1]), pk(y1[2], y1[3])});
        }
    }
    bar();
    *(GAS u4*)((char*)out + oo) = lds_ld<u4>(yl);
    if (wv < 2) {
        const int r2 = tid < 56 ? 14 : 15, c2 = tid + 1024 - 72 * r2;
        *(GAS u4*)((char*)out + (u32)((s00 + 16 + 256 * (r2 - 8)) * (4 * DM) + 2 * DM + c2 * 16)) = lds_ld<u4>(LB_Y + r2 * OP + c2 * 16);
    }
    STAMP(11);
    STAMP_OUT(0);
}

DI f2 cexpa(float ar2, float air, float p) {
    const float e = __builtin_amdgcn_exp2f(ar2 * p), t = __builtin_amdgcn_fractf(air * p);
    return f2{e * __builtin_amdgcn_cosf(t), e * __builtin_amdgcn_sinf(t)};
}
DI u4 ld_k(int o) {
    const u2 a = *(const volatile LAS u2*)(u32)o, b = *(const volatile LAS u2*)(u32)(o + 8);
    return u4{a[0], a[1], b[0], b[1]};
}
DI void put8(int o, u4 a, u4 b) {
    lds_st<u4>(o, a); lds_st<u4>(o + 16, b);
    lds_st<u32>(o + 8316, a[0]); lds_st<u4>(o + 8320, u4{a[1], a[2], a[3], b[0]});
    lds_st<u2>(o + 8336, u2{b[1], b[2]}); lds_st<u32>(o + 8344, b[3]);
}
DI void put4(int o, u4 a) {
    lds_st<u4>(o, a);
    lds_st<u32>(o + 8316, a[0]); lds_st<u2>(o + 8320, u2{a[1], a[2]}); lds_st<u32>(o + 8328, a[3]);
}
DI void put2(int o, u32 a, u32 b) { lds_st<u2>(o, u2{a, b}); lds_st<u32>(o + 8316, a); lds_st<u32>(o + 8320, b); }

DI void a_rows_out(int yb, int ystr, char* o) {
    u2 y[6];
#pragma unroll
    for (int c = 0; c < 6; c++) y[c] = lds_ld<u2>(yb + c * ystr);
#pragma unroll
    for (int r = 0; r < 4; r++) {
        u3 v;
#pragma unroll
        for (int c = 0; c < 3; c++) v[c] = __builtin_amdgcn_perm(y[2 * c + 1][r >> 1], y[2 * c][r >> 1], (r & 1) ? 0x07060302u : 0x05040100u);
        *(u3*)(o + r * (4 * DM)) = v;
    }
}

DI void sig(int f) {
    asm volatile("s_waitcnt lgkmcnt(0)" ::: "memory");
    if ((threadIdx.x & 63) == 0) __hip_atomic_fetch_add((LAS u32*)(u32)(LA_F + 4 * f), 1u, __ATOMIC_RELAXED, __HIP_MEMORY_SCOPE_WORKGROUP);
}
template <int SLEEP = 0> DI void await(int f, u32 n) {
    while ((u32)__builtin_amdgcn_readfirstlane(*(const volatile LAS u32*)(u32)(LA_F + 4 * f)) < n) if (SLEEP) __builtin_amdgcn_s_sleep(SLEEP);
    asm volatile("" ::: "memory");
}
DI float exp_bf(float x) {
    const float t = x * 1.44269504f, r = __builtin_fmaf(x, 1.92596299e-8f, __builtin_fmaf(x, 1.44269504f, -t));
    const float e = __builtin_amdgcn_exp2f(t);
    return __builtin_fmaf(e * 0.693147181f, r, e);
}

DI void head_a(const u16* __restrict__ u, const u16* __restrict__ lar, const u16* __restrict__ aimg,
               const u16* __restrict__ cre, const u16* __restrict__ cim, const u16* __restrict__ ldt,
               const u16* __restrict__ dsk, u16* __restrict__ out, char* ws, u32 ptag_) {
    const int tid = threadIdx.x, lane = tid & 63, c16 = lane & 15, g = lane >> 4;
    const int wv = __builtin_amdgcn_readfirstlane(tid >> 6);
    STAMP_DECL;
    const int ai_ = blockIdx.x - A0;
    const int d0 = 6 * ((ai_ & 7) * 12 + (ai_ >> 3));
    const f4 Z = {0.f, 0.f, 0.f, 0.f};

    if (wv >= 12) {
        __builtin_amdgcn_s_setprio(3);
        const int t = tid - 768;
        if (t < 16) lds_st<u4>(LA_F + t * 16, u4{0u, 0u, 0u, 0u});
        if (t < 180) lds_st<u4>(LA_U + (t / 30) * 4608 + (t % 30) * 16, u4{0u, 0u, 0u, 0u});
        bar();
#pragma unroll
        for (int i = 0; i < 2; i++)
            if (i == 0 || wv < 14) {
                const int r = min((ai_ >> 3) + 12 * (128 * i + (t >> 1)), SEQ - 1);
                u32 keep;
                asm volatile("s_mov_b32 %0, m0\n\ts_mov_b32 m0, %3\n\ts_nop 4\n\tglobal_load_lds_dword %1, %2\n\ts_mov_b32 m0, %0"
                             : "=&s"(keep) : "v"(r * (2 * DM) + (((ai_ & 7) * 144) & ~127) + (t & 1) * 128), "s"(u), "s"(LA_D) : "memory");
            }
        u3 ua[4], ub[4];
#pragma unroll
        for (int r = 0; r < 4; r++) ua[r] = *(const u3*)(u + (4 * t + r) * DM + d0);
#pragma unroll
        for (int c = 0; c < 6; c++) {
            const u32 sel = (c & 1) ? 0x07060302u : 0x05040100u;
            lds_st<u2>(LA_U + c * 4608 + 480 + t * 8, u2{__builtin_amdgcn_perm(ua[1][c >> 1], ua[0][c >> 1], sel), __builtin_amdgcn_perm(ua[3][c >> 1], ua[2][c >> 1], sel)});
        }
        asm volatile("" ::: "memory");
#pragma unroll
        for (int r = 0; r < 4; r++) ub[r] = *(const u3*)(u + (1024 + 4 * t + r) * DM + d0);
        sig(FU1);
        STAMP(7);
#pragma unroll
        for (int c = 0; c < 6; c++) {
            const u32 sel = (c & 1) ? 0x07060302u : 0x05040100u;
            lds_st<u2>(LA_U + c * 4608 + 480 + 2048 + t * 8, u2{__builtin_amdgcn_perm(ub[1][c >> 1], ub[0][c >> 1], sel), __builtin_amdgcn_perm(ub[3][c >> 1], ub[2][c >> 1], sel)});
        }
        sig(FU2);
        STAMP(8);
        const int tile = wv - 12;
        char* o = (char*)(out + 4 * t * (2 * DM) + d0);
        await<2>(FY1, 12);
        STAMP(11);
        a_rows_out(LA_Q + ((tile >> 1) ^ 1) * 2576 + (tile & 1) * 1024 + lane * 8, 5152, o);
        STAMP(9);
        await<1>(FY2, 12);
        STAMP(12);
        a_rows_out(LA_K + (2 * ((tile >> 1) ^ 1) + (tile & 1)) * 4160 + lane * 8, 16640, o + 1024 * (4 * DM));
        STAMP(10);
        STAMP_OUT(768);
        return;
    }
    const int ch = wv >> 1, hf = wv & 1;
    const u16 p_d = dsk[d0 + ch];
    {
        const int po = (d0 + ch) * NS + 64 * hf + lane;
        const u16 p_l = lar[po], p_a = aimg[po], p_r = cre[po], p_i = cim[po], p_t = ldt[d0 + ch];
        asm volatile("" ::: "memory");
        __builtin_amdgcn_s_barrier();
        __builtin_amdgcn_s_setprio(3);
        const float dt = rbf(exp_bf(bf2f(p_t)));
        const float ar2 = -rbf(exp_bf(bf2f(p_l))) * dt * 1.44269504f, air = bf2f(p_a) * dt * 0.159154943f;
        const f2 C = {2.f * dt * bf2f(p_r), 2.f * dt * bf2f(p_i)};
        STAMP(1);
        const f2 z1 = cexpa(ar2, air, 1.f), z8c = cexpa(ar2, -air, 8.f), z128 = cexpa(ar2, air, 128.f);
        const int gb = LA_K + ch * 16640 + hf * 4608, gw = gb + lane * 4, gr = gb + c16 * 288 + g * 16;
        u4 bfr[4];
        {
            const f2 z16c = cmul(z8c, z8c);
            f2 w0 = {1.f, 0.f}, w1 = z8c;
#pragma unroll
            for (int r = 0; r < 16; r += 2) {
                lds_st<u32>(gw + r * 288, pk(w0.x, w0.y)); lds_st<u32>(gw + (r + 1) * 288, pk(w1.x, w1.y));
                if (r < 14) { w0 = r ? cmul(w0, z16c) : z16c; w1 = cmul(w1, z16c); }
            }
#pragma unroll
            for (int s = 0; s < 4; s++) bfr[s] = lds_ld<u4>(gr + 64 * s);
        }
        f2 e[8];
        e[0] = C;
#pragma unroll
        for (int j = 1; j < 8; j++) e[j] = cmul(e[j - 1], z1);
        const f2 z256 = cmul(z128, z128), z512 = cmul(z256, z256), z1024 = cmul(z512, z512);
        const int q = LA_Q + wv * 2576;
#pragma unroll
        for (int t = 0; t < 3; t++) {
            if (t == 0) {
#pragma unroll
                for (int j = 0; j < 8; j++) {
                    lds_st<u32>(gw + j * 288, pk(e[j].x, e[j].y));
                    const f2 f = cmul(e[j], z128);
                    lds_st<u32>(gw + (8 + j) * 288, pk(f.x, f.y));
                }
            } else if (t == 1) {
                f2 f[6];
#pragma unroll
                for (int j = 0; j < 4; j++) f[j] = cmul(e[2 * j], z256);
                f[4] = cmul(e[0], z512); f[5] = cmul(e[4], z512);
#pragma unroll
                for (int j = 0; j < 4; j++) {
                    lds_st<u32>(gw + j * 288, pk(f[j].x, f[j].y));
                    const f2 h = cmul(f[j], z128);
                    lds_st<u32>(gw + (4 + j) * 288, pk(h.x, h.y));
                    lds_st<u32>(gw + (8 + 2 * j) * 288, pk(f[4].x, f[4].y));
                    lds_st<u32>(gw + (9 + 2 * j) * 288, pk(f[5].x, f[5].y));
                    if (j < 3) { f[4] = cmul(f[4], z128); f[5] = cmul(f[5], z128); }
                }
            } else {
                f2 f0 = cmul(C, z1024), f1 = cmul(f0, z128);
#pragma unroll
                for (int k = 0; k < 9; k += 2) {
                    lds_st<u32>(gw + k * 288, pk(f0.x, f0.y));
                    if (k < 8) { lds_st<u32>(gw + (k + 1) * 288, pk(f1.x, f1.y)); f0 = cmul(f0, z256); }
                    if (k < 6) f1 = cmul(f1, z256);
                }
            }
            f4 acc = Z;
#pragma unroll
            for (int s = 0; s < 4; s++) acc = mfma(lds_ld<u4>(gr + 64 * s), bfr[s], acc);
            if (t == 0) lds_st<f4>(q + (4 * (g & 1) + 128 * (g >> 1) + 8 * c16) * 4, acc);
            else if (t == 1) {
                if (g < 2) lds_st<f4>(q + (256 + 64 * g + 4 * c16) * 4, acc);
                else {
                    const int o = q + (384 + 64 * (g - 2) + 2 * c16) * 4;
                    lds_st<f2>(o, f2{acc[0], acc[1]}); lds_st<f2>(o + 128, f2{acc[2], acc[3]});
                }
            } else {
                if (g < 2) {
#pragma unroll
                    for (int i = 0; i < 4; i++) lds_st<float>(q + (512 + 64 * g + 16 * i + c16) * 4, acc[i]);
                } else if (lane == 32) lds_st<float>(q + 640 * 4, acc[0]);
            }
        }
    }
    sig(FQ + ch);
    STAMP(2);
    await(FQ + ch, 2);
    {
        const LAS float* qa = (const LAS float*)(u32)(LA_Q + ch * 5152);
        const LAS float* qb = qa + 644;
        const int kE = LA_K + ch * 16640, kO = kE + 4160;
        if (hf == 0) {
            const int j = 512 + 2 * lane;
            const float q0 = qa[j] + qb[j], q1 = qa[j + 1] + qb[j + 1], q2 = qa[j + 2] + qb[j + 2];
            const u32 a = pk(q2, q2), b = pk(q1, q1), c = pk(q0, q0);
            const int y = (504 - 8 * lane) * 4;
            put8(kE + y, u4{a, a, b, b}, u4{b, pk(q1, q0), c, c});
            put8(kO + y, u4{a, pk(q2, q1), b, b}, u4{b, c, c, c});
            if (lane < 36) {
                const int cp = lane / 9;
                if (cp >= 2 || lane != 9 * cp) lds_st<u32>(kE + cp * 4160 + (1023 + lane - 9 * cp) * 4, 0u);
            }
        } else {
            {
                const int j = 384 + 2 * lane;
                const float q0 = qa[j] + qb[j], q1 = qa[j + 1] + qb[j + 1], q2 = qa[j + 2] + qb[j + 2];
                const u32 b = pk(q1, q1), c = pk(q0, q0);
                const int y = (764 - 4 * lane) * 4;
                put4(kE + y, u4{pk(q2, q2), b, pk(q1, q0), c});
                put4(kO + y, u4{pk(q2, q1), b, c, c});
            }
            {
                const int j = 256 + 2 * lane;
                const float q0 = qa[j] + qb[j], q1 = qa[j + 1] + qb[j + 1], q2 = qa[j + 2] + qb[j + 2];
                const float qm = lane ? q0 : qa[255] + qb[255];
                const int y = (894 - 2 * lane) * 4;
                put2(kE + y, pk(q2, q1), pk(q0, q0));
                put2(kO + y, pk(q1, q0), pk(q0, qm));
            }
            {
                f4 k = *(const LAS f4*)(qa + 4 * lane) + *(const LAS f4*)(qb + 4 * lane);
                if (lane == 0) k[0] += bf2f(p_d);
                const float km = lane ? qa[4 * lane - 1] + qb[4 * lane - 1] : 0.f;
                const int y = (1022 - 2 * lane) * 4;
                put2(kE + y, pk(k[3], k[2]), pk(k[1], k[0]));
                put2(kO + y, pk(k[2], k[1]), pk(k[0], km));
            }
        }
    }
    sig(FK + ch);
    const int x0 = 2031 - c16 + 8 * g;
    int ka = LA_K + ch * 16640 + (x0 & 3) * 4160 + (x0 >> 2) * 8 - 256 * hf - 192 - 3584;
    int ubs = LA_U + ch * 4608 + 480 + (16 * c16 + 8 * g - 16) * 2 - 256 * hf - 192;
    pin(ka); pin(ubs);
    if (wv < 4) __builtin_amdgcn_s_setprio(0);
    else if (wv < 8) __builtin_amdgcn_s_setprio(1);
    else __builtin_amdgcn_s_setprio(2);
    await(FK + ch, 2);
    STAMP(13);
    await<1>(FU1, 4);
    STAMP(3);
    f4 acc[4];
#define KFR(rho, p) ld_k(ka + 192 - 64 * (rho) + 3584 - 512 * (p))
#define UFR(rho, q) lds_ld<u4>(ubs + 192 - 64 * (rho) + 512 * (q))
#define SB __builtin_amdgcn_sched_barrier(0)
#define PINACC do { pin(acc[0]); pin(acc[1]); pin(acc[2]); pin(acc[3]); } while (0)
#define TRIANGLE(q0)                                                                          \
    {                                                                                         \
        u4 A[2][4], B[2][4];                                                                  \
        _Pragma("unroll") for (int p = 0; p < 4; p++) { B[0][p] = UFR(0, q0 + p); A[0][p] = KFR(0, p); } \
        _Pragma("unroll") for (int rho = 0; rho < 4; rho++)                                   \
            _Pragma("unroll") for (int p = 0; p < 4; p++) {                                   \
                SB;                                                                           \
                if (rho < 3) { B[(rho + 1) & 1][p] = UFR(rho + 1, q0 + p); A[(rho + 1) & 1][p] = KFR(rho + 1, p); } \
                _Pragma("unroll") for (int q = 0; p + q < 4; q++) acc[p + q] = mfma(A[rho & 1][p], B[rho & 1][q], acc[p + q]); \
            }                                                                                 \
        SB;                                                                                   \
        PINACC;                                                                               \
    }
#define EXCHANGE(mine, other, str, fx, fy)                                                    \
    {                                                                                         \
        if (hf) { lds_st<f4>((mine) + lane * 16, acc[0]); lds_st<f4>((mine) + (str) + lane * 16, acc[1]); acc[0] = acc[2]; acc[1] = acc[3]; } \
        else { lds_st<f4>((mine) + lane * 16, acc[2]); lds_st<f4>((mine) + (str) + lane * 16, acc[3]); } \
        sig(fx + ch);                                                                         \
        await(fx + ch, 2);                                                                    \
        const f4 y0 = lds_ld<f4>((other) + lane * 16) + acc[0], y1 = lds_ld<f4>((other) + (str) + lane * 16) + acc[1]; \
        const int w = (other) + (16 * c16 + 4 * g) * 2;                                       \
        lds_st<u2>(w, u2{pk(y0[0], y0[1]), pk(y0[2], y0[3])});                                \
        lds_st<u2>(w + (str), u2{pk(y1[0], y1[1]), pk(y1[2], y1[3])});                        \
        sig(fy);                                                                              \
    }
#pragma unroll
    for (int i = 0; i < 4; i++) acc[i] = Z;
    TRIANGLE(0);
    STAMP(4);
    EXCHANGE(LA_Q + wv * 2576, LA_Q + (wv ^ 1) * 2576, 1024, FX1, FY1);
#pragma unroll
    for (int i = 0; i < 4; i++) acc[i] = Z;
    {
        u4 A[8], B[2][4];
#pragma unroll
        for (int p = 0; p < 4; p++) B[0][p] = UFR(0, p);
#pragma unroll
        for (int p = 1; p < 8; p++) A[p] = KFR(0, p);
#pragma unroll
        for (int rho = 0; rho < 4; rho++)
#pragma unroll
            for (int p = 1; p < 8; p++) {
                SB;
                if (rho < 3 && p < 5) B[(rho + 1) & 1][p - 1] = UFR(rho + 1, p - 1);
#pragma unroll
                for (int q = 0; q < 4; q++)
                    if (p + q >= 4 && p + q < 8) acc[p + q - 4] = mfma(A[p], B[rho & 1][q], acc[p + q - 4]);
                SB;
                if (rho < 3) A[p] = KFR(rho + 1, p);
            }
        PINACC;
    }
    sig(F2A + ch);
    STAMP(14);
    await<1>(FU2, 4);
    TRIANGLE(4);
    STAMP(5);
    await(F2A + ch, 2);
    EXCHANGE(LA_K + ch * 16640 + 2 * hf * 4160, LA_K + ch * 16640 + 2 * (hf ^ 1) * 4160, 4160, FX2, FY2);
    STAMP(6);
    STAMP_OUT(0);
}

__global__ void __launch_bounds__(NT) fused_kernel(const u16* __restrict__ u, const u16* __restrict__ lar, const u16* __restrict__ aimg,
                                                   const u16* __restrict__ cre, const u16* __restrict__ cim, const u16* __restrict__ ldt,
                                                   const u16* __restrict__ dsk, const u16* __restrict__ x, const u16* __restrict__ wr,
                                                   const u16* __restrict__ wi, u16* __restrict__ out, char* ws) {
    const char* entry;
    asm volatile("s_getpc_b64 %0" : "=s"(entry));
    const u32 ptag_ = 0x5A3C1234u;
    if (blockIdx.x == 150) { const GAS u32* e_ = (const GAS u32*)(ws + (6u << 20) + (1u << 19));
        const unsigned long long s0 = __builtin_amdgcn_s_memrealtime();
        if (threadIdx.x < 128) while (__hip_atomic_load(e_ + 64 + threadIdx.x * 32, __ATOMIC_RELAXED, __HIP_MEMORY_SCOPE_AGENT) != ptag_ && __builtin_amdgcn_s_memrealtime() - s0 < 10000ull) __builtin_amdgcn_s_sleep(2);
        __syncthreads();
        const unsigned long long t0 = __builtin_amdgcn_s_memrealtime(); while (__builtin_amdgcn_s_memrealtime() - t0 < 5000ull) __builtin_amdgcn_s_sleep(1);
        if (threadIdx.x < 256) __hip_atomic_store((GAS u32*)e_ + 64 + threadIdx.x * 32, 0u, __ATOMIC_RELAXED, __HIP_MEMORY_SCOPE_AGENT);
        return; }
    if (__builtin_expect(blockIdx.x < NB2, 1)) head_b(entry, x, wr, wi, out, ws, ptag_);
    else if (blockIdx.x >= A0) { const unsigned long long t0_ = __builtin_amdgcn_s_memrealtime(); while (__builtin_amdgcn_s_memrealtime() - t0_ < 2000ull) __builtin_amdgcn_s_sleep(8); head_a(u, lar, aimg, cre, cim, ldt, dsk, out, ws, ptag_); }
    else warm_code(entry);
}

extern "C" void kernel_launch(void* const* d_in, const int* in_sizes, int n_in, void* d_out, int out_size, void* d_ws,
                              size_t ws_size, hipStream_t stream) {
    (void)hipFuncSetAttribute((const void*)fused_kernel, hipFuncAttributeMaxDynamicSharedMemorySize, LDS_BYTES);
    fused_kernel<<<GRID, NT, LDS_BYTES, stream>>>((const u16*)d_in[0], (const u16*)d_in[1], (const u16*)d_in[2], (const u16*)d_in[3],
                                                  (const u16*)d_in[4], (const u16*)d_in[5], (const u16*)d_in[6], (const u16*)d_in[7],
                                                  (const u16*)d_in[8], (const u16*)d_in[9], (u16*)d_out, (char*)d_ws);
}
```

```cpp
#define POLL_SLEEP 2
#include <hip/hip_runtime.h>

typedef unsigned short u16;
typedef unsigned int u32;
typedef float f2 __attribute__((ext_vector_type(2)));
typedef float f4 __attribute__((ext_vector_type(4)));
typedef u32 u2 __attribute__((ext_vector_type(2)));
typedef u32 u4 __attribute__((ext_vector_type(4)));
typedef int i8v __attribute__((ext_vector_type(8)));
typedef short s4 __attribute__((ext_vector_type(4)));
typedef short s8 __attribute__((ext_vector_type(8)));
typedef __bf16 bf2 __attribute__((ext_vector_type(2)));
#define DI __device__ __forceinline__
#define LAS __attribute__((address_space(3)))
#define GAS __attribute__((address_space(1)))
#define CAS __attribute__((address_space(4)))
#pragma clang diagnostic ignored "-Wint-to-pointer-cast"
struct u3 { u32 v[3]; DI u32& operator[](int i) { return v[i]; } DI const u32& operator[](int i) const { return v[i]; } };

constexpr int SEQ = 2048, DM = 576, NS = 128, DF = 289;
constexpr int NB1 = 128;
constexpr int NB2 = 145;
constexpr int A0 = 152;
constexpr int NA = 96;
constexpr int GRID = A0 + NA;
constexpr int NT = 1024;

constexpr u32 WS_EPOCH = 0, WS_F1 = 4096, WS_F2 = WS_F1 + 16 * 16 * 128, WS_X1 = 1u << 20, WS_Y1 = 4u << 20, WS_USED = 8u << 20;
#define STAMP(i)
#define STAMP_DECL
#define STAMP_OUT(T)

constexpr int F_SA = 0, F_EO = 4;
constexpr int F_ARE = 9, F_AIM = 10, F_BRE = 11, F_BIM = 12, F_G = 13;
constexpr int F_F16 = 21, F_GI = 23, F_AI = 31, F_M1C = 33;
constexpr int F_VAR = 37;
constexpr int F_END = F_VAR + 40;
constexpr int ET = 452, ER = 28, E2V = 580, E2U = 36, OP = 1168;
constexpr int LB_X = 0, LB_I = 0, LB_Y = 0;
constexpr int LB_E = 16 * OP;
constexpr int LB_O = LB_E, LB_E2 = LB_E;
constexpr int LB_S = LB_E + 16 * ET * 4;
constexpr int LB_W = LB_S + 1200 * 16;
constexpr int LB_T = LB_W + 2 * 256 * 36;
constexpr int LB_G = LB_T + (F_VAR + 16) * 1024;
constexpr int LB_END = LB_G + 16;
struct CosTab { double c[2048]; double c9[9], s9[9]; };
constexpr double tay_cos(double x) { double t = 1, s = 1; for (int k = 1; k <= 14; k++) { t *= -x * x / ((2 * k - 1) * (2 * k)); s += t; } return s; }
constexpr double tay_sin(double x) { double t = x, s = x; for (int k = 1; k <= 14; k++) { t *= -x * x / ((2 * k + 1) * (2 * k)); s += t; } return s; }
constexpr CosTab make_cos() {
    CosTab t = {};
    const double PI2 = 6.283185307179586476925;
    for (int k = 0; k <= 512; k++) {
        const double v = k == 512 ? 0.0 : (k <= 256 ? tay_cos(PI2 * k / 2048) : tay_sin(PI2 * (512 - k) / 2048));
        t.c[k] = v; t.c[(2048 - k) & 2047] = v; t.c[1024 - k] = -v; t.c[(1024 + k) & 2047] = -v;
    }
    t.c[512] = 0.0; t.c[1536] = 0.0;
    for (int k = 0; k < 9; k++) { const double a = PI2 * (k <= 4 ? k : k - 9) / 9; t.c9[k] = tay_cos(a); t.s9[k] = tay_sin(a); }
    return t;
}
constexpr CosTab CT = make_cos();
constexpr double cs(int n) { return CT.c[n & 2047]; }
constexpr double sn(int n) { return CT.c[(n - 512) & 2047]; }
constexpr u32 bfr(double x) { const u32 b = __builtin_bit_cast(u32, (float)x); return (b + 0x7fffu + ((b >> 16) & 1u)) >> 16; }
constexpr u32 pkc(double lo, double hi) { return bfr(lo) | (bfr(hi) << 16); }
constexpr u32 f32c(double x) { return __builtin_bit_cast(u32, (float)x); }
template <int N> struct Frags { u32 w[N * 256]; };
constexpr u32 w_re(double amp, int n) { return pkc(amp * cs(n), amp * sn(n)); }
constexpr u32 w_im(double amp, int n) { return pkc(-amp * sn(n), amp * cs(n)); }
constexpr Frags<F_VAR> make_t0() {
    Frags<F_VAR> t = {};
    const double R2 = 1.4142135623730951;
    for (int l = 0; l < 64; l++)
        for (int i = 0; i < 4; i++) {
            const int c = l & 15, g = l >> 4, r = 4 * g + i, o = l * 4 + i;
            {
                const int b = c >> 2, tq = c & 3;
                const double amp = b ? 1.0 : R2;
                const int n0 = 512 * tq * g + (b ? 256 * b * (2 * i) : -256), n1 = 512 * tq * g + (b ? 256 * b * (2 * i + 1) : 256);
                t.w[F_ARE * 256 + o] = pkc(amp * cs(n0), amp * cs(n1));
                t.w[F_AIM * 256 + o] = pkc(-amp * sn(n0), -amp * sn(n1));
            }
            for (int w = 0; w < 9; w++) {
                const int sh = 16 * (i & 1), oo = w * 128 + l * 2 + (i >> 1);
                t.w[F_EO * 256 + oo] |= (u32)((c * ET + r * ER + 2 * ((7 * w + r) % 9)) * 4) << sh;
                if (w < 8) {
                    const int k = (9 * (w & 3) + 36 * r + 64 * (c % 9)) % DM, slot = w < 4 ? (k <= 288 ? 2 * k : 2 * (DM - k) + 1) : k;
                    t.w[F_SA * 256 + oo] |= (u32)(slot * 8 + (slot >> 6) * 16) << sh;
                }
            }
            if (r < 9) { const int k = (r * c) % 9; t.w[F_BRE * 256 + o] = pkc(CT.c9[k], CT.s9[k]); t.w[F_BIM * 256 + o] = pkc(-CT.s9[k], CT.c9[k]); }
            for (int q = 0; q < 4; q++) {
                t.w[(F_G + 2 * q) * 256 + o] = w_re(1.0, 32 * r * (q + 4 * c)); t.w[(F_G + 2 * q + 1) * 256 + o] = w_im(1.0, 32 * r * (q + 4 * c));
                t.w[(F_GI + 2 * q) * 256 + o] = w_re(1.0, 32 * c * (q + 4 * r)); t.w[(F_GI + 2 * q + 1) * 256 + o] = w_im(1.0, 32 * c * (q + 4 * r));
            }
            t.w[F_F16 * 256 + o] = w_re(1.0, 128 * r * c); t.w[(F_F16 + 1) * 256 + o] = w_im(1.0, 128 * r * c);
            for (int h = 0; h < 2; h++) {
                const int j = 4 * h + (c >> 2), n = 512 * i * (c & 3) + (g ? 256 * g * j : ((j & 1) ? 256 : -256));
                t.w[(F_AI + h) * 256 + o] = w_re(g ? 2.0 : R2, n);
                const int m = 16 * r * (4 * h + 8 * c) - 512 * h;
                t.w[(F_M1C + 2 * h) * 256 + o] = pkc(0.5 * cs(m), -0.5 * sn(m));
                t.w[(F_M1C + 2 * h + 1) * 256 + o] = pkc(-0.5 * sn(m), -0.5 * cs(m));
            }
        }
    return t;
}
constexpr Frags<40> make_t1() {
    Frags<40> t = {};
    for (int v = 0; v < 5; v++)
        for (int l = 0; l < 64; l++)
            for (int i = 0; i < 4; i++) {
                const int be = v < 3 ? v + 1 : 4 * (v - 3), c = l & 15, r = 4 * (l >> 4) + i, o = v * 2048 + l * 4 + i;
                const double amp = v < 3 ? 1.0 : 0.5;
                const int n1 = 16 * r * (be + 8 * c) + (v == 4 ? 512 : 0), n4 = 16 * c * (be + 8 * r);
                t.w[o] = w_re(amp, n1); t.w[o + 256] = w_im(amp, n1);
                t.w[o + 512] = w_re(1.0, n4); t.w[o + 768] = w_im(1.0, n4);
                t.w[o + 1024] = f32c(cs(r * (be + 8 * c))); t.w[o + 1280] = f32c(-sn(r * (be + 8 * c)));
                t.w[o + 1536] = f32c(cs(c * (be + 8 * r))); t.w[o + 1792] = f32c(-sn(c * (be + 8 * r)));
            }
    return t;
}
constexpr int qidx(int l) {
    if (l <= 256) return l;
    const int sh = l < 512 ? 1 : (l < 1024 ? 2 : 3), h = 1 << (sh - 1), m = l >> sh, r = l & ((1 << sh) - 1);
    return 128 * sh + m + ((r > h || (r == h && (m & 1))) ? 1 : 0);
}
constexpr Frags<8> make_t2() {
    Frags<8> t = {};
    for (int hf = 0; hf < 2; hf++)
        for (int l = 0; l < 64; l++) {
            const int sg[3] = {17 + 4 * hf, 33 + 4 * hf, 32 + 4 * hf}, back[3] = {8, 4, 0};
            for (int k = 0; k < 3; k++) {
                const int L = 32 * sg[k] + 16 + (l & 15) - 8 * (l >> 4), q0 = qidx(L - 7);
                for (int i = 0; i < 4; i++) {
                    const u32 a = 2 * (qidx(L - 2 * i) - q0), b = 2 * (qidx(L - 2 * i - 1) - q0);
                    t.w[(4 * hf + k) * 256 + l * 4 + i] = a | ((a + 1) << 8) | (b << 16) | ((b + 1) << 24);
                }
                t.w[(4 * hf + 3) * 256 + l * 4 + k] = (u32)((q0 - back[k] - 384) * 8);
            }
            const int e = 543 - 2 * (l & 15) - (l >> 4);
            t.w[(4 * hf + 3) * 256 + l * 4 + 3] = (u32)(qidx(e) * 4) | ((u32)(qidx(e - 1) * 4) << 16);
        }
    return t;
}
struct Tables { Frags<F_VAR> a; Frags<40> b; Frags<8> c; };
constexpr Frags<8> T2 = make_t2();
constexpr Frags<F_VAR> T0 = make_t0();
constexpr Frags<40> T1 = make_t1();
static __device__ const __attribute__((aligned(128))) Tables TAB = {T0, T1, T2};
constexpr int LA_G = 0;
constexpr int LA_K = LA_G + 12 * 4608;
constexpr int KI = 1344, KC = 4 * KI + 2048;
constexpr int LA_U = LA_K + 6 * KC;
constexpr int LA_Q = LA_U + 6 * 4608;
constexpr int LA_F = LA_Q + 12 * 2576;
constexpr int LA_D = LA_F + 16;
constexpr int LA_END = LA_D + 256;
constexpr int FU0 = 0, FU2 = 1;
constexpr int LDS_BYTES = (LA_END > LB_END ? LA_END : LB_END);
static_assert(LDS_BYTES <= 160 * 1024 && LDS_BYTES > 80 * 1024, "one workgroup per CU");

DI u32 pk(float a, float b) { f2 v = {a, b}; return __builtin_bit_cast(u32, __builtin_convertvector(v, bf2)); }
DI float blo(u32 v) { return __uint_as_float(v << 16); }
DI float bhi(u32 v) { return __uint_as_float(v & 0xffff0000u); }
DI float bf2f(u16 v) { return __uint_as_float((u32)v << 16); }
DI float rbf(float x) { return blo(pk(x, x)); }
DI f2 cmul(f2 a, f2 b) { f2 t = a.xx * b; f2 s = {-b.y, b.x}; return __builtin_elementwise_fma(a.yy, s, t); }
DI f4 mfma(u4 a, u4 b, f4 c) { return __builtin_amdgcn_mfma_f32_16x16x32_bf16(__builtin_bit_cast(s8, a), __builtin_bit_cast(s8, b), c, 0, 0, 0); }
DI u4 pack4(f4 re, f4 im) { u4 r = {pk(re[0], im[0]), pk(re[1], im[1]), pk(re[2], im[2]), pk(re[3], im[3])}; return r; }
DI u4 twid(f4 re, f4 im, f4 wr, f4 wi) { return pack4(__builtin_elementwise_fma(re, wr, -(im * wi)), __builtin_elementwise_fma(re, wi, im * wr)); }
DI int mod9(int x) { return x - 9 * ((x * 57) >> 9); }
template <class T> DI void pin(T& v) { asm volatile("" : "+v"(v)); }

template <class T> DI T lds_ld(int off) { return *(const LAS T*)(u32)off; }
template <class T> DI void lds_st(int off, T v) { *(LAS T*)(u32)off = v; }
DI void bar() { __syncthreads(); }
#define PRIO(n) __builtin_amdgcn_s_setprio(n)
#define PRIOB(n)
DI void glds16(const void* g, u32 off, int lds) {
    u32 keep;
    asm volatile("s_mov_b32 %0, m0\n\ts_mov_b32 m0, %3\n\ts_nop 4\n\tglobal_load_lds_dwordx4 %1, %2\n\ts_mov_b32 m0, %0" : "=&s"(keep) : "v"(off), "s"(g), "s"(lds) : "memory");
}

DI void publish_tag(u32 epoch) {
    u32 t;
    asm volatile("v_add_u32 %0, 0x9E3779B1, %1\n\tds_write_b32 %2, %0\n\ts_waitcnt lgkmcnt(0)" : "=&v"(t) : "v"(epoch), "v"(LB_G) : "memory");
}
DI void wait_flag(__amdgpu_buffer_rsrc_t r, u32 off, u32 tag, bool on) {
    if (on) {
        const unsigned long long t0 = __builtin_amdgcn_s_memrealtime();
        while (!__all(__builtin_amdgcn_raw_buffer_load_b32(r, off, 0, 16) == tag)) {
            if (__builtin_amdgcn_s_memrealtime() - t0 > 5000000ull) break;
            __builtin_amdgcn_s_sleep(POLL_SLEEP);
            asm volatile("" ::: "memory");
        }
    }
    asm volatile("" ::: "memory");
}

constexpr int CODE_LINES = 128;
DI void warm_code(const char* entry) {
    if (threadIdx.x < CODE_LINES) { const u32 t = *(const GAS u32*)(entry + threadIdx.x * 128); asm volatile("" :: "v"(t)); }
}

DI void head_b(const char* entry, const u16* __restrict__ x, const u16* __restrict__ wr, const u16* __restrict__ wi,
               u16* __restrict__ out, char* ws) {
    const int tid = threadIdx.x, lane = tid & 63, c16 = lane & 15, g = lane >> 4;
    const int wv = __builtin_amdgcn_readfirstlane(tid >> 6);
    const int id = blockIdx.x;
    STAMP_DECL;
    const bool rows = id < NB1;
    const int s00 = 32 * (id >> 4) + (id & 15);
    const char* tab = (const char*)&TAB;
    const int R = (tid * 911) >> 16;
    const u32 xoff = (s00 + 16 * (R >> 3) + 256 * (R & 7)) * (2 * DM) + (tid - R * 72) * 16;
    asm volatile("" :: "s"(x), "s"(wr), "s"(wi), "s"(out), "s"(ws), "v"(xoff));
    const __amdgpu_buffer_rsrc_t rws = __builtin_amdgcn_make_buffer_rsrc(ws, 0, WS_USED, 0x00020000);
    u32 epoch = 0;
    if (wv == 0) asm volatile("global_load_dword %0, %0, %1 sc1" : "+v"(epoch) : "s"(ws) : "memory");
    if (rows) {
        glds16(x, xoff, LB_X + wv * 1024);
        if (wv < 2) glds16(x, (s00 + 16 + 1536) * (2 * DM) + (tid < 56 ? (tid + 16) * 16 : 256 * 2 * DM + (tid - 56) * 16), LB_X + 16384 + wv * 1024);
        else glds16(tab, tid * 16 - 2048, LB_T + (wv - 2) * 1024);
        if (wv >= 9) glds16(tab, tid * 16 + 5 * 1024, LB_T + (wv + 5) * 1024);
    }
    const int u2i = (id & 7) ? (id & 7) * 18 + 1 + (id >> 3) : (id == 136 ? 0 : (id >> 3) + (id < 136 ? 1 : 0));
    const bool star = u2i >= 108;
    const int q36 = min((u2i * 57) >> 11, 3), kg = u2i - 36 * q36, bq = (q36 + 1) & 3;
    const bool mir = !star && kg >= 18;
    const int w0 = star ? min(16 * kg, 562) : (mir ? (2048 - bq) * (2 * DF) + 1122 - 32 * kg : bq * (2 * DF) + 32 * kg);
    __builtin_amdgcn_s_barrier();
    const u32 woff = w0 + ((tid >> 1) & 255) * (mir ? -16 * DF : 16 * DF) + (tid & 1) * (star ? 8 * DF : 16);
    u4 wra, wrb;
    if (wv >= 8) {
        __builtin_memcpy(&wra, (const char*)wr + woff, 16);
        __builtin_memcpy(&wrb, (const char*)wi + woff, 16);
    }
    __builtin_amdgcn_sched_barrier(0);
    const f4 Z = {0.f, 0.f, 0.f, 0.f};
    const int tau = wv & 3, bw = wv >> 2;
    int tl = LB_T + lane * 16;
    int lin = LB_S + (tid + (tid >> 5)) * 16;
    pin(tl); pin(lin);
    int sadr[4];

    if (rows) {
        if (tid < 256) {
            const int o = LB_E + 72 + (tid & 15) * (ER * 4) + (tid >> 4) * (ET * 4);
            lds_st<u2>(o, u2{0u, 0u}); lds_st<u4>(o + 8, u4{0u, 0u, 0u, 0u});
        }
        int xo = LB_X + (c16 >> 2) * 1152 + 2 * (144 * ((g - wv) & 3) + 16 * wv + 4 * (c16 & 3));
        int bo = LB_E + (wv * ET + c16 * ER + min(8 * g, 16)) * 4;
        int go = WS_X1 + (tid >> 3) * 16384 + id * 128 + (tid & 7) * 16;
        int ta = LB_T + lane * 8;
        pin(xo); pin(bo); pin(go); pin(ta);
        STAMP(1);
        asm volatile("s_cmp_lt_u32 %0, 8\n\ts_cbranch_scc0 .Lw%=\n\ts_waitcnt vmcnt(0)\n.Lw%=:\n\ts_waitcnt vmcnt(2)" :: "s"(wv) : "memory", "scc");
        if (wv == 0) publish_tag(epoch);
        bar();
        STAMP(2);
        if (wv < 9) {
            u4 d[2];
#pragma unroll
            for (int s = 0; s < 2; s++) {
                const s4 lo = __builtin_amdgcn_ds_read_tr16_b64_v4i16((LAS s4*)(u32)(xo + s * 9216));
                const s4 hi = __builtin_amdgcn_ds_read_tr16_b64_v4i16((LAS s4*)(u32)(xo + s * 9216 + 4608));
                d[s] = __builtin_bit_cast(u4, __builtin_shufflevector(lo, hi, 0, 1, 2, 3, 4, 5, 6, 7));
            }
            const u4 are = lds_ld<u4>(tl + F_ARE * 1024), aim = lds_ld<u4>(tl + F_AIM * 1024);
            const u2 eo = lds_ld<u2>(ta + F_EO * 1024 + wv * 512);
#pragma unroll
            for (int s = 0; s < 2; s++) d[s] = pack4(mfma(d[s], are, Z), mfma(d[s], aim, Z));
#pragma unroll
            for (int i = 0; i < 4; i++) lds_st<u2>(LB_E + ((i & 1) ? eo[i >> 1] >> 16 : eo[i >> 1] & 0xffff), u2{d[0][i], d[1][i]});
        }
        u4 bre = lds_ld<u4>(tl + F_BRE * 1024), bim = lds_ld<u4>(tl + F_BIM * 1024);
        u4 gre = lds_ld<u4>(tl + (F_G + 2 * tau) * 1024), gim = lds_ld<u4>(tl + (F_G + 2 * tau + 1) * 1024);
        {
            const u2 sa = lds_ld<u2>(ta + F_SA * 1024 + (tau + (bw ? 4 : 0)) * 512);
            const int base = LB_S + ((bw + 3) & 3) * (DM * 8 + 144);
#pragma unroll
            for (int i = 0; i < 4; i++) { sadr[i] = base + ((i & 1) ? sa[i >> 1] >> 16 : sa[i >> 1] & 0xffff); pin(sadr[i]); }
        }
        pin(bre); pin(bim); pin(gre); pin(gim);
        bar();
        STAMP(13);
        {
            PRIOB(2);
            const u4 p0 = lds_ld<u4>(bo), p1 = lds_ld<u4>(bo + 16);
            u4 d[2];
            f4 re[2], im[2];
#pragma unroll
            for (int s = 0; s < 2; s++) { const u4 a = {p0[s], p0[2 + s], p1[s], p1[2 + s]}; re[s] = mfma(a, bre, Z); im[s] = mfma(a, bim, Z); }
            PRIOB(1);
#pragma unroll
            for (int s = 0; s < 2; s++) { const u4 t = pack4(re[s], im[s]); re[s] = mfma(gre, t, Z); im[s] = mfma(gim, t, Z); }
            PRIOB(0);
#pragma unroll
            for (int s = 0; s < 2; s++) d[s] = pack4(re[s], im[s]);
            if (c16 < 9) {
#pragma unroll
                for (int i = 0; i < 4; i++) lds_st<u2>(sadr[i], u2{d[0][i], d[1][i]});
                if (wv == 0 && c16 == 0 && !(g & 1)) lds_st<u2>(sadr[0] + 8, u2{d[0][0], d[1][0]});
            }
        }
        STAMP(14);
        bar();
        STAMP(3);
        __builtin_amdgcn_raw_buffer_store_b128(lds_ld<u4>(lin), rws, go, 0, 16);
        if (tid <= 128) __builtin_amdgcn_raw_buffer_store_b128(lds_ld<u4>(lin + 1056 * 16), rws, go, 128 * 16384, 16);
    } else {
        if (wv < 8 && (kg == 0 || kg == (star ? 36 : 18))) {
            const int m = tid & 255;
            lds_st<u16>(LB_W + tid * 36 + 32, ((wv >> 2) ? wi : wr)[((mir ? bq + 8 * m : 2048 - bq - 8 * m) & 2047) * DF + (kg ? 288 : 0)]);
        }
        warm_code(entry);
        asm volatile("s_waitcnt vmcnt(0)" ::: "memory");
        if (wv == 0) publish_tag(epoch);
        bar();
    }
    const u32 tag = lds_ld<u32>(LB_G);
    if (wv >= 8) {
        const int o = LB_W + ((tid >> 1) & 255) * 36 + (tid & 1) * 16;
#pragma unroll
        for (int i = 0; i < 4; i++) { lds_st<u32>(o + 4 * i, wra[i]); lds_st<u32>(o + 9216 + 4 * i, wrb[i]); }
    }
    asm volatile("s_waitcnt vmcnt(0)" ::: "memory");
    if (rows && lane == 0) __builtin_amdgcn_raw_buffer_store_b32(tag, rws, WS_F1 + (wv * 16 + (id >> 3)) * 128 + (id & 7) * 4, 0, 16);
    STAMP(4);
    glds16(tab, F_F16 * 1024 + tid * 16, LB_T + F_F16 * 1024 + wv * 1024);
    if (star || wv < 8) glds16(tab, (F_VAR + 8 * q36) * 1024 + tid * 16, LB_T + F_VAR * 1024 + wv * 1024);
    asm volatile("s_waitcnt vmcnt(0)" ::: "memory");
    bar();
    STAMP(12);

    const int typ = star ? (wv & 1) : 0;
    const int tv = tl + (F_VAR + 8 * typ) * 1024;
    u4 fre = lds_ld<u4>(tl + F_F16 * 1024), fim = lds_ld<u4>(tl + (F_F16 + 1) * 1024);
    u4 m1r = lds_ld<u4>(tv), m1i = lds_ld<u4>(tv + 1024), m4r = lds_ld<u4>(tv + 2048), m4i = lds_ld<u4>(tv + 3072);
    f4 twr = lds_ld<f4>(tv + 4096), twi = lds_ld<f4>(tv + 5120), tur = lds_ld<f4>(tv + 6144), tui = lds_ld<f4>(tv + 7168);
    u4 mcr = lds_ld<u4>(tl + (F_M1C + 2 * typ) * 1024), mci = lds_ld<u4>(tl + (F_M1C + 2 * typ + 1) * 1024);
    f4 wfr, wfi;
    {
        const int kcol = star ? 8 * kg + (wv >> 1) : 16 * kg + wv;
        const int cc = star ? 8 * typ + (u2i < NB2 - 1 ? (wv >> 1) : 7) : (mir ? 15 - wv : wv);
        const int wo = LB_W + (c16 + 64 * g) * 36 + cc * 2;
#pragma unroll
        for (int i = 0; i < 4; i++) {
            wfr[i] = bf2f(lds_ld<u16>(wo + i * 576));
            wfi[i] = bf2f(lds_ld<u16>(wo + i * 576 + 9216));
        }
        float sc = 1.f / (float)(SEQ * DM);
        if (kcol == 0 || kcol == 288) {
            sc *= 0.5f;
#pragma unroll
            for (int i = 0; i < 4; i++) {
                const int m = c16 + 64 * g + 16 * i;
                const int o = LB_W + (typ ? (255 - m) * 36 + cc * 2 : m * 36 + 32);
                wfr[i] += bf2f(lds_ld<u16>(o));
                wfi[i] -= bf2f(lds_ld<u16>(o + 9216));
            }
        }
        wfr *= sc; wfi *= (mir ? -sc : sc);
    }
    f4 wnr = -wfr, wni = -wfi;
    int io = (32 * g + c16) * 128 + (((wv >> 1) ^ (c16 >> 1)) * 16);
    int so = (tid >> 3) * 128 + (((tid & 7) ^ ((tid >> 4) & 7)) * 16);
    pin(fre); pin(fim); pin(m1r); pin(m1i); pin(m4r); pin(m4i); pin(twr); pin(twi); pin(tur); pin(tui); pin(mcr); pin(mci);
    int gi = WS_X1 + u2i * 16384 + tid * 16, gq = WS_Y1 + (tid >> 3) * (NB2 * 128) + u2i * 128 + (tid & 7) * 16;
    int i1 = LB_I + io + (star ? 0 : (wv & 1) * 8);
    io += LB_O + (wv & 1) * 8;
    pin(wfr); pin(wnr); pin(wni); pin(io); pin(so); pin(gi); pin(gq); pin(i1);
    wait_flag(rws, WS_F1 + (((u2i >> 3) & 15) * 16 + wv) * 128 + lane * 4, tag, lane < 8);
    STAMP(5);
    lds_st<u4>(LB_I + so, __builtin_amdgcn_raw_buffer_load_b128(rws, gi, 0, 16));
    bar();
    STAMP(6);
    {
        const int o1 = i1;
        PRIOB(3);
        const u2 a0 = lds_ld<u2>(o1), a1 = lds_ld<u2>(o1 + 2048);
        const u4 a = {a0[0], a0[1], a1[0], a1[1]};
        f4 re = mfma(a, m1r, Z), im = mfma(a, m1i, Z);
        if (star) {
            const u2 b0 = lds_ld<u2>(o1 + 8), b1 = lds_ld<u2>(o1 + 8 + 2048);
            const u4 b = {b0[0], b0[1], b1[0], b1[1]};
            re = mfma(b, mcr, re); im = mfma(b, mci, im);
        }
        u4 t = twid(re, im, twr, twi);
        re = mfma(fre, t, Z); im = mfma(fim, t, Z);
        PRIOB(2);
        t = pack4(__builtin_elementwise_fma(re, wfr, im * wni), __builtin_elementwise_fma(re, wni, im * wnr));
        re = mfma(t, fre, Z); im = mfma(t, fim, Z);
        PRIOB(1);
        t = twid(re, im, tur, tui);
        re = mfma(m4r, t, Z); im = mfma(m4i, t, Z);
        PRIOB(0);
        t = pack4(re, im);
        lds_st<u2>(io, u2{t[0], t[1]}); lds_st<u2>(io + 2048, u2{t[2], t[3]});
    }
    bar();
    STAMP(7);
    {
        u4 v = lds_ld<u4>(LB_O + so);
        if (star) {
            u4 w;
#pragma unroll
            for (int s = 0; s < 2; s++) {
                const float ar = blo(v[s]), ai = bhi(v[s]), cr = blo(v[2 + s]), ci = bhi(v[2 + s]);
                w[s] = pk(ar + ci, ai - cr); w[2 + s] = pk(ar - ci, -ai - cr);
            }
            v = w;
        }
        if (u2i < NB2 - 1 || (tid & 7) == 0)
            __builtin_amdgcn_raw_buffer_store_b128(v, rws, gq, 0, 16);
    }
    asm volatile("s_waitcnt vmcnt(0)" ::: "memory");
    if (lane == 0) __builtin_amdgcn_raw_buffer_store_b32(tag, rws, WS_F2 + (wv * 19 + (u2i >> 3)) * 128 + (u2i & 7) * 4, 0, 16);
    STAMP(8);
    if (!rows) { STAMP_OUT(0); return; }

    u4 gre = lds_ld<u4>(tl + (F_GI + 2 * tau) * 1024), gim = lds_ld<u4>(tl + (F_GI + 2 * tau + 1) * 1024);
    u4 bre = lds_ld<u4>(tl + F_BRE * 1024), bim = lds_ld<u4>(tl + F_BIM * 1024);
    u4 ai0 = lds_ld<u4>(tl + F_AI * 1024), ai1 = lds_ld<u4>(tl + (F_AI + 1) * 1024);
    int eadr[4];
#pragma unroll
    for (int i = 0; i < 4; i++) {
        const int r = 4 * g + i;
        eadr[i] = LB_E2 + (mod9(4 * (c16 + 18 - r)) * E2V + r * E2U + 2 * wv) * 4;
        pin(eadr[i]);
    }
    int ao = LB_E2 + (wv * E2V + c16 * E2U + 8 * g) * 4;
    int gy = WS_Y1 + id * (NB2 * 128) + tid * 16;
    int yo = LB_Y + (c16 >> 2) * OP + 2 * (144 * (((c16 & 3) - wv) & 3) + 16 * wv + 4 * g);
    int yl = LB_Y + tid * 16 + R * (OP - 1152);
    u32 oo = 2 * xoff - (tid - R * 72) * 16 + 2 * DM;
    pin(gre); pin(gim); pin(bre); pin(bim); pin(ai0); pin(ai1); pin(ao); pin(gy); pin(yo); pin(yl); pin(oo);
    {
        const int un = 8 * wv + lane + (lane >= 8 ? 120 : 0);
        wait_flag(rws, WS_F2 + ((id >> 3) * 19 + (un >> 3)) * 128 + (lane & 7) * 4, tag, lane < 16 && un < NB2);
    }
    STAMP(9);
    {
        const u4 v = __builtin_amdgcn_raw_buffer_load_b128(rws, gy, 0, 16);
        if (tid <= 128) lds_st<u4>(lin + 1056 * 16, __builtin_amdgcn_raw_buffer_load_b128(rws, gy, 16384, 16));
        lds_st<u4>(lin, v);
    }
    bar();
    if (id == 0 && tid == 0) __builtin_amdgcn_raw_buffer_store_b32(tag, rws, WS_EPOCH, 0, 16);
    STAMP(10);
    {
        PRIOB(2);
        u4 d[2];
        u2 p[4];
#pragma unroll
        for (int i = 0; i < 4; i++) p[i] = lds_ld<u2>(sadr[i]);
        f4 re[2], im[2];
#pragma unroll
        for (int s = 0; s < 2; s++) { const u4 a = {p[0][s], p[1][s], p[2][s], p[3][s]}; re[s] = mfma(a, gre, Z); im[s] = mfma(a, gim, Z); }
        PRIOB(1);
#pragma unroll
        for (int s = 0; s < 2; s++) { const u4 t = pack4(re[s], im[s]); re[s] = mfma(t, bre, Z); im[s] = mfma(t, bim, Z); }
        PRIOB(0);
#pragma unroll
        for (int s = 0; s < 2; s++) d[s] = pack4(re[s], im[s]);
        if (c16 < 9) {
#pragma unroll
            for (int i = 0; i < 4; i++) lds_st<u2>(eadr[i], u2{d[0][i], d[1][i]});
        }
    }
    STAMP(15);
    bar();
    if (wv < 9) {
        const u4 p0 = lds_ld<u4>(ao), p1 = lds_ld<u4>(ao + 16);
#pragma unroll
        for (int s = 0; s < 2; s++) {
            const u4 a = {p0[s], p0[2 + s], p1[s], p1[2 + s]};
            const f4 y0 = mfma(a, ai0, Z), y1 = mfma(a, ai1, Z);
            lds_st<u2>(yo + s * (8 * OP), u2{pk(y0[0], y0[1]), pk(y0[2], y0[3])});
            lds_st<u2>(yo + s * (8 * OP) + 4 * OP, u2{pk(y1[0], y1[1]), pk(y1[2], y1[3])});
        }
    }
    bar();
    *(GAS u4*)((char*)out + oo) = lds_ld<u4>(yl);
    if (wv < 2) {
        const int r2 = tid < 56 ? 14 : 15, c2 = tid + 1024 - 72 * r2;
        *(GAS u4*)((char*)out + (u32)((s00 + 16 + 256 * (r2 - 8)) * (4 * DM) + 2 * DM + c2 * 16)) = lds_ld<u4>(LB_Y + r2 * OP + c2 * 16);
    }
    STAMP(11);
    STAMP_OUT(0);
}

DI f2 cexpa(float ar2, float air, float p) {
    const float e = __builtin_amdgcn_exp2f(ar2 * p), t = __builtin_amdgcn_fractf(air * p);
    return f2{e * __builtin_amdgcn_cosf(t), e * __builtin_amdgcn_sinf(t)};
}
DI u4 ld_k(int o) {
    const u2 a = *(const volatile LAS u2*)(u32)o, b = *(const volatile LAS u2*)(u32)(o + 8);
    return u4{a[0], a[1], b[0], b[1]};
}
DI void a_rows_out(int yb, int ystr, char* o) {
    u2 y[6];
#pragma unroll
    for (int c = 0; c < 6; c++) y[c] = lds_ld<u2>(yb + c * ystr);
#pragma unroll
    for (int r = 0; r < 4; r++) {
        u3 v;
#pragma unroll
        for (int c = 0; c < 3; c++) v[c] = __builtin_amdgcn_perm(y[2 * c + 1][r >> 1], y[2 * c][r >> 1], (r & 1) ? 0x07060302u : 0x05040100u);
        *(u3*)(o + r * (4 * DM)) = v;
    }
}
template <int R> DI void stage(u4 v) {
    asm volatile("ds_write_addtid_b32 %0 offset:%4\n\tds_write_addtid_b32 %1 offset:%5\n\tds_write_addtid_b32 %2 offset:%6\n\tds_write_addtid_b32 %3 offset:%7"
                 :: "v"(v[0]), "v"(v[1]), "v"(v[2]), "v"(v[3]), "i"(288 * R), "i"(288 * R + 288), "i"(288 * R + 576), "i"(288 * R + 864) : "memory");
}
#define CM_MUL(d, a, b) "v_pk_mul_f32 " d ", " a ", " b " op_sel_hi:[0,1]\n\t"
#define CM_FMA(d, a, b) "v_pk_fma_f32 " d ", " a ", " b ", " d " op_sel:[1,1,0] op_sel_hi:[1,0,1] neg_lo:[1,0,0]\n\t"
struct C4 { f2 a, b, c, d; };
DI C4 cmul4(f2 a0, f2 b0, f2 a1, f2 b1, f2 a2, f2 b2, f2 a3, f2 b3) {
    C4 r;
    asm("s_nop 0\n\t" CM_MUL("%0", "%4", "%5") CM_MUL("%1", "%6", "%7") CM_MUL("%2", "%8", "%9") CM_MUL("%3", "%10", "%11")
        CM_FMA("%0", "%4", "%5") CM_FMA("%1", "%6", "%7") CM_FMA("%2", "%8", "%9") CM_FMA("%3", "%10", "%11") "s_nop 0"
        : "=&v"(r.a), "=&v"(r.b), "=&v"(r.c), "=&v"(r.d) : "v"(a0), "v"(b0), "v"(a1), "v"(b1), "v"(a2), "v"(b2), "v"(a3), "v"(b3));
    return r;
}
DI C4 cmul4(const C4& a, f2 b) { return cmul4(a.a, b, a.b, b, a.c, b, a.d, b); }
DI u4 pk4(const C4& v) { return u4{pk(v.a.x, v.a.y), pk(v.b.x, v.b.y), pk(v.c.x, v.c.y), pk(v.d.x, v.d.y)}; }

DI void sig(int f) {
    asm volatile("s_waitcnt lgkmcnt(0)" ::: "memory");
    if ((threadIdx.x & 63) == 0) __hip_atomic_fetch_add((LAS u32*)(u32)(LA_F + 4 * f), 1u, __ATOMIC_RELAXED, __HIP_MEMORY_SCOPE_WORKGROUP);
}
template <int SLEEP = 0> DI void await(int f, u32 n) {
    while ((u32)__builtin_amdgcn_readfirstlane(*(const volatile LAS u32*)(u32)(LA_F + 4 * f)) < n) if (SLEEP) __builtin_amdgcn_s_sleep(SLEEP);
    asm volatile("" ::: "memory");
}
DI float exp_bf(float x) {
    const float t = x * 1.44269504f, r = __builtin_fmaf(x, 1.92596299e-8f, __builtin_fmaf(x, 1.44269504f, -t));
    const float e = __builtin_amdgcn_exp2f(t);
    return __builtin_fmaf(e * 0.693147181f, r, e);
}

struct APar { u32 l, a, r, i, t, d; u4 sel2, sel3, selx, tadr; };
DI void head_a(const u16* __restrict__ u, APar pr, u16* __restrict__ out, char* ws) {
    const int tid = threadIdx.x, lane = tid & 63, c16 = lane & 15, g = lane >> 4;
    const int wv = __builtin_amdgcn_readfirstlane(tid >> 6);
    STAMP_DECL;
    const int ai_ = blockIdx.x - A0;
    const int d0 = 6 * ((ai_ & 7) * 12 + (ai_ >> 3));
    const f4 Z = {0.f, 0.f, 0.f, 0.f};

    if (wv >= 12) {
        __builtin_amdgcn_s_setprio(3);
        const int t = tid - 768;
        if (t == 0) lds_st<u2>(LA_F, u2{0u, 0u});
        if (t < 180) lds_st<u4>(LA_U + (t / 30) * 4608 + (t % 30) * 16, u4{0u, 0u, 0u, 0u});
        bar();
#pragma unroll
        for (int i = 0; i < 2; i++)
            if (i == 0 || wv < 14) {
                const int r = min((ai_ >> 3) + 12 * (128 * i + (t >> 1)), SEQ - 1);
                u32 keep;
                asm volatile("s_mov_b32 %0, m0\n\ts_mov_b32 m0, %3\n\ts_nop 4\n\tglobal_load_lds_dword %1, %2\n\ts_mov_b32 m0, %0"
                             : "=&s"(keep) : "v"(r * (2 * DM) + (((ai_ & 7) * 144) & ~127) + (t & 1) * 128), "s"(u), "s"(LA_D) : "memory");
            }
        u3 ua[4], ub[4];
#pragma unroll
        for (int r = 0; r < 4; r++) ua[r] = *(const u3*)(u + (4 * t + r) * DM + d0);
        sig(FU0);
        await<1>(FU0, 4);
#pragma unroll
        for (int r = 0; r < 4; r++) ub[r] = *(const u3*)(u + (1024 + 4 * t + r) * DM + d0);
        asm volatile("" ::: "memory");
#pragma unroll
        for (int c = 0; c < 6; c++) {
            const u32 sel = (c & 1) ? 0x07060302u : 0x05040100u;
            lds_st<u2>(LA_U + c * 4608 + 480 + t * 8, u2{__builtin_amdgcn_perm(ua[1][c >> 1], ua[0][c >> 1], sel), __builtin_amdgcn_perm(ua[3][c >> 1], ua[2][c >> 1], sel)});
        }
        STAMP(7);
        bar();
        bar();
#pragma unroll
        for (int c = 0; c < 6; c++) {
            const u32 sel = (c & 1) ? 0x07060302u : 0x05040100u;
            lds_st<u2>(LA_U + c * 4608 + 480 + 2048 + t * 8, u2{__builtin_amdgcn_perm(ub[1][c >> 1], ub[0][c >> 1], sel), __builtin_amdgcn_perm(ub[3][c >> 1], ub[2][c >> 1], sel)});
        }
        sig(FU2);
        STAMP(8);
        const int tile = wv - 12, yl = ((lane >> 2) + 16 * (lane & 3)) * 8;
        char* o = (char*)(out + 4 * t * (2 * DM) + d0);
        bar();
        bar();
        STAMP(11);
        a_rows_out(LA_G + ((tile >> 1) ^ 1) * 4608 + (tile & 1) * 1024 + yl, 9216, o);
        STAMP(9);
        bar();
        bar();
        STAMP(12);
        a_rows_out(LA_G + ((tile >> 1) ^ 1) * 4608 + (tile & 1) * 1024 + 2048 + yl, 9216, o + 1024 * (4 * DM));
        STAMP(10);
        STAMP_OUT(768);
        return;
    }
    const int ch = wv >> 1, hf = wv & 1;
    {
        __builtin_amdgcn_s_barrier();
        asm volatile("s_waitcnt vmcnt(4)" : "+v"(pr.l), "+v"(pr.a), "+v"(pr.r), "+v"(pr.i), "+v"(pr.t), "+v"(pr.d) :: "memory");
        PRIO(3);
        const float dt = rbf(exp_bf(blo(pr.t)));
        const float ar2 = -rbf(exp_bf(blo(pr.l))) * dt * 1.44269504f, air = blo(pr.a) * dt * 0.159154943f;
        const f2 C = {2.f * dt * blo(pr.r), 2.f * dt * blo(pr.i)};
        STAMP(1);
        const f2 z1 = cexpa(ar2, air, 1.f), z8c = cexpa(ar2, -air, 8.f), z128 = cexpa(ar2, air, 128.f);
        const int gr = LA_G + wv * 4608 + c16 * 288 + g * 16, q = LA_Q + wv * 2576;
        asm volatile("s_mov_b32 m0, %0\n\ts_nop 0" :: "s"(LA_G + wv * 4608) : "memory");
#define SB __builtin_amdgcn_sched_barrier(0)
#define STAGE16(v) do { stage<0>(v[0]); stage<4>(v[1]); stage<8>(v[2]); stage<12>(v[3]); } while (0)
#define READ4(f) do { _Pragma("unroll") for (int s = 0; s < 4; s++) f[s] = lds_ld<u4>(gr + 64 * s); SB; } while (0)
        u4 v[4], bfr[4], afr[4];
        const C4 s1 = cmul4(z8c, z8c, z1, z1, z128, z128, C, z1);
        const C4 s2 = cmul4(s1.a, s1.a, z8c, s1.a, s1.c, s1.c, C, s1.b);
        {
            C4 w = {f2{1.f, 0.f}, z8c, s1.a, s2.b};
            v[0] = pk4(w);
#pragma unroll
            for (int i = 1; i < 4; i++) { w = cmul4(w, s2.a); v[i] = pk4(w); }
        }
        STAGE16(v); READ4(bfr);
        PRIO(2);
        const C4 s3 = cmul4(s1.d, s1.b, s1.b, s1.b, s2.c, s2.c, s2.c, z128);
        const C4 e0 = {C, s1.d, s2.d, s3.a}, e1 = cmul4(e0, s3.b);
        v[0] = pk4(e0); v[1] = pk4(e1); v[2] = pk4(cmul4(e0, z128)); v[3] = pk4(cmul4(e1, z128));
        STAGE16(v); READ4(afr);
        PRIO(1);
        {
            const C4 f = cmul4(C4{e0.a, e0.c, e1.a, e1.c}, s1.c), h = cmul4(e0.a, s2.c, e1.a, s2.c, e0.a, s3.d, e1.a, s3.d);
            v[0] = pk4(f); v[1] = pk4(cmul4(f, z128)); v[2] = pk4(h); v[3] = pk4(cmul4(h, s1.c));
        }
        SB;
        f4 acc = Z;
#pragma unroll
        for (int s = 0; s < 4; s++) acc = mfma(afr[s], bfr[s], acc);
        STAGE16(v); READ4(afr);
        PRIO(0);
        lds_st<f4>(q + (4 * (g & 1) + 128 * (g >> 1) + 8 * c16) * 4, acc);
        {
            const f2 f0 = cmul(C, s3.c), f1 = cmul(f0, z128);
            const C4 f = cmul4(f0, s1.c, f1, s1.c, f0, s3.c, f0, s3.c), h = cmul4(C4{f0, f1, f.a, f.b}, s2.c);
            v[0] = pk4(C4{f0, f1, f.a, f.b}); v[1] = pk4(h); v[2][0] = pk(f.c.x, f.c.y);
        }
        SB;
        acc = Z;
#pragma unroll
        for (int s = 0; s < 4; s++) acc = mfma(afr[s], bfr[s], acc);
        stage<0>(v[0]); stage<4>(v[1]);
        asm volatile("ds_write_addtid_b32 %0 offset:%1" :: "v"(v[2][0]), "i"(288 * 8) : "memory");
        READ4(afr);
        if (g < 2) lds_st<f4>(q + (256 + 64 * g + 4 * c16) * 4, acc);
        else {
            const int o = q + (384 + 64 * (g - 2) + 2 * c16) * 4;
            lds_st<f2>(o, f2{acc[0], acc[1]}); lds_st<f2>(o + 128, f2{acc[2], acc[3]});
        }
        acc = Z;
#pragma unroll
        for (int s = 0; s < 4; s++) acc = mfma(afr[s], bfr[s], acc);
        if (g < 2) {
#pragma unroll
            for (int i = 0; i < 4; i++) lds_st<float>(q + (512 + 64 * g + 16 * i + c16) * 4, acc[i]);
        } else if (lane == 32) lds_st<float>(q + 640 * 4, acc[0]);
    }
    STAMP(2);
    asm volatile("s_waitcnt vmcnt(0) lgkmcnt(0)" : "+v"(pr.sel2), "+v"(pr.sel3), "+v"(pr.selx), "+v"(pr.tadr) :: "memory");
    const u4 sel2 = pr.sel2, sel3 = pr.sel3, selx = pr.selx, tadr = pr.tadr;
    bar();
    STAMP(9);
    const int k0 = LA_K + ch * KC;
    {
        const LAS float* qa = (const LAS float*)(u32)(LA_Q + ch * 5152);
        const LAS float* qb = qa + 644;
        u2 w[4];
        int y;
        if (hf == 0) {
            const int j = 384 + 4 * lane;
            const f4 a = *(const LAS f4*)(qa + j) + *(const LAS f4*)(qb + j), b = *(const LAS f4*)(qa + j + 4) + *(const LAS f4*)(qb + j + 4);
            const u32 ix = tadr[3];
            const float e0 = qa[(ix & 0xffff) >> 2] + qb[(ix & 0xffff) >> 2], e1 = qa[ix >> 18] + qb[ix >> 18];
            f4 k = *(const LAS f4*)(qa + 4 * lane) + *(const LAS f4*)(qb + 4 * lane);
            f4 p = *(const LAS f4*)(qa + 4 * lane - 4) + *(const LAS f4*)(qb + 4 * lane - 4);
            const u32 p23 = pk(a[2], a[3]), p34 = pk(a[3], b[0]);
            lds_st<u4>(k0 + 4 * KI + lane * 32, u4{pk(a[0], a[1]), p23, pk(a[1], a[2]), p34});
            lds_st<u4>(k0 + 4 * KI + lane * 32 + 16, u4{p23, pk(b[0], b[1]), p34, pk(b[1], b[2])});
            lds_st<u32>(k0 + g * KI + c16 * 4, pk(e0, e1));
            if (lane == 0) { k[0] += blo(pr.d); p = Z; }
            const u32 c = pk(k[1], k[0]), d = pk(k[0], p[3]);
            w[0] = u2{pk(k[3], k[2]), c}; w[1] = u2{pk(k[2], k[1]), d}; w[2] = u2{c, pk(p[3], p[2])}; w[3] = u2{d, pk(p[2], p[1])};
            y = k0 + (270 - 2 * lane) * 4;
        } else {
            const int j = 256 + 2 * lane;
            const float q0 = qa[j] + qb[j], q1 = qa[j + 1] + qb[j + 1], q2 = qa[j + 2] + qb[j + 2];
            const float p1 = qa[j - 1] + qb[j - 1], p2 = qa[j - 2] + qb[j - 2], p3 = qa[j - 3] + qb[j - 3];
            const u32 c = pk(q0, q0), d = pk(q0, p1), e = pk(p1, p2), f = lane ? c : d;
            w[0] = u2{pk(q2, q1), c}; w[1] = u2{pk(q1, q0), f}; w[2] = u2{c, lane ? d : e}; w[3] = u2{f, lane ? e : pk(p2, p3)};
            y = k0 + (142 - 2 * lane) * 4;
            if (lane < 32) lds_st<u32>(k0 + (lane >> 3) * KI + (272 + (lane & 7)) * 4, 0u);
        }
#pragma unroll
        for (int i = 0; i < 4; i++) lds_st<u2>(y + i * KI, w[i]);
    }
    const int x0 = 2031 - c16 + 8 * g;
    int ka = k0 + (x0 & 3) * KI + (x0 >> 2) * 8 - 3008 - 256 * hf - 192 - 512;
    int ubs = LA_U + ch * 4608 + 480 + (16 * c16 + 8 * g - 16) * 2 - 256 * hf - 192;
    int q2a = k0 + 4 * KI + tadr[0], q3a = k0 + 4 * KI + tadr[1], qxa = k0 + 4 * KI + tadr[2];
    pin(ka); pin(ubs); pin(q2a); pin(q3a); pin(qxa);
    STAMP(13);
    bar();
    STAMP(3);
#define KFR(rho, p) ld_k(ka + 192 - 64 * (rho) + 512 - 512 * (p))
#define UFR(rho, q) lds_ld<u4>(ubs + 192 - 64 * (rho) + 512 * (q))
#define QFR(rho, p) ((p) < 4 ? lds_ld<u2>(q2a + 64 * (8 * ((p) - 2) + (rho))) : ((p) == 4 && (rho) == 0 ? lds_ld<u2>(qxa) : lds_ld<u2>(q3a + 32 * (8 * ((p) - 4) + (rho)))))
#define QSEL(rho, p) ((p) < 4 ? sel2 : ((p) == 4 && (rho) == 0 ? selx : sel3))
#define QBUILD(r, s) u4{__builtin_amdgcn_perm(r[1], r[0], s[0]), __builtin_amdgcn_perm(r[1], r[0], s[1]), __builtin_amdgcn_perm(r[1], r[0], s[2]), __builtin_amdgcn_perm(r[1], r[0], s[3])}
#define FIRST2(dst) do { if (hf) { const u2 r_ = QFR(0, 2); dst = QBUILD(r_, sel2); } else dst = ld_k(ka - 320); } while (0)
    f4 acc[8];
#pragma unroll
    for (int i = 0; i < 8; i++) acc[i] = Z;
    {
        u4 A[2], T[2], B[2][4];
        u2 R[8];
#pragma unroll
        for (int p = 0; p < 4; p++) B[0][p] = UFR(0, p);
        A[0] = KFR(0, 0); A[1] = KFR(0, 1);
#pragma unroll
        for (int p = 3; p < 8; p++) R[p] = QFR(0, p);
        FIRST2(T[0]);
#pragma unroll
        for (int rho = 0; rho < 4; rho++)
#pragma unroll
            for (int p = 0; p < 8; p++) {
                SB;
                if (p == 0) { if (rho == 0) PRIO(3); else if (rho == 1) PRIO(2); else if (rho == 2) PRIO(1); else PRIO(0); }
                if (rho < 3 && p < 4) B[(rho + 1) & 1][p] = UFR(rho + 1, p);
                if (p >= 1 && p < 7 && !(p == 1 && rho == 0)) T[(p + 1) & 1] = QBUILD(R[p + 1], QSEL(rho, p + 1));
#pragma unroll
                for (int q = 0; q < 4; q++)
                    if (p + q < 8) acc[p + q] = mfma(p < 2 ? A[p] : T[p & 1], B[rho & 1][q], acc[p + q]);
                SB;
                if (rho < 3) {
                    if (p < 2) A[p] = KFR(rho + 1, p);
                    if (p >= 1 && p < 7) R[p + 1] = QFR(rho + 1, p + 1);
                }
            }
    }
#pragma unroll
    for (int i = 0; i < 8; i++) pin(acc[i]);
    STAMP(4);
#define EXCHANGE(t0, off)                                                                     \
    {                                                                                         \
        const int mine = LA_G + wv * 4608 + (off), other = LA_G + (wv ^ 1) * 4608 + (off);     \
        if (hf) { lds_st<f4>(mine + lane * 16, acc[t0]); lds_st<f4>(mine + 1024 + lane * 16, acc[t0 + 1]); acc[t0] = acc[t0 + 2]; acc[t0 + 1] = acc[t0 + 3]; } \
        else { lds_st<f4>(mine + lane * 16, acc[t0 + 2]); lds_st<f4>(mine + 1024 + lane * 16, acc[t0 + 3]); } \
        bar();                                                                                \
        const f4 y0 = lds_ld<f4>(other + lane * 16) + acc[t0], y1 = lds_ld<f4>(other + 1024 + lane * 16) + acc[t0 + 1]; \
        lds_st<u2>(other + lane * 8, u2{pk(y0[0], y0[1]), pk(y0[2], y0[3])});                 \
        lds_st<u2>(other + 1024 + lane * 8, u2{pk(y1[0], y1[1]), pk(y1[2], y1[3])});          \
    }
    await<1>(FU2, 4);
    EXCHANGE(0, 0);
    {
        u4 A[2][2], T[2], B[2][4];
        u2 R[4];
#pragma unroll
        for (int p = 0; p < 4; p++) B[0][p] = UFR(0, 4 + p);
        A[0][0] = KFR(0, 0); A[0][1] = KFR(0, 1); R[3] = QFR(0, 3);
        FIRST2(T[0]);
        bar();
        STAMP(14);
#pragma unroll
        for (int rho = 0; rho < 4; rho++)
#pragma unroll
            for (int p = 0; p < 4; p++) {
                SB;
                if (p == 0) { if (rho == 0) PRIO(3); else if (rho == 1) PRIO(2); else if (rho == 2) PRIO(1); else PRIO(0); }
                if (rho < 3) { B[(rho + 1) & 1][p] = UFR(rho + 1, 4 + p); if (p < 2) A[(rho + 1) & 1][p] = KFR(rho + 1, p); }
                if (p == 0 && rho > 0) T[0] = QBUILD(R[2], sel2);
                if (p == 1) T[1] = QBUILD(R[3], sel2);
#pragma unroll
                for (int q = 0; p + q < 4; q++) acc[4 + p + q] = mfma(p < 2 ? A[rho & 1][p] : T[p & 1], B[rho & 1][q], acc[4 + p + q]);
                SB;
                if (rho < 3 && p < 2) R[2 + p] = QFR(rho + 1, 2 + p);
            }
        SB;
    }
#pragma unroll
    for (int i = 4; i < 8; i++) pin(acc[i]);
    STAMP(5);
    EXCHANGE(4, 2048);
    bar();
    STAMP(6);
    STAMP_OUT(0);
}

__global__ void __launch_bounds__(NT) fused_kernel(const u16* __restrict__ u, const u16* __restrict__ lar, const u16* __restrict__ aimg,
                                                   const u16* __restrict__ cre, const u16* __restrict__ cim, const u16* __restrict__ ldt,
                                                   const u16* __restrict__ dsk, const u16* __restrict__ x, const u16* __restrict__ wr,
                                                   const u16* __restrict__ wi, u16* __restrict__ out, char* ws) {
    const char* entry;
    asm volatile("s_getpc_b64 %0" : "=s"(entry));
    APar pr;
    if (blockIdx.x >= A0 && __builtin_amdgcn_readfirstlane(threadIdx.x) < 768) {
        const int a = blockIdx.x - A0, d = 6 * ((a & 7) * 12 + (a >> 3)) + __builtin_amdgcn_readfirstlane(threadIdx.x >> 7);
        asm volatile("global_load_ushort %0, %6, %8\n\tglobal_load_ushort %1, %6, %9\n\tglobal_load_ushort %2, %6, %10\n\t"
                     "global_load_ushort %3, %6, %11\n\tglobal_load_ushort %4, %7, %12\n\tglobal_load_ushort %5, %7, %13"
                     : "=&v"(pr.l), "=&v"(pr.a), "=&v"(pr.r), "=&v"(pr.i), "=&v"(pr.t), "=&v"(pr.d)
                     : "v"((d * NS + (threadIdx.x & 127)) * 2), "v"(d * 2), "s"(lar), "s"(aimg), "s"(cre), "s"(cim), "s"(ldt), "s"(dsk) : "memory");
        asm volatile("global_load_dwordx4 %0, %4, %5\n\tglobal_load_dwordx4 %1, %4, %5 offset:1024\n\t"
                     "global_load_dwordx4 %2, %4, %5 offset:2048\n\tglobal_load_dwordx4 %3, %4, %5 offset:3072"
                     : "=&v"(pr.sel2), "=&v"(pr.sel3), "=&v"(pr.selx), "=&v"(pr.tadr)
                     : "v"((threadIdx.x & 63) * 16 + (threadIdx.x & 64) * 64), "s"((const char*)&TAB + F_END * 1024) : "memory");
    }
    if (__builtin_expect(blockIdx.x < NB2, 1)) head_b(entry, x, wr, wi, out, ws);
    else if (blockIdx.x >= A0) head_a(u, pr, out, ws);
    else warm_code(entry);
}

extern "C" void kernel_launch(void* const* d_in, const int* in_sizes, int n_in, void* d_out, int out_size, void* d_ws,
                              size_t ws_size, hipStream_t stream) {
    (void)hipFuncSetAttribute((const void*)fused_kernel, hipFuncAttributeMaxDynamicSharedMemorySize, LDS_BYTES);
    fused_kernel<<<GRID, NT, LDS_BYTES, stream>>>((const u16*)d_in[0], (const u16*)d_in[1], (const u16*)d_in[2], (const u16*)d_in[3],
                                                  (const u16*)d_in[4], (const u16*)d_in[5], (const u16*)d_in[6], (const u16*)d_in[7],
                                                  (const u16*)d_in[8], (const u16*)d_in[9], (u16*)d_out, (char*)d_ws);
}
```

```cpp
#define POLL_SLEEP 6
#include <hip/hip_runtime.h>

typedef unsigned short u16;
typedef unsigned int u32;
typedef float f2 __attribute__((ext_vector_type(2)));
typedef float f4 __attribute__((ext_vector_type(4)));
typedef u32 u2 __attribute__((ext_vector_type(2)));
typedef u32 u4 __attribute__((ext_vector_type(4)));
typedef int i8v __attribute__((ext_vector_type(8)));
typedef short s4 __attribute__((ext_vector_type(4)));
typedef short s8 __attribute__((ext_vector_type(8)));
typedef __bf16 bf2 __attribute__((ext_vector_type(2)));
#define DI __device__ __forceinline__
#define LAS __attribute__((address_space(3)))
#define GAS __attribute__((address_space(1)))
#define CAS __attribute__((address_space(4)))
#pragma clang diagnostic ignored "-Wint-to-pointer-cast"
struct u3 { u32 v[3]; DI u32& operator[](int i) { return v[i]; } DI const u32& operator[](int i) const { return v[i]; } };

constexpr int SEQ = 2048, DM = 576, NS = 128, DF = 289;
constexpr int NB1 = 128;
constexpr int NB2 = 145;
constexpr int A0 = 152;
constexpr int NA = 96;
constexpr int GRID = A0 + NA;
constexpr int NT = 1024;

constexpr u32 WS_EPOCH = 0, WS_F1 = 4096, WS_F2 = WS_F1 + 16 * 16 * 128, WS_X1 = 1u << 20, WS_Y1 = 4u << 20, WS_USED = 8u << 20;
#define STAMP(i) do { if ((i) == PK && HEADSEL) { __builtin_amdgcn_sched_barrier(0); tk_ = __builtin_amdgcn_s_memrealtime(); __builtin_amdgcn_sched_barrier(0); } } while (0)
#define STAMP_DECL const unsigned long long t0_ = __builtin_amdgcn_s_memrealtime(); unsigned long long tk_ = t0_
#define STAMP_OUT(T) do { if (HEADSEL && wv == PW) while (__builtin_amdgcn_s_memrealtime() - t0_ < 3000ull + 20ull * (tk_ - t0_)) __builtin_amdgcn_s_sleep(2); } while (0)
#define PK 1
#define PW 0
#define HEADSEL (blockIdx.x < NB2)

constexpr int F_SA = 0, F_EO = 4;
constexpr int F_ARE = 9, F_AIM = 10, F_BRE = 11, F_BIM = 12, F_G = 13;
constexpr int F_F16 = 21, F_GI = 23, F_AI = 31, F_M1C = 33;
constexpr int F_VAR = 37;
constexpr int F_END = F_VAR + 40;
constexpr int ET = 452, ER = 28, E2V = 580, E2U = 36, OP = 1168;
constexpr int LB_X = 0, LB_I = 0, LB_Y = 0;
constexpr int LB_E = 16 * OP;
constexpr int LB_O = LB_E, LB_E2 = LB_E;
constexpr int LB_S = LB_E + 16 * ET * 4;
constexpr int LB_W = LB_S + 1200 * 16;
constexpr int LB_T = LB_W + 2 * 256 * 36;
constexpr int LB_G = LB_T + (F_VAR + 16) * 1024;
constexpr int LB_END = LB_G + 16;
struct CosTab { double c[2048]; double c9[9], s9[9]; };
constexpr double tay_cos(double x) { double t = 1, s = 1; for (int k = 1; k <= 14; k++) { t *= -x * x / ((2 * k - 1) * (2 * k)); s += t; } return s; }
constexpr double tay_sin(double x) { double t = x, s = x; for (int k = 1; k <= 14; k++) { t *= -x * x / ((2 * k + 1) * (2 * k)); s += t; } return s; }
constexpr CosTab make_cos() {
    CosTab t = {};
    const double PI2 = 6.283185307179586476925;
    for (int k = 0; k <= 512; k++) {
        const double v = k == 512 ? 0.0 : (k <= 256 ? tay_cos(PI2 * k / 2048) : tay_sin(PI2 * (512 - k) / 2048));
        t.c[k] = v; t.c[(2048 - k) & 2047] = v; t.c[1024 - k] = -v; t.c[(1024 + k) & 2047] = -v;
    }
    t.c[512] = 0.0; t.c[1536] = 0.0;
    for (int k = 0; k < 9; k++) { const double a = PI2 * (k <= 4 ? k : k - 9) / 9; t.c9[k] = tay_cos(a); t.s9[k] = tay_sin(a); }
    return t;
}
constexpr CosTab CT = make_cos();
constexpr double cs(int n) { return CT.c[n & 2047]; }
constexpr double sn(int n) { return CT.c[(n - 512) & 2047]; }
constexpr u32 bfr(double x) { const u32 b = __builtin_bit_cast(u32, (float)x); return (b + 0x7fffu + ((b >> 16) & 1u)) >> 16; }
constexpr u32 pkc(double lo, double hi) { return bfr(lo) | (bfr(hi) << 16); }
constexpr u32 f32c(double x) { return __builtin_bit_cast(u32, (float)x); }
template <int N> struct Frags { u32 w[N * 256]; };
constexpr u32 w_re(double amp, int n) { return pkc(amp * cs(n), amp * sn(n)); }
constexpr u32 w_im(double amp, int n) { return pkc(-amp * sn(n), amp * cs(n)); }
constexpr Frags<F_VAR> make_t0() {
    Frags<F_VAR> t = {};
    const double R2 = 1.4142135623730951;
    for (int l = 0; l < 64; l++)
        for (int i = 0; i < 4; i++) {
            const int c = l & 15, g = l >> 4, r = 4 * g + i, o = l * 4 + i;
            {
                const int b = c >> 2, tq = c & 3;
                const double amp = b ? 1.0 : R2;
                const int n0 = 512 * tq * g + (b ? 256 * b * (2 * i) : -256), n1 = 512 * tq * g + (b ? 256 * b * (2 * i + 1) : 256);
                t.w[F_ARE * 256 + o] = pkc(amp * cs(n0), amp * cs(n1));
                t.w[F_AIM * 256 + o] = pkc(-amp * sn(n0), -amp * sn(n1));
            }
            for (int w = 0; w < 9; w++) {
                const int sh = 16 * (i & 1), oo = w * 128 + l * 2 + (i >> 1);
                t.w[F_EO * 256 + oo] |= (u32)((c * ET + r * ER + 2 * ((7 * w + r) % 9)) * 4) << sh;
                if (w < 8) {
                    const int k = (9 * (w & 3) + 36 * r + 64 * (c % 9)) % DM, slot = w < 4 ? (k <= 288 ? 2 * k : 2 * (DM - k) + 1) : k;
                    t.w[F_SA * 256 + oo] |= (u32)(slot * 8 + (slot >> 6) * 16) << sh;
                }
            }
            if (r < 9) { const int k = (r * c) % 9; t.w[F_BRE * 256 + o] = pkc(CT.c9[k], CT.s9[k]); t.w[F_BIM * 256 + o] = pkc(-CT.s9[k], CT.c9[k]); }
            for (int q = 0; q < 4; q++) {
                t.w[(F_G + 2 * q) * 256 + o] = w_re(1.0, 32 * r * (q + 4 * c)); t.w[(F_G + 2 * q + 1) * 256 + o] = w_im(1.0, 32 * r * (q + 4 * c));
                t.w[(F_GI + 2 * q) * 256 + o] = w_re(1.0, 32 * c * (q + 4 * r)); t.w[(F_GI + 2 * q + 1) * 256 + o] = w_im(1.0, 32 * c * (q + 4 * r));
            }
            t.w[F_F16 * 256 + o] = w_re(1.0, 128 * r * c); t.w[(F_F16 + 1) * 256 + o] = w_im(1.0, 128 * r * c);
            for (int h = 0; h < 2; h++) {
                const int j = 4 * h + (c >> 2), n = 512 * i * (c & 3) + (g ? 256 * g * j : ((j & 1) ? 256 : -256));
                t.w[(F_AI + h) * 256 + o] = w_re(g ? 2.0 : R2, n);
                const int m = 16 * r * (4 * h + 8 * c) - 512 * h;
                t.w[(F_M1C + 2 * h) * 256 + o] = pkc(0.5 * cs(m), -0.5 * sn(m));
                t.w[(F_M1C + 2 * h + 1) * 256 + o] = pkc(-0.5 * sn(m), -0.5 * cs(m));
            }
        }
    return t;
}
constexpr Frags<40> make_t1() {
    Frags<40> t = {};
    for (int v = 0; v < 5; v++)
        for (int l = 0; l < 64; l++)
            for (int i = 0; i < 4; i++) {
                const int be = v < 3 ? v + 1 : 4 * (v - 3), c = l & 15, r = 4 * (l >> 4) + i, o = v * 2048 + l * 4 + i;
                const double amp = v < 3 ? 1.0 : 0.5;
                const int n1 = 16 * r * (be + 8 * c) + (v == 4 ? 512 : 0), n4 = 16 * c * (be + 8 * r);
                t.w[o] = w_re(amp, n1); t.w[o + 256] = w_im(amp, n1);
                t.w[o + 512] = w_re(1.0, n4); t.w[o + 768] = w_im(1.0, n4);
                t.w[o + 1024] = f32c(cs(r * (be + 8 * c))); t.w[o + 1280] = f32c(-sn(r * (be + 8 * c)));
                t.w[o + 1536] = f32c(cs(c * (be + 8 * r))); t.w[o + 1792] = f32c(-sn(c * (be + 8 * r)));
            }
    return t;
}
constexpr int qidx(int l) {
    if (l <= 256) return l;
    const int sh = l < 512 ? 1 : (l < 1024 ? 2 : 3), h = 1 << (sh - 1), m = l >> sh, r = l & ((1 << sh) - 1);
    return 128 * sh + m + ((r > h || (r == h && (m & 1))) ? 1 : 0);
}
constexpr Frags<8> make_t2() {
    Frags<8> t = {};
    for (int hf = 0; hf < 2; hf++)
        for (int l = 0; l < 64; l++) {
            const int sg[3] = {17 + 4 * hf, 33 + 4 * hf, 32 + 4 * hf}, back[3] = {8, 4, 0};
            for (int k = 0; k < 3; k++) {
                const int L = 32 * sg[k] + 16 + (l & 15) - 8 * (l >> 4), q0 = qidx(L - 7);
                for (int i = 0; i < 4; i++) {
                    const u32 a = 2 * (qidx(L - 2 * i) - q0), b = 2 * (qidx(L - 2 * i - 1) - q0);
                    t.w[(4 * hf + k) * 256 + l * 4 + i] = a | ((a + 1) << 8) | (b << 16) | ((b + 1) << 24);
                }
                t.w[(4 * hf + 3) * 256 + l * 4 + k] = (u32)((q0 - back[k] - 384) * 8);
            }
            const int e = 543 - 2 * (l & 15) - (l >> 4);
            t.w[(4 * hf + 3) * 256 + l * 4 + 3] = (u32)(qidx(e) * 4) | ((u32)(qidx(e - 1) * 4) << 16);
        }
    return t;
}
struct Tables { Frags<F_VAR> a; Frags<40> b; Frags<8> c; };
constexpr Frags<8> T2 = make_t2();
constexpr Frags<F_VAR> T0 = make_t0();
constexpr Frags<40> T1 = make_t1();
static __device__ const __attribute__((aligned(128))) Tables TAB = {T0, T1, T2};
constexpr int LA_G = 0;
constexpr int LA_K = LA_G + 12 * 4608;
constexpr int KI = 1344, KC = 4 * KI + 2048;
constexpr int LA_U = LA_K + 6 * KC;
constexpr int LA_Q = LA_U + 6 * 4608;
constexpr int LA_F = LA_Q + 12 * 2576;
constexpr int LA_D = LA_F + 16;
constexpr int LA_END = LA_D + 256;
constexpr int FU0 = 0, FU2 = 1;
constexpr int LDS_BYTES = (LA_END > LB_END ? LA_END : LB_END);
static_assert(LDS_BYTES <= 160 * 1024 && LDS_BYTES > 80 * 1024, "one workgroup per CU");

DI u32 pk(float a, float b) { f2 v = {a, b}; return __builtin_bit_cast(u32, __builtin_convertvector(v, bf2)); }
DI float blo(u32 v) { return __uint_as_float(v << 16); }
DI float bhi(u32 v) { return __uint_as_float(v & 0xffff0000u); }
DI float bf2f(u16 v) { return __uint_as_float((u32)v << 16); }
DI float rbf(float x) { return blo(pk(x, x)); }
DI f2 cmul(f2 a, f2 b) { f2 t = a.xx * b; f2 s = {-b.y, b.x}; return __builtin_elementwise_fma(a.yy, s, t); }
DI f4 mfma(u4 a, u4 b, f4 c) { return __builtin_amdgcn_mfma_f32_16x16x32_bf16(__builtin_bit_cast(s8, a), __builtin_bit_cast(s8, b), c, 0, 0, 0); }
DI u4 pack4(f4 re, f4 im) { u4 r = {pk(re[0], im[0]), pk(re[1], im[1]), pk(re[2], im[2]), pk(re[3], im[3])}; return r; }
DI u4 twid(f4 re, f4 im, f4 wr, f4 wi) { return pack4(__builtin_elementwise_fma(re, wr, -(im * wi)), __builtin_elementwise_fma(re, wi, im * wr)); }
DI int mod9(int x) { return x - 9 * ((x * 57) >> 9); }
template <class T> DI void pin(T& v) { asm volatile("" : "+v"(v)); }

template <class T> DI T lds_ld(int off) { return *(const LAS T*)(u32)off; }
template <class T> DI void lds_st(int off, T v) { *(LAS T*)(u32)off = v; }
DI void bar() { __syncthreads(); }
#define PRIO(n) __builtin_amdgcn_s_setprio(n)
#define PRIOB(n)
DI void glds16(const void* g, u32 off, int lds) {
    u32 keep;
    asm volatile("s_mov_b32 %0, m0\n\ts_mov_b32 m0, %3\n\ts_nop 4\n\tglobal_load_lds_dwordx4 %1, %2\n\ts_mov_b32 m0, %0" : "=&s"(keep) : "v"(off), "s"(g), "s"(lds) : "memory");
}

DI void publish_tag(u32 epoch) {
    u32 t;
    asm volatile("v_add_u32 %0, 0x9E3779B1, %1\n\tds_write_b32 %2, %0\n\ts_waitcnt lgkmcnt(0)" : "=&v"(t) : "v"(epoch), "v"(LB_G) : "memory");
}
DI void wait_flag(__amdgpu_buffer_rsrc_t r, u32 off, u32 tag, bool on) {
    if (on) {
        const unsigned long long t0 = __builtin_amdgcn_s_memrealtime();
        while (!__all(__builtin_amdgcn_raw_buffer_load_b32(r, off, 0, 16) == tag)) {
            if (__builtin_amdgcn_s_memrealtime() - t0 > 5000000ull) break;
            __builtin_amdgcn_s_sleep(POLL_SLEEP);
            asm volatile("" ::: "memory");
        }
    }
    asm volatile("" ::: "memory");
}

constexpr int CODE_LINES = 128;
DI void warm_code(const char* entry) {
    if (threadIdx.x < CODE_LINES) { const u32 t = *(const GAS u32*)(entry + threadIdx.x * 128); asm volatile("" :: "v"(t)); }
}

DI void head_b(const char* entry, const u16* __restrict__ x, const u16* __restrict__ wr, const u16* __restrict__ wi,
               u16* __restrict__ out, char* ws) {
    const int tid = threadIdx.x, lane = tid & 63, c16 = lane & 15, g = lane >> 4;
    const int wv = __builtin_amdgcn_readfirstlane(tid >> 6);
    const int id = blockIdx.x;
    STAMP_DECL;
    const bool rows = id < NB1;
    const int s00 = 32 * (id >> 4) + (id & 15);
    const char* tab = (const char*)&TAB;
    const int R = (tid * 911) >> 16;
    const u32 xoff = (s00 + 16 * (R >> 3) + 256 * (R & 7)) * (2 * DM) + (tid - R * 72) * 16;
    asm volatile("" :: "s"(x), "s"(wr), "s"(wi), "s"(out), "s"(ws), "v"(xoff));
    const __amdgpu_buffer_rsrc_t rws = __builtin_amdgcn_make_buffer_rsrc(ws, 0, WS_USED, 0x00020000);
    u32 epoch = 0;
    if (wv == 0) asm volatile("global_load_dword %0, %0, %1 sc1" : "+v"(epoch) : "s"(ws) : "memory");
    if (rows) {
        glds16(x, xoff, LB_X + wv * 1024);
        if (wv < 2) glds16(x, (s00 + 16 + 1536) * (2 * DM) + (tid < 56 ? (tid + 16) * 16 : 256 * 2 * DM + (tid - 56) * 16), LB_X + 16384 + wv * 1024);
        else glds16(tab, tid * 16 - 2048, LB_T + (wv - 2) * 1024);
        if (wv >= 9) glds16(tab, tid * 16 + 5 * 1024, LB_T + (wv + 5) * 1024);
    }
    const int u2i = (id & 7) ? (id & 7) * 18 + 1 + (id >> 3) : (id == 136 ? 0 : (id >> 3) + (id < 136 ? 1 : 0));
    const bool star = u2i >= 108;
    const int q36 = min((u2i * 57) >> 11, 3), kg = u2i - 36 * q36, bq = (q36 + 1) & 3;
    const bool mir = !star && kg >= 18;
    const int w0 = star ? min(16 * kg, 562) : (mir ? (2048 - bq) * (2 * DF) + 1122 - 32 * kg : bq * (2 * DF) + 32 * kg);
    __builtin_amdgcn_s_barrier();
    const u32 woff = w0 + ((tid >> 1) & 255) * (mir ? -16 * DF : 16 * DF) + (tid & 1) * (star ? 8 * DF : 16);
    u4 wra, wrb;
    if (wv >= 8) {
        __builtin_memcpy(&wra, (const char*)wr + woff, 16);
        __builtin_memcpy(&wrb, (const char*)wi + woff, 16);
    }
    __builtin_amdgcn_sched_barrier(0);
    const f4 Z = {0.f, 0.f, 0.f, 0.f};
    const int tau = wv & 3, bw = wv >> 2;
    int tl = LB_T + lane * 16;
    int lin = LB_S + (tid + (tid >> 5)) * 16;
    pin(tl); pin(lin);
    int sadr[4];

    if (rows) {
        if (tid < 256) {
            const int o = LB_E + 72 + (tid & 15) * (ER * 4) + (tid >> 4) * (ET * 4);
            lds_st<u2>(o, u2{0u, 0u}); lds_st<u4>(o + 8, u4{0u, 0u, 0u, 0u});
        }
        int xo = LB_X + (c16 >> 2) * 1152 + 2 * (144 * ((g - wv) & 3) + 16 * wv + 4 * (c16 & 3));
        int bo = LB_E + (wv * ET + c16 * ER + min(8 * g, 16)) * 4;
        int go = WS_X1 + (tid >> 3) * 16384 + id * 128 + (tid & 7) * 16;
        int ta = LB_T + lane * 8;
        pin(xo); pin(bo); pin(go); pin(ta);
        STAMP(1);
        asm volatile("s_cmp_lt_u32 %0, 8\n\ts_cbranch_scc0 .Lw%=\n\ts_waitcnt vmcnt(0)\n.Lw%=:\n\ts_waitcnt vmcnt(2)" :: "s"(wv) : "memory", "scc");
        if (wv == 0) publish_tag(epoch);
        bar();
        STAMP(2);
        if (wv < 9) {
            u4 d[2];
#pragma unroll
            for (int s = 0; s < 2; s++) {
                const s4 lo = __builtin_amdgcn_ds_read_tr16_b64_v4i16((LAS s4*)(u32)(xo + s * 9216));
                const s4 hi = __builtin_amdgcn_ds_read_tr16_b64_v4i16((LAS s4*)(u32)(xo + s * 9216 + 4608));
                d[s] = __builtin_bit_cast(u4, __builtin_shufflevector(lo, hi, 0, 1, 2, 3, 4, 5, 6, 7));
            }
            const u4 are = lds_ld<u4>(tl + F_ARE * 1024), aim = lds_ld<u4>(tl + F_AIM * 1024);
            const u2 eo = lds_ld<u2>(ta + F_EO * 1024 + wv * 512);
#pragma unroll
            for (int s = 0; s < 2; s++) d[s] = pack4(mfma(d[s], are, Z), mfma(d[s], aim, Z));
#pragma unroll
            for (int i = 0; i < 4; i++) lds_st<u2>(LB_E + ((i & 1) ? eo[i >> 1] >> 16 : eo[i >> 1] & 0xffff), u2{d[0][i], d[1][i]});
        }
        u4 bre = lds_ld<u4>(tl + F_BRE * 1024), bim = lds_ld<u4>(tl + F_BIM * 1024);
        u4 gre = lds_ld<u4>(tl + (F_G + 2 * tau) * 1024), gim = lds_ld<u4>(tl + (F_G + 2 * tau + 1) * 1024);
        {
            const u2 sa = lds_ld<u2>(ta + F_SA * 1024 + (tau + (bw ? 4 : 0)) * 512);
            const int base = LB_S + ((bw + 3) & 3) * (DM * 8 + 144);
#pragma unroll
            for (int i = 0; i < 4; i++) { sadr[i] = base + ((i & 1) ? sa[i >> 1] >> 16 : sa[i >> 1] & 0xffff); pin(sadr[i]); }
        }
        pin(bre); pin(bim); pin(gre); pin(gim);
        bar();
        STAMP(13);
        {
            PRIOB(2);
            const u4 p0 = lds_ld<u4>(bo), p1 = lds_ld<u4>(bo + 16);
            u4 d[2];
            f4 re[2], im[2];
#pragma unroll
            for (int s = 0; s < 2; s++) { const u4 a = {p0[s], p0[2 + s], p1[s], p1[2 + s]}; re[s] = mfma(a, bre, Z); im[s] = mfma(a, bim, Z); }
            PRIOB(1);
#pragma unroll
            for (int s = 0; s < 2; s++) { const u4 t = pack4(re[s], im[s]); re[s] = mfma(gre, t, Z); im[s] = mfma(gim, t, Z); }
            PRIOB(0);
#pragma unroll
            for (int s = 0; s < 2; s++) d[s] = pack4(re[s], im[s]);
            if (c16 < 9) {
#pragma unroll
                for (int i = 0; i < 4; i++) lds_st<u2>(sadr[i], u2{d[0][i], d[1][i]});
                if (wv == 0 && c16 == 0 && !(g & 1)) lds_st<u2>(sadr[0] + 8, u2{d[0][0], d[1][0]});
            }
        }
        STAMP(14);
        bar();
        STAMP(3);
        __builtin_amdgcn_raw_buffer_store_b128(lds_ld<u4>(lin), rws, go, 0, 16);
        if (tid <= 128) __builtin_amdgcn_raw_buffer_store_b128(lds_ld<u4>(lin + 1056 * 16), rws, go, 128 * 16384, 16);
    } else {
        if (wv < 8 && (kg == 0 || kg == (star ? 36 : 18))) {
            const int m = tid & 255;
            lds_st<u16>(LB_W + tid * 36 + 32, ((wv >> 2) ? wi : wr)[((mir ? bq + 8 * m : 2048 - bq - 8 * m) & 2047) * DF + (kg ? 288 : 0)]);
        }
        warm_code(entry);
        asm volatile("s_waitcnt vmcnt(0)" ::: "memory");
        if (wv == 0) publish_tag(epoch);
        bar();
    }
    const u32 tag = lds_ld<u32>(LB_G);
    if (wv >= 8) {
        const int o = LB_W + ((tid >> 1) & 255) * 36 + (tid & 1) * 16;
#pragma unroll
        for (int i = 0; i < 4; i++) { lds_st<u32>(o + 4 * i, wra[i]); lds_st<u32>(o + 9216 + 4 * i, wrb[i]); }
    }
    asm volatile("s_waitcnt vmcnt(0)" ::: "memory");
    if (rows && lane == 0) __builtin_amdgcn_raw_buffer_store_b32(tag, rws, WS_F1 + (wv * 16 + (id >> 3)) * 128 + (id & 7) * 4, 0, 16);
    STAMP(4);
    glds16(tab, F_F16 * 1024 + tid * 16, LB_T + F_F16 * 1024 + wv * 1024);
    if (star || wv < 8) glds16(tab, (F_VAR + 8 * q36) * 1024 + tid * 16, LB_T + F_VAR * 1024 + wv * 1024);
    asm volatile("s_waitcnt vmcnt(0)" ::: "memory");
    bar();
    STAMP(12);

    const int typ = star ? (wv & 1) : 0;
    const int tv = tl + (F_VAR + 8 * typ) * 1024;
    u4 fre = lds_ld<u4>(tl + F_F16 * 1024), fim = lds_ld<u4>(tl + (F_F16 + 1) * 1024);
    u4 m1r = lds_ld<u4>(tv), m1i = lds_ld<u4>(tv + 1024), m4r = lds_ld<u4>(tv + 2048), m4i = lds_ld<u4>(tv + 3072);
    f4 twr = lds_ld<f4>(tv + 4096), twi = lds_ld<f4>(tv + 5120), tur = lds_ld<f4>(tv + 6144), tui = lds_ld<f4>(tv + 7168);
    u4 mcr = lds_ld<u4>(tl + (F_M1C + 2 * typ) * 1024), mci = lds_ld<u4>(tl + (F_M1C + 2 * typ + 1) * 1024);
    f4 wfr, wfi;
    {
        const int kcol = star ? 8 * kg + (wv >> 1) : 16 * kg + wv;
        const int cc = star ? 8 * typ + (u2i < NB2 - 1 ? (wv >> 1) : 7) : (mir ? 15 - wv : wv);
        const int wo = LB_W + (c16 + 64 * g) * 36 + cc * 2;
#pragma unroll
        for (int i = 0; i < 4; i++) {
            wfr[i] = bf2f(lds_ld<u16>(wo + i * 576));
            wfi[i] = bf2f(lds_ld<u16>(wo + i * 576 + 9216));
        }
        float sc = 1.f / (float)(SEQ * DM);
        if (kcol == 0 || kcol == 288) {
            sc *= 0.5f;
#pragma unroll
            for (int i = 0; i < 4; i++) {
                const int m = c16 + 64 * g + 16 * i;
                const int o = LB_W + (typ ? (255 - m) * 36 + cc * 2 : m * 36 + 32);
                wfr[i] += bf2f(lds_ld<u16>(o));
                wfi[i] -= bf2f(lds_ld<u16>(o + 9216));
            }
        }
        wfr *= sc; wfi *= (mir ? -sc : sc);
    }
    f4 wnr = -wfr, wni = -wfi;
    int io = (32 * g + c16) * 128 + (((wv >> 1) ^ (c16 >> 1)) * 16);
    int so = (tid >> 3) * 128 + (((tid & 7) ^ ((tid >> 4) & 7)) * 16);
    pin(fre); pin(fim); pin(m1r); pin(m1i); pin(m4r); pin(m4i); pin(twr); pin(twi); pin(tur); pin(tui); pin(mcr); pin(mci);
    int gi = WS_X1 + u2i * 16384 + tid * 16, gq = WS_Y1 + (tid >> 3) * (NB2 * 128) + u2i * 128 + (tid & 7) * 16;
    int i1 = LB_I + io + (star ? 0 : (wv & 1) * 8);
    io += LB_O + (wv & 1) * 8;
    pin(wfr); pin(wnr); pin(wni); pin(io); pin(so); pin(gi); pin(gq); pin(i1);
    wait_flag(rws, WS_F1 + (((u2i >> 3) & 15) * 16 + wv) * 128 + lane * 4, tag, lane < 8);
    STAMP(5);
    lds_st<u4>(LB_I + so, __builtin_amdgcn_raw_buffer_load_b128(rws, gi, 0, 16));
    bar();
    STAMP(6);
    {
        const int o1 = i1;
        PRIOB(3);
        const u2 a0 = lds_ld<u2>(o1), a1 = lds_ld<u2>(o1 + 2048);
        const u4 a = {a0[0], a0[1], a1[0], a1[1]};
        f4 re = mfma(a, m1r, Z), im = mfma(a, m1i, Z);
        if (star) {
            const u2 b0 = lds_ld<u2>(o1 + 8), b1 = lds_ld<u2>(o1 + 8 + 2048);
            const u4 b = {b0[0], b0[1], b1[0], b1[1]};
            re = mfma(b, mcr, re); im = mfma(b, mci, im);
        }
        u4 t = twid(re, im, twr, twi);
        re = mfma(fre, t, Z); im = mfma(fim, t, Z);
        PRIOB(2);
        t = pack4(__builtin_elementwise_fma(re, wfr, im * wni), __builtin_elementwise_fma(re, wni, im * wnr));
        re = mfma(t, fre, Z); im = mfma(t, fim, Z);
        PRIOB(1);
        t = twid(re, im, tur, tui);
        re = mfma(m4r, t, Z); im = mfma(m4i, t, Z);
        PRIOB(0);
        t = pack4(re, im);
        lds_st<u2>(io, u2{t[0], t[1]}); lds_st<u2>(io + 2048, u2{t[2], t[3]});
    }
    bar();
    STAMP(7);
    {
        u4 v = lds_ld<u4>(LB_O + so);
        if (star) {
            u4 w;
#pragma unroll
            for (int s = 0; s < 2; s++) {
                const float ar = blo(v[s]), ai = bhi(v[s]), cr = blo(v[2 + s]), ci = bhi(v[2 + s]);
                w[s] = pk(ar + ci, ai - cr); w[2 + s] = pk(ar - ci, -ai - cr);
            }
            v = w;
        }
        if (u2i < NB2 - 1 || (tid & 7) == 0)
            __builtin_amdgcn_raw_buffer_store_b128(v, rws, gq, 0, 16);
    }
    asm volatile("s_waitcnt vmcnt(0)" ::: "memory");
    if (lane == 0) __builtin_amdgcn_raw_buffer_store_b32(tag, rws, WS_F2 + (wv * 19 + (u2i >> 3)) * 128 + (u2i & 7) * 4, 0, 16);
    STAMP(8);
    if (!rows) { STAMP_OUT(0); return; }

    u4 gre = lds_ld<u4>(tl + (F_GI + 2 * tau) * 1024), gim = lds_ld<u4>(tl + (F_GI + 2 * tau + 1) * 1024);
    u4 bre = lds_ld<u4>(tl + F_BRE * 1024), bim = lds_ld<u4>(tl + F_BIM * 1024);
    u4 ai0 = lds_ld<u4>(tl + F_AI * 1024), ai1 = lds_ld<u4>(tl + (F_AI + 1) * 1024);
    int eadr[4];
#pragma unroll
    for (int i = 0; i < 4; i++) {
        const int r = 4 * g + i;
        eadr[i] = LB_E2 + (mod9(4 * (c16 + 18 - r)) * E2V + r * E2U + 2 * wv) * 4;
        pin(eadr[i]);
    }
    int ao = LB_E2 + (wv * E2V + c16 * E2U + 8 * g) * 4;
    int gy = WS_Y1 + id * (NB2 * 128) + tid * 16;
    int yo = LB_Y + (c16 >> 2) * OP + 2 * (144 * (((c16 & 3) - wv) & 3) + 16 * wv + 4 * g);
    int yl = LB_Y + tid * 16 + R * (OP - 1152);
    u32 oo = 2 * xoff - (tid - R * 72) * 16 + 2 * DM;
    pin(gre); pin(gim); pin(bre); pin(bim); pin(ai0); pin(ai1); pin(ao); pin(gy); pin(yo); pin(yl); pin(oo);
    {
        const int un = 8 * wv + lane + (lane >= 8 ? 120 : 0);
        wait_flag(rws, WS_F2 + ((id >> 3) * 19 + (un >> 3)) * 128 + (lane & 7) * 4, tag, lane < 16 && un < NB2);
    }
    STAMP(9);
    {
        const u4 v = __builtin_amdgcn_raw_buffer_load_b128(rws, gy, 0, 16);
        if (tid <= 128) lds_st<u4>(lin + 1056 * 16, __builtin_amdgcn_raw_buffer_load_b128(rws, gy, 16384, 16));
        lds_st<u4>(lin, v);
    }
    bar();
    if (id == 0 && tid == 0) __builtin_amdgcn_raw_buffer_store_b32(tag, rws, WS_EPOCH, 0, 16);
    STAMP(10);
    {
        PRIOB(2);
        u4 d[2];
        u2 p[4];
#pragma unroll
        for (int i = 0; i < 4; i++) p[i] = lds_ld<u2>(sadr[i]);
        f4 re[2], im[2];
#pragma unroll
        for (int s = 0; s < 2; s++) { const u4 a = {p[0][s], p[1][s], p[2][s], p[3][s]}; re[s] = mfma(a, gre, Z); im[s] = mfma(a, gim, Z); }
        PRIOB(1);
#pragma unroll
        for (int s = 0; s < 2; s++) { const u4 t = pack4(re[s], im[s]); re[s] = mfma(t, bre, Z); im[s] = mfma(t, bim, Z); }
        PRIOB(0);
#pragma unroll
        for (int s = 0; s < 2; s++) d[s] = pack4(re[s], im[s]);
        if (c16 < 9) {
#pragma unroll
            for (int i = 0; i < 4; i++) lds_st<u2>(eadr[i], u2{d[0][i], d[1][i]});
        }
    }
    STAMP(15);
    bar();
    if (wv < 9) {
        const u4 p0 = lds_ld<u4>(ao), p1 = lds_ld<u4>(ao + 16);
#pragma unroll
        for (int s = 0; s < 2; s++) {
            const u4 a = {p0[s], p0[2 + s], p1[s], p1[2 + s]};
            const f4 y0 = mfma(a, ai0, Z), y1 = mfma(a, ai1, Z);
            lds_st<u2>(yo + s * (8 * OP), u2{pk(y0[0], y0[1]), pk(y0[2], y0[3])});
            lds_st<u2>(yo + s * (8 * OP) + 4 * OP, u2{pk(y1[0], y1[1]), pk(y1[2], y1[3])});
        }
    }
    bar();
    *(GAS u4*)((char*)out + oo) = lds_ld<u4>(yl);
    if (wv < 2) {
        const int r2 = tid < 56 ? 14 : 15, c2 = tid + 1024 - 72 * r2;
        *(GAS u4*)((char*)out + (u32)((s00 + 16 + 256 * (r2 - 8)) * (4 * DM) + 2 * DM + c2 * 16)) = lds_ld<u4>(LB_Y + r2 * OP + c2 * 16);
    }
    STAMP(11);
    STAMP_OUT(0);
}

DI f2 cexpa(float ar2, float air, float p) {
    const float e = __builtin_amdgcn_exp2f(ar2 * p), t = __builtin_amdgcn_fractf(air * p);
    return f2{e * __builtin_amdgcn_cosf(t), e * __builtin_amdgcn_sinf(t)};
}
DI u4 ld_k(int o) {
    const u2 a = *(const volatile LAS u2*)(u32)o, b = *(const volatile LAS u2*)(u32)(o + 8);
    return u4{a[0], a[1], b[0], b[1]};
}
DI void a_rows_out(int yb, int ystr, char* o) {
    u2 y[6];
#pragma unroll
    for (int c = 0; c < 6; c++) y[c] = lds_ld<u2>(yb + c * ystr);
#pragma unroll
    for (int r = 0; r < 4; r++) {
        u3 v;
#pragma unroll
        for (int c = 0; c < 3; c++) v[c] = __builtin_amdgcn_perm(y[2 * c + 1][r >> 1], y[2 * c][r >> 1], (r & 1) ? 0x07060302u : 0x05040100u);
        *(u3*)(o + r * (4 * DM)) = v;
    }
}
template <int R> DI void stage(u4 v) {
    asm volatile("ds_write_addtid_b32 %0 offset:%4\n\tds_write_addtid_b32 %1 offset:%5\n\tds_write_addtid_b32 %2 offset:%6\n\tds_write_addtid_b32 %3 offset:%7"
                 :: "v"(v[0]), "v"(v[1]), "v"(v[2]), "v"(v[3]), "i"(288 * R), "i"(288 * R + 288), "i"(288 * R + 576), "i"(288 * R + 864) : "memory");
}
#define CM_MUL(d, a, b) "v_pk_mul_f32 " d ", " a ", " b " op_sel_hi:[0,1]\n\t"
#define CM_FMA(d, a, b) "v_pk_fma_f32 " d ", " a ", " b ", " d " op_sel:[1,1,0] op_sel_hi:[1,0,1] neg_lo:[1,0,0]\n\t"
struct C4 { f2 a, b, c, d; };
DI C4 cmul4(f2 a0, f2 b0, f2 a1, f2 b1, f2 a2, f2 b2, f2 a3, f2 b3) {
    C4 r;
    asm("s_nop 0\n\t" CM_MUL("%0", "%4", "%5") CM_MUL("%1", "%6", "%7") CM_MUL("%2", "%8", "%9") CM_MUL("%3", "%10", "%11")
        CM_FMA("%0", "%4", "%5") CM_FMA("%1", "%6", "%7") CM_FMA("%2", "%8", "%9") CM_FMA("%3", "%10", "%11") "s_nop 0"
        : "=&v"(r.a), "=&v"(r.b), "=&v"(r.c), "=&v"(r.d) : "v"(a0), "v"(b0), "v"(a1), "v"(b1), "v"(a2), "v"(b2), "v"(a3), "v"(b3));
    return r;
}
DI C4 cmul4(const C4& a, f2 b) { return cmul4(a.a, b, a.b, b, a.c, b, a.d, b); }
DI u4 pk4(const C4& v) { return u4{pk(v.a.x, v.a.y), pk(v.b.x, v.b.y), pk(v.c.x, v.c.y), pk(v.d.x, v.d.y)}; }

DI void sig(int f) {
    asm volatile("s_waitcnt lgkmcnt(0)" ::: "memory");
    if ((threadIdx.x & 63) == 0) __hip_atomic_fetch_add((LAS u32*)(u32)(LA_F + 4 * f), 1u, __ATOMIC_RELAXED, __HIP_MEMORY_SCOPE_WORKGROUP);
}
template <int SLEEP = 0> DI void await(int f, u32 n) {
    while ((u32)__builtin_amdgcn_readfirstlane(*(const volatile LAS u32*)(u32)(LA_F + 4 * f)) < n) if (SLEEP) __builtin_amdgcn_s_sleep(SLEEP);
    asm volatile("" ::: "memory");
}
DI float exp_bf(float x) {
    const float t = x * 1.44269504f, r = __builtin_fmaf(x, 1.92596299e-8f, __builtin_fmaf(x, 1.44269504f, -t));
    const float e = __builtin_amdgcn_exp2f(t);
    return __builtin_fmaf(e * 0.693147181f, r, e);
}

struct APar { u32 l, a, r, i, t, d; u4 sel2, sel3, selx, tadr; };
DI void head_a(const u16* __restrict__ u, APar pr, u16* __restrict__ out, char* ws) {
    const int tid = threadIdx.x, lane = tid & 63, c16 = lane & 15, g = lane >> 4;
    const int wv = __builtin_amdgcn_readfirstlane(tid >> 6);
    STAMP_DECL;
    const int ai_ = blockIdx.x - A0;
    const int d0 = 6 * ((ai_ & 7) * 12 + (ai_ >> 3));
    const f4 Z = {0.f, 0.f, 0.f, 0.f};

    if (wv >= 12) {
        __builtin_amdgcn_s_setprio(3);
        const int t = tid - 768;
        if (t == 0) lds_st<u2>(LA_F, u2{0u, 0u});
        if (t < 180) lds_st<u4>(LA_U + (t / 30) * 4608 + (t % 30) * 16, u4{0u, 0u, 0u, 0u});
        bar();
#pragma unroll
        for (int i = 0; i < 2; i++)
            if (i == 0 || wv < 14) {
                const int r = min((ai_ >> 3) + 12 * (128 * i + (t >> 1)), SEQ - 1);
                u32 keep;
                asm volatile("s_mov_b32 %0, m0\n\ts_mov_b32 m0, %3\n\ts_nop 4\n\tglobal_load_lds_dword %1, %2\n\ts_mov_b32 m0, %0"
                             : "=&s"(keep) : "v"(r * (2 * DM) + (((ai_ & 7) * 144) & ~127) + (t & 1) * 128), "s"(u), "s"(LA_D) : "memory");
            }
        u3 ua[4], ub[4];
#pragma unroll
        for (int r = 0; r < 4; r++) ua[r] = *(const u3*)(u + (4 * t + r) * DM + d0);
        sig(FU0);
        await<1>(FU0, 4);
#pragma unroll
        for (int r = 0; r < 4; r++) ub[r] = *(const u3*)(u + (1024 + 4 * t + r) * DM + d0);
        asm volatile("" ::: "memory");
#pragma unroll
        for (int c = 0; c < 6; c++) {
            const u32 sel = (c & 1) ? 0x07060302u : 0x05040100u;
            lds_st<u2>(LA_U + c * 4608 + 480 + t * 8, u2{__builtin_amdgcn_perm(ua[1][c >> 1], ua[0][c >> 1], sel), __builtin_amdgcn_perm(ua[3][c >> 1], ua[2][c >> 1], sel)});
        }
        STAMP(7);
        bar();
        bar();
#pragma unroll
        for (int c = 0; c < 6; c++) {
            const u32 sel = (c & 1) ? 0x07060302u : 0x05040100u;
            lds_st<u2>(LA_U + c * 4608 + 480 + 2048 + t * 8, u2{__builtin_amdgcn_perm(ub[1][c >> 1], ub[0][c >> 1], sel), __builtin_amdgcn_perm(ub[3][c >> 1], ub[2][c >> 1], sel)});
        }
        sig(FU2);
        STAMP(8);
        const int tile = wv - 12, yl = ((lane >> 2) + 16 * (lane & 3)) * 8;
        char* o = (char*)(out + 4 * t * (2 * DM) + d0);
        bar();
        bar();
        STAMP(11);
        a_rows_out(LA_G + ((tile >> 1) ^ 1) * 4608 + (tile & 1) * 1024 + yl, 9216, o);
        STAMP(9);
        bar();
        bar();
        STAMP(12);
        a_rows_out(LA_G + ((tile >> 1) ^ 1) * 4608 + (tile & 1) * 1024 + 2048 + yl, 9216, o + 1024 * (4 * DM));
        STAMP(10);
        STAMP_OUT(768);
        return;
    }
    const int ch = wv >> 1, hf = wv & 1;
    {
        __builtin_amdgcn_s_barrier();
        asm volatile("s_waitcnt vmcnt(4)" : "+v"(pr.l), "+v"(pr.a), "+v"(pr.r), "+v"(pr.i), "+v"(pr.t), "+v"(pr.d) :: "memory");
        PRIO(3);
        const float dt = rbf(exp_bf(blo(pr.t)));
        const float ar2 = -rbf(exp_bf(blo(pr.l))) * dt * 1.44269504f, air = blo(pr.a) * dt * 0.159154943f;
        const f2 C = {2.f * dt * blo(pr.r), 2.f * dt * blo(pr.i)};
        STAMP(1);
        const f2 z1 = cexpa(ar2, air, 1.f), z8c = cexpa(ar2, -air, 8.f), z128 = cexpa(ar2, air, 128.f);
        const int gr = LA_G + wv * 4608 + c16 * 288 + g * 16, q = LA_Q + wv * 2576;
        asm volatile("s_mov_b32 m0, %0\n\ts_nop 0" :: "s"(LA_G + wv * 4608) : "memory");
#define SB __builtin_amdgcn_sched_barrier(0)
#define STAGE16(v) do { stage<0>(v[0]); stage<4>(v[1]); stage<8>(v[2]); stage<12>(v[3]); } while (0)
#define READ4(f) do { _Pragma("unroll") for (int s = 0; s < 4; s++) f[s] = lds_ld<u4>(gr + 64 * s); SB; } while (0)
        u4 v[4], bfr[4], afr[4];
        const C4 s1 = cmul4(z8c, z8c, z1, z1, z128, z128, C, z1);
        const C4 s2 = cmul4(s1.a, s1.a, z8c, s1.a, s1.c, s1.c, C, s1.b);
        {
            C4 w = {f2{1.f, 0.f}, z8c, s1.a, s2.b};
            v[0] = pk4(w);
#pragma unroll
            for (int i = 1; i < 4; i++) { w = cmul4(w, s2.a); v[i] = pk4(w); }
        }
        STAGE16(v); READ4(bfr);
        PRIO(2);
        const C4 s3 = cmul4(s1.d, s1.b, s1.b, s1.b, s2.c, s2.c, s2.c, z128);
        const C4 e0 = {C, s1.d, s2.d, s3.a}, e1 = cmul4(e0, s3.b);
        v[0] = pk4(e0); v[1] = pk4(e1); v[2] = pk4(cmul4(e0, z128)); v[3] = pk4(cmul4(e1, z128));
        STAGE16(v); READ4(afr);
        PRIO(1);
        {
            const C4 f = cmul4(C4{e0.a, e0.c, e1.a, e1.c}, s1.c), h = cmul4(e0.a, s2.c, e1.a, s2.c, e0.a, s3.d, e1.a, s3.d);
            v[0] = pk4(f); v[1] = pk4(cmul4(f, z128)); v[2] = pk4(h); v[3] = pk4(cmul4(h, s1.c));
        }
        SB;
        f4 acc = Z;
#pragma unroll
        for (int s = 0; s < 4; s++) acc = mfma(afr[s], bfr[s], acc);
        STAGE16(v); READ4(afr);
        PRIO(0);
        lds_st<f4>(q + (4 * (g & 1) + 128 * (g >> 1) + 8 * c16) * 4, acc);
        {
            const f2 f0 = cmul(C, s3.c), f1 = cmul(f0, z128);
            const C4 f = cmul4(f0, s1.c, f1, s1.c, f0, s3.c, f0, s3.c), h = cmul4(C4{f0, f1, f.a, f.b}, s2.c);
            v[0] = pk4(C4{f0, f1, f.a, f.b}); v[1] = pk4(h); v[2][0] = pk(f.c.x, f.c.y);
        }
        SB;
        acc = Z;
#pragma unroll
        for (int s = 0; s < 4; s++) acc = mfma(afr[s], bfr[s], acc);
        stage<0>(v[0]); stage<4>(v[1]);
        asm volatile("ds_write_addtid_b32 %0 offset:%1" :: "v"(v[2][0]), "i"(288 * 8) : "memory");
        READ4(afr);
        if (g < 2) lds_st<f4>(q + (256 + 64 * g + 4 * c16) * 4, acc);
        else {
            const int o = q + (384 + 64 * (g - 2) + 2 * c16) * 4;
            lds_st<f2>(o, f2{acc[0], acc[1]}); lds_st<f2>(o + 128, f2{acc[2], acc[3]});
        }
        acc = Z;
#pragma unroll
        for (int s = 0; s < 4; s++) acc = mfma(afr[s], bfr[s], acc);
        if (g < 2) {
#pragma unroll
            for (int i = 0; i < 4; i++) lds_st<float>(q + (512 + 64 * g + 16 * i + c16) * 4, acc[i]);
        } else if (lane == 32) lds_st<float>(q + 640 * 4, acc[0]);
    }
    STAMP(2);
    asm volatile("s_waitcnt vmcnt(0) lgkmcnt(0)" : "+v"(pr.sel2), "+v"(pr.sel3), "+v"(pr.selx), "+v"(pr.tadr) :: "memory");
    const u4 sel2 = pr.sel2, sel3 = pr.sel3, selx = pr.selx, tadr = pr.tadr;
    bar();
    STAMP(9);
    const int k0 = LA_K + ch * KC;
    {
        const LAS float* qa = (const LAS float*)(u32)(LA_Q + ch * 5152);
        const LAS float* qb = qa + 644;
        u2 w[4];
        int y;
        if (hf == 0) {
            const int j = 384 + 4 * lane;
            const f4 a = *(const LAS f4*)(qa + j) + *(const LAS f4*)(qb + j), b = *(const LAS f4*)(qa + j + 4) + *(const LAS f4*)(qb + j + 4);
            const u32 ix = tadr[3];
            const float e0 = qa[(ix & 0xffff) >> 2] + qb[(ix & 0xffff) >> 2], e1 = qa[ix >> 18] + qb[ix >> 18];
            f4 k = *(const LAS f4*)(qa + 4 * lane) + *(const LAS f4*)(qb + 4 * lane);
            f4 p = *(const LAS f4*)(qa + 4 * lane - 4) + *(const LAS f4*)(qb + 4 * lane - 4);
            const u32 p23 = pk(a[2], a[3]), p34 = pk(a[3], b[0]);
            lds_st<u4>(k0 + 4 * KI + lane * 32, u4{pk(a[0], a[1]), p23, pk(a[1], a[2]), p34});
            lds_st<u4>(k0 + 4 * KI + lane * 32 + 16, u4{p23, pk(b[0], b[1]), p34, pk(b[1], b[2])});
            lds_st<u32>(k0 + g * KI + c16 * 4, pk(e0, e1));
            if (lane == 0) { k[0] += blo(pr.d); p = Z; }
            const u32 c = pk(k[1], k[0]), d = pk(k[0], p[3]);
            w[0] = u2{pk(k[3], k[2]), c}; w[1] = u2{pk(k[2], k[1]), d}; w[2] = u2{c, pk(p[3], p[2])}; w[3] = u2{d, pk(p[2], p[1])};
            y = k0 + (270 - 2 * lane) * 4;
        } else {
            const int j = 256 + 2 * lane;
            const float q0 = qa[j] + qb[j], q1 = qa[j + 1] + qb[j + 1], q2 = qa[j + 2] + qb[j + 2];
            const float p1 = qa[j - 1] + qb[j - 1], p2 = qa[j - 2] + qb[j - 2], p3 = qa[j - 3] + qb[j - 3];
            const u32 c = pk(q0, q0), d = pk(q0, p1), e = pk(p1, p2), f = lane ? c : d;
            w[0] = u2{pk(q2, q1), c}; w[1] = u2{pk(q1, q0), f}; w[2] = u2{c, lane ? d : e}; w[3] = u2{f, lane ? e : pk(p2, p3)};
            y = k0 + (142 - 2 * lane) * 4;
            if (lane < 32) lds_st<u32>(k0 + (lane >> 3) * KI + (272 + (lane & 7)) * 4, 0u);
        }
#pragma unroll
        for (int i = 0; i < 4; i++) lds_st<u2>(y + i * KI, w[i]);
    }
    const int x0 = 2031 - c16 + 8 * g;
    int ka = k0 + (x0 & 3) * KI + (x0 >> 2) * 8 - 3008 - 256 * hf - 192 - 512;
    int ubs = LA_U + ch * 4608 + 480 + (16 * c16 + 8 * g - 16) * 2 - 256 * hf - 192;
    int q2a = k0 + 4 * KI + tadr[0], q3a = k0 + 4 * KI + tadr[1], qxa = k0 + 4 * KI + tadr[2];
    pin(ka); pin(ubs); pin(q2a); pin(q3a); pin(qxa);
    STAMP(13);
    bar();
    STAMP(3);
#define KFR(rho, p) ld_k(ka + 192 - 64 * (rho) + 512 - 512 * (p))
#define UFR(rho, q) lds_ld<u4>(ubs + 192 - 64 * (rho) + 512 * (q))
#define QFR(rho, p) ((p) < 4 ? lds_ld<u2>(q2a + 64 * (8 * ((p) - 2) + (rho))) : ((p) == 4 && (rho) == 0 ? lds_ld<u2>(qxa) : lds_ld<u2>(q3a + 32 * (8 * ((p) - 4) + (rho)))))
#define QSEL(rho, p) ((p) < 4 ? sel2 : ((p) == 4 && (rho) == 0 ? selx : sel3))
#define QBUILD(r, s) u4{__builtin_amdgcn_perm(r[1], r[0], s[0]), __builtin_amdgcn_perm(r[1], r[0], s[1]), __builtin_amdgcn_perm(r[1], r[0], s[2]), __builtin_amdgcn_perm(r[1], r[0], s[3])}
#define FIRST2(dst) do { if (hf) { const u2 r_ = QFR(0, 2); dst = QBUILD(r_, sel2); } else dst = ld_k(ka - 320); } while (0)
    f4 acc[8];
#pragma unroll
    for (int i = 0; i < 8; i++) acc[i] = Z;
    {
        u4 A[2], T[2], B[2][4];
        u2 R[8];
#pragma unroll
        for (int p = 0; p < 4; p++) B[0][p] = UFR(0, p);
        A[0] = KFR(0, 0); A[1] = KFR(0, 1);
#pragma unroll
        for (int p = 3; p < 8; p++) R[p] = QFR(0, p);
        FIRST2(T[0]);
#pragma unroll
        for (int rho = 0; rho < 4; rho++)
#pragma unroll
            for (int p = 0; p < 8; p++) {
                SB;
                if (p == 0) { if (rho == 0) PRIO(3); else if (rho == 1) PRIO(2); else if (rho == 2) PRIO(1); else PRIO(0); }
                if (rho < 3 && p < 4) B[(rho + 1) & 1][p] = UFR(rho + 1, p);
                if (p >= 1 && p < 7 && !(p == 1 && rho == 0)) T[(p + 1) & 1] = QBUILD(R[p + 1], QSEL(rho, p + 1));
#pragma unroll
                for (int q = 0; q < 4; q++)
                    if (p + q < 8) acc[p + q] = mfma(p < 2 ? A[p] : T[p & 1], B[rho & 1][q], acc[p + q]);
                SB;
                if (rho < 3) {
                    if (p < 2) A[p] = KFR(rho + 1, p);
                    if (p >= 1 && p < 7) R[p + 1] = QFR(rho + 1, p + 1);
                }
            }
    }
#pragma unroll
    for (int i = 0; i < 8; i++) pin(acc[i]);
    STAMP(4);
#define EXCHANGE(t0, off)                                                                     \
    {                                                                                         \
        const int mine = LA_G + wv * 4608 + (off), other = LA_G + (wv ^ 1) * 4608 + (off);     \
        if (hf) { lds_st<f4>(mine + lane * 16, acc[t0]); lds_st<f4>(mine + 1024 + lane * 16, acc[t0 + 1]); acc[t0] = acc[t0 + 2]; acc[t0 + 1] = acc[t0 + 3]; } \
        else { lds_st<f4>(mine + lane * 16, acc[t0 + 2]); lds_st<f4>(mine + 1024 + lane * 16, acc[t0 + 3]); } \
        bar();                                                                                \
        const f4 y0 = lds_ld<f4>(other + lane * 16) + acc[t0], y1 = lds_ld<f4>(other + 1024 + lane * 16) + acc[t0 + 1]; \
        lds_st<u2>(other + lane * 8, u2{pk(y0[0], y0[1]), pk(y0[2], y0[3])});                 \
        lds_st<u2>(other + 1024 + lane * 8, u2{pk(y1[0], y1[1]), pk(y1[2], y1[3])});          \
    }
    await<1>(FU2, 4);
    EXCHANGE(0, 0);
    {
        u4 A[2][2], T[2], B[2][4];
        u2 R[4];
#pragma unroll
        for (int p = 0; p < 4; p++) B[0][p] = UFR(0, 4 + p);
        A[0][0] = KFR(0, 0); A[0][1] = KFR(0, 1); R[3] = QFR(0, 3);
        FIRST2(T[0]);
        bar();
        STAMP(14);
#pragma unroll
        for (int rho = 0; rho < 4; rho++)
#pragma unroll
            for (int p = 0; p < 4; p++) {
                SB;
                if (p == 0) { if (rho == 0) PRIO(3); else if (rho == 1) PRIO(2); else if (rho == 2) PRIO(1); else PRIO(0); }
                if (rho < 3) { B[(rho + 1) & 1][p] = UFR(rho + 1, 4 + p); if (p < 2) A[(rho + 1) & 1][p] = KFR(rho + 1, p); }
                if (p == 0 && rho > 0) T[0] = QBUILD(R[2], sel2);
                if (p == 1) T[1] = QBUILD(R[3], sel2);
#pragma unroll
                for (int q = 0; p + q < 4; q++) acc[4 + p + q] = mfma(p < 2 ? A[rho & 1][p] : T[p & 1], B[rho & 1][q], acc[4 + p + q]);
                SB;
                if (rho < 3 && p < 2) R[2 + p] = QFR(rho + 1, 2 + p);
            }
        SB;
    }
#pragma unroll
    for (int i = 4; i < 8; i++) pin(acc[i]);
    STAMP(5);
    EXCHANGE(4, 2048);
    bar();
    STAMP(6);
    STAMP_OUT(0);
}

__global__ void __launch_bounds__(NT) fused_kernel(const u16* __restrict__ u, const u16* __restrict__ lar, const u16* __restrict__ aimg,
                                                   const u16* __restrict__ cre, const u16* __restrict__ cim, const u16* __restrict__ ldt,
                                                   const u16* __restrict__ dsk, const u16* __restrict__ x, const u16* __restrict__ wr,
                                                   const u16* __restrict__ wi, u16* __restrict__ out, char* ws) {
    const char* entry;
    asm volatile("s_getpc_b64 %0" : "=s"(entry));
    APar pr;
    if (blockIdx.x >= A0 && __builtin_amdgcn_readfirstlane(threadIdx.x) < 768) {
        const int a = blockIdx.x - A0, d = 6 * ((a & 7) * 12 + (a >> 3)) + __builtin_amdgcn_readfirstlane(threadIdx.x >> 7);
        asm volatile("global_load_ushort %0, %6, %8\n\tglobal_load_ushort %1, %6, %9\n\tglobal_load_ushort %2, %6, %10\n\t"
                     "global_load_ushort %3, %6, %11\n\tglobal_load_ushort %4, %7, %12\n\tglobal_load_ushort %5, %7, %13"
                     : "=&v"(pr.l), "=&v"(pr.a), "=&v"(pr.r), "=&v"(pr.i), "=&v"(pr.t), "=&v"(pr.d)
                     : "v"((d * NS + (threadIdx.x & 127)) * 2), "v"(d * 2), "s"(lar), "s"(aimg), "s"(cre), "s"(cim), "s"(ldt), "s"(dsk) : "memory");
        asm volatile("global_load_dwordx4 %0, %4, %5\n\tglobal_load_dwordx4 %1, %4, %5 offset:1024\n\t"
                     "global_load_dwordx4 %2, %4, %5 offset:2048\n\tglobal_load_dwordx4 %3, %4, %5 offset:3072"
                     : "=&v"(pr.sel2), "=&v"(pr.sel3), "=&v"(pr.selx), "=&v"(pr.tadr)
                     : "v"((threadIdx.x & 63) * 16 + (threadIdx.x & 64) * 64), "s"((const char*)&TAB + F_END * 1024) : "memory");
    }
    if (__builtin_expect(blockIdx.x < NB2, 1)) head_b(entry, x, wr, wi, out, ws);
    else if (blockIdx.x >= A0) head_a(u, pr, out, ws);
    else warm_code(entry);
}

extern "C" void kernel_launch(void* const* d_in, const int* in_sizes, int n_in, void* d_out, int out_size, void* d_ws,
                              size_t ws_size, hipStream_t stream) {
    (void)hipFuncSetAttribute((const void*)fused_kernel, hipFuncAttributeMaxDynamicSharedMemorySize, LDS_BYTES);
    fused_kernel<<<GRID, NT, LDS_BYTES, stream>>>((const u16*)d_in[0], (const u16*)d_in[1], (const u16*)d_in[2], (const u16*)d_in[3],
                                                  (const u16*)d_in[4], (const u16*)d_in[5], (const u16*)d_in[6], (const u16*)d_in[7],
                                                  (const u16*)d_in[8], (const u16*)d_in[9], (u16*)d_out, (char*)d_ws);
}
```

```cpp
#define POLL_SLEEP 6
#include <hip/hip_runtime.h>

typedef unsigned short u16;
typedef unsigned int u32;
typedef float f2 __attribute__((ext_vector_type(2)));
typedef float f4 __attribute__((ext_vector_type(4)));
typedef u32 u2 __attribute__((ext_vector_type(2)));
typedef u32 u4 __attribute__((ext_vector_type(4)));
typedef int i8v __attribute__((ext_vector_type(8)));
typedef short s4 __attribute__((ext_vector_type(4)));
typedef short s8 __attribute__((ext_vector_type(8)));
typedef __bf16 bf2 __attribute__((ext_vector_type(2)));
#define DI __device__ __forceinline__
#define LAS __attribute__((address_space(3)))
#define GAS __attribute__((address_space(1)))
#define CAS __attribute__((address_space(4)))
#pragma clang diagnostic ignored "-Wint-to-pointer-cast"
struct u3 { u32 v[3]; DI u32& operator[](int i) { return v[i]; } DI const u32& operator[](int i) const { return v[i]; } };

constexpr int SEQ = 2048, DM = 576, NS = 128, DF = 289;
constexpr int NB1 = 128;
constexpr int NB2 = 145;
constexpr int A0 = 152;
constexpr int NA = 96;
constexpr int GRID = A0 + NA;
constexpr int NT = 1024;

constexpr u32 WS_EPOCH = 0, WS_F1 = 4096, WS_F2 = WS_F1 + 16 * 16 * 128, WS_X1 = 1u << 20, WS_Y1 = 4u << 20, WS_USED = 8u << 20;
#define STAMP(i)
#define STAMP_DECL
#define STAMP_OUT(T)

constexpr int F_SA = 0, F_EO = 4;
constexpr int F_ARE = 9, F_AIM = 10, F_BRE = 11, F_BIM = 12, F_G = 13;
constexpr int F_F16 = 21, F_GI = 23, F_AI = 31, F_M1C = 33;
constexpr int F_VAR = 37;
constexpr int F_END = F_VAR + 40;
constexpr int ET = 452, ER = 28, E2V = 580, E2U = 36, OP = 1168;
constexpr int LB_X = 0, LB_I = 0, LB_Y = 0;
constexpr int LB_E = 16 * OP;
constexpr int LB_O = LB_E, LB_E2 = LB_E;
constexpr int LB_S = LB_E + 16 * ET * 4;
constexpr int LB_W = LB_S + 1200 * 16;
constexpr int LB_T = LB_W + 2 * 256 * 36;
constexpr int LB_G = LB_T + (F_VAR + 16) * 1024;
constexpr int LB_END = LB_G + 16;
struct CosTab { double c[2048]; double c9[9], s9[9]; };
constexpr double tay_cos(double x) { double t = 1, s = 1; for (int k = 1; k <= 14; k++) { t *= -x * x / ((2 * k - 1) * (2 * k)); s += t; } return s; }
constexpr double tay_sin(double x) { double t = x, s = x; for (int k = 1; k <= 14; k++) { t *= -x * x / ((2 * k + 1) * (2 * k)); s += t; } return s; }
constexpr CosTab make_cos() {
    CosTab t = {};
    const double PI2 = 6.283185307179586476925;
    for (int k = 0; k <= 512; k++) {
        const double v = k == 512 ? 0.0 : (k <= 256 ? tay_cos(PI2 * k / 2048) : tay_sin(PI2 * (512 - k) / 2048));
        t.c[k] = v; t.c[(2048 - k) & 2047] = v; t.c[1024 - k] = -v; t.c[(1024 + k) & 2047] = -v;
    }
    t.c[512] = 0.0; t.c[1536] = 0.0;
    for (int k = 0; k < 9; k++) { const double a = PI2 * (k <= 4 ? k : k - 9) / 9; t.c9[k] = tay_cos(a); t.s9[k] = tay_sin(a); }
    return t;
}
constexpr CosTab CT = make_cos();
constexpr double cs(int n) { return CT.c[n & 2047]; }
constexpr double sn(int n) { return CT.c[(n - 512) & 2047]; }
constexpr u32 bfr(double x) { const u32 b = __builtin_bit_cast(u32, (float)x); return (b + 0x7fffu + ((b >> 16) & 1u)) >> 16; }
constexpr u32 pkc(double lo, double hi) { return bfr(lo) | (bfr(hi) << 16); }
constexpr u32 f32c(double x) { return __builtin_bit_cast(u32, (float)x); }
template <int N> struct Frags { u32 w[N * 256]; };
constexpr u32 w_re(double amp, int n) { return pkc(amp * cs(n), amp * sn(n)); }
constexpr u32 w_im(double amp, int n) { return pkc(-amp * sn(n), amp * cs(n)); }
constexpr Frags<F_VAR> make_t0() {
    Frags<F_VAR> t = {};
    const double R2 = 1.4142135623730951;
    for (int l = 0; l < 64; l++)
        for (int i = 0; i < 4; i++) {
            const int c = l & 15, g = l >> 4, r = 4 * g + i, o = l * 4 + i;
            {
                const int b = c >> 2, tq = c & 3;
                const double amp = b ? 1.0 : R2;
                const int n0 = 512 * tq * g + (b ? 256 * b * (2 * i) : -256), n1 = 512 * tq * g + (b ? 256 * b * (2 * i + 1) : 256);
                t.w[F_ARE * 256 + o] = pkc(amp * cs(n0), amp * cs(n1));
                t.w[F_AIM * 256 + o] = pkc(-amp * sn(n0), -amp * sn(n1));
            }
            for (int w = 0; w < 9; w++) {
                const int sh = 16 * (i & 1), oo = w * 128 + l * 2 + (i >> 1);
                t.w[F_EO * 256 + oo] |= (u32)((c * ET + r * ER + 2 * ((7 * w + r) % 9)) * 4) << sh;
                if (w < 8) {
                    const int k = (9 * (w & 3) + 36 * r + 64 * (c % 9)) % DM, slot = w < 4 ? (k <= 288 ? 2 * k : 2 * (DM - k) + 1) : k;
                    t.w[F_SA * 256 + oo] |= (u32)(slot * 8 + (slot >> 6) * 16) << sh;
                }
            }
            if (r < 9) { const int k = (r * c) % 9; t.w[F_BRE * 256 + o] = pkc(CT.c9[k], CT.s9[k]); t.w[F_BIM * 256 + o] = pkc(-CT.s9[k], CT.c9[k]); }
            for (int q = 0; q < 4; q++) {
                t.w[(F_G + 2 * q) * 256 + o] = w_re(1.0, 32 * r * (q + 4 * c)); t.w[(F_G + 2 * q + 1) * 256 + o] = w_im(1.0, 32 * r * (q + 4 * c));
                t.w[(F_GI + 2 * q) * 256 + o] = w_re(1.0, 32 * c * (q + 4 * r)); t.w[(F_GI + 2 * q + 1) * 256 + o] = w_im(1.0, 32 * c * (q + 4 * r));
            }
            t.w[F_F16 * 256 + o] = w_re(1.0, 128 * r * c); t.w[(F_F16 + 1) * 256 + o] = w_im(1.0, 128 * r * c);
            for (int h = 0; h < 2; h++) {
                const int j = 4 * h + (c >> 2), n = 512 * i * (c & 3) + (g ? 256 * g * j : ((j & 1) ? 256 : -256));
                t.w[(F_AI + h) * 256 + o] = w_re(g ? 2.0 : R2, n);
                const int m = 16 * r * (4 * h + 8 * c) - 512 * h;
                t.w[(F_M1C + 2 * h) * 256 + o] = pkc(0.5 * cs(m), -0.5 * sn(m));
                t.w[(F_M1C + 2 * h + 1) * 256 + o] = pkc(-0.5 * sn(m), -0.5 * cs(m));
            }
        }
    return t;
}
constexpr Frags<40> make_t1() {
    Frags<40> t = {};
    for (int v = 0; v < 5; v++)
        for (int l = 0; l < 64; l++)
            for (int i = 0; i < 4; i++) {
                const int be = v < 3 ? v + 1 : 4 * (v - 3), c = l & 15, r = 4 * (l >> 4) + i, o = v * 2048 + l * 4 + i;
                const double amp = v < 3 ? 1.0 : 0.5;
                const int n1 = 16 * r * (be + 8 * c) + (v == 4 ? 512 : 0), n4 = 16 * c * (be + 8 * r);
                t.w[o] = w_re(amp, n1); t.w[o + 256] = w_im(amp, n1);
                t.w[o + 512] = w_re(1.0, n4); t.w[o + 768] = w_im(1.0, n4);
                t.w[o + 1024] = f32c(cs(r * (be + 8 * c))); t.w[o + 1280] = f32c(-sn(r * (be + 8 * c)));
                t.w[o + 1536] = f32c(cs(c * (be + 8 * r))); t.w[o + 1792] = f32c(-sn(c * (be + 8 * r)));
            }
    return t;
}
constexpr int qidx(int l) {
    if (l <= 256) return l;
    const int sh = l < 512 ? 1 : (l < 1024 ? 2 : 3), h = 1 << (sh - 1), m = l >> sh, r = l & ((1 << sh) - 1);
    return 128 * sh + m + ((r > h || (r == h && (m & 1))) ? 1 : 0);
}
constexpr Frags<8> make_t2() {
    Frags<8> t = {};
    for (int hf = 0; hf < 2; hf++)
        for (int l = 0; l < 64; l++) {
            const int sg[3] = {17 + 4 * hf, 33 + 4 * hf, 32 + 4 * hf}, back[3] = {8, 4, 0};
            for (int k = 0; k < 3; k++) {
                const int L = 32 * sg[k] + 16 + (l & 15) - 8 * (l >> 4), q0 = qidx(L - 7);
                for (int i = 0; i < 4; i++) {
                    const u32 a = 2 * (qidx(L - 2 * i) - q0), b = 2 * (qidx(L - 2 * i - 1) - q0);
                    t.w[(4 * hf + k) * 256 + l * 4 + i] = a | ((a + 1) << 8) | (b << 16) | ((b + 1) << 24);
                }
                t.w[(4 * hf + 3) * 256 + l * 4 + k] = (u32)((q0 - back[k] - 384) * 8);
            }
            const int e = 543 - 2 * (l & 15) - (l >> 4);
            t.w[(4 * hf + 3) * 256 + l * 4 + 3] = (u32)(qidx(e) * 4) | ((u32)(qidx(e - 1) * 4) << 16);
        }
    return t;
}
struct Tables { Frags<F_VAR> a; Frags<40> b; Frags<8> c; };
constexpr Frags<8> T2 = make_t2();
constexpr Frags<F_VAR> T0 = make_t0();
constexpr Frags<40> T1 = make_t1();
static __device__ const __attribute__((aligned(128))) Tables TAB = {T0, T1, T2};
constexpr int LA_G = 0;
constexpr int LA_K = LA_G + 12 * 4608;
constexpr int KI = 1344, KC = 4 * KI + 2048;
constexpr int LA_U = LA_K + 6 * KC;
constexpr int LA_Q = LA_U + 6 * 4608;
constexpr int LA_F = LA_Q + 12 * 2576;
constexpr int LA_D = LA_F + 16;
constexpr int LA_END = LA_D + 256;
constexpr int FU0 = 0, FU2 = 1;
constexpr int LDS_BYTES = (LA_END > LB_END ? LA_END : LB_END);
static_assert(LDS_BYTES <= 160 * 1024 && LDS_BYTES > 80 * 1024, "one workgroup per CU");

DI u32 pk(float a, float b) { f2 v = {a, b}; return __builtin_bit_cast(u32, __builtin_convertvector(v, bf2)); }
DI float blo(u32 v) { return __uint_as_float(v << 16); }
DI float bhi(u32 v) { return __uint_as_float(v & 0xffff0000u); }
DI float bf2f(u16 v) { return __uint_as_float((u32)v << 16); }
DI float rbf(float x) { return blo(pk(x, x)); }
DI f2 cmul(f2 a, f2 b) { f2 t = a.xx * b; f2 s = {-b.y, b.x}; return __builtin_elementwise_fma(a.yy, s, t); }
DI f4 mfma(u4 a, u4 b, f4 c) { return __builtin_amdgcn_mfma_f32_16x16x32_bf16(__builtin_bit_cast(s8, a), __builtin_bit_cast(s8, b), c, 0, 0, 0); }
DI u4 pack4(f4 re, f4 im) { u4 r = {pk(re[0], im[0]), pk(re[1], im[1]), pk(re[2], im[2]), pk(re[3], im[3])}; return r; }
DI u4 twid(f4 re, f4 im, f4 wr, f4 wi) { return pack4(__builtin_elementwise_fma(re, wr, -(im * wi)), __builtin_elementwise_fma(re, wi, im * wr)); }
DI int mod9(int x) { return x - 9 * ((x * 57) >> 9); }
template <class T> DI void pin(T& v) { asm volatile("" : "+v"(v)); }

template <class T> DI T lds_ld(int off) { return *(const LAS T*)(u32)off; }
template <class T> DI void lds_st(int off, T v) { *(LAS T*)(u32)off = v; }
DI void bar() { __syncthreads(); }
#define PRIO(n) __builtin_amdgcn_s_setprio(n)
#define PRIOB(n)
DI void glds16(const void* g, u32 off, int lds) {
    u32 keep;
    asm volatile("s_mov_b32 %0, m0\n\ts_mov_b32 m0, %3\n\ts_nop 4\n\tglobal_load_lds_dwordx4 %1, %2\n\ts_mov_b32 m0, %0" : "=&s"(keep) : "v"(off), "s"(g), "s"(lds) : "memory");
}

DI void publish_tag(u32 epoch) {
    u32 t;
    asm volatile("v_add_u32 %0, 0x9E3779B1, %1\n\tds_write_b32 %2, %0\n\ts_waitcnt lgkmcnt(0)" : "=&v"(t) : "v"(epoch), "v"(LB_G) : "memory");
}
DI void wait_flag(__amdgpu_buffer_rsrc_t r, u32 off, u32 tag, bool on) {
    if (on) {
        const unsigned long long t0 = __builtin_amdgcn_s_memrealtime();
        while (!__all(__builtin_amdgcn_raw_buffer_load_b32(r, off, 0, 16) == tag)) {
            if (__builtin_amdgcn_s_memrealtime() - t0 > 5000000ull) break;
            __builtin_amdgcn_s_sleep(POLL_SLEEP);
            asm volatile("" ::: "memory");
        }
    }
    asm volatile("" ::: "memory");
}

constexpr int CODE_LINES = 128;
DI void warm_code(const char* entry) {
    if (threadIdx.x < CODE_LINES) { const u32 t = *(const GAS u32*)(entry + threadIdx.x * 128); asm volatile("" :: "v"(t)); }
}

DI void head_b(const char* entry, const u16* __restrict__ x, const u16* __restrict__ wr, const u16* __restrict__ wi,
               u16* __restrict__ out, char* ws) {
    const int tid = threadIdx.x, lane = tid & 63, c16 = lane & 15, g = lane >> 4;
    const int wv = __builtin_amdgcn_readfirstlane(tid >> 6);
    const int id = blockIdx.x;
    STAMP_DECL;
    const bool rows = id < NB1;
    const int s00 = 32 * (id >> 4) + (id & 15);
    const char* tab = (const char*)&TAB;
    const int R = (tid * 911) >> 16;
    const u32 xoff = (s00 + 16 * (R >> 3) + 256 * (R & 7)) * (2 * DM) + (tid - R * 72) * 16;
    asm volatile("" :: "s"(x), "s"(wr), "s"(wi), "s"(out), "s"(ws), "v"(xoff));
    const __amdgpu_buffer_rsrc_t rws = __builtin_amdgcn_make_buffer_rsrc(ws, 0, WS_USED, 0x00020000);
    STAMP(13);
    u32 epoch = 0;
    if (wv == 0) asm volatile("global_load_dword %0, %0, %1 sc1" : "+v"(epoch) : "s"(ws) : "memory");
    if (rows) {
        glds16(x, xoff, LB_X + wv * 1024);
        if (wv < 2) glds16(x, (s00 + 16 + 1536) * (2 * DM) + (tid < 56 ? (tid + 16) * 16 : 256 * 2 * DM + (tid - 56) * 16), LB_X + 16384 + wv * 1024);
        else glds16(tab, tid * 16 - 2048, LB_T + (wv - 2) * 1024);
        if (wv >= 9) glds16(tab, tid * 16 + 5 * 1024, LB_T + (wv + 5) * 1024);
    }
    const int u2i = (id & 7) ? (id & 7) * 18 + 1 + (id >> 3) : (id == 136 ? 0 : (id >> 3) + (id < 136 ? 1 : 0));
    const bool star = u2i >= 108;
    const int q36 = min((u2i * 57) >> 11, 3), kg = u2i - 36 * q36, bq = (q36 + 1) & 3;
    const bool mir = !star && kg >= 18;
    const int w0 = star ? min(16 * kg, 562) : (mir ? (2048 - bq) * (2 * DF) + 1122 - 32 * kg : bq * (2 * DF) + 32 * kg);
    __builtin_amdgcn_s_barrier();
    STAMP(14);
    const u32 woff = w0 + ((tid >> 1) & 255) * (mir ? -16 * DF : 16 * DF) + (tid & 1) * (star ? 8 * DF : 16);
    u4 wra, wrb;
    if (wv >= 8) {
        __builtin_memcpy(&wra, (const char*)wr + woff, 16);
        __builtin_memcpy(&wrb, (const char*)wi + woff, 16);
    }
    __builtin_amdgcn_sched_barrier(0);
    const f4 Z = {0.f, 0.f, 0.f, 0.f};
    const int tau = wv & 3, bw = wv >> 2;
    int tl = LB_T + lane * 16;
    int lin = LB_S + (tid + (tid >> 5)) * 16;
    pin(tl); pin(lin);
    int sadr[4];

    if (rows) {
        if (tid < 256) {
            const int o = LB_E + 72 + (tid & 15) * (ER * 4) + (tid >> 4) * (ET * 4);
            lds_st<u2>(o, u2{0u, 0u}); lds_st<u4>(o + 8, u4{0u, 0u, 0u, 0u});
        }
        int xo = LB_X + (c16 >> 2) * 1152 + 2 * (144 * ((g - wv) & 3) + 16 * wv + 4 * (c16 & 3));
        int bo = LB_E + (wv * ET + c16 * ER + min(8 * g, 16)) * 4;
        int go = WS_X1 + (tid >> 3) * 16384 + id * 128 + (tid & 7) * 16;
        int ta = LB_T + lane * 8;
        pin(xo); pin(bo); pin(go); pin(ta);
        STAMP(1);
        asm volatile("s_cmp_lt_u32 %0, 8\n\ts_cbranch_scc0 .Lw%=\n\ts_waitcnt vmcnt(0)\n.Lw%=:\n\ts_waitcnt vmcnt(2)" :: "s"(wv) : "memory", "scc");
        if (wv == 0) publish_tag(epoch);
        bar();
        STAMP(2);
        if (wv < 9) {
            u4 d[2];
#pragma unroll
            for (int s = 0; s < 2; s++) {
                const s4 lo = __builtin_amdgcn_ds_read_tr16_b64_v4i16((LAS s4*)(u32)(xo + s * 9216));
                const s4 hi = __builtin_amdgcn_ds_read_tr16_b64_v4i16((LAS s4*)(u32)(xo + s * 9216 + 4608));
                d[s] = __builtin_bit_cast(u4, __builtin_shufflevector(lo, hi, 0, 1, 2, 3, 4, 5, 6, 7));
            }
            const u4 are = lds_ld<u4>(tl + F_ARE * 1024), aim = lds_ld<u4>(tl + F_AIM * 1024);
            const u2 eo = lds_ld<u2>(ta + F_EO * 1024 + wv * 512);
#pragma unroll
            for (int s = 0; s < 2; s++) d[s] = pack4(mfma(d[s], are, Z), mfma(d[s], aim, Z));
#pragma unroll
            for (int i = 0; i < 4; i++) lds_st<u2>(LB_E + ((i & 1) ? eo[i >> 1] >> 16 : eo[i >> 1] & 0xffff), u2{d[0][i], d[1][i]});
        }
        u4 bre = lds_ld<u4>(tl + F_BRE * 1024), bim = lds_ld<u4>(tl + F_BIM * 1024);
        u4 gre = lds_ld<u4>(tl + (F_G + 2 * tau) * 1024), gim = lds_ld<u4>(tl + (F_G + 2 * tau + 1) * 1024);
        {
            const u2 sa = lds_ld<u2>(ta + F_SA * 1024 + (tau + (bw ? 4 : 0)) * 512);
            const int base = LB_S + ((bw + 3) & 3) * (DM * 8 + 144);
#pragma unroll
            for (int i = 0; i < 4; i++) { sadr[i] = base + ((i & 1) ? sa[i >> 1] >> 16 : sa[i >> 1] & 0xffff); pin(sadr[i]); }
        }
        pin(bre); pin(bim); pin(gre); pin(gim);
        bar();
        {
            PRIOB(2);
            const u4 p0 = lds_ld<u4>(bo), p1 = lds_ld<u4>(bo + 16);
            u4 d[2];
            f4 re[2], im[2];
#pragma unroll
            for (int s = 0; s < 2; s++) { const u4 a = {p0[s], p0[2 + s], p1[s], p1[2 + s]}; re[s] = mfma(a, bre, Z); im[s] = mfma(a, bim, Z); }
            PRIOB(1);
#pragma unroll
            for (int s = 0; s < 2; s++) { const u4 t = pack4(re[s], im[s]); re[s] = mfma(gre, t, Z); im[s] = mfma(gim, t, Z); }
            PRIOB(0);
#pragma unroll
            for (int s = 0; s < 2; s++) d[s] = pack4(re[s], im[s]);
            if (c16 < 9) {
#pragma unroll
                for (int i = 0; i < 4; i++) lds_st<u2>(sadr[i], u2{d[0][i], d[1][i]});
                if (wv == 0 && c16 == 0 && !(g & 1)) lds_st<u2>(sadr[0] + 8, u2{d[0][0], d[1][0]});
            }
        }
        bar();
        STAMP(3);
        __builtin_amdgcn_raw_buffer_store_b128(lds_ld<u4>(lin), rws, go, 0, 16);
        if (tid <= 128) __builtin_amdgcn_raw_buffer_store_b128(lds_ld<u4>(lin + 1056 * 16), rws, go, 128 * 16384, 16);
    } else {
        if (wv < 8 && (kg == 0 || kg == (star ? 36 : 18))) {
            const int m = tid & 255;
            lds_st<u16>(LB_W + tid * 36 + 32, ((wv >> 2) ? wi : wr)[((mir ? bq + 8 * m : 2048 - bq - 8 * m) & 2047) * DF + (kg ? 288 : 0)]);
        }
        warm_code(entry);
        asm volatile("s_waitcnt vmcnt(0)" ::: "memory");
        if (wv == 0) publish_tag(epoch);
        bar();
    }
    const u32 tag = lds_ld<u32>(LB_G);
    if (wv >= 8) {
        const int o = LB_W + ((tid >> 1) & 255) * 36 + (tid & 1) * 16;
#pragma unroll
        for (int i = 0; i < 4; i++) { lds_st<u32>(o + 4 * i, wra[i]); lds_st<u32>(o + 9216 + 4 * i, wrb[i]); }
    }
    asm volatile("s_waitcnt vmcnt(0)" ::: "memory");
    if (rows && lane == 0) __builtin_amdgcn_raw_buffer_store_b32(tag, rws, WS_F1 + (wv * 16 + (id >> 3)) * 128 + (id & 7) * 4, 0, 16);
    STAMP(4);
    glds16(tab, F_F16 * 1024 + tid * 16, LB_T + F_F16 * 1024 + wv * 1024);
    if (star || wv < 8) glds16(tab, (F_VAR + 8 * q36) * 1024 + tid * 16, LB_T + F_VAR * 1024 + wv * 1024);
    asm volatile("s_waitcnt vmcnt(0)" ::: "memory");
    bar();
    STAMP(12);

    const int typ = star ? (wv & 1) : 0;
    const int tv = tl + (F_VAR + 8 * typ) * 1024;
    u4 fre = lds_ld<u4>(tl + F_F16 * 1024), fim = lds_ld<u4>(tl + (F_F16 + 1) * 1024);
    u4 m1r = lds_ld<u4>(tv), m1i = lds_ld<u4>(tv + 1024), m4r = lds_ld<u4>(tv + 2048), m4i = lds_ld<u4>(tv + 3072);
    f4 twr = lds_ld<f4>(tv + 4096), twi = lds_ld<f4>(tv + 5120), tur = lds_ld<f4>(tv + 6144), tui = lds_ld<f4>(tv + 7168);
    u4 mcr = lds_ld<u4>(tl + (F_M1C + 2 * typ) * 1024), mci = lds_ld<u4>(tl + (F_M1C + 2 * typ + 1) * 1024);
    f4 wfr, wfi;
    {
        const int kcol = star ? 8 * kg + (wv >> 1) : 16 * kg + wv;
        const int cc = star ? 8 * typ + (u2i < NB2 - 1 ? (wv >> 1) : 7) : (mir ? 15 - wv : wv);
        const int wo = LB_W + (c16 + 64 * g) * 36 + cc * 2;
#pragma unroll
        for (int i = 0; i < 4; i++) {
            wfr[i] = bf2f(lds_ld<u16>(wo + i * 576));
            wfi[i] = bf2f(lds_ld<u16>(wo + i * 576 + 9216));
        }
        float sc = 1.f / (float)(SEQ * DM);
        if (kcol == 0 || kcol == 288) {
            sc *= 0.5f;
#pragma unroll
            for (int i = 0; i < 4; i++) {
                const int m = c16 + 64 * g + 16 * i;
                const int o = LB_W + (typ ? (255 - m) * 36 + cc * 2 : m * 36 + 32);
                wfr[i] += bf2f(lds_ld<u16>(o));
                wfi[i] -= bf2f(lds_ld<u16>(o + 9216));
            }
        }
        wfr *= sc; wfi *= (mir ? -sc : sc);
    }
    f4 wnr = -wfr, wni = -wfi;
    int io = (32 * g + c16) * 128 + (((wv >> 1) ^ (c16 >> 1)) * 16);
    int so = (tid >> 3) * 128 + (((tid & 7) ^ ((tid >> 4) & 7)) * 16);
    pin(fre); pin(fim); pin(m1r); pin(m1i); pin(m4r); pin(m4i); pin(twr); pin(twi); pin(tur); pin(tui); pin(mcr); pin(mci);
    int gi = WS_X1 + u2i * 16384 + tid * 16, gq = WS_Y1 + (tid >> 3) * (NB2 * 128) + u2i * 128 + (tid & 7) * 16;
    int i1 = LB_I + io + (star ? 0 : (wv & 1) * 8);
    io += LB_O + (wv & 1) * 8;
    pin(wfr); pin(wnr); pin(wni); pin(io); pin(so); pin(gi); pin(gq); pin(i1);
    wait_flag(rws, WS_F1 + (((u2i >> 3) & 15) * 16 + wv) * 128 + lane * 4, tag, lane < 8);
    STAMP(5);
    lds_st<u4>(LB_I + so, __builtin_amdgcn_raw_buffer_load_b128(rws, gi, 0, 16));
    bar();
    STAMP(6);
    {
        const int o1 = i1;
        PRIOB(3);
        const u2 a0 = lds_ld<u2>(o1), a1 = lds_ld<u2>(o1 + 2048);
        const u4 a = {a0[0], a0[1], a1[0], a1[1]};
        f4 re = mfma(a, m1r, Z), im = mfma(a, m1i, Z);
        if (star) {
            const u2 b0 = lds_ld<u2>(o1 + 8), b1 = lds_ld<u2>(o1 + 8 + 2048);
            const u4 b = {b0[0], b0[1], b1[0], b1[1]};
            re = mfma(b, mcr, re); im = mfma(b, mci, im);
        }
        u4 t = twid(re, im, twr, twi);
        re = mfma(fre, t, Z); im = mfma(fim, t, Z);
        PRIOB(2);
        t = pack4(__builtin_elementwise_fma(re, wfr, im * wni), __builtin_elementwise_fma(re, wni, im * wnr));
        re = mfma(t, fre, Z); im = mfma(t, fim, Z);
        PRIOB(1);
        t = twid(re, im, tur, tui);
        re = mfma(m4r, t, Z); im = mfma(m4i, t, Z);
        PRIOB(0);
        t = pack4(re, im);
        lds_st<u2>(io, u2{t[0], t[1]}); lds_st<u2>(io + 2048, u2{t[2], t[3]});
    }
    bar();
    STAMP(7);
    {
        u4 v = lds_ld<u4>(LB_O + so);
        if (star) {
            u4 w;
#pragma unroll
            for (int s = 0; s < 2; s++) {
                const float ar = blo(v[s]), ai = bhi(v[s]), cr = blo(v[2 + s]), ci = bhi(v[2 + s]);
                w[s] = pk(ar + ci, ai - cr); w[2 + s] = pk(ar - ci, -ai - cr);
            }
            v = w;
        }
        if (u2i < NB2 - 1 || (tid & 7) == 0)
            __builtin_amdgcn_raw_buffer_store_b128(v, rws, gq, 0, 16);
    }
    asm volatile("s_waitcnt vmcnt(0)" ::: "memory");
    if (lane == 0) __builtin_amdgcn_raw_buffer_store_b32(tag, rws, WS_F2 + (wv * 19 + (u2i >> 3)) * 128 + (u2i & 7) * 4, 0, 16);
    STAMP(8);
    if (!rows) { STAMP_OUT(0); return; }

    u4 gre = lds_ld<u4>(tl + (F_GI + 2 * tau) * 1024), gim = lds_ld<u4>(tl + (F_GI + 2 * tau + 1) * 1024);
    u4 bre = lds_ld<u4>(tl + F_BRE * 1024), bim = lds_ld<u4>(tl + F_BIM * 1024);
    u4 ai0 = lds_ld<u4>(tl + F_AI * 1024), ai1 = lds_ld<u4>(tl + (F_AI + 1) * 1024);
    int eadr[4];
#pragma unroll
    for (int i = 0; i < 4; i++) {
        const int r = 4 * g + i;
        eadr[i] = LB_E2 + (mod9(4 * (c16 + 18 - r)) * E2V + r * E2U + 2 * wv) * 4;
        pin(eadr[i]);
    }
    int ao = LB_E2 + (wv * E2V + c16 * E2U + 8 * g) * 4;
    int gy = WS_Y1 + id * (NB2 * 128) + tid * 16;
    int yo = LB_Y + (c16 >> 2) * OP + 2 * (144 * (((c16 & 3) - wv) & 3) + 16 * wv + 4 * g);
    int yl = LB_Y + tid * 16 + R * (OP - 1152);
    u32 oo = 2 * xoff - (tid - R * 72) * 16 + 2 * DM;
    pin(gre); pin(gim); pin(bre); pin(bim); pin(ai0); pin(ai1); pin(ao); pin(gy); pin(yo); pin(yl); pin(oo);
    {
        const int un = 8 * wv + lane + (lane >= 8 ? 120 : 0);
        wait_flag(rws, WS_F2 + ((id >> 3) * 19 + (un >> 3)) * 128 + (lane & 7) * 4, tag, lane < 16 && un < NB2);
    }
    STAMP(9);
    {
        const u4 v = __builtin_amdgcn_raw_buffer_load_b128(rws, gy, 0, 16);
        if (tid <= 128) lds_st<u4>(lin + 1056 * 16, __builtin_amdgcn_raw_buffer_load_b128(rws, gy, 16384, 16));
        lds_st<u4>(lin, v);
    }
    bar();
    if (id == 0 && tid == 0) __builtin_amdgcn_raw_buffer_store_b32(tag, rws, WS_EPOCH, 0, 16);
    STAMP(10);
    {
        PRIOB(2);
        u4 d[2];
        u2 p[4];
#pragma unroll
        for (int i = 0; i < 4; i++) p[i] = lds_ld<u2>(sadr[i]);
        f4 re[2], im[2];
#pragma unroll
        for (int s = 0; s < 2; s++) { const u4 a = {p[0][s], p[1][s], p[2][s], p[3][s]}; re[s] = mfma(a, gre, Z); im[s] = mfma(a, gim, Z); }
        PRIOB(1);
#pragma unroll
        for (int s = 0; s < 2; s++) { const u4 t = pack4(re[s], im[s]); re[s] = mfma(t, bre, Z); im[s] = mfma(t, bim, Z); }
        PRIOB(0);
#pragma unroll
        for (int s = 0; s < 2; s++) d[s] = pack4(re[s], im[s]);
        if (c16 < 9) {
#pragma unroll
            for (int i = 0; i < 4; i++) lds_st<u2>(eadr[i], u2{d[0][i], d[1][i]});
        }
    }
    STAMP(15);
    bar();
    if (wv < 9) {
        const u4 p0 = lds_ld<u4>(ao), p1 = lds_ld<u4>(ao + 16);
#pragma unroll
        for (int s = 0; s < 2; s++) {
            const u4 a = {p0[s], p0[2 + s], p1[s], p1[2 + s]};
            const f4 y0 = mfma(a, ai0, Z), y1 = mfma(a, ai1, Z);
            lds_st<u2>(yo + s * (8 * OP), u2{pk(y0[0], y0[1]), pk(y0[2], y0[3])});
            lds_st<u2>(yo + s * (8 * OP) + 4 * OP, u2{pk(y1[0], y1[1]), pk(y1[2], y1[3])});
        }
    }
    bar();
    *(GAS u4*)((char*)out + oo) = lds_ld<u4>(yl);
    if (wv < 2) {
        const int r2 = tid < 56 ? 14 : 15, c2 = tid + 1024 - 72 * r2;
        *(GAS u4*)((char*)out + (u32)((s00 + 16 + 256 * (r2 - 8)) * (4 * DM) + 2 * DM + c2 * 16)) = lds_ld<u4>(LB_Y + r2 * OP + c2 * 16);
    }
    STAMP(11);
    STAMP_OUT(0);
}

DI f2 cexpa(float ar2, float air, float p) {
    const float e = __builtin_amdgcn_exp2f(ar2 * p), t = __builtin_amdgcn_fractf(air * p);
    return f2{e * __builtin_amdgcn_cosf(t), e * __builtin_amdgcn_sinf(t)};
}
DI u4 ld_k(int o) {
    const u2 a = *(const volatile LAS u2*)(u32)o, b = *(const volatile LAS u2*)(u32)(o + 8);
    return u4{a[0], a[1], b[0], b[1]};
}
DI void a_rows_out(int yb, int ystr, char* o) {
    u2 y[6];
#pragma unroll
    for (int c = 0; c < 6; c++) y[c] = lds_ld<u2>(yb + c * ystr);
#pragma unroll
    for (int r = 0; r < 4; r++) {
        u3 v;
#pragma unroll
        for (int c = 0; c < 3; c++) v[c] = __builtin_amdgcn_perm(y[2 * c + 1][r >> 1], y[2 * c][r >> 1], (r & 1) ? 0x07060302u : 0x05040100u);
        *(u3*)(o + r * (4 * DM)) = v;
    }
}
template <int R> DI void stage(u4 v) {
    asm volatile("ds_write_addtid_b32 %0 offset:%4\n\tds_write_addtid_b32 %1 offset:%5\n\tds_write_addtid_b32 %2 offset:%6\n\tds_write_addtid_b32 %3 offset:%7"
                 :: "v"(v[0]), "v"(v[1]), "v"(v[2]), "v"(v[3]), "i"(288 * R), "i"(288 * R + 288), "i"(288 * R + 576), "i"(288 * R + 864) : "memory");
}
#define CM_MUL(d, a, b) "v_pk_mul_f32 " d ", " a ", " b " op_sel_hi:[0,1]\n\t"
#define CM_FMA(d, a, b) "v_pk_fma_f32 " d ", " a ", " b ", " d " op_sel:[1,1,0] op_sel_hi:[1,0,1] neg_lo:[1,0,0]\n\t"
struct C4 { f2 a, b, c, d; };
DI C4 cmul4(f2 a0, f2 b0, f2 a1, f2 b1, f2 a2, f2 b2, f2 a3, f2 b3) {
    C4 r;
    asm("s_nop 0\n\t" CM_MUL("%0", "%4", "%5") CM_MUL("%1", "%6", "%7") CM_MUL("%2", "%8", "%9") CM_MUL("%3", "%10", "%11")
        CM_FMA("%0", "%4", "%5") CM_FMA("%1", "%6", "%7") CM_FMA("%2", "%8", "%9") CM_FMA("%3", "%10", "%11") "s_nop 0"
        : "=&v"(r.a), "=&v"(r.b), "=&v"(r.c), "=&v"(r.d) : "v"(a0), "v"(b0), "v"(a1), "v"(b1), "v"(a2), "v"(b2), "v"(a3), "v"(b3));
    return r;
}
DI C4 cmul4(const C4& a, f2 b) { return cmul4(a.a, b, a.b, b, a.c, b, a.d, b); }
DI u4 pk4(const C4& v) { return u4{pk(v.a.x, v.a.y), pk(v.b.x, v.b.y), pk(v.c.x, v.c.y), pk(v.d.x, v.d.y)}; }

DI void sig(int f) {
    asm volatile("s_waitcnt lgkmcnt(0)" ::: "memory");
    if ((threadIdx.x & 63) == 0) __hip_atomic_fetch_add((LAS u32*)(u32)(LA_F + 4 * f), 1u, __ATOMIC_RELAXED, __HIP_MEMORY_SCOPE_WORKGROUP);
}
template <int SLEEP = 0> DI void await(int f, u32 n) {
    while ((u32)__builtin_amdgcn_readfirstlane(*(const volatile LAS u32*)(u32)(LA_F + 4 * f)) < n) if (SLEEP) __builtin_amdgcn_s_sleep(SLEEP);
    asm volatile("" ::: "memory");
}
DI float exp_bf(float x) {
    const float t = x * 1.44269504f, r = __builtin_fmaf(x, 1.92596299e-8f, __builtin_fmaf(x, 1.44269504f, -t));
    const float e = __builtin_amdgcn_exp2f(t);
    return __builtin_fmaf(e * 0.693147181f, r, e);
}

struct APar { u32 l, a, r, i, t, d; u4 sel2, sel3, selx, tadr; };
DI void head_a(const u16* __restrict__ u, APar pr, u16* __restrict__ out, char* ws) {
    const int tid = threadIdx.x, lane = tid & 63, c16 = lane & 15, g = lane >> 4;
    const int wv = __builtin_amdgcn_readfirstlane(tid >> 6);
    STAMP_DECL;
    const int ai_ = blockIdx.x - A0;
    const int d0 = 6 * ((ai_ & 7) * 12 + (ai_ >> 3));
    const f4 Z = {0.f, 0.f, 0.f, 0.f};

    if (wv >= 12) {
        __builtin_amdgcn_s_setprio(3);
        const int t = tid - 768;
        if (t == 0) lds_st<u2>(LA_F, u2{0u, 0u});
        if (t < 180) lds_st<u4>(LA_U + (t / 30) * 4608 + (t % 30) * 16, u4{0u, 0u, 0u, 0u});
        bar();
#pragma unroll
        for (int i = 0; i < 2; i++)
            if (i == 0 || wv < 14) {
                const int r = min((ai_ >> 3) + 12 * (128 * i + (t >> 1)), SEQ - 1);
                u32 keep;
                asm volatile("s_mov_b32 %0, m0\n\ts_mov_b32 m0, %3\n\ts_nop 4\n\tglobal_load_lds_dword %1, %2\n\ts_mov_b32 m0, %0"
                             : "=&s"(keep) : "v"(r * (2 * DM) + (((ai_ & 7) * 144) & ~127) + (t & 1) * 128), "s"(u), "s"(LA_D) : "memory");
            }
        u3 ua[4], ub[4];
#pragma unroll
        for (int r = 0; r < 4; r++) ua[r] = *(const u3*)(u + (4 * t + r) * DM + d0);
        sig(FU0);
        await<1>(FU0, 4);
#pragma unroll
        for (int r = 0; r < 4; r++) ub[r] = *(const u3*)(u + (1024 + 4 * t + r) * DM + d0);
        asm volatile("" ::: "memory");
#pragma unroll
        for (int c = 0; c < 6; c++) {
            const u32 sel = (c & 1) ? 0x07060302u : 0x05040100u;
            lds_st<u2>(LA_U + c * 4608 + 480 + t * 8, u2{__builtin_amdgcn_perm(ua[1][c >> 1], ua[0][c >> 1], sel), __builtin_amdgcn_perm(ua[3][c >> 1], ua[2][c >> 1], sel)});
        }
        STAMP(7);
        bar();
        bar();
#pragma unroll
        for (int c = 0; c < 6; c++) {
            const u32 sel = (c & 1) ? 0x07060302u : 0x05040100u;
            lds_st<u2>(LA_U + c * 4608 + 480 + 2048 + t * 8, u2{__builtin_amdgcn_perm(ub[1][c >> 1], ub[0][c >> 1], sel), __builtin_amdgcn_perm(ub[3][c >> 1], ub[2][c >> 1], sel)});
        }
        sig(FU2);
        STAMP(8);
        const int tile = wv - 12, yl = ((lane >> 2) + 16 * (lane & 3)) * 8;
        char* o = (char*)(out + 4 * t * (2 * DM) + d0);
        bar();
        bar();
        STAMP(11);
        a_rows_out(LA_G + ((tile >> 1) ^ 1) * 4608 + (tile & 1) * 1024 + yl, 9216, o);
        STAMP(9);
        bar();
        bar();
        STAMP(12);
        a_rows_out(LA_G + ((tile >> 1) ^ 1) * 4608 + (tile & 1) * 1024 + 2048 + yl, 9216, o + 1024 * (4 * DM));
        STAMP(10);
        STAMP_OUT(768);
        return;
    }
    const int ch = wv >> 1, hf = wv & 1;
    {
        __builtin_amdgcn_s_barrier();
        asm volatile("s_waitcnt vmcnt(4)" : "+v"(pr.l), "+v"(pr.a), "+v"(pr.r), "+v"(pr.i), "+v"(pr.t), "+v"(pr.d) :: "memory");
        PRIO(3);
        const float dt = rbf(exp_bf(blo(pr.t)));
        const float ar2 = -rbf(exp_bf(blo(pr.l))) * dt * 1.44269504f, air = blo(pr.a) * dt * 0.159154943f;
        const f2 C = {2.f * dt * blo(pr.r), 2.f * dt * blo(pr.i)};
        STAMP(1);
        const f2 z1 = cexpa(ar2, air, 1.f), z8c = cexpa(ar2, -air, 8.f), z128 = cexpa(ar2, air, 128.f);
        const int gr = LA_G + wv * 4608 + c16 * 288 + g * 16, q = LA_Q + wv * 2576;
        asm volatile("s_mov_b32 m0, %0\n\ts_nop 0" :: "s"(LA_G + wv * 4608) : "memory");
#define SB __builtin_amdgcn_sched_barrier(0)
#define STAGE16(v) do { stage<0>(v[0]); stage<4>(v[1]); stage<8>(v[2]); stage<12>(v[3]); } while (0)
#define READ4(f) do { _Pragma("unroll") for (int s = 0; s < 4; s++) f[s] = lds_ld<u4>(gr + 64 * s); SB; } while (0)
        u4 v[4], bfr[4], afr[4];
        const C4 s1 = cmul4(z8c, z8c, z1, z1, z128, z128, C, z1);
        const C4 s2 = cmul4(s1.a, s1.a, z8c, s1.a, s1.c, s1.c, C, s1.b);
        {
            C4 w = {f2{1.f, 0.f}, z8c, s1.a, s2.b};
            v[0] = pk4(w);
#pragma unroll
            for (int i = 1; i < 4; i++) { w = cmul4(w, s2.a); v[i] = pk4(w); }
        }
        STAGE16(v); READ4(bfr);
        PRIO(2);
        const C4 s3 = cmul4(s1.d, s1.b, s1.b, s1.b, s2.c, s2.c, s2.c, z128);
        const C4 e0 = {C, s1.d, s2.d, s3.a}, e1 = cmul4(e0, s3.b);
        v[0] = pk4(e0); v[1] = pk4(e1); v[2] = pk4(cmul4(e0, z128)); v[3] = pk4(cmul4(e1, z128));
        STAGE16(v); READ4(afr);
        PRIO(1);
        {
            const C4 f = cmul4(C4{e0.a, e0.c, e1.a, e1.c}, s1.c), h = cmul4(e0.a, s2.c, e1.a, s2.c, e0.a, s3.d, e1.a, s3.d);
            v[0] = pk4(f); v[1] = pk4(cmul4(f, z128)); v[2] = pk4(h); v[3] = pk4(cmul4(h, s1.c));
        }
        SB;
        f4 acc = Z;
#pragma unroll
        for (int s = 0; s < 4; s++) acc = mfma(afr[s], bfr[s], acc);
        STAGE16(v); READ4(afr);
        PRIO(0);
        lds_st<f4>(q + (4 * (g & 1) + 128 * (g >> 1) + 8 * c16) * 4, acc);
        {
            const f2 f0 = cmul(C, s3.c), f1 = cmul(f0, z128);
            const C4 f = cmul4(f0, s1.c, f1, s1.c, f0, s3.c, f0, s3.c), h = cmul4(C4{f0, f1, f.a, f.b}, s2.c);
            v[0] = pk4(C4{f0, f1, f.a, f.b}); v[1] = pk4(h); v[2][0] = pk(f.c.x, f.c.y);
        }
        SB;
        acc = Z;
#pragma unroll
        for (int s = 0; s < 4; s++) acc = mfma(afr[s], bfr[s], acc);
        stage<0>(v[0]); stage<4>(v[1]);
        asm volatile("ds_write_addtid_b32 %0 offset:%1" :: "v"(v[2][0]), "i"(288 * 8) : "memory");
        READ4(afr);
        if (g < 2) lds_st<f4>(q + (256 + 64 * g + 4 * c16) * 4, acc);
        else {
            const int o = q + (384 + 64 * (g - 2) + 2 * c16) * 4;
            lds_st<f2>(o, f2{acc[0], acc[1]}); lds_st<f2>(o + 128, f2{acc[2], acc[3]});
        }
        acc = Z;
#pragma unroll
        for (int s = 0; s < 4; s++) acc = mfma(afr[s], bfr[s], acc);
        if (g < 2) {
#pragma unroll
            for (int i = 0; i < 4; i++) lds_st<float>(q + (512 + 64 * g + 16 * i + c16) * 4, acc[i]);
        } else if (lane == 32) lds_st<float>(q + 640 * 4, acc[0]);
    }
    STAMP(2);
    asm volatile("s_waitcnt vmcnt(0) lgkmcnt(0)" : "+v"(pr.sel2), "+v"(pr.sel3), "+v"(pr.selx), "+v"(pr.tadr) :: "memory");
    const u4 sel2 = pr.sel2, sel3 = pr.sel3, selx = pr.selx, tadr = pr.tadr;
    bar();
    STAMP(9);
    const int k0 = LA_K + ch * KC;
    {
        const LAS float* qa = (const LAS float*)(u32)(LA_Q + ch * 5152);
        const LAS float* qb = qa + 644;
        u2 w[4];
        int y;
        if (hf == 0) {
            const int j = 384 + 4 * lane;
            const f4 a = *(const LAS f4*)(qa + j) + *(const LAS f4*)(qb + j), b = *(const LAS f4*)(qa + j + 4) + *(const LAS f4*)(qb + j + 4);
            const u32 ix = tadr[3];
            const float e0 = qa[(ix & 0xffff) >> 2] + qb[(ix & 0xffff) >> 2], e1 = qa[ix >> 18] + qb[ix >> 18];
            f4 k = *(const LAS f4*)(qa + 4 * lane) + *(const LAS f4*)(qb + 4 * lane);
            f4 p = *(const LAS f4*)(qa + 4 * lane - 4) + *(const LAS f4*)(qb + 4 * lane - 4);
            const u32 p23 = pk(a[2], a[3]), p34 = pk(a[3], b[0]);
            lds_st<u4>(k0 + 4 * KI + lane * 32, u4{pk(a[0], a[1]), p23, pk(a[1], a[2]), p34});
            lds_st<u4>(k0 + 4 * KI + lane * 32 + 16, u4{p23, pk(b[0], b[1]), p34, pk(b[1], b[2])});
            lds_st<u32>(k0 + g * KI + c16 * 4, pk(e0, e1));
            if (lane == 0) { k[0] += blo(pr.d); p = Z; }
            const u32 c = pk(k[1], k[0]), d = pk(k[0], p[3]);
            w[0] = u2{pk(k[3], k[2]), c}; w[1] = u2{pk(k[2], k[1]), d}; w[2] = u2{c, pk(p[3], p[2])}; w[3] = u2{d, pk(p[2], p[1])};
            y = k0 + (270 - 2 * lane) * 4;
        } else {
            const int j = 256 + 2 * lane;
            const float q0 = qa[j] + qb[j], q1 = qa[j + 1] + qb[j + 1], q2 = qa[j + 2] + qb[j + 2];
            const float p1 = qa[j - 1] + qb[j - 1], p2 = qa[j - 2] + qb[j - 2], p3 = qa[j - 3] + qb[j - 3];
            const u32 c = pk(q0, q0), d = pk(q0, p1), e = pk(p1, p2), f = lane ? c : d;
            w[0] = u2{pk(q2, q1), c}; w[1] = u2{pk(q1, q0), f}; w[2] = u2{c, lane ? d : e}; w[3] = u2{f, lane ? e : pk(p2, p3)};
            y = k0 + (142 - 2 * lane) * 4;
            if (lane < 32) lds_st<u32>(k0 + (lane >> 3) * KI + (272 + (lane & 7)) * 4, 0u);
        }
#pragma unroll
        for (int i = 0; i < 4; i++) lds_st<u2>(y + i * KI, w[i]);
    }
    const int x0 = 2031 - c16 + 8 * g;
    int ka = k0 + (x0 & 3) * KI + (x0 >> 2) * 8 - 3008 - 256 * hf - 192 - 512;
    int ubs = LA_U + ch * 4608 + 480 + (16 * c16 + 8 * g - 16) * 2 - 256 * hf - 192;
    int q2a = k0 + 4 * KI + tadr[0], q3a = k0 + 4 * KI + tadr[1], qxa = k0 + 4 * KI + tadr[2];
    pin(ka); pin(ubs); pin(q2a); pin(q3a); pin(qxa);
    STAMP(13);
    bar();
    STAMP(3);
#define KFR(rho, p) ld_k(ka + 192 - 64 * (rho) + 512 - 512 * (p))
#define UFR(rho, q) lds_ld<u4>(ubs + 192 - 64 * (rho) + 512 * (q))
#define QFR(rho, p) ((p) < 4 ? lds_ld<u2>(q2a + 64 * (8 * ((p) - 2) + (rho))) : ((p) == 4 && (rho) == 0 ? lds_ld<u2>(qxa) : lds_ld<u2>(q3a + 32 * (8 * ((p) - 4) + (rho)))))
#define QSEL(rho, p) ((p) < 4 ? sel2 : ((p) == 4 && (rho) == 0 ? selx : sel3))
#define QBUILD(r, s) u4{__builtin_amdgcn_perm(r[1], r[0], s[0]), __builtin_amdgcn_perm(r[1], r[0], s[1]), __builtin_amdgcn_perm(r[1], r[0], s[2]), __builtin_amdgcn_perm(r[1], r[0], s[3])}
#define FIRST2(dst) do { if (hf) { const u2 r_ = QFR(0, 2); dst = QBUILD(r_, sel2); } else dst = ld_k(ka - 320); } while (0)
    f4 acc[8];
#pragma unroll
    for (int i = 0; i < 8; i++) acc[i] = Z;
    {
        u4 A[2], T[2], B[2][4];
        u2 R[8];
#pragma unroll
        for (int p = 0; p < 4; p++) B[0][p] = UFR(0, p);
        A[0] = KFR(0, 0); A[1] = KFR(0, 1);
#pragma unroll
        for (int p = 3; p < 8; p++) R[p] = QFR(0, p);
        FIRST2(T[0]);
#pragma unroll
        for (int rho = 0; rho < 4; rho++)
#pragma unroll
            for (int p = 0; p < 8; p++) {
                SB;
                if (p == 0) { if (rho == 0) PRIO(3); else if (rho == 1) PRIO(2); else if (rho == 2) PRIO(1); else PRIO(0); }
                if (rho < 3 && p < 4) B[(rho + 1) & 1][p] = UFR(rho + 1, p);
                if (p >= 1 && p < 7 && !(p == 1 && rho == 0)) T[(p + 1) & 1] = QBUILD(R[p + 1], QSEL(rho, p + 1));
#pragma unroll
                for (int q = 0; q < 4; q++)
                    if (p + q < 8) acc[p + q] = mfma(p < 2 ? A[p] : T[p & 1], B[rho & 1][q], acc[p + q]);
                SB;
                if (rho < 3) {
                    if (p < 2) A[p] = KFR(rho + 1, p);
                    if (p >= 1 && p < 7) R[p + 1] = QFR(rho + 1, p + 1);
                }
            }
    }
#pragma unroll
    for (int i = 0; i < 8; i++) pin(acc[i]);
    STAMP(4);
#define EXCHANGE(t0, off)                                                                     \
    {                                                                                         \
        const int mine = LA_G + wv * 4608 + (off), other = LA_G + (wv ^ 1) * 4608 + (off);     \
        if (hf) { lds_st<f4>(mine + lane * 16, acc[t0]); lds_st<f4>(mine + 1024 + lane * 16, acc[t0 + 1]); acc[t0] = acc[t0 + 2]; acc[t0 + 1] = acc[t0 + 3]; } \
        else { lds_st<f4>(mine + lane * 16, acc[t0 + 2]); lds_st<f4>(mine + 1024 + lane * 16, acc[t0 + 3]); } \
        bar();                                                                                \
        const f4 y0 = lds_ld<f4>(other + lane * 16) + acc[t0], y1 = lds_ld<f4>(other + 1024 + lane * 16) + acc[t0 + 1]; \
        lds_st<u2>(other + lane * 8, u2{pk(y0[0], y0[1]), pk(y0[2], y0[3])});                 \
        lds_st<u2>(other + 1024 + lane * 8, u2{pk(y1[0], y1[1]), pk(y1[2], y1[3])});          \
    }
    await<1>(FU2, 4);
    EXCHANGE(0, 0);
    {
        u4 A[2][2], T[2], B[2][4];
        u2 R[4];
#pragma unroll
        for (int p = 0; p < 4; p++) B[0][p] = UFR(0, 4 + p);
        A[0][0] = KFR(0, 0); A[0][1] = KFR(0, 1); R[3] = QFR(0, 3);
        FIRST2(T[0]);
        bar();
        STAMP(14);
#pragma unroll
        for (int rho = 0; rho < 4; rho++)
#pragma unroll
            for (int p = 0; p < 4; p++) {
                SB;
                if (p == 0) { if (rho == 0) PRIO(3); else if (rho == 1) PRIO(2); else if (rho == 2) PRIO(1); else PRIO(0); }
                if (rho < 3) { B[(rho + 1) & 1][p] = UFR(rho + 1, 4 + p); if (p < 2) A[(rho + 1) & 1][p] = KFR(rho + 1, p); }
                if (p == 0 && rho > 0) T[0] = QBUILD(R[2], sel2);
                if (p == 1) T[1] = QBUILD(R[3], sel2);
#pragma unroll
                for (int q = 0; p + q < 4; q++) acc[4 + p + q] = mfma(p < 2 ? A[rho & 1][p] : T[p & 1], B[rho & 1][q], acc[4 + p + q]);
                SB;
                if (rho < 3 && p < 2) R[2 + p] = QFR(rho + 1, 2 + p);
            }
        SB;
    }
#pragma unroll
    for (int i = 4; i < 8; i++) pin(acc[i]);
    STAMP(5);
    EXCHANGE(4, 2048);
    bar();
    STAMP(6);
    STAMP_OUT(0);
}

__global__ void __launch_bounds__(NT) __attribute__((aligned(4096))) fused_kernel(const u16* __restrict__ u, const u16* __restrict__ lar, const u16* __restrict__ aimg,
                                                   const u16* __restrict__ cre, const u16* __restrict__ cim, const u16* __restrict__ ldt,
                                                   const u16* __restrict__ dsk, const u16* __restrict__ x, const u16* __restrict__ wr,
                                                   const u16* __restrict__ wi, u16* __restrict__ out, char* ws) {
    const char* entry;
    asm volatile("s_getpc_b64 %0" : "=s"(entry));
    APar pr;
    if (blockIdx.x >= A0 && __builtin_amdgcn_readfirstlane(threadIdx.x) < 768) {
        const int a = blockIdx.x - A0, d = 6 * ((a & 7) * 12 + (a >> 3)) + __builtin_amdgcn_readfirstlane(threadIdx.x >> 7);
        asm volatile("global_load_ushort %0, %6, %8\n\tglobal_load_ushort %1, %6, %9\n\tglobal_load_ushort %2, %6, %10\n\t"
                     "global_load_ushort %3, %6, %11\n\tglobal_load_ushort %4, %7, %12\n\tglobal_load_ushort %5, %7, %13"
                     : "=&v"(pr.l), "=&v"(pr.a), "=&v"(pr.r), "=&v"(pr.i), "=&v"(pr.t), "=&v"(pr.d)
                     : "v"((d * NS + (threadIdx.x & 127)) * 2), "v"(d * 2), "s"(lar), "s"(aimg), "s"(cre), "s"(cim), "s"(ldt), "s"(dsk) : "memory");
        asm volatile("global_load_dwordx4 %0, %4, %5\n\tglobal_load_dwordx4 %1, %4, %5 offset:1024\n\t"
                     "global_load_dwordx4 %2, %4, %5 offset:2048\n\tglobal_load_dwordx4 %3, %4, %5 offset:3072"
                     : "=&v"(pr.sel2), "=&v"(pr.sel3), "=&v"(pr.selx), "=&v"(pr.tadr)
                     : "v"((threadIdx.x & 63) * 16 + (threadIdx.x & 64) * 64), "s"((const char*)&TAB + F_END * 1024) : "memory");
    }
    if (__builtin_expect(blockIdx.x < NB2, 1)) head_b(entry, x, wr, wi, out, ws);
    else if (blockIdx.x >= A0) head_a(u, pr, out, ws);
    else warm_code(entry);
}

extern "C" void kernel_launch(void* const* d_in, const int* in_sizes, int n_in, void* d_out, int out_size, void* d_ws,
                              size_t ws_size, hipStream_t stream) {
    (void)hipFuncSetAttribute((const void*)fused_kernel, hipFuncAttributeMaxDynamicSharedMemorySize, LDS_BYTES);
    fused_kernel<<<GRID, NT, LDS_BYTES, stream>>>((const u16*)d_in[0], (const u16*)d_in[1], (const u16*)d_in[2], (const u16*)d_in[3],
                                                  (const u16*)d_in[4], (const u16*)d_in[5], (const u16*)d_in[6], (const u16*)d_in[7],
                                                  (const u16*)d_in[8], (const u16*)d_in[9], (u16*)d_out, (char*)d_ws);
}
```

```cpp
#define POLL_SLEEP 6
#include <hip/hip_runtime.h>

typedef unsigned short u16;
typedef unsigned int u32;
typedef float f2 __attribute__((ext_vector_type(2)));
typedef float f4 __attribute__((ext_vector_type(4)));
typedef u32 u2 __attribute__((ext_vector_type(2)));
typedef u32 u4 __attribute__((ext_vector_type(4)));
typedef int i8v __attribute__((ext_vector_type(8)));
typedef short s4 __attribute__((ext_vector_type(4)));
typedef short s8 __attribute__((ext_vector_type(8)));
typedef __bf16 bf2 __attribute__((ext_vector_type(2)));
#define DI __device__ __forceinline__
#define LAS __attribute__((address_space(3)))
#define GAS __attribute__((address_space(1)))
#define CAS __attribute__((address_space(4)))
#pragma clang diagnostic ignored "-Wint-to-pointer-cast"
struct u3 { u32 v[3]; DI u32& operator[](int i) { return v[i]; } DI const u32& operator[](int i) const { return v[i]; } };

constexpr int SEQ = 2048, DM = 576, NS = 128, DF = 289;
constexpr int NB1 = 128;
constexpr int NB2 = 145;
constexpr int A0 = 152;
constexpr int NA = 96;
constexpr int GRID = A0 + NA;
constexpr int NT = 1024;

constexpr u32 WS_EPOCH = 0, WS_F1 = 4096, WS_F2 = WS_F1 + 16 * 16 * 128, WS_X1 = 1u << 20, WS_Y1 = 4u << 20, WS_USED = 8u << 20;
#define STAMP(i) do { if ((i) == PK && HEADSEL) { __builtin_amdgcn_sched_barrier(0); tk_ = __builtin_amdgcn_s_memrealtime(); __builtin_amdgcn_sched_barrier(0); } } while (0)
#define STAMP_DECL const unsigned long long t0_ = __builtin_amdgcn_s_memrealtime(); unsigned long long tk_ = t0_
#define STAMP_OUT(T) do { if (HEADSEL && wv == PW) while (__builtin_amdgcn_s_memrealtime() - t0_ < 3000ull + 20ull * (tk_ - t0_)) __builtin_amdgcn_s_sleep(2); } while (0)
#define PK 1
#define PW 8
#define HEADSEL (blockIdx.x >= A0)

constexpr int F_SA = 0, F_EO = 4;
constexpr int F_ARE = 9, F_AIM = 10, F_BRE = 11, F_BIM = 12, F_G = 13;
constexpr int F_F16 = 21, F_GI = 23, F_AI = 31, F_M1C = 33;
constexpr int F_VAR = 37;
constexpr int F_END = F_VAR + 40;
constexpr int ET = 452, ER = 28, E2V = 580, E2U = 36, OP = 1168;
constexpr int LB_X = 0, LB_I = 0, LB_Y = 0;
constexpr int LB_E = 16 * OP;
constexpr int LB_O = LB_E, LB_E2 = LB_E;
constexpr int LB_S = LB_E + 16 * ET * 4;
constexpr int LB_W = LB_S + 1200 * 16;
constexpr int LB_T = LB_W + 2 * 256 * 36;
constexpr int LB_G = LB_T + (F_VAR + 16) * 1024;
constexpr int LB_END = LB_G + 16;
struct CosTab { double c[2048]; double c9[9], s9[9]; };
constexpr double tay_cos(double x) { double t = 1, s = 1; for (int k = 1; k <= 14; k++) { t *= -x * x / ((2 * k - 1) * (2 * k)); s += t; } return s; }
constexpr double tay_sin(double x) { double t = x, s = x; for (int k = 1; k <= 14; k++) { t *= -x * x / ((2 * k + 1) * (2 * k)); s += t; } return s; }
constexpr CosTab make_cos() {
    CosTab t = {};
    const double PI2 = 6.283185307179586476925;
    for (int k = 0; k <= 512; k++) {
        const double v = k == 512 ? 0.0 : (k <= 256 ? tay_cos(PI2 * k / 2048) : tay_sin(PI2 * (512 - k) / 2048));
        t.c[k] = v; t.c[(2048 - k) & 2047] = v; t.c[1024 - k] = -v; t.c[(1024 + k) & 2047] = -v;
    }
    t.c[512] = 0.0; t.c[1536] = 0.0;
    for (int k = 0; k < 9; k++) { const double a = PI2 * (k <= 4 ? k : k - 9) / 9; t.c9[k] = tay_cos(a); t.s9[k] = tay_sin(a); }
    return t;
}
constexpr CosTab CT = make_cos();
constexpr double cs(int n) { return CT.c[n & 2047]; }
constexpr double sn(int n) { return CT.c[(n - 512) & 2047]; }
constexpr u32 bfr(double x) { const u32 b = __builtin_bit_cast(u32, (float)x); return (b + 0x7fffu + ((b >> 16) & 1u)) >> 16; }
constexpr u32 pkc(double lo, double hi) { return bfr(lo) | (bfr(hi) << 16); }
constexpr u32 f32c(double x) { return __builtin_bit_cast(u32, (float)x); }
template <int N> struct Frags { u32 w[N * 256]; };
constexpr u32 w_re(double amp, int n) { return pkc(amp * cs(n), amp * sn(n)); }
constexpr u32 w_im(double amp, int n) { return pkc(-amp * sn(n), amp * cs(n)); }
constexpr Frags<F_VAR> make_t0() {
    Frags<F_VAR> t = {};
    const double R2 = 1.4142135623730951;
    for (int l = 0; l < 64; l++)
        for (int i = 0; i < 4; i++) {
            const int c = l & 15, g = l >> 4, r = 4 * g + i, o = l * 4 + i;
            {
                const int b = c >> 2, tq = c & 3;
                const double amp = b ? 1.0 : R2;
                const int n0 = 512 * tq * g + (b ? 256 * b * (2 * i) : -256), n1 = 512 * tq * g + (b ? 256 * b * (2 * i + 1) : 256);
                t.w[F_ARE * 256 + o] = pkc(amp * cs(n0), amp * cs(n1));
                t.w[F_AIM * 256 + o] = pkc(-amp * sn(n0), -amp * sn(n1));
            }
            for (int w = 0; w < 9; w++) {
                const int sh = 16 * (i & 1), oo = w * 128 + l * 2 + (i >> 1);
                t.w[F_EO * 256 + oo] |= (u32)((c * ET + r * ER + 2 * ((7 * w + r) % 9)) * 4) << sh;
                if (w < 8) {
                    const int k = (9 * (w & 3) + 36 * r + 64 * (c % 9)) % DM, slot = w < 4 ? (k <= 288 ? 2 * k : 2 * (DM - k) + 1) : k;
                    t.w[F_SA * 256 + oo] |= (u32)(slot * 8 + (slot >> 6) * 16) << sh;
                }
            }
            if (r < 9) { const int k = (r * c) % 9; t.w[F_BRE * 256 + o] = pkc(CT.c9[k], CT.s9[k]); t.w[F_BIM * 256 + o] = pkc(-CT.s9[k], CT.c9[k]); }
            for (int q = 0; q < 4; q++) {
                t.w[(F_G + 2 * q) * 256 + o] = w_re(1.0, 32 * r * (q + 4 * c)); t.w[(F_G + 2 * q + 1) * 256 + o] = w_im(1.0, 32 * r * (q + 4 * c));
                t.w[(F_GI + 2 * q) * 256 + o] = w_re(1.0, 32 * c * (q + 4 * r)); t.w[(F_GI + 2 * q + 1) * 256 + o] = w_im(1.0, 32 * c * (q + 4 * r));
            }
            t.w[F_F16 * 256 + o] = w_re(1.0, 128 * r * c); t.w[(F_F16 + 1) * 256 + o] = w_im(1.0, 128 * r * c);
            for (int h = 0; h < 2; h++) {
                const int j = 4 * h + (c >> 2), n = 512 * i * (c & 3) + (g ? 256 * g * j : ((j & 1) ? 256 : -256));
                t.w[(F_AI + h) * 256 + o] = w_re(g ? 2.0 : R2, n);
                const int m = 16 * r * (4 * h + 8 * c) - 512 * h;
                t.w[(F_M1C + 2 * h) * 256 + o] = pkc(0.5 * cs(m), -0.5 * sn(m));
                t.w[(F_M1C + 2 * h + 1) * 256 + o] = pkc(-0.5 * sn(m), -0.5 * cs(m));
            }
        }
    return t;
}
constexpr Frags<40> make_t1() {
    Frags<40> t = {};
    for (int v = 0; v < 5; v++)
        for (int l = 0; l < 64; l++)
            for (int i = 0; i < 4; i++) {
                const int be = v < 3 ? v + 1 : 4 * (v - 3), c = l & 15, r = 4 * (l >> 4) + i, o = v * 2048 + l * 4 + i;
                const double amp = v < 3 ? 1.0 : 0.5;
                const int n1 = 16 * r * (be + 8 * c) + (v == 4 ? 512 : 0), n4 = 16 * c * (be + 8 * r);
                t.w[o] = w_re(amp, n1); t.w[o + 256] = w_im(amp, n1);
                t.w[o + 512] = w_re(1.0, n4); t.w[o + 768] = w_im(1.0, n4);
                t.w[o + 1024] = f32c(cs(r * (be + 8 * c))); t.w[o + 1280] = f32c(-sn(r * (be + 8 * c)));
                t.w[o + 1536] = f32c(cs(c * (be + 8 * r))); t.w[o + 1792] = f32c(-sn(c * (be + 8 * r)));
            }
    return t;
}
constexpr int qidx(int l) {
    if (l <= 256) return l;
    const int sh = l < 512 ? 1 : (l < 1024 ? 2 : 3), h = 1 << (sh - 1), m = l >> sh, r = l & ((1 << sh) - 1);
    return 128 * sh + m + ((r > h || (r == h && (m & 1))) ? 1 : 0);
}
constexpr Frags<8> make_t2() {
    Frags<8> t = {};
    for (int hf = 0; hf < 2; hf++)
        for (int l = 0; l < 64; l++) {
            const int sg[3] = {17 + 4 * hf, 33 + 4 * hf, 32 + 4 * hf}, back[3] = {8, 4, 0};
            for (int k = 0; k < 3; k++) {
                const int L = 32 * sg[k] + 16 + (l & 15) - 8 * (l >> 4), q0 = qidx(L - 7);
                for (int i = 0; i < 4; i++) {
                    const u32 a = 2 * (qidx(L - 2 * i) - q0), b = 2 * (qidx(L - 2 * i - 1) - q0);
                    t.w[(4 * hf + k) * 256 + l * 4 + i] = a | ((a + 1) << 8) | (b << 16) | ((b + 1) << 24);
                }
                t.w[(4 * hf + 3) * 256 + l * 4 + k] = (u32)((q0 - back[k] - 384) * 8);
            }
            const int e = 543 - 2 * (l & 15) - (l >> 4);
            t.w[(4 * hf + 3) * 256 + l * 4 + 3] = (u32)(qidx(e) * 4) | ((u32)(qidx(e - 1) * 4) << 16);
        }
    return t;
}
struct Tables { Frags<F_VAR> a; Frags<40> b; Frags<8> c; };
constexpr Frags<8> T2 = make_t2();
constexpr Frags<F_VAR> T0 = make_t0();
constexpr Frags<40> T1 = make_t1();
static __device__ const __attribute__((aligned(128))) Tables TAB = {T0, T1, T2};
constexpr int LA_G = 0;
constexpr int LA_K = LA_G + 12 * 4608;
constexpr int KI = 1344, KC = 4 * KI + 2048;
constexpr int LA_U = LA_K + 6 * KC;
constexpr int LA_Q = LA_U + 6 * 4608;
constexpr int LA_F = LA_Q + 12 * 2576;
constexpr int LA_D = LA_F + 16;
constexpr int LA_END = LA_D + 256;
constexpr int FU0 = 0, FU2 = 1;
constexpr int LDS_BYTES = (LA_END > LB_END ? LA_END : LB_END);
static_assert(LDS_BYTES <= 160 * 1024 && LDS_BYTES > 80 * 1024, "one workgroup per CU");

DI u32 pk(float a, float b) { f2 v = {a, b}; return __builtin_bit_cast(u32, __builtin_convertvector(v, bf2)); }
DI float blo(u32 v) { return __uint_as_float(v << 16); }
DI float bhi(u32 v) { return __uint_as_float(v & 0xffff0000u); }
DI float bf2f(u16 v) { return __uint_as_float((u32)v << 16); }
DI float rbf(float x) { return blo(pk(x, x)); }
DI f2 cmul(f2 a, f2 b) { f2 t = a.xx * b; f2 s = {-b.y, b.x}; return __builtin_elementwise_fma(a.yy, s, t); }
DI f4 mfma(u4 a, u4 b, f4 c) { return __builtin_amdgcn_mfma_f32_16x16x32_bf16(__builtin_bit_cast(s8, a), __builtin_bit_cast(s8, b), c, 0, 0, 0); }
DI u4 pack4(f4 re, f4 im) { u4 r = {pk(re[0], im[0]), pk(re[1], im[1]), pk(re[2], im[2]), pk(re[3], im[3])}; return r; }
DI u4 twid(f4 re, f4 im, f4 wr, f4 wi) { return pack4(__builtin_elementwise_fma(re, wr, -(im * wi)), __builtin_elementwise_fma(re, wi, im * wr)); }
DI int mod9(int x) { return x - 9 * ((x * 57) >> 9); }
template <class T> DI void pin(T& v) { asm volatile("" : "+v"(v)); }

template <class T> DI T lds_ld(int off) { return *(const LAS T*)(u32)off; }
template <class T> DI void lds_st(int off, T v) { *(LAS T*)(u32)off = v; }
DI void bar() { __syncthreads(); }
#define PRIO(n) __builtin_amdgcn_s_setprio(n)
#define PRIOB(n)
DI void glds16(const void* g, u32 off, int lds) {
    u32 keep;
    asm volatile("s_mov_b32 %0, m0\n\ts_mov_b32 m0, %3\n\ts_nop 4\n\tglobal_load_lds_dwordx4 %1, %2\n\ts_mov_b32 m0, %0" : "=&s"(keep) : "v"(off), "s"(g), "s"(lds) : "memory");
}

DI void publish_tag(u32 epoch) {
    u32 t;
    asm volatile("v_add_u32 %0, 0x9E3779B1, %1\n\tds_write_b32 %2, %0\n\ts_waitcnt lgkmcnt(0)" : "=&v"(t) : "v"(epoch), "v"(LB_G) : "memory");
}
DI void wait_flag(__amdgpu_buffer_rsrc_t r, u32 off, u32 tag, bool on) {
    if (on) {
        const unsigned long long t0 = __builtin_amdgcn_s_memrealtime();
        while (!__all(__builtin_amdgcn_raw_buffer_load_b32(r, off, 0, 16) == tag)) {
            if (__builtin_amdgcn_s_memrealtime() - t0 > 5000000ull) break;
            __builtin_amdgcn_s_sleep(POLL_SLEEP);
            asm volatile("" ::: "memory");
        }
    }
    asm volatile("" ::: "memory");
}

constexpr int CODE_LINES = 128;
DI void warm_code(const char* entry) {
    if (threadIdx.x < CODE_LINES) { const u32 t = *(const GAS u32*)(entry + threadIdx.x * 128); asm volatile("" :: "v"(t)); }
}

DI void head_b(const char* entry, const u16* __restrict__ x, const u16* __restrict__ wr, const u16* __restrict__ wi,
               u16* __restrict__ out, char* ws) {
    const int tid = threadIdx.x, lane = tid & 63, c16 = lane & 15, g = lane >> 4;
    const int wv = __builtin_amdgcn_readfirstlane(tid >> 6);
    const int id = blockIdx.x;
    STAMP_DECL;
    const bool rows = id < NB1;
    const int s00 = 32 * (id >> 4) + (id & 15);
    const char* tab = (const char*)&TAB;
    const int R = (tid * 911) >> 16;
    const u32 xoff = (s00 + 16 * (R >> 3) + 256 * (R & 7)) * (2 * DM) + (tid - R * 72) * 16;
    asm volatile("" :: "s"(x), "s"(wr), "s"(wi), "s"(out), "s"(ws), "v"(xoff));
    const __amdgpu_buffer_rsrc_t rws = __builtin_amdgcn_make_buffer_rsrc(ws, 0, WS_USED, 0x00020000);
    STAMP(13);
    u32 epoch = 0;
    if (wv == 0) asm volatile("global_load_dword %0, %0, %1 sc1" : "+v"(epoch) : "s"(ws) : "memory");
    if (rows) {
        glds16(x, xoff, LB_X + wv * 1024);
        if (wv < 2) glds16(x, (s00 + 16 + 1536) * (2 * DM) + (tid < 56 ? (tid + 16) * 16 : 256 * 2 * DM + (tid - 56) * 16), LB_X + 16384 + wv * 1024);
        else glds16(tab, tid * 16 - 2048, LB_T + (wv - 2) * 1024);
        if (wv >= 9) glds16(tab, tid * 16 + 5 * 1024, LB_T + (wv + 5) * 1024);
    }
    const int u2i = (id & 7) ? (id & 7) * 18 + 1 + (id >> 3) : (id == 136 ? 0 : (id >> 3) + (id < 136 ? 1 : 0));
    const bool star = u2i >= 108;
    const int q36 = min((u2i * 57) >> 11, 3), kg = u2i - 36 * q36, bq = (q36 + 1) & 3;
    const bool mir = !star && kg >= 18;
    const int w0 = star ? min(16 * kg, 562) : (mir ? (2048 - bq) * (2 * DF) + 1122 - 32 * kg : bq * (2 * DF) + 32 * kg);
    __builtin_amdgcn_s_barrier();
    STAMP(14);
    const u32 woff = w0 + ((tid >> 1) & 255) * (mir ? -16 * DF : 16 * DF) + (tid & 1) * (star ? 8 * DF : 16);
    u4 wra, wrb;
    if (wv >= 8) {
        __builtin_memcpy(&wra, (const char*)wr + woff, 16);
        __builtin_memcpy(&wrb, (const char*)wi + woff, 16);
    }
    __builtin_amdgcn_sched_barrier(0);
    const f4 Z = {0.f, 0.f, 0.f, 0.f};
    const int tau = wv & 3, bw = wv >> 2;
    int tl = LB_T + lane * 16;
    int lin = LB_S + (tid + (tid >> 5)) * 16;
    pin(tl); pin(lin);
    int sadr[4];

    if (rows) {
        if (tid < 256) {
            const int o = LB_E + 72 + (tid & 15) * (ER * 4) + (tid >> 4) * (ET * 4);
            lds_st<u2>(o, u2{0u, 0u}); lds_st<u4>(o + 8, u4{0u, 0u, 0u, 0u});
        }
        int xo = LB_X + (c16 >> 2) * 1152 + 2 * (144 * ((g - wv) & 3) + 16 * wv + 4 * (c16 & 3));
        int bo = LB_E + (wv * ET + c16 * ER + min(8 * g, 16)) * 4;
        int go = WS_X1 + (tid >> 3) * 16384 + id * 128 + (tid & 7) * 16;
        int ta = LB_T + lane * 8;
        pin(xo); pin(bo); pin(go); pin(ta);
        STAMP(1);
        asm volatile("s_cmp_lt_u32 %0, 8\n\ts_cbranch_scc0 .Lw%=\n\ts_waitcnt vmcnt(0)\n.Lw%=:\n\ts_waitcnt vmcnt(2)" :: "s"(wv) : "memory", "scc");
        if (wv == 0) publish_tag(epoch);
        bar();
        STAMP(2);
        if (wv < 9) {
            u4 d[2];
#pragma unroll
            for (int s = 0; s < 2; s++) {
                const s4 lo = __builtin_amdgcn_ds_read_tr16_b64_v4i16((LAS s4*)(u32)(xo + s * 9216));
                const s4 hi = __builtin_amdgcn_ds_read_tr16_b64_v4i16((LAS s4*)(u32)(xo + s * 9216 + 4608));
                d[s] = __builtin_bit_cast(u4, __builtin_shufflevector(lo, hi, 0, 1, 2, 3, 4, 5, 6, 7));
            }
            const u4 are = lds_ld<u4>(tl + F_ARE * 1024), aim = lds_ld<u4>(tl + F_AIM * 1024);
            const u2 eo = lds_ld<u2>(ta + F_EO * 1024 + wv * 512);
#pragma unroll
            for (int s = 0; s < 2; s++) d[s] = pack4(mfma(d[s], are, Z), mfma(d[s], aim, Z));
#pragma unroll
            for (int i = 0; i < 4; i++) lds_st<u2>(LB_E + ((i & 1) ? eo[i >> 1] >> 16 : eo[i >> 1] & 0xffff), u2{d[0][i], d[1][i]});
        }
        u4 bre = lds_ld<u4>(tl + F_BRE * 1024), bim = lds_ld<u4>(tl + F_BIM * 1024);
        u4 gre = lds_ld<u4>(tl + (F_G + 2 * tau) * 1024), gim = lds_ld<u4>(tl + (F_G + 2 * tau + 1) * 1024);
        {
            const u2 sa = lds_ld<u2>(ta + F_SA * 1024 + (tau + (bw ? 4 : 0)) * 512);
            const int base = LB_S + ((bw + 3) & 3) * (DM * 8 + 144);
#pragma unroll
            for (int i = 0; i < 4; i++) { sadr[i] = base + ((i & 1) ? sa[i >> 1] >> 16 : sa[i >> 1] & 0xffff); pin(sadr[i]); }
        }
        pin(bre); pin(bim); pin(gre); pin(gim);
        bar();
        {
            PRIOB(2);
            const u4 p0 = lds_ld<u4>(bo), p1 = lds_ld<u4>(bo + 16);
            u4 d[2];
            f4 re[2], im[2];
#pragma unroll
            for (int s = 0; s < 2; s++) { const u4 a = {p0[s], p0[2 + s], p1[s], p1[2 + s]}; re[s] = mfma(a, bre, Z); im[s] = mfma(a, bim, Z); }
            PRIOB(1);
#pragma unroll
            for (int s = 0; s < 2; s++) { const u4 t = pack4(re[s], im[s]); re[s] = mfma(gre, t, Z); im[s] = mfma(gim, t, Z); }
            PRIOB(0);
#pragma unroll
            for (int s = 0; s < 2; s++) d[s] = pack4(re[s], im[s]);
            if (c16 < 9) {
#pragma unroll
                for (int i = 0; i < 4; i++) lds_st<u2>(sadr[i], u2{d[0][i], d[1][i]});
                if (wv == 0 && c16 == 0 && !(g & 1)) lds_st<u2>(sadr[0] + 8, u2{d[0][0], d[1][0]});
            }
        }
        bar();
        STAMP(3);
        __builtin_amdgcn_raw_buffer_store_b128(lds_ld<u4>(lin), rws, go, 0, 16);
        if (tid <= 128) __builtin_amdgcn_raw_buffer_store_b128(lds_ld<u4>(lin + 1056 * 16), rws, go, 128 * 16384, 16);
    } else {
        if (wv < 8 && (kg == 0 || kg == (star ? 36 : 18))) {
            const int m = tid & 255;
            lds_st<u16>(LB_W + tid * 36 + 32, ((wv >> 2) ? wi : wr)[((mir ? bq + 8 * m : 2048 - bq - 8 * m) & 2047) * DF + (kg ? 288 : 0)]);
        }
        warm_code(entry);
        asm volatile("s_waitcnt vmcnt(0)" ::: "memory");
        if (wv == 0) publish_tag(epoch);
        bar();
    }
    const u32 tag = lds_ld<u32>(LB_G);
    if (wv >= 8) {
        const int o = LB_W + ((tid >> 1) & 255) * 36 + (tid & 1) * 16;
#pragma unroll
        for (int i = 0; i < 4; i++) { lds_st<u32>(o + 4 * i, wra[i]); lds_st<u32>(o + 9216 + 4 * i, wrb[i]); }
    }
    asm volatile("s_waitcnt vmcnt(0)" ::: "memory");
    if (rows && lane == 0) __builtin_amdgcn_raw_buffer_store_b32(tag, rws, WS_F1 + (wv * 16 + (id >> 3)) * 128 + (id & 7) * 4, 0, 16);
    STAMP(4);
    glds16(tab, F_F16 * 1024 + tid * 16, LB_T + F_F16 * 1024 + wv * 1024);
    if (star || wv < 8) glds16(tab, (F_VAR + 8 * q36) * 1024 + tid * 16, LB_T + F_VAR * 1024 + wv * 1024);
    asm volatile("s_waitcnt vmcnt(0)" ::: "memory");
    bar();
    STAMP(12);

    const int typ = star ? (wv & 1) : 0;
    const int tv = tl + (F_VAR + 8 * typ) * 1024;
    u4 fre = lds_ld<u4>(tl + F_F16 * 1024), fim = lds_ld<u4>(tl + (F_F16 + 1) * 1024);
    u4 m1r = lds_ld<u4>(tv), m1i = lds_ld<u4>(tv + 1024), m4r = lds_ld<u4>(tv + 2048), m4i = lds_ld<u4>(tv + 3072);
    f4 twr = lds_ld<f4>(tv + 4096), twi = lds_ld<f4>(tv + 5120), tur = lds_ld<f4>(tv + 6144), tui = lds_ld<f4>(tv + 7168);
    u4 mcr = lds_ld<u4>(tl + (F_M1C + 2 * typ) * 1024), mci = lds_ld<u4>(tl + (F_M1C + 2 * typ + 1) * 1024);
    f4 wfr, wfi;
    {
        const int kcol = star ? 8 * kg + (wv >> 1) : 16 * kg + wv;
        const int cc = star ? 8 * typ + (u2i < NB2 - 1 ? (wv >> 1) : 7) : (mir ? 15 - wv : wv);
        const int wo = LB_W + (c16 + 64 * g) * 36 + cc * 2;
#pragma unroll
        for (int i = 0; i < 4; i++) {
            wfr[i] = bf2f(lds_ld<u16>(wo + i * 576));
            wfi[i] = bf2f(lds_ld<u16>(wo + i * 576 + 9216));
        }
        float sc = 1.f / (float)(SEQ * DM);
        if (kcol == 0 || kcol == 288) {
            sc *= 0.5f;
#pragma unroll
            for (int i = 0; i < 4; i++) {
                const int m = c16 + 64 * g + 16 * i;
                const int o = LB_W + (typ ? (255 - m) * 36 + cc * 2 : m * 36 + 32);
                wfr[i] += bf2f(lds_ld<u16>(o));
                wfi[i] -= bf2f(lds_ld<u16>(o + 9216));
            }
        }
        wfr *= sc; wfi *= (mir ? -sc : sc);
    }
    f4 wnr = -wfr, wni = -wfi;
    int io = (32 * g + c16) * 128 + (((wv >> 1) ^ (c16 >> 1)) * 16);
    int so = (tid >> 3) * 128 + (((tid & 7) ^ ((tid >> 4) & 7)) * 16);
    pin(fre); pin(fim); pin(m1r); pin(m1i); pin(m4r); pin(m4i); pin(twr); pin(twi); pin(tur); pin(tui); pin(mcr); pin(mci);
    int gi = WS_X1 + u2i * 16384 + tid * 16, gq = WS_Y1 + (tid >> 3) * (NB2 * 128) + u2i * 128 + (tid & 7) * 16;
    int i1 = LB_I + io + (star ? 0 : (wv & 1) * 8);
    io += LB_O + (wv & 1) * 8;
    pin(wfr); pin(wnr); pin(wni); pin(io); pin(so); pin(gi); pin(gq); pin(i1);
    wait_flag(rws, WS_F1 + (((u2i >> 3) & 15) * 16 + wv) * 128 + lane * 4, tag, lane < 8);
    STAMP(5);
    lds_st<u4>(LB_I + so, __builtin_amdgcn_raw_buffer_load_b128(rws, gi, 0, 16));
    bar();
    STAMP(6);
    {
        const int o1 = i1;
        PRIOB(3);
        const u2 a0 = lds_ld<u2>(o1), a1 = lds_ld<u2>(o1 + 2048);
        const u4 a = {a0[0], a0[1], a1[0], a1[1]};
        f4 re = mfma(a, m1r, Z), im = mfma(a, m1i, Z);
        if (star) {
            const u2 b0 = lds_ld<u2>(o1 + 8), b1 = lds_ld<u2>(o1 + 8 + 2048);
            const u4 b = {b0[0], b0[1], b1[0], b1[1]};
            re = mfma(b, mcr, re); im = mfma(b, mci, im);
        }
        u4 t = twid(re, im, twr, twi);
        re = mfma(fre, t, Z); im = mfma(fim, t, Z);
        PRIOB(2);
        t = pack4(__builtin_elementwise_fma(re, wfr, im * wni), __builtin_elementwise_fma(re, wni, im * wnr));
        re = mfma(t, fre, Z); im = mfma(t, fim, Z);
        PRIOB(1);
        t = twid(re, im, tur, tui);
        re = mfma(m4r, t, Z); im = mfma(m4i, t, Z);
        PRIOB(0);
        t = pack4(re, im);
        lds_st<u2>(io, u2{t[0], t[1]}); lds_st<u2>(io + 2048, u2{t[2], t[3]});
    }
    bar();
    STAMP(7);
    {
        u4 v = lds_ld<u4>(LB_O + so);
        if (star) {
            u4 w;
#pragma unroll
            for (int s = 0; s < 2; s++) {
                const float ar = blo(v[s]), ai = bhi(v[s]), cr = blo(v[2 + s]), ci = bhi(v[2 + s]);
                w[s] = pk(ar + ci, ai - cr); w[2 + s] = pk(ar - ci, -ai - cr);
            }
            v = w;
        }
        if (u2i < NB2 - 1 || (tid & 7) == 0)
            __builtin_amdgcn_raw_buffer_store_b128(v, rws, gq, 0, 16);
    }
    asm volatile("s_waitcnt vmcnt(0)" ::: "memory");
    if (lane == 0) __builtin_amdgcn_raw_buffer_store_b32(tag, rws, WS_F2 + (wv * 19 + (u2i >> 3)) * 128 + (u2i & 7) * 4, 0, 16);
    STAMP(8);
    if (!rows) { STAMP_OUT(0); return; }

    u4 gre = lds_ld<u4>(tl + (F_GI + 2 * tau) * 1024), gim = lds_ld<u4>(tl + (F_GI + 2 * tau + 1) * 1024);
    u4 bre = lds_ld<u4>(tl + F_BRE * 1024), bim = lds_ld<u4>(tl + F_BIM * 1024);
    u4 ai0 = lds_ld<u4>(tl + F_AI * 1024), ai1 = lds_ld<u4>(tl + (F_AI + 1) * 1024);
    int eadr[4];
#pragma unroll
    for (int i = 0; i < 4; i++) {
        const int r = 4 * g + i;
        eadr[i] = LB_E2 + (mod9(4 * (c16 + 18 - r)) * E2V + r * E2U + 2 * wv) * 4;
        pin(eadr[i]);
    }
    int ao = LB_E2 + (wv * E2V + c16 * E2U + 8 * g) * 4;
    int gy = WS_Y1 + id * (NB2 * 128) + tid * 16;
    int yo = LB_Y + (c16 >> 2) * OP + 2 * (144 * (((c16 & 3) - wv) & 3) + 16 * wv + 4 * g);
    int yl = LB_Y + tid * 16 + R * (OP - 1152);
    u32 oo = 2 * xoff - (tid - R * 72) * 16 + 2 * DM;
    pin(gre); pin(gim); pin(bre); pin(bim); pin(ai0); pin(ai1); pin(ao); pin(gy); pin(yo); pin(yl); pin(oo);
    {
        const int un = 8 * wv + lane + (lane >= 8 ? 120 : 0);
        wait_flag(rws, WS_F2 + ((id >> 3) * 19 + (un >> 3)) * 128 + (lane & 7) * 4, tag, lane < 16 && un < NB2);
    }
    STAMP(9);
    {
        const u4 v = __builtin_amdgcn_raw_buffer_load_b128(rws, gy, 0, 16);
        if (tid <= 128) lds_st<u4>(lin + 1056 * 16, __builtin_amdgcn_raw_buffer_load_b128(rws, gy, 16384, 16));
        lds_st<u4>(lin, v);
    }
    bar();
    if (id == 0 && tid == 0) __builtin_amdgcn_raw_buffer_store_b32(tag, rws, WS_EPOCH, 0, 16);
    STAMP(10);
    {
        PRIOB(2);
        u4 d[2];
        u2 p[4];
#pragma unroll
        for (int i = 0; i < 4; i++) p[i] = lds_ld<u2>(sadr[i]);
        f4 re[2], im[2];
#pragma unroll
        for (int s = 0; s < 2; s++) { const u4 a = {p[0][s], p[1][s], p[2][s], p[3][s]}; re[s] = mfma(a, gre, Z); im[s] = mfma(a, gim, Z); }
        PRIOB(1);
#pragma unroll
        for (int s = 0; s < 2; s++) { const u4 t = pack4(re[s], im[s]); re[s] = mfma(t, bre, Z); im[s] = mfma(t, bim, Z); }
        PRIOB(0);
#pragma unroll
        for (int s = 0; s < 2; s++) d[s] = pack4(re[s], im[s]);
        if (c16 < 9) {
#pragma unroll
            for (int i = 0; i < 4; i++) lds_st<u2>(eadr[i], u2{d[0][i], d[1][i]});
        }
    }
    STAMP(15);
    bar();
    if (wv < 9) {
        const u4 p0 = lds_ld<u4>(ao), p1 = lds_ld<u4>(ao + 16);
#pragma unroll
        for (int s = 0; s < 2; s++) {
            const u4 a = {p0[s], p0[2 + s], p1[s], p1[2 + s]};
            const f4 y0 = mfma(a, ai0, Z), y1 = mfma(a, ai1, Z);
            lds_st<u2>(yo + s * (8 * OP), u2{pk(y0[0], y0[1]), pk(y0[2], y0[3])});
            lds_st<u2>(yo + s * (8 * OP) + 4 * OP, u2{pk(y1[0], y1[1]), pk(y1[2], y1[3])});
        }
    }
    bar();
    *(GAS u4*)((char*)out + oo) = lds_ld<u4>(yl);
    if (wv < 2) {
        const int r2 = tid < 56 ? 14 : 15, c2 = tid + 1024 - 72 * r2;
        *(GAS u4*)((char*)out + (u32)((s00 + 16 + 256 * (r2 - 8)) * (4 * DM) + 2 * DM + c2 * 16)) = lds_ld<u4>(LB_Y + r2 * OP + c2 * 16);
    }
    STAMP(11);
    STAMP_OUT(0);
}

DI f2 cexpa(float ar2, float air, float p) {
    const float e = __builtin_amdgcn_exp2f(ar2 * p), t = __builtin_amdgcn_fractf(air * p);
    return f2{e * __builtin_amdgcn_cosf(t), e * __builtin_amdgcn_sinf(t)};
}
DI u4 ld_k(int o) {
    const u2 a = *(const volatile LAS u2*)(u32)o, b = *(const volatile LAS u2*)(u32)(o + 8);
    return u4{a[0], a[1], b[0], b[1]};
}
DI void a_rows_out(int yb, int ystr, char* o) {
    u2 y[6];
#pragma unroll
    for (int c = 0; c < 6; c++) y[c] = lds_ld<u2>(yb + c * ystr);
#pragma unroll
    for (int r = 0; r < 4; r++) {
        u3 v;
#pragma unroll
        for (int c = 0; c < 3; c++) v[c] = __builtin_amdgcn_perm(y[2 * c + 1][r >> 1], y[2 * c][r >> 1], (r & 1) ? 0x07060302u : 0x05040100u);
        *(u3*)(o + r * (4 * DM)) = v;
    }
}
template <int R> DI void stage(u4 v) {
    asm volatile("ds_write_addtid_b32 %0 offset:%4\n\tds_write_addtid_b32 %1 offset:%5\n\tds_write_addtid_b32 %2 offset:%6\n\tds_write_addtid_b32 %3 offset:%7"
                 :: "v"(v[0]), "v"(v[1]), "v"(v[2]), "v"(v[3]), "i"(288 * R), "i"(288 * R + 288), "i"(288 * R + 576), "i"(288 * R + 864) : "memory");
}
#define CM_MUL(d, a, b) "v_pk_mul_f32 " d ", " a ", " b " op_sel_hi:[0,1]\n\t"
#define CM_FMA(d, a, b) "v_pk_fma_f32 " d ", " a ", " b ", " d " op_sel:[1,1,0] op_sel_hi:[1,0,1] neg_lo:[1,0,0]\n\t"
struct C4 { f2 a, b, c, d; };
DI C4 cmul4(f2 a0, f2 b0, f2 a1, f2 b1, f2 a2, f2 b2, f2 a3, f2 b3) {
    C4 r;
    asm("s_nop 0\n\t" CM_MUL("%0", "%4", "%5") CM_MUL("%1", "%6", "%7") CM_MUL("%2", "%8", "%9") CM_MUL("%3", "%10", "%11")
        CM_FMA("%0", "%4", "%5") CM_FMA("%1", "%6", "%7") CM_FMA("%2", "%8", "%9") CM_FMA("%3", "%10", "%11") "s_nop 0"
        : "=&v"(r.a), "=&v"(r.b), "=&v"(r.c), "=&v"(r.d) : "v"(a0), "v"(b0), "v"(a1), "v"(b1), "v"(a2), "v"(b2), "v"(a3), "v"(b3));
    return r;
}
DI C4 cmul4(const C4& a, f2 b) { return cmul4(a.a, b, a.b, b, a.c, b, a.d, b); }
DI u4 pk4(const C4& v) { return u4{pk(v.a.x, v.a.y), pk(v.b.x, v.b.y), pk(v.c.x, v.c.y), pk(v.d.x, v.d.y)}; }

DI void sig(int f) {
    asm volatile("s_waitcnt lgkmcnt(0)" ::: "memory");
    if ((threadIdx.x & 63) == 0) __hip_atomic_fetch_add((LAS u32*)(u32)(LA_F + 4 * f), 1u, __ATOMIC_RELAXED, __HIP_MEMORY_SCOPE_WORKGROUP);
}
template <int SLEEP = 0> DI void await(int f, u32 n) {
    while ((u32)__builtin_amdgcn_readfirstlane(*(const volatile LAS u32*)(u32)(LA_F + 4 * f)) < n) if (SLEEP) __builtin_amdgcn_s_sleep(SLEEP);
    asm volatile("" ::: "memory");
}
DI float exp_bf(float x) {
    const float t = x * 1.44269504f, r = __builtin_fmaf(x, 1.92596299e-8f, __builtin_fmaf(x, 1.44269504f, -t));
    const float e = __builtin_amdgcn_exp2f(t);
    return __builtin_fmaf(e * 0.693147181f, r, e);
}

struct APar { u32 l, a, r, i, t, d; u4 sel2, sel3, selx, tadr; };
DI void head_a(const u16* __restrict__ u, APar pr, u16* __restrict__ out, char* ws) {
    const int tid = threadIdx.x, lane = tid & 63, c16 = lane & 15, g = lane >> 4;
    const int wv = __builtin_amdgcn_readfirstlane(tid >> 6);
    STAMP_DECL;
    const int ai_ = blockIdx.x - A0;
    const int d0 = 6 * ((ai_ & 7) * 12 + (ai_ >> 3));
    const f4 Z = {0.f, 0.f, 0.f, 0.f};

    if (wv >= 12) {
        __builtin_amdgcn_s_setprio(3);
        const int t = tid - 768;
        if (t == 0) lds_st<u2>(LA_F, u2{0u, 0u});
        if (t < 180) lds_st<u4>(LA_U + (t / 30) * 4608 + (t % 30) * 16, u4{0u, 0u, 0u, 0u});
        bar();
#pragma unroll
        for (int i = 0; i < 2; i++)
            if (i == 0 || wv < 14) {
                const int r = min((ai_ >> 3) + 12 * (128 * i + (t >> 1)), SEQ - 1);
                u32 keep;
                asm volatile("s_mov_b32 %0, m0\n\ts_mov_b32 m0, %3\n\ts_nop 4\n\tglobal_load_lds_dword %1, %2\n\ts_mov_b32 m0, %0"
                             : "=&s"(keep) : "v"(r * (2 * DM) + (((ai_ & 7) * 144) & ~127) + (t & 1) * 128), "s"(u), "s"(LA_D) : "memory");
            }
        u3 ua[4], ub[4];
#pragma unroll
        for (int r = 0; r < 4; r++) ua[r] = *(const u3*)(u + (4 * t + r) * DM + d0);
        sig(FU0);
        await<1>(FU0, 4);
#pragma unroll
        for (int r = 0; r < 4; r++) ub[r] = *(const u3*)(u + (1024 + 4 * t + r) * DM + d0);
        asm volatile("" ::: "memory");
#pragma unroll
        for (int c = 0; c < 6; c++) {
            const u32 sel = (c & 1) ? 0x07060302u : 0x05040100u;
            lds_st<u2>(LA_U + c * 4608 + 480 + t * 8, u2{__builtin_amdgcn_perm(ua[1][c >> 1], ua[0][c >> 1], sel), __builtin_amdgcn_perm(ua[3][c >> 1], ua[2][c >> 1], sel)});
        }
        STAMP(7);
        bar();
        bar();
#pragma unroll
        for (int c = 0; c < 6; c++) {
            const u32 sel = (c & 1) ? 0x07060302u : 0x05040100u;
            lds_st<u2>(LA_U + c * 4608 + 480 + 2048 + t * 8, u2{__builtin_amdgcn_perm(ub[1][c >> 1], ub[0][c >> 1], sel), __builtin_amdgcn_perm(ub[3][c >> 1], ub[2][c >> 1], sel)});
        }
        sig(FU2);
        STAMP(8);
        const int tile = wv - 12, yl = ((lane >> 2) + 16 * (lane & 3)) * 8;
        char* o = (char*)(out + 4 * t * (2 * DM) + d0);
        bar();
        bar();
        STAMP(11);
        a_rows_out(LA_G + ((tile >> 1) ^ 1) * 4608 + (tile & 1) * 1024 + yl, 9216, o);
        STAMP(9);
        bar();
        bar();
        STAMP(12);
        a_rows_out(LA_G + ((tile >> 1) ^ 1) * 4608 + (tile & 1) * 1024 + 2048 + yl, 9216, o + 1024 * (4 * DM));
        STAMP(10);
        STAMP_OUT(768);
        return;
    }
    const int ch = wv >> 1, hf = wv & 1;
    {
        __builtin_amdgcn_s_barrier();
        asm volatile("s_waitcnt vmcnt(4)" : "+v"(pr.l), "+v"(pr.a), "+v"(pr.r), "+v"(pr.i), "+v"(pr.t), "+v"(pr.d) :: "memory");
        PRIO(3);
        const float dt = rbf(exp_bf(blo(pr.t)));
        const float ar2 = -rbf(exp_bf(blo(pr.l))) * dt * 1.44269504f, air = blo(pr.a) * dt * 0.159154943f;
        const f2 C = {2.f * dt * blo(pr.r), 2.f * dt * blo(pr.i)};
        STAMP(1);
        const f2 z1 = cexpa(ar2, air, 1.f), z8c = cexpa(ar2, -air, 8.f), z128 = cexpa(ar2, air, 128.f);
        const int gr = LA_G + wv * 4608 + c16 * 288 + g * 16, q = LA_Q + wv * 2576;
        asm volatile("s_mov_b32 m0, %0\n\ts_nop 0" :: "s"(LA_G + wv * 4608) : "memory");
#define SB __builtin_amdgcn_sched_barrier(0)
#define STAGE16(v) do { stage<0>(v[0]); stage<4>(v[1]); stage<8>(v[2]); stage<12>(v[3]); } while (0)
#define READ4(f) do { _Pragma("unroll") for (int s = 0; s < 4; s++) f[s] = lds_ld<u4>(gr + 64 * s); SB; } while (0)
        u4 v[4], bfr[4], afr[4];
        const C4 s1 = cmul4(z8c, z8c, z1, z1, z128, z128, C, z1);
        const C4 s2 = cmul4(s1.a, s1.a, z8c, s1.a, s1.c, s1.c, C, s1.b);
        {
            C4 w = {f2{1.f, 0.f}, z8c, s1.a, s2.b};
            v[0] = pk4(w);
#pragma unroll
            for (int i = 1; i < 4; i++) { w = cmul4(w, s2.a); v[i] = pk4(w); }
        }
        STAGE16(v); READ4(bfr);
        PRIO(2);
        const C4 s3 = cmul4(s1.d, s1.b, s1.b, s1.b, s2.c, s2.c, s2.c, z128);
        const C4 e0 = {C, s1.d, s2.d, s3.a}, e1 = cmul4(e0, s3.b);
        v[0] = pk4(e0); v[1] = pk4(e1); v[2] = pk4(cmul4(e0, z128)); v[3] = pk4(cmul4(e1, z128));
        STAGE16(v); READ4(afr);
        PRIO(1);
        {
            const C4 f = cmul4(C4{e0.a, e0.c, e1.a, e1.c}, s1.c), h = cmul4(e0.a, s2.c, e1.a, s2.c, e0.a, s3.d, e1.a, s3.d);
            v[0] = pk4(f); v[1] = pk4(cmul4(f, z128)); v[2] = pk4(h); v[3] = pk4(cmul4(h, s1.c));
        }
        SB;
        f4 acc = Z;
#pragma unroll
        for (int s = 0; s < 4; s++) acc = mfma(afr[s], bfr[s], acc);
        STAGE16(v); READ4(afr);
        PRIO(0);
        lds_st<f4>(q + (4 * (g & 1) + 128 * (g >> 1) + 8 * c16) * 4, acc);
        {
            const f2 f0 = cmul(C, s3.c), f1 = cmul(f0, z128);
            const C4 f = cmul4(f0, s1.c, f1, s1.c, f0, s3.c, f0, s3.c), h = cmul4(C4{f0, f1, f.a, f.b}, s2.c);
            v[0] = pk4(C4{f0, f1, f.a, f.b}); v[1] = pk4(h); v[2][0] = pk(f.c.x, f.c.y);
        }
        SB;
        acc = Z;
#pragma unroll
        for (int s = 0; s < 4; s++) acc = mfma(afr[s], bfr[s], acc);
        stage<0>(v[0]); stage<4>(v[1]);
        asm volatile("ds_write_addtid_b32 %0 offset:%1" :: "v"(v[2][0]), "i"(288 * 8) : "memory");
        READ4(afr);
        if (g < 2) lds_st<f4>(q + (256 + 64 * g + 4 * c16) * 4, acc);
        else {
            const int o = q + (384 + 64 * (g - 2) + 2 * c16) * 4;
            lds_st<f2>(o, f2{acc[0], acc[1]}); lds_st<f2>(o + 128, f2{acc[2], acc[3]});
        }
        acc = Z;
#pragma unroll
        for (int s = 0; s < 4; s++) acc = mfma(afr[s], bfr[s], acc);
        if (g < 2) {
#pragma unroll
            for (int i = 0; i < 4; i++) lds_st<float>(q + (512 + 64 * g + 16 * i + c16) * 4, acc[i]);
        } else if (lane == 32) lds_st<float>(q + 640 * 4, acc[0]);
    }
    STAMP(2);
    asm volatile("s_waitcnt vmcnt(0) lgkmcnt(0)" : "+v"(pr.sel2), "+v"(pr.sel3), "+v"(pr.selx), "+v"(pr.tadr) :: "memory");
    const u4 sel2 = pr.sel2, sel3 = pr.sel3, selx = pr.selx, tadr = pr.tadr;
    bar();
    STAMP(9);
    const int k0 = LA_K + ch * KC;
    {
        const LAS float* qa = (const LAS float*)(u32)(LA_Q + ch * 5152);
        const LAS float* qb = qa + 644;
        u2 w[4];
        int y;
        if (hf == 0) {
            const int j = 384 + 4 * lane;
            const f4 a = *(const LAS f4*)(qa + j) + *(const LAS f4*)(qb + j), b = *(const LAS f4*)(qa + j + 4) + *(const LAS f4*)(qb + j + 4);
            const u32 ix = tadr[3];
            const float e0 = qa[(ix & 0xffff) >> 2] + qb[(ix & 0xffff) >> 2], e1 = qa[ix >> 18] + qb[ix >> 18];
            f4 k = *(const LAS f4*)(qa + 4 * lane) + *(const LAS f4*)(qb + 4 * lane);
            f4 p = *(const LAS f4*)(qa + 4 * lane - 4) + *(const LAS f4*)(qb + 4 * lane - 4);
            const u32 p23 = pk(a[2], a[3]), p34 = pk(a[3], b[0]);
            lds_st<u4>(k0 + 4 * KI + lane * 32, u4{pk(a[0], a[1]), p23, pk(a[1], a[2]), p34});
            lds_st<u4>(k0 + 4 * KI + lane * 32 + 16, u4{p23, pk(b[0], b[1]), p34, pk(b[1], b[2])});
            lds_st<u32>(k0 + g * KI + c16 * 4, pk(e0, e1));
            if (lane == 0) { k[0] += blo(pr.d); p = Z; }
            const u32 c = pk(k[1], k[0]), d = pk(k[0], p[3]);
            w[0] = u2{pk(k[3], k[2]), c}; w[1] = u2{pk(k[2], k[1]), d}; w[2] = u2{c, pk(p[3], p[2])}; w[3] = u2{d, pk(p[2], p[1])};
            y = k0 + (270 - 2 * lane) * 4;
        } else {
            const int j = 256 + 2 * lane;
            const float q0 = qa[j] + qb[j], q1 = qa[j + 1] + qb[j + 1], q2 = qa[j + 2] + qb[j + 2];
            const float p1 = qa[j - 1] + qb[j - 1], p2 = qa[j - 2] + qb[j - 2], p3 = qa[j - 3] + qb[j - 3];
            const u32 c = pk(q0, q0), d = pk(q0, p1), e = pk(p1, p2), f = lane ? c : d;
            w[0] = u2{pk(q2, q1), c}; w[1] = u2{pk(q1, q0), f}; w[2] = u2{c, lane ? d : e}; w[3] = u2{f, lane ? e : pk(p2, p3)};
            y = k0 + (142 - 2 * lane) * 4;
            if (lane < 32) lds_st<u32>(k0 + (lane >> 3) * KI + (272 + (lane & 7)) * 4, 0u);
        }
#pragma unroll
        for (int i = 0; i < 4; i++) lds_st<u2>(y + i * KI, w[i]);
    }
    const int x0 = 2031 - c16 + 8 * g;
    int ka = k0 + (x0 & 3) * KI + (x0 >> 2) * 8 - 3008 - 256 * hf - 192 - 512;
    int ubs = LA_U + ch * 4608 + 480 + (16 * c16 + 8 * g - 16) * 2 - 256 * hf - 192;
    int q2a = k0 + 4 * KI + tadr[0], q3a = k0 + 4 * KI + tadr[1], qxa = k0 + 4 * KI + tadr[2];
    pin(ka); pin(ubs); pin(q2a); pin(q3a); pin(qxa);
    STAMP(13);
    bar();
    STAMP(3);
#define KFR(rho, p) ld_k(ka + 192 - 64 * (rho) + 512 - 512 * (p))
#define UFR(rho, q) lds_ld<u4>(ubs + 192 - 64 * (rho) + 512 * (q))
#define QFR(rho, p) ((p) < 4 ? lds_ld<u2>(q2a + 64 * (8 * ((p) - 2) + (rho))) : ((p) == 4 && (rho) == 0 ? lds_ld<u2>(qxa) : lds_ld<u2>(q3a + 32 * (8 * ((p) - 4) + (rho)))))
#define QSEL(rho, p) ((p) < 4 ? sel2 : ((p) == 4 && (rho) == 0 ? selx : sel3))
#define QBUILD(r, s) u4{__builtin_amdgcn_perm(r[1], r[0], s[0]), __builtin_amdgcn_perm(r[1], r[0], s[1]), __builtin_amdgcn_perm(r[1], r[0], s[2]), __builtin_amdgcn_perm(r[1], r[0], s[3])}
#define FIRST2(dst) do { if (hf) { const u2 r_ = QFR(0, 2); dst = QBUILD(r_, sel2); } else dst = ld_k(ka - 320); } while (0)
    f4 acc[8];
#pragma unroll
    for (int i = 0; i < 8; i++) acc[i] = Z;
    {
        u4 A[2], T[2], B[2][4];
        u2 R[8];
#pragma unroll
        for (int p = 0; p < 4; p++) B[0][p] = UFR(0, p);
        A[0] = KFR(0, 0); A[1] = KFR(0, 1);
#pragma unroll
        for (int p = 3; p < 8; p++) R[p] = QFR(0, p);
        FIRST2(T[0]);
#pragma unroll
        for (int rho = 0; rho < 4; rho++)
#pragma unroll
            for (int p = 0; p < 8; p++) {
                SB;
                if (p == 0) { if (rho == 0) PRIO(3); else if (rho == 1) PRIO(2); else if (rho == 2) PRIO(1); else PRIO(0); }
                if (rho < 3 && p < 4) B[(rho + 1) & 1][p] = UFR(rho + 1, p);
                if (p >= 1 && p < 7 && !(p == 1 && rho == 0)) T[(p + 1) & 1] = QBUILD(R[p + 1], QSEL(rho, p + 1));
#pragma unroll
                for (int q = 0; q < 4; q++)
                    if (p + q < 8) acc[p + q] = mfma(p < 2 ? A[p] : T[p & 1], B[rho & 1][q], acc[p + q]);
                SB;
                if (rho < 3) {
                    if (p < 2) A[p] = KFR(rho + 1, p);
                    if (p >= 1 && p < 7) R[p + 1] = QFR(rho + 1, p + 1);
                }
            }
    }
#pragma unroll
    for (int i = 0; i < 8; i++) pin(acc[i]);
    STAMP(4);
#define EXCHANGE(t0, off)                                                                     \
    {                                                                                         \
        const int mine = LA_G + wv * 4608 + (off), other = LA_G + (wv ^ 1) * 4608 + (off);     \
        if (hf) { lds_st<f4>(mine + lane * 16, acc[t0]); lds_st<f4>(mine + 1024 + lane * 16, acc[t0 + 1]); acc[t0] = acc[t0 + 2]; acc[t0 + 1] = acc[t0 + 3]; } \
        else { lds_st<f4>(mine + lane * 16, acc[t0 + 2]); lds_st<f4>(mine + 1024 + lane * 16, acc[t0 + 3]); } \
        bar();                                                                                \
        const f4 y0 = lds_ld<f4>(other + lane * 16) + acc[t0], y1 = lds_ld<f4>(other + 1024 + lane * 16) + acc[t0 + 1]; \
        lds_st<u2>(other + lane * 8, u2{pk(y0[0], y0[1]), pk(y0[2], y0[3])});                 \
        lds_st<u2>(other + 1024 + lane * 8, u2{pk(y1[0], y1[1]), pk(y1[2], y1[3])});          \
    }
    await<1>(FU2, 4);
    EXCHANGE(0, 0);
    {
        u4 A[2][2], T[2], B[2][4];
        u2 R[4];
#pragma unroll
        for (int p = 0; p < 4; p++) B[0][p] = UFR(0, 4 + p);
        A[0][0] = KFR(0, 0); A[0][1] = KFR(0, 1); R[3] = QFR(0, 3);
        FIRST2(T[0]);
        bar();
        STAMP(14);
#pragma unroll
        for (int rho = 0; rho < 4; rho++)
#pragma unroll
            for (int p = 0; p < 4; p++) {
                SB;
                if (p == 0) { if (rho == 0) PRIO(3); else if (rho == 1) PRIO(2); else if (rho == 2) PRIO(1); else PRIO(0); }
                if (rho < 3) { B[(rho + 1) & 1][p] = UFR(rho + 1, 4 + p); if (p < 2) A[(rho + 1) & 1][p] = KFR(rho + 1, p); }
                if (p == 0 && rho > 0) T[0] = QBUILD(R[2], sel2);
                if (p == 1) T[1] = QBUILD(R[3], sel2);
#pragma unroll
                for (int q = 0; p + q < 4; q++) acc[4 + p + q] = mfma(p < 2 ? A[rho & 1][p] : T[p & 1], B[rho & 1][q], acc[4 + p + q]);
                SB;
                if (rho < 3 && p < 2) R[2 + p] = QFR(rho + 1, 2 + p);
            }
        SB;
    }
#pragma unroll
    for (int i = 4; i < 8; i++) pin(acc[i]);
    STAMP(5);
    EXCHANGE(4, 2048);
    bar();
    STAMP(6);
    STAMP_OUT(0);
}

__global__ void __launch_bounds__(NT) __attribute__((aligned(4096))) fused_kernel(const u16* __restrict__ u, const u16* __restrict__ lar, const u16* __restrict__ aimg,
                                                   const u16* __restrict__ cre, const u16* __restrict__ cim, const u16* __restrict__ ldt,
                                                   const u16* __restrict__ dsk, const u16* __restrict__ x, const u16* __restrict__ wr,
                                                   const u16* __restrict__ wi, u16* __restrict__ out, char* ws) {
    const char* entry;
    asm volatile("s_getpc_b64 %0" : "=s"(entry));
    APar pr;
    if (blockIdx.x >= A0 && __builtin_amdgcn_readfirstlane(threadIdx.x) < 768) {
        const int a = blockIdx.x - A0, d = 6 * ((a & 7) * 12 + (a >> 3)) + __builtin_amdgcn_readfirstlane(threadIdx.x >> 7);
        asm volatile("global_load_ushort %0, %6, %8\n\tglobal_load_ushort %1, %6, %9\n\tglobal_load_ushort %2, %6, %10\n\t"
                     "global_load_ushort %3, %6, %11\n\tglobal_load_ushort %4, %7, %12\n\tglobal_load_ushort %5, %7, %13"
                     : "=&v"(pr.l), "=&v"(pr.a), "=&v"(pr.r), "=&v"(pr.i), "=&v"(pr.t), "=&v"(pr.d)
                     : "v"((d * NS + (threadIdx.x & 127)) * 2), "v"(d * 2), "s"(lar), "s"(aimg), "s"(cre), "s"(cim), "s"(ldt), "s"(dsk) : "memory");
        asm volatile("global_load_dwordx4 %0, %4, %5\n\tglobal_load_dwordx4 %1, %4, %5 offset:1024\n\t"
                     "global_load_dwordx4 %2, %4, %5 offset:2048\n\tglobal_load_dwordx4 %3, %4, %5 offset:3072"
                     : "=&v"(pr.sel2), "=&v"(pr.sel3), "=&v"(pr.selx), "=&v"(pr.tadr)
                     : "v"((threadIdx.x & 63) * 16 + (threadIdx.x & 64) * 64), "s"((const char*)&TAB + F_END * 1024) : "memory");
    }
    if (__builtin_expect(blockIdx.x < NB2, 1)) head_b(entry, x, wr, wi, out, ws);
    else if (blockIdx.x >= A0) head_a(u, pr, out, ws);
    else warm_code(entry);
}

extern "C" void kernel_launch(void* const* d_in, const int* in_sizes, int n_in, void* d_out, int out_size, void* d_ws,
                              size_t ws_size, hipStream_t stream) {
    (void)hipFuncSetAttribute((const void*)fused_kernel, hipFuncAttributeMaxDynamicSharedMemorySize, LDS_BYTES);
    fused_kernel<<<GRID, NT, LDS_BYTES, stream>>>((const u16*)d_in[0], (const u16*)d_in[1], (const u16*)d_in[2], (const u16*)d_in[3],
                                                  (const u16*)d_in[4], (const u16*)d_in[5], (const u16*)d_in[6], (const u16*)d_in[7],
                                                  (const u16*)d_in[8], (const u16*)d_in[9], (u16*)d_out, (char*)d_ws);
}
```

```cpp
#define POLL_SLEEP 6
#include <hip/hip_runtime.h>

typedef unsigned short u16;
typedef unsigned int u32;
typedef float f2 __attribute__((ext_vector_type(2)));
typedef float f4 __attribute__((ext_vector_type(4)));
typedef u32 u2 __attribute__((ext_vector_type(2)));
typedef u32 u4 __attribute__((ext_vector_type(4)));
typedef int i8v __attribute__((ext_vector_type(8)));
typedef short s4 __attribute__((ext_vector_type(4)));
typedef short s8 __attribute__((ext_vector_type(8)));
typedef __bf16 bf2 __attribute__((ext_vector_type(2)));
#define DI __device__ __forceinline__
#define LAS __attribute__((address_space(3)))
#define GAS __attribute__((address_space(1)))
#define CAS __attribute__((address_space(4)))
#pragma clang diagnostic ignored "-Wint-to-pointer-cast"
struct u3 { u32 v[3]; DI u32& operator[](int i) { return v[i]; } DI const u32& operator[](int i) const { return v[i]; } };

constexpr int SEQ = 2048, DM = 576, NS = 128, DF = 289;
constexpr int NB1 = 128;
constexpr int NB2 = 145;
constexpr int A0 = 152;
constexpr int NA = 96;
constexpr int GRID = A0 + NA;
constexpr int NT = 1024;

constexpr u32 WS_EPOCH = 0, WS_F1 = 4096, WS_F2 = WS_F1 + 16 * 16 * 128, WS_X1 = 1u << 20, WS_Y1 = 4u << 20, WS_USED = 8u << 20;
#define STAMP(i)
#define STAMP_DECL
#define STAMP_OUT(T)

constexpr int F_SA = 0, F_EO = 4;
constexpr int F_ARE = 9, F_AIM = 10, F_BRE = 11, F_BIM = 12, F_G = 13;
constexpr int F_F16 = 21, F_GI = 23, F_AI = 31, F_M1C = 33;
constexpr int F_VAR = 37;
constexpr int F_END = F_VAR + 40;
constexpr int ET = 452, ER = 28, E2V = 580, E2U = 36, OP = 1168;
constexpr int LB_X = 0, LB_I = 0, LB_Y = 0;
constexpr int LB_E = 16 * OP;
constexpr int LB_O = LB_E, LB_E2 = LB_E;
constexpr int LB_S = LB_E + 16 * ET * 4;
constexpr int LB_W = LB_S + 1200 * 16;
constexpr int LB_T = LB_W + 2 * 256 * 36;
constexpr int LB_G = LB_T + (F_VAR + 16) * 1024;
constexpr int LB_END = LB_G + 16;
struct CosTab { double c[2048]; double c9[9], s9[9]; };
constexpr double tay_cos(double x) { double t = 1, s = 1; for (int k = 1; k <= 14; k++) { t *= -x * x / ((2 * k - 1) * (2 * k)); s += t; } return s; }
constexpr double tay_sin(double x) { double t = x, s = x; for (int k = 1; k <= 14; k++) { t *= -x * x / ((2 * k + 1) * (2 * k)); s += t; } return s; }
constexpr CosTab make_cos() {
    CosTab t = {};
    const double PI2 = 6.283185307179586476925;
    for (int k = 0; k <= 512; k++) {
        const double v = k == 512 ? 0.0 : (k <= 256 ? tay_cos(PI2 * k / 2048) : tay_sin(PI2 * (512 - k) / 2048));
        t.c[k] = v; t.c[(2048 - k) & 2047] = v; t.c[1024 - k] = -v; t.c[(1024 + k) & 2047] = -v;
    }
    t.c[512] = 0.0; t.c[1536] = 0.0;
    for (int k = 0; k < 9; k++) { const double a = PI2 * (k <= 4 ? k : k - 9) / 9; t.c9[k] = tay_cos(a); t.s9[k] = tay_sin(a); }
    return t;
}
constexpr CosTab CT = make_cos();
constexpr double cs(int n) { return CT.c[n & 2047]; }
constexpr double sn(int n) { return CT.c[(n - 512) & 2047]; }
constexpr u32 bfr(double x) { const u32 b = __builtin_bit_cast(u32, (float)x); return (b + 0x7fffu + ((b >> 16) & 1u)) >> 16; }
constexpr u32 pkc(double lo, double hi) { return bfr(lo) | (bfr(hi) << 16); }
constexpr u32 f32c(double x) { return __builtin_bit_cast(u32, (float)x); }
template <int N> struct Frags { u32 w[N * 256]; };
constexpr u32 w_re(double amp, int n) { return pkc(amp * cs(n), amp * sn(n)); }
constexpr u32 w_im(double amp, int n) { return pkc(-amp * sn(n), amp * cs(n)); }
constexpr Frags<F_VAR> make_t0() {
    Frags<F_VAR> t = {};
    const double R2 = 1.4142135623730951;
    for (int l = 0; l < 64; l++)
        for (int i = 0; i < 4; i++) {
            const int c = l & 15, g = l >> 4, r = 4 * g + i, o = l * 4 + i;
            {
                const int b = c >> 2, tq = c & 3;
                const double amp = b ? 1.0 : R2;
                const int n0 = 512 * tq * g + (b ? 256 * b * (2 * i) : -256), n1 = 512 * tq * g + (b ? 256 * b * (2 * i + 1) : 256);
                t.w[F_ARE * 256 + o] = pkc(amp * cs(n0), amp * cs(n1));
                t.w[F_AIM * 256 + o] = pkc(-amp * sn(n0), -amp * sn(n1));
            }
            for (int w = 0; w < 9; w++) {
                const int sh = 16 * (i & 1), oo = w * 128 + l * 2 + (i >> 1);
                t.w[F_EO * 256 + oo] |= (u32)((c * ET + r * ER + 2 * ((7 * w + r) % 9)) * 4) << sh;
                if (w < 8) {
                    const int k = (9 * (w & 3) + 36 * r + 64 * (c % 9)) % DM, slot = w < 4 ? (k <= 288 ? 2 * k : 2 * (DM - k) + 1) : k;
                    t.w[F_SA * 256 + oo] |= (u32)(slot * 8 + (slot >> 6) * 16) << sh;
                }
            }
            if (r < 9) { const int k = (r * c) % 9; t.w[F_BRE * 256 + o] = pkc(CT.c9[k], CT.s9[k]); t.w[F_BIM * 256 + o] = pkc(-CT.s9[k], CT.c9[k]); }
            for (int q = 0; q < 4; q++) {
                t.w[(F_G + 2 * q) * 256 + o] = w_re(1.0, 32 * r * (q + 4 * c)); t.w[(F_G + 2 * q + 1) * 256 + o] = w_im(1.0, 32 * r * (q + 4 * c));
                t.w[(F_GI + 2 * q) * 256 + o] = w_re(1.0, 32 * c * (q + 4 * r)); t.w[(F_GI + 2 * q + 1) * 256 + o] = w_im(1.0, 32 * c * (q + 4 * r));
            }
            t.w[F_F16 * 256 + o] = w_re(1.0, 128 * r * c); t.w[(F_F16 + 1) * 256 + o] = w_im(1.0, 128 * r * c);
            for (int h = 0; h < 2; h++) {
                const int j = 4 * h + (c >> 2), n = 512 * i * (c & 3) + (g ? 256 * g * j : ((j & 1) ? 256 : -256));
                t.w[(F_AI + h) * 256 + o] = w_re(g ? 2.0 : R2, n);
                const int m = 16 * r * (4 * h + 8 * c) - 512 * h;
                t.w[(F_M1C + 2 * h) * 256 + o] = pkc(0.5 * cs(m), -0.5 * sn(m));
                t.w[(F_M1C + 2 * h + 1) * 256 + o] = pkc(-0.5 * sn(m), -0.5 * cs(m));
            }
        }
    return t;
}
constexpr Frags<40> make_t1() {
    Frags<40> t = {};
    for (int v = 0; v < 5; v++)
        for (int l = 0; l < 64; l++)
            for (int i = 0; i < 4; i++) {
                const int be = v < 3 ? v + 1 : 4 * (v - 3), c = l & 15, r = 4 * (l >> 4) + i, o = v * 2048 + l * 4 + i;
                const double amp = v < 3 ? 1.0 : 0.5;
                const int n1 = 16 * r * (be + 8 * c) + (v == 4 ? 512 : 0), n4 = 16 * c * (be + 8 * r);
                t.w[o] = w_re(amp, n1); t.w[o + 256] = w_im(amp, n1);
                t.w[o + 512] = w_re(1.0, n4); t.w[o + 768] = w_im(1.0, n4);
                t.w[o + 1024] = f32c(cs(r * (be + 8 * c))); t.w[o + 1280] = f32c(-sn(r * (be + 8 * c)));
                t.w[o + 1536] = f32c(cs(c * (be + 8 * r))); t.w[o + 1792] = f32c(-sn(c * (be + 8 * r)));
            }
    return t;
}
constexpr int qidx(int l) {
    if (l <= 256) return l;
    const int sh = l < 512 ? 1 : (l < 1024 ? 2 : 3), h = 1 << (sh - 1), m = l >> sh, r = l & ((1 << sh) - 1);
    return 128 * sh + m + ((r > h || (r == h && (m & 1))) ? 1 : 0);
}
constexpr Frags<8> make_t2() {
    Frags<8> t = {};
    for (int hf = 0; hf < 2; hf++)
        for (int l = 0; l < 64; l++) {
            const int sg[3] = {17 + 4 * hf, 33 + 4 * hf, 32 + 4 * hf}, back[3] = {8, 4, 0};
            for (int k = 0; k < 3; k++) {
                const int L = 32 * sg[k] + 16 + (l & 15) - 8 * (l >> 4), q0 = qidx(L - 7);
                for (int i = 0; i < 4; i++) {
                    const u32 a = 2 * (qidx(L - 2 * i) - q0), b = 2 * (qidx(L - 2 * i - 1) - q0);
                    t.w[(4 * hf + k) * 256 + l * 4 + i] = a | ((a + 1) << 8) | (b << 16) | ((b + 1) << 24);
                }
                t.w[(4 * hf + 3) * 256 + l * 4 + k] = (u32)((q0 - back[k] - 384) * 8);
            }
            const int e = 543 - 2 * (l & 15) - (l >> 4);
            t.w[(4 * hf + 3) * 256 + l * 4 + 3] = (u32)(qidx(e) * 4) | ((u32)(qidx(e - 1) * 4) << 16);
        }
    return t;
}
struct Tables { Frags<F_VAR> a; Frags<40> b; Frags<8> c; };
constexpr Frags<8> T2 = make_t2();
constexpr Frags<F_VAR> T0 = make_t0();
constexpr Frags<40> T1 = make_t1();
static __device__ const __attribute__((aligned(128))) Tables TAB = {T0, T1, T2};
constexpr int LA_G = 0;
constexpr int LA_K = LA_G + 12 * 4608;
constexpr int KI = 1344, KC = 4 * KI + 2048;
constexpr int LA_U = LA_K + 6 * KC;
constexpr int LA_Q = LA_U + 6 * 4608;
constexpr int LA_F = LA_Q + 12 * 2576;
constexpr int LA_D = LA_F + 16;
constexpr int LA_END = LA_D + 256;
constexpr int FU0 = 0, FU2 = 1;
constexpr int LDS_BYTES = (LA_END > LB_END ? LA_END : LB_END);
static_assert(LDS_BYTES <= 160 * 1024 && LDS_BYTES > 80 * 1024, "one workgroup per CU");

DI u32 pk(float a, float b) { f2 v = {a, b}; return __builtin_bit_cast(u32, __builtin_convertvector(v, bf2)); }
DI float blo(u32 v) { return __uint_as_float(v << 16); }
DI float bhi(u32 v) { return __uint_as_float(v & 0xffff0000u); }
DI float bf2f(u16 v) { return __uint_as_float((u32)v << 16); }
DI float rbf(float x) { return blo(pk(x, x)); }
DI f2 cmul(f2 a, f2 b) { f2 t = a.xx * b; f2 s = {-b.y, b.x}; return __builtin_elementwise_fma(a.yy, s, t); }
DI f4 mfma(u4 a, u4 b, f4 c) { return __builtin_amdgcn_mfma_f32_16x16x32_bf16(__builtin_bit_cast(s8, a), __builtin_bit_cast(s8, b), c, 0, 0, 0); }
DI u4 pack4(f4 re, f4 im) { u4 r = {pk(re[0], im[0]), pk(re[1], im[1]), pk(re[2], im[2]), pk(re[3], im[3])}; return r; }
DI u4 twid(f4 re, f4 im, f4 wr, f4 wi) { return pack4(__builtin_elementwise_fma(re, wr, -(im * wi)), __builtin_elementwise_fma(re, wi, im * wr)); }
DI int mod9(int x) { return x - 9 * ((x * 57) >> 9); }
template <class T> DI void pin(T& v) { asm volatile("" : "+v"(v)); }

template <class T> DI T lds_ld(int off) { return *(const LAS T*)(u32)off; }
template <class T> DI void lds_st(int off, T v) { *(LAS T*)(u32)off = v; }
DI void bar() { __syncthreads(); }
#define PRIO(n) __builtin_amdgcn_s_setprio(n)
#define PRIOB(n)
DI void glds16(const void* g, u32 off, int lds) {
    u32 keep;
    asm volatile("s_mov_b32 %0, m0\n\ts_mov_b32 m0, %3\n\ts_nop 4\n\tglobal_load_lds_dwordx4 %1, %2\n\ts_mov_b32 m0, %0" : "=&s"(keep) : "v"(off), "s"(g), "s"(lds) : "memory");
}

DI void publish_tag(u32 epoch) {
    u32 t;
    asm volatile("v_add_u32 %0, 0x9E3779B1, %1\n\tds_write_b32 %2, %0\n\ts_waitcnt lgkmcnt(0)" : "=&v"(t) : "v"(epoch), "v"(LB_G) : "memory");
}
DI void wait_flag(__amdgpu_buffer_rsrc_t r, u32 off, u32 tag, bool on) {
    if (on) {
        const unsigned long long t0 = __builtin_amdgcn_s_memrealtime();
        while (!__all(__builtin_amdgcn_raw_buffer_load_b32(r, off, 0, 16) == tag)) {
            if (__builtin_amdgcn_s_memrealtime() - t0 > 5000000ull) break;
            __builtin_amdgcn_s_sleep(POLL_SLEEP);
            asm volatile("" ::: "memory");
        }
    }
    asm volatile("" ::: "memory");
}

constexpr u32 POISON = 0xFFFFFFFFu;
DI bool unset(u4 v) { return max(max(v[0], v[1]), max(v[2], v[3])) == POISON; }
#define DPOLL_SLEEP 1
template <bool TWO> DI void load_set(__amdgpu_buffer_rsrc_t r, u32 off, bool on, u4& a, bool on2, u4& b) {
    const unsigned long long t0 = __builtin_amdgcn_s_memrealtime();
    bool ma = on, mb = TWO && on2;
    for (;;) {
        if (ma) a = __builtin_amdgcn_raw_buffer_load_b128(r, off, 0, 16);
        if (TWO && mb) b = __builtin_amdgcn_raw_buffer_load_b128(r, off, 16384, 16);
        ma = ma && unset(a);
        if (TWO) mb = mb && unset(b);
        if (!__any(ma || mb) || __builtin_amdgcn_s_memrealtime() - t0 > 5000000ull) break;
        __builtin_amdgcn_s_sleep(DPOLL_SLEEP);
        asm volatile("" ::: "memory");
    }
}
constexpr int CODE_LINES = 128;
DI void warm_code(const char* entry) {
    if (threadIdx.x < CODE_LINES) { const u32 t = *(const GAS u32*)(entry + threadIdx.x * 128); asm volatile("" :: "v"(t)); }
}

DI void head_b(const char* entry, const u16* __restrict__ x, const u16* __restrict__ wr, const u16* __restrict__ wi,
               u16* __restrict__ out, char* ws) {
    const int tid = threadIdx.x, lane = tid & 63, c16 = lane & 15, g = lane >> 4;
    const int wv = __builtin_amdgcn_readfirstlane(tid >> 6);
    const int id = blockIdx.x;
    STAMP_DECL;
    const bool rows = id < NB1;
    const int s00 = 32 * (id >> 4) + (id & 15);
    const char* tab = (const char*)&TAB;
    const int R = (tid * 911) >> 16;
    const u32 xoff = (s00 + 16 * (R >> 3) + 256 * (R & 7)) * (2 * DM) + (tid - R * 72) * 16;
    asm volatile("" :: "s"(x), "s"(wr), "s"(wi), "s"(out), "s"(ws), "v"(xoff));
    const __amdgpu_buffer_rsrc_t rws = __builtin_amdgcn_make_buffer_rsrc(ws, 0, WS_USED, 0x00020000);
    STAMP(13);
    int gy = WS_Y1 + id * (NB2 * 128) + tid * 16;
    u32 epoch = 0;
    if (wv == 0) asm volatile("global_load_dword %0, %0, %1 sc1" : "+v"(epoch) : "s"(ws) : "memory");
    if (rows) {
        glds16(x, xoff, LB_X + wv * 1024);
        if (wv < 2) glds16(x, (s00 + 16 + 1536) * (2 * DM) + (tid < 56 ? (tid + 16) * 16 : 256 * 2 * DM + (tid - 56) * 16), LB_X + 16384 + wv * 1024);
        else glds16(tab, tid * 16 - 2048, LB_T + (wv - 2) * 1024);
        if (wv >= 9) glds16(tab, tid * 16 + 5 * 1024, LB_T + (wv + 5) * 1024);
        const u4 P = {POISON, POISON, POISON, POISON};
        __builtin_amdgcn_raw_buffer_store_b128(P, rws, gy, 0, 16);
        if (tid <= 128) __builtin_amdgcn_raw_buffer_store_b128(P, rws, gy, 16384, 16);
    }
    const int u2i = (id & 7) ? (id & 7) * 18 + 1 + (id >> 3) : (id == 136 ? 0 : (id >> 3) + (id < 136 ? 1 : 0));
    const bool star = u2i >= 108;
    const int q36 = min((u2i * 57) >> 11, 3), kg = u2i - 36 * q36, bq = (q36 + 1) & 3;
    const bool mir = !star && kg >= 18;
    const int w0 = star ? min(16 * kg, 562) : (mir ? (2048 - bq) * (2 * DF) + 1122 - 32 * kg : bq * (2 * DF) + 32 * kg);
    __builtin_amdgcn_s_barrier();
    STAMP(14);
    const u32 woff = w0 + ((tid >> 1) & 255) * (mir ? -16 * DF : 16 * DF) + (tid & 1) * (star ? 8 * DF : 16);
    u4 wra, wrb;
    if (wv >= 8) {
        __builtin_memcpy(&wra, (const char*)wr + woff, 16);
        __builtin_memcpy(&wrb, (const char*)wi + woff, 16);
    }
    __builtin_amdgcn_sched_barrier(0);
    const f4 Z = {0.f, 0.f, 0.f, 0.f};
    const int tau = wv & 3, bw = wv >> 2;
    int tl = LB_T + lane * 16;
    int lin = LB_S + (tid + (tid >> 5)) * 16;
    pin(tl); pin(lin);
    int sadr[4];

    if (rows) {
        if (tid < 256) {
            const int o = LB_E + 72 + (tid & 15) * (ER * 4) + (tid >> 4) * (ET * 4);
            lds_st<u2>(o, u2{0u, 0u}); lds_st<u4>(o + 8, u4{0u, 0u, 0u, 0u});
        }
        int xo = LB_X + (c16 >> 2) * 1152 + 2 * (144 * ((g - wv) & 3) + 16 * wv + 4 * (c16 & 3));
        int bo = LB_E + (wv * ET + c16 * ER + min(8 * g, 16)) * 4;
        int go = WS_X1 + (tid >> 3) * 16384 + id * 128 + (tid & 7) * 16;
        int ta = LB_T + lane * 8;
        pin(xo); pin(bo); pin(go); pin(ta);
        STAMP(1);
        asm volatile("s_cmp_lt_u32 %0, 8\n\ts_cbranch_scc0 .Lw%=\n\ts_waitcnt vmcnt(0)\n.Lw%=:\n\ts_waitcnt vmcnt(2)" :: "s"(wv) : "memory", "scc");
        if (wv == 0) publish_tag(epoch);
        bar();
        STAMP(2);
        if (wv < 9) {
            u4 d[2];
#pragma unroll
            for (int s = 0; s < 2; s++) {
                const s4 lo = __builtin_amdgcn_ds_read_tr16_b64_v4i16((LAS s4*)(u32)(xo + s * 9216));
                const s4 hi = __builtin_amdgcn_ds_read_tr16_b64_v4i16((LAS s4*)(u32)(xo + s * 9216 + 4608));
                d[s] = __builtin_bit_cast(u4, __builtin_shufflevector(lo, hi, 0, 1, 2, 3, 4, 5, 6, 7));
            }
            const u4 are = lds_ld<u4>(tl + F_ARE * 1024), aim = lds_ld<u4>(tl + F_AIM * 1024);
            const u2 eo = lds_ld<u2>(ta + F_EO * 1024 + wv * 512);
#pragma unroll
            for (int s = 0; s < 2; s++) d[s] = pack4(mfma(d[s], are, Z), mfma(d[s], aim, Z));
#pragma unroll
            for (int i = 0; i < 4; i++) lds_st<u2>(LB_E + ((i & 1) ? eo[i >> 1] >> 16 : eo[i >> 1] & 0xffff), u2{d[0][i], d[1][i]});
        }
        u4 bre = lds_ld<u4>(tl + F_BRE * 1024), bim = lds_ld<u4>(tl + F_BIM * 1024);
        u4 gre = lds_ld<u4>(tl + (F_G + 2 * tau) * 1024), gim = lds_ld<u4>(tl + (F_G + 2 * tau + 1) * 1024);
        {
            const u2 sa = lds_ld<u2>(ta + F_SA * 1024 + (tau + (bw ? 4 : 0)) * 512);
            const int base = LB_S + ((bw + 3) & 3) * (DM * 8 + 144);
#pragma unroll
            for (int i = 0; i < 4; i++) { sadr[i] = base + ((i & 1) ? sa[i >> 1] >> 16 : sa[i >> 1] & 0xffff); pin(sadr[i]); }
        }
        pin(bre); pin(bim); pin(gre); pin(gim);
        bar();
        {
            PRIOB(2);
            const u4 p0 = lds_ld<u4>(bo), p1 = lds_ld<u4>(bo + 16);
            u4 d[2];
            f4 re[2], im[2];
#pragma unroll
            for (int s = 0; s < 2; s++) { const u4 a = {p0[s], p0[2 + s], p1[s], p1[2 + s]}; re[s] = mfma(a, bre, Z); im[s] = mfma(a, bim, Z); }
            PRIOB(1);
#pragma unroll
            for (int s = 0; s < 2; s++) { const u4 t = pack4(re[s], im[s]); re[s] = mfma(gre, t, Z); im[s] = mfma(gim, t, Z); }
            PRIOB(0);
#pragma unroll
            for (int s = 0; s < 2; s++) d[s] = pack4(re[s], im[s]);
            if (c16 < 9) {
#pragma unroll
                for (int i = 0; i < 4; i++) lds_st<u2>(sadr[i], u2{d[0][i], d[1][i]});
                if (wv == 0 && c16 == 0 && !(g & 1)) lds_st<u2>(sadr[0] + 8, u2{d[0][0], d[1][0]});
            }
        }
        bar();
        STAMP(3);
        __builtin_amdgcn_raw_buffer_store_b128(lds_ld<u4>(lin), rws, go, 0, 16);
        if (tid <= 128) __builtin_amdgcn_raw_buffer_store_b128(lds_ld<u4>(lin + 1056 * 16), rws, go, 128 * 16384, 16);
    } else {
        if (wv < 8 && (kg == 0 || kg == (star ? 36 : 18))) {
            const int m = tid & 255;
            lds_st<u16>(LB_W + tid * 36 + 32, ((wv >> 2) ? wi : wr)[((mir ? bq + 8 * m : 2048 - bq - 8 * m) & 2047) * DF + (kg ? 288 : 0)]);
        }
        warm_code(entry);
        asm volatile("s_waitcnt vmcnt(0)" ::: "memory");
        if (wv == 0) publish_tag(epoch);
        bar();
    }
    const u32 tag = lds_ld<u32>(LB_G);
    if (wv >= 8) {
        const int o = LB_W + ((tid >> 1) & 255) * 36 + (tid & 1) * 16;
#pragma unroll
        for (int i = 0; i < 4; i++) { lds_st<u32>(o + 4 * i, wra[i]); lds_st<u32>(o + 9216 + 4 * i, wrb[i]); }
    }
    asm volatile("s_waitcnt vmcnt(0)" ::: "memory");
    if (rows && lane == 0) __builtin_amdgcn_raw_buffer_store_b32(tag, rws, WS_F1 + (wv * 16 + (id >> 3)) * 128 + (id & 7) * 4, 0, 16);
    STAMP(4);
    glds16(tab, F_F16 * 1024 + tid * 16, LB_T + F_F16 * 1024 + wv * 1024);
    if (star || wv < 8) glds16(tab, (F_VAR + 8 * q36) * 1024 + tid * 16, LB_T + F_VAR * 1024 + wv * 1024);
    asm volatile("s_waitcnt vmcnt(0)" ::: "memory");
    bar();
    STAMP(12);

    const int typ = star ? (wv & 1) : 0;
    const int tv = tl + (F_VAR + 8 * typ) * 1024;
    u4 fre = lds_ld<u4>(tl + F_F16 * 1024), fim = lds_ld<u4>(tl + (F_F16 + 1) * 1024);
    u4 m1r = lds_ld<u4>(tv), m1i = lds_ld<u4>(tv + 1024), m4r = lds_ld<u4>(tv + 2048), m4i = lds_ld<u4>(tv + 3072);
    f4 twr = lds_ld<f4>(tv + 4096), twi = lds_ld<f4>(tv + 5120), tur = lds_ld<f4>(tv + 6144), tui = lds_ld<f4>(tv + 7168);
    u4 mcr = lds_ld<u4>(tl + (F_M1C + 2 * typ) * 1024), mci = lds_ld<u4>(tl + (F_M1C + 2 * typ + 1) * 1024);
    f4 wfr, wfi;
    {
        const int kcol = star ? 8 * kg + (wv >> 1) : 16 * kg + wv;
        const int cc = star ? 8 * typ + (u2i < NB2 - 1 ? (wv >> 1) : 7) : (mir ? 15 - wv : wv);
        const int wo = LB_W + (c16 + 64 * g) * 36 + cc * 2;
#pragma unroll
        for (int i = 0; i < 4; i++) {
            wfr[i] = bf2f(lds_ld<u16>(wo + i * 576));
            wfi[i] = bf2f(lds_ld<u16>(wo + i * 576 + 9216));
        }
        float sc = 1.f / (float)(SEQ * DM);
        if (kcol == 0 || kcol == 288) {
            sc *= 0.5f;
#pragma unroll
            for (int i = 0; i < 4; i++) {
                const int m = c16 + 64 * g + 16 * i;
                const int o = LB_W + (typ ? (255 - m) * 36 + cc * 2 : m * 36 + 32);
                wfr[i] += bf2f(lds_ld<u16>(o));
                wfi[i] -= bf2f(lds_ld<u16>(o + 9216));
            }
        }
        wfr *= sc; wfi *= (mir ? -sc : sc);
    }
    f4 wnr = -wfr, wni = -wfi;
    int io = (32 * g + c16) * 128 + (((wv >> 1) ^ (c16 >> 1)) * 16);
    int so = (tid >> 3) * 128 + (((tid & 7) ^ ((tid >> 4) & 7)) * 16);
    pin(fre); pin(fim); pin(m1r); pin(m1i); pin(m4r); pin(m4i); pin(twr); pin(twi); pin(tur); pin(tui); pin(mcr); pin(mci);
    int gi = WS_X1 + u2i * 16384 + tid * 16, gq = WS_Y1 + (tid >> 3) * (NB2 * 128) + u2i * 128 + (tid & 7) * 16;
    int i1 = LB_I + io + (star ? 0 : (wv & 1) * 8);
    io += LB_O + (wv & 1) * 8;
    pin(wfr); pin(wnr); pin(wni); pin(io); pin(so); pin(gi); pin(gq); pin(i1);
    wait_flag(rws, WS_F1 + (((u2i >> 3) & 15) * 16 + wv) * 128 + lane * 4, tag, lane < 8);
    STAMP(5);
    lds_st<u4>(LB_I + so, __builtin_amdgcn_raw_buffer_load_b128(rws, gi, 0, 16));
    bar();
    STAMP(6);
    {
        const int o1 = i1;
        PRIOB(3);
        const u2 a0 = lds_ld<u2>(o1), a1 = lds_ld<u2>(o1 + 2048);
        const u4 a = {a0[0], a0[1], a1[0], a1[1]};
        f4 re = mfma(a, m1r, Z), im = mfma(a, m1i, Z);
        if (star) {
            const u2 b0 = lds_ld<u2>(o1 + 8), b1 = lds_ld<u2>(o1 + 8 + 2048);
            const u4 b = {b0[0], b0[1], b1[0], b1[1]};
            re = mfma(b, mcr, re); im = mfma(b, mci, im);
        }
        u4 t = twid(re, im, twr, twi);
        re = mfma(fre, t, Z); im = mfma(fim, t, Z);
        PRIOB(2);
        t = pack4(__builtin_elementwise_fma(re, wfr, im * wni), __builtin_elementwise_fma(re, wni, im * wnr));
        re = mfma(t, fre, Z); im = mfma(t, fim, Z);
        PRIOB(1);
        t = twid(re, im, tur, tui);
        re = mfma(m4r, t, Z); im = mfma(m4i, t, Z);
        PRIOB(0);
        t = pack4(re, im);
        lds_st<u2>(io, u2{t[0], t[1]}); lds_st<u2>(io + 2048, u2{t[2], t[3]});
    }
    bar();
    STAMP(7);
    {
        u4 v = lds_ld<u4>(LB_O + so);
        if (star) {
            u4 w;
#pragma unroll
            for (int s = 0; s < 2; s++) {
                const float ar = blo(v[s]), ai = bhi(v[s]), cr = blo(v[2 + s]), ci = bhi(v[2 + s]);
                w[s] = pk(ar + ci, ai - cr); w[2 + s] = pk(ar - ci, -ai - cr);
            }
            v = w;
        }
        if (u2i < NB2 - 1 || (tid & 7) == 0)
            __builtin_amdgcn_raw_buffer_store_b128(v, rws, gq, 0, 16);
    }
    STAMP(8);
    if (!rows) { STAMP_OUT(0); return; }

    u4 gre = lds_ld<u4>(tl + (F_GI + 2 * tau) * 1024), gim = lds_ld<u4>(tl + (F_GI + 2 * tau + 1) * 1024);
    u4 bre = lds_ld<u4>(tl + F_BRE * 1024), bim = lds_ld<u4>(tl + F_BIM * 1024);
    u4 ai0 = lds_ld<u4>(tl + F_AI * 1024), ai1 = lds_ld<u4>(tl + (F_AI + 1) * 1024);
    int eadr[4];
#pragma unroll
    for (int i = 0; i < 4; i++) {
        const int r = 4 * g + i;
        eadr[i] = LB_E2 + (mod9(4 * (c16 + 18 - r)) * E2V + r * E2U + 2 * wv) * 4;
        pin(eadr[i]);
    }
    int ao = LB_E2 + (wv * E2V + c16 * E2U + 8 * g) * 4;
    int yo = LB_Y + (c16 >> 2) * OP + 2 * (144 * (((c16 & 3) - wv) & 3) + 16 * wv + 4 * g);
    int yl = LB_Y + tid * 16 + R * (OP - 1152);
    u32 oo = 2 * xoff - (tid - R * 72) * 16 + 2 * DM;
    pin(gre); pin(gim); pin(bre); pin(bim); pin(ai0); pin(ai1); pin(ao); pin(gy); pin(yo); pin(yl); pin(oo);
    STAMP(9);
    {
        u4 v, w;
        if (wv < 3) { load_set<true>(rws, gy, true, v, tid <= 128, w); if (tid <= 128) lds_st<u4>(lin + 1056 * 16, w); }
        else load_set<false>(rws, gy, true, v, false, w);
        lds_st<u4>(lin, v);
    }
    bar();
    if (id == 0 && tid == 0) __builtin_amdgcn_raw_buffer_store_b32(tag, rws, WS_EPOCH, 0, 16);
    STAMP(10);
    {
        PRIOB(2);
        u4 d[2];
        u2 p[4];
#pragma unroll
        for (int i = 0; i < 4; i++) p[i] = lds_ld<u2>(sadr[i]);
        f4 re[2], im[2];
#pragma unroll
        for (int s = 0; s < 2; s++) { const u4 a = {p[0][s], p[1][s], p[2][s], p[3][s]}; re[s] = mfma(a, gre, Z); im[s] = mfma(a, gim, Z); }
        PRIOB(1);
#pragma unroll
        for (int s = 0; s < 2; s++) { const u4 t = pack4(re[s], im[s]); re[s] = mfma(t, bre, Z); im[s] = mfma(t, bim, Z); }
        PRIOB(0);
#pragma unroll
        for (int s = 0; s < 2; s++) d[s] = pack4(re[s], im[s]);
        if (c16 < 9) {
#pragma unroll
            for (int i = 0; i < 4; i++) lds_st<u2>(eadr[i], u2{d[0][i], d[1][i]});
        }
    }
    STAMP(15);
    bar();
    if (wv < 9) {
        const u4 p0 = lds_ld<u4>(ao), p1 = lds_ld<u4>(ao + 16);
#pragma unroll
        for (int s = 0; s < 2; s++) {
            const u4 a = {p0[s], p0[2 + s], p1[s], p1[2 + s]};
            const f4 y0 = mfma(a, ai0, Z), y1 = mfma(a, ai1, Z);
            lds_st<u2>(yo + s * (8 * OP), u2{pk(y0[0], y0[1]), pk(y0[2], y0[3])});
            lds_st<u2>(yo + s * (8 * OP) + 4 * OP, u2{pk(y1[0], y1[1]), pk(y1[2], y1[3])});
        }
    }
    bar();
    *(GAS u4*)((char*)out + oo) = lds_ld<u4>(yl);
    if (wv < 2) {
        const int r2 = tid < 56 ? 14 : 15, c2 = tid + 1024 - 72 * r2;
        *(GAS u4*)((char*)out + (u32)((s00 + 16 + 256 * (r2 - 8)) * (4 * DM) + 2 * DM + c2 * 16)) = lds_ld<u4>(LB_Y + r2 * OP + c2 * 16);
    }
    STAMP(11);
    STAMP_OUT(0);
}

DI f2 cexpa(float ar2, float air, float p) {
    const float e = __builtin_amdgcn_exp2f(ar2 * p), t = __builtin_amdgcn_fractf(air * p);
    return f2{e * __builtin_amdgcn_cosf(t), e * __builtin_amdgcn_sinf(t)};
}
DI u4 ld_k(int o) {
    const u2 a = *(const volatile LAS u2*)(u32)o, b = *(const volatile LAS u2*)(u32)(o + 8);
    return u4{a[0], a[1], b[0], b[1]};
}
DI void a_rows_out(int yb, int ystr, char* o) {
    u2 y[6];
#pragma unroll
    for (int c = 0; c < 6; c++) y[c] = lds_ld<u2>(yb + c * ystr);
#pragma unroll
    for (int r = 0; r < 4; r++) {
        u3 v;
#pragma unroll
        for (int c = 0; c < 3; c++) v[c] = __builtin_amdgcn_perm(y[2 * c + 1][r >> 1], y[2 * c][r >> 1], (r & 1) ? 0x07060302u : 0x05040100u);
        *(u3*)(o + r * (4 * DM)) = v;
    }
}
template <int R> DI void stage(u4 v) {
    asm volatile("ds_write_addtid_b32 %0 offset:%4\n\tds_write_addtid_b32 %1 offset:%5\n\tds_write_addtid_b32 %2 offset:%6\n\tds_write_addtid_b32 %3 offset:%7"
                 :: "v"(v[0]), "v"(v[1]), "v"(v[2]), "v"(v[3]), "i"(288 * R), "i"(288 * R + 288), "i"(288 * R + 576), "i"(288 * R + 864) : "memory");
}
#define CM_MUL(d, a, b) "v_pk_mul_f32 " d ", " a ", " b " op_sel_hi:[0,1]\n\t"
#define CM_FMA(d, a, b) "v_pk_fma_f32 " d ", " a ", " b ", " d " op_sel:[1,1,0] op_sel_hi:[1,0,1] neg_lo:[1,0,0]\n\t"
struct C4 { f2 a, b, c, d; };
DI C4 cmul4(f2 a0, f2 b0, f2 a1, f2 b1, f2 a2, f2 b2, f2 a3, f2 b3) {
    C4 r;
    asm("s_nop 0\n\t" CM_MUL("%0", "%4", "%5") CM_MUL("%1", "%6", "%7") CM_MUL("%2", "%8", "%9") CM_MUL("%3", "%10", "%11")
        CM_FMA("%0", "%4", "%5") CM_FMA("%1", "%6", "%7") CM_FMA("%2", "%8", "%9") CM_FMA("%3", "%10", "%11") "s_nop 0"
        : "=&v"(r.a), "=&v"(r.b), "=&v"(r.c), "=&v"(r.d) : "v"(a0), "v"(b0), "v"(a1), "v"(b1), "v"(a2), "v"(b2), "v"(a3), "v"(b3));
    return r;
}
DI C4 cmul4(const C4& a, f2 b) { return cmul4(a.a, b, a.b, b, a.c, b, a.d, b); }
DI u4 pk4(const C4& v) { return u4{pk(v.a.x, v.a.y), pk(v.b.x, v.b.y), pk(v.c.x, v.c.y), pk(v.d.x, v.d.y)}; }

DI void sig(int f) {
    asm volatile("s_waitcnt lgkmcnt(0)" ::: "memory");
    if ((threadIdx.x & 63) == 0) __hip_atomic_fetch_add((LAS u32*)(u32)(LA_F + 4 * f), 1u, __ATOMIC_RELAXED, __HIP_MEMORY_SCOPE_WORKGROUP);
}
template <int SLEEP = 0> DI void await(int f, u32 n) {
    while ((u32)__builtin_amdgcn_readfirstlane(*(const volatile LAS u32*)(u32)(LA_F + 4 * f)) < n) if (SLEEP) __builtin_amdgcn_s_sleep(SLEEP);
    asm volatile("" ::: "memory");
}
DI float exp_bf(float x) {
    const float t = x * 1.44269504f, r = __builtin_fmaf(x, 1.92596299e-8f, __builtin_fmaf(x, 1.44269504f, -t));
    const float e = __builtin_amdgcn_exp2f(t);
    return __builtin_fmaf(e * 0.693147181f, r, e);
}

struct APar { u32 l, a, r, i, t, d; u4 sel2, sel3, selx, tadr; };
DI void head_a(const u16* __restrict__ u, APar pr, u16* __restrict__ out, char* ws) {
    const int tid = threadIdx.x, lane = tid & 63, c16 = lane & 15, g = lane >> 4;
    const int wv = __builtin_amdgcn_readfirstlane(tid >> 6);
    STAMP_DECL;
    const int ai_ = blockIdx.x - A0;
    const int d0 = 6 * ((ai_ & 7) * 12 + (ai_ >> 3));
    const f4 Z = {0.f, 0.f, 0.f, 0.f};

    if (wv >= 12) {
        __builtin_amdgcn_s_setprio(3);
        const int t = tid - 768;
        if (t == 0) lds_st<u2>(LA_F, u2{0u, 0u});
        if (t < 180) lds_st<u4>(LA_U + (t / 30) * 4608 + (t % 30) * 16, u4{0u, 0u, 0u, 0u});
        bar();
#pragma unroll
        for (int i = 0; i < 2; i++)
            if (i == 0 || wv < 14) {
                const int r = min((ai_ >> 3) + 12 * (128 * i + (t >> 1)), SEQ - 1);
                u32 keep;
                asm volatile("s_mov_b32 %0, m0\n\ts_mov_b32 m0, %3\n\ts_nop 4\n\tglobal_load_lds_dword %1, %2\n\ts_mov_b32 m0, %0"
                             : "=&s"(keep) : "v"(r * (2 * DM) + (((ai_ & 7) * 144) & ~127) + (t & 1) * 128), "s"(u), "s"(LA_D) : "memory");
            }
        u3 ua[4], ub[4];
#pragma unroll
        for (int r = 0; r < 4; r++) ua[r] = *(const u3*)(u + (4 * t + r) * DM + d0);
        sig(FU0);
        await<1>(FU0, 4);
#pragma unroll
        for (int r = 0; r < 4; r++) ub[r] = *(const u3*)(u + (1024 + 4 * t + r) * DM + d0);
        asm volatile("" ::: "memory");
#pragma unroll
        for (int c = 0; c < 6; c++) {
            const u32 sel = (c & 1) ? 0x07060302u : 0x05040100u;
            lds_st<u2>(LA_U + c * 4608 + 480 + t * 8, u2{__builtin_amdgcn_perm(ua[1][c >> 1], ua[0][c >> 1], sel), __builtin_amdgcn_perm(ua[3][c >> 1], ua[2][c >> 1], sel)});
        }
        STAMP(7);
        bar();
        bar();
#pragma unroll
        for (int c = 0; c < 6; c++) {
            const u32 sel = (c & 1) ? 0x07060302u : 0x05040100u;
            lds_st<u2>(LA_U + c * 4608 + 480 + 2048 + t * 8, u2{__builtin_amdgcn_perm(ub[1][c >> 1], ub[0][c >> 1], sel), __builtin_amdgcn_perm(ub[3][c >> 1], ub[2][c >> 1], sel)});
        }
        sig(FU2);
        STAMP(8);
        const int tile = wv - 12, yl = ((lane >> 2) + 16 * (lane & 3)) * 8;
        char* o = (char*)(out + 4 * t * (2 * DM) + d0);
        bar();
        bar();
        STAMP(11);
        a_rows_out(LA_G + ((tile >> 1) ^ 1) * 4608 + (tile & 1) * 1024 + yl, 9216, o);
        STAMP(9);
        bar();
        bar();
        STAMP(12);
        a_rows_out(LA_G + ((tile >> 1) ^ 1) * 4608 + (tile & 1) * 1024 + 2048 + yl, 9216, o + 1024 * (4 * DM));
        STAMP(10);
        STAMP_OUT(768);
        return;
    }
    const int ch = wv >> 1, hf = wv & 1;
    {
        __builtin_amdgcn_s_barrier();
        asm volatile("s_waitcnt vmcnt(4)" : "+v"(pr.l), "+v"(pr.a), "+v"(pr.r), "+v"(pr.i), "+v"(pr.t), "+v"(pr.d) :: "memory");
        PRIO(3);
        const float dt = rbf(exp_bf(blo(pr.t)));
        const float ar2 = -rbf(exp_bf(blo(pr.l))) * dt * 1.44269504f, air = blo(pr.a) * dt * 0.159154943f;
        const f2 C = {2.f * dt * blo(pr.r), 2.f * dt * blo(pr.i)};
        STAMP(1);
        const f2 z1 = cexpa(ar2, air, 1.f), z8c = cexpa(ar2, -air, 8.f), z128 = cexpa(ar2, air, 128.f);
        const int gr = LA_G + wv * 4608 + c16 * 288 + g * 16, q = LA_Q + wv * 2576;
        asm volatile("s_mov_b32 m0, %0\n\ts_nop 0" :: "s"(LA_G + wv * 4608) : "memory");
#define SB __builtin_amdgcn_sched_barrier(0)
#define STAGE16(v) do { stage<0>(v[0]); stage<4>(v[1]); stage<8>(v[2]); stage<12>(v[3]); } while (0)
#define READ4(f) do { _Pragma("unroll") for (int s = 0; s < 4; s++) f[s] = lds_ld<u4>(gr + 64 * s); SB; } while (0)
        u4 v[4], bfr[4], afr[4];
        const C4 s1 = cmul4(z8c, z8c, z1, z1, z128, z128, C, z1);
        const C4 s2 = cmul4(s1.a, s1.a, z8c, s1.a, s1.c, s1.c, C, s1.b);
        {
            C4 w = {f2{1.f, 0.f}, z8c, s1.a, s2.b};
            v[0] = pk4(w);
#pragma unroll
            for (int i = 1; i < 4; i++) { w = cmul4(w, s2.a); v[i] = pk4(w); }
        }
        STAGE16(v); READ4(bfr);
        PRIO(2);
        const C4 s3 = cmul4(s1.d, s1.b, s1.b, s1.b, s2.c, s2.c, s2.c, z128);
        const C4 e0 = {C, s1.d, s2.d, s3.a}, e1 = cmul4(e0, s3.b);
        v[0] = pk4(e0); v[1] = pk4(e1); v[2] = pk4(cmul4(e0, z128)); v[3] = pk4(cmul4(e1, z128));
        STAGE16(v); READ4(afr);
        PRIO(1);
        {
            const C4 f = cmul4(C4{e0.a, e0.c, e1.a, e1.c}, s1.c), h = cmul4(e0.a, s2.c, e1.a, s2.c, e0.a, s3.d, e1.a, s3.d);
            v[0] = pk4(f); v[1] = pk4(cmul4(f, z128)); v[2] = pk4(h); v[3] = pk4(cmul4(h, s1.c));
        }
        SB;
        f4 acc = Z;
#pragma unroll
        for (int s = 0; s < 4; s++) acc = mfma(afr[s], bfr[s], acc);
        STAGE16(v); READ4(afr);
        PRIO(0);
        lds_st<f4>(q + (4 * (g & 1) + 128 * (g >> 1) + 8 * c16) * 4, acc);
        {
            const f2 f0 = cmul(C, s3.c), f1 = cmul(f0, z128);
            const C4 f = cmul4(f0, s1.c, f1, s1.c, f0, s3.c, f0, s3.c), h = cmul4(C4{f0, f1, f.a, f.b}, s2.c);
            v[0] = pk4(C4{f0, f1, f.a, f.b}); v[1] = pk4(h); v[2][0] = pk(f.c.x, f.c.y);
        }
        SB;
        acc = Z;
#pragma unroll
        for (int s = 0; s < 4; s++) acc = mfma(afr[s], bfr[s], acc);
        stage<0>(v[0]); stage<4>(v[1]);
        asm volatile("ds_write_addtid_b32 %0 offset:%1" :: "v"(v[2][0]), "i"(288 * 8) : "memory");
        READ4(afr);
        if (g < 2) lds_st<f4>(q + (256 + 64 * g + 4 * c16) * 4, acc);
        else {
            const int o = q + (384 + 64 * (g - 2) + 2 * c16) * 4;
            lds_st<f2>(o, f2{acc[0], acc[1]}); lds_st<f2>(o + 128, f2{acc[2], acc[3]});
        }
        acc = Z;
#pragma unroll
        for (int s = 0; s < 4; s++) acc = mfma(afr[s], bfr[s], acc);
        if (g < 2) {
#pragma unroll
            for (int i = 0; i < 4; i++) lds_st<float>(q + (512 + 64 * g + 16 * i + c16) * 4, acc[i]);
        } else if (lane == 32) lds_st<float>(q + 640 * 4, acc[0]);
    }
    STAMP(2);
    asm volatile("s_waitcnt vmcnt(0) lgkmcnt(0)" : "+v"(pr.sel2), "+v"(pr.sel3), "+v"(pr.selx), "+v"(pr.tadr) :: "memory");
    const u4 sel2 = pr.sel2, sel3 = pr.sel3, selx = pr.selx, tadr = pr.tadr;
    bar();
    STAMP(9);
    const int k0 = LA_K + ch * KC;
    {
        const LAS float* qa = (const LAS float*)(u32)(LA_Q + ch * 5152);
        const LAS float* qb = qa + 644;
        u2 w[4];
        int y;
        if (hf == 0) {
            const int j = 384 + 4 * lane;
            const f4 a = *(const LAS f4*)(qa + j) + *(const LAS f4*)(qb + j), b = *(const LAS f4*)(qa + j + 4) + *(const LAS f4*)(qb + j + 4);
            const u32 ix = tadr[3];
            const float e0 = qa[(ix & 0xffff) >> 2] + qb[(ix & 0xffff) >> 2], e1 = qa[ix >> 18] + qb[ix >> 18];
            f4 k = *(const LAS f4*)(qa + 4 * lane) + *(const LAS f4*)(qb + 4 * lane);
            f4 p = *(const LAS f4*)(qa + 4 * lane - 4) + *(const LAS f4*)(qb + 4 * lane - 4);
            const u32 p23 = pk(a[2], a[3]), p34 = pk(a[3], b[0]);
            lds_st<u4>(k0 + 4 * KI + lane * 32, u4{pk(a[0], a[1]), p23, pk(a[1], a[2]), p34});
            lds_st<u4>(k0 + 4 * KI + lane * 32 + 16, u4{p23, pk(b[0], b[1]), p34, pk(b[1], b[2])});
            lds_st<u32>(k0 + g * KI + c16 * 4, pk(e0, e1));
            if (lane == 0) { k[0] += blo(pr.d); p = Z; }
            const u32 c = pk(k[1], k[0]), d = pk(k[0], p[3]);
            w[0] = u2{pk(k[3], k[2]), c}; w[1] = u2{pk(k[2], k[1]), d}; w[2] = u2{c, pk(p[3], p[2])}; w[3] = u2{d, pk(p[2], p[1])};
            y = k0 + (270 - 2 * lane) * 4;
        } else {
            const int j = 256 + 2 * lane;
            const float q0 = qa[j] + qb[j], q1 = qa[j + 1] + qb[j + 1], q2 = qa[j + 2] + qb[j + 2];
            const float p1 = qa[j - 1] + qb[j - 1], p2 = qa[j - 2] + qb[j - 2], p3 = qa[j - 3] + qb[j - 3];
            const u32 c = pk(q0, q0), d = pk(q0, p1), e = pk(p1, p2), f = lane ? c : d;
            w[0] = u2{pk(q2, q1), c}; w[1] = u2{pk(q1, q0), f}; w[2] = u2{c, lane ? d : e}; w[3] = u2{f, lane ? e : pk(p2, p3)};
            y = k0 + (142 - 2 * lane) * 4;
            if (lane < 32) lds_st<u32>(k0 + (lane >> 3) * KI + (272 + (lane & 7)) * 4, 0u);
        }
#pragma unroll
        for (int i = 0; i < 4; i++) lds_st<u2>(y + i * KI, w[i]);
    }
    const int x0 = 2031 - c16 + 8 * g;
    int ka = k0 + (x0 & 3) * KI + (x0 >> 2) * 8 - 3008 - 256 * hf - 192 - 512;
    int ubs = LA_U + ch * 4608 + 480 + (16 * c16 + 8 * g - 16) * 2 - 256 * hf - 192;
    int q2a = k0 + 4 * KI + tadr[0], q3a = k0 + 4 * KI + tadr[1], qxa = k0 + 4 * KI + tadr[2];
    pin(ka); pin(ubs); pin(q2a); pin(q3a); pin(qxa);
    STAMP(13);
    bar();
    STAMP(3);
#define KFR(rho, p) ld_k(ka + 192 - 64 * (rho) + 512 - 512 * (p))
#define UFR(rho, q) lds_ld<u4>(ubs + 192 - 64 * (rho) + 512 * (q))
#define QFR(rho, p) ((p) < 4 ? lds_ld<u2>(q2a + 64 * (8 * ((p) - 2) + (rho))) : ((p) == 4 && (rho) == 0 ? lds_ld<u2>(qxa) : lds_ld<u2>(q3a + 32 * (8 * ((p) - 4) + (rho)))))
#define QSEL(rho, p) ((p) < 4 ? sel2 : ((p) == 4 && (rho) == 0 ? selx : sel3))
#define QBUILD(r, s) u4{__builtin_amdgcn_perm(r[1], r[0], s[0]), __builtin_amdgcn_perm(r[1], r[0], s[1]), __builtin_amdgcn_perm(r[1], r[0], s[2]), __builtin_amdgcn_perm(r[1], r[0], s[3])}
#define FIRST2(dst) do { if (hf) { const u2 r_ = QFR(0, 2); dst = QBUILD(r_, sel2); } else dst = ld_k(ka - 320); } while (0)
    f4 acc[8];
#pragma unroll
    for (int i = 0; i < 8; i++) acc[i] = Z;
    {
        u4 A[2], T[2], B[2][4];
        u2 R[8];
#pragma unroll
        for (int p = 0; p < 4; p++) B[0][p] = UFR(0, p);
        A[0] = KFR(0, 0); A[1] = KFR(0, 1);
#pragma unroll
        for (int p = 3; p < 8; p++) R[p] = QFR(0, p);
        FIRST2(T[0]);
#pragma unroll
        for (int rho = 0; rho < 4; rho++)
#pragma unroll
            for (int p = 0; p < 8; p++) {
                SB;
                if (p == 0) { if (rho == 0) PRIO(3); else if (rho == 1) PRIO(2); else if (rho == 2) PRIO(1); else PRIO(0); }
                if (rho < 3 && p < 4) B[(rho + 1) & 1][p] = UFR(rho + 1, p);
                if (p >= 1 && p < 7 && !(p == 1 && rho == 0)) T[(p + 1) & 1] = QBUILD(R[p + 1], QSEL(rho, p + 1));
#pragma unroll
                for (int q = 0; q < 4; q++)
                    if (p + q < 8) acc[p + q] = mfma(p < 2 ? A[p] : T[p & 1], B[rho & 1][q], acc[p + q]);
                SB;
                if (rho < 3) {
                    if (p < 2) A[p] = KFR(rho + 1, p);
                    if (p >= 1 && p < 7) R[p + 1] = QFR(rho + 1, p + 1);
                }
            }
    }
#pragma unroll
    for (int i = 0; i < 8; i++) pin(acc[i]);
    STAMP(4);
#define EXCHANGE(t0, off)                                                                     \
    {                                                                                         \
        const int mine = LA_G + wv * 4608 + (off), other = LA_G + (wv ^ 1) * 4608 + (off);     \
        if (hf) { lds_st<f4>(mine + lane * 16, acc[t0]); lds_st<f4>(mine + 1024 + lane * 16, acc[t0 + 1]); acc[t0] = acc[t0 + 2]; acc[t0 + 1] = acc[t0 + 3]; } \
        else { lds_st<f4>(mine + lane * 16, acc[t0 + 2]); lds_st<f4>(mine + 1024 + lane * 16, acc[t0 + 3]); } \
        bar();                                                                                \
        const f4 y0 = lds_ld<f4>(other + lane * 16) + acc[t0], y1 = lds_ld<f4>(other + 1024 + lane * 16) + acc[t0 + 1]; \
        lds_st<u2>(other + lane * 8, u2{pk(y0[0], y0[1]), pk(y0[2], y0[3])});                 \
        lds_st<u2>(other + 1024 + lane * 8, u2{pk(y1[0], y1[1]), pk(y1[2], y1[3])});          \
    }
    await<1>(FU2, 4);
    EXCHANGE(0, 0);
    {
        u4 A[2][2], T[2], B[2][4];
        u2 R[4];
#pragma unroll
        for (int p = 0; p < 4; p++) B[0][p] = UFR(0, 4 + p);
        A[0][0] = KFR(0, 0); A[0][1] = KFR(0, 1); R[3] = QFR(0, 3);
        FIRST2(T[0]);
        bar();
        STAMP(14);
#pragma unroll
        for (int rho = 0; rho < 4; rho++)
#pragma unroll
            for (int p = 0; p < 4; p++) {
                SB;
                if (p == 0) { if (rho == 0) PRIO(3); else if (rho == 1) PRIO(2); else if (rho == 2) PRIO(1); else PRIO(0); }
                if (rho < 3) { B[(rho + 1) & 1][p] = UFR(rho + 1, 4 + p); if (p < 2) A[(rho + 1) & 1][p] = KFR(rho + 1, p); }
                if (p == 0 && rho > 0) T[0] = QBUILD(R[2], sel2);
                if (p == 1) T[1] = QBUILD(R[3], sel2);
#pragma unroll
                for (int q = 0; p + q < 4; q++) acc[4 + p + q] = mfma(p < 2 ? A[rho & 1][p] : T[p & 1], B[rho & 1][q], acc[4 + p + q]);
                SB;
                if (rho < 3 && p < 2) R[2 + p] = QFR(rho + 1, 2 + p);
            }
        SB;
    }
#pragma unroll
    for (int i = 4; i < 8; i++) pin(acc[i]);
    STAMP(5);
    EXCHANGE(4, 2048);
    bar();
    STAMP(6);
    STAMP_OUT(0);
}

__global__ void __launch_bounds__(NT) __attribute__((aligned(4096))) fused_kernel(const u16* __restrict__ u, const u16* __restrict__ lar, const u16* __restrict__ aimg,
                                                   const u16* __restrict__ cre, const u16* __restrict__ cim, const u16* __restrict__ ldt,
                                                   const u16* __restrict__ dsk, const u16* __restrict__ x, const u16* __restrict__ wr,
                                                   const u16* __restrict__ wi, u16* __restrict__ out, char* ws) {
    const char* entry;
    asm volatile("s_getpc_b64 %0" : "=s"(entry));
    APar pr;
    if (blockIdx.x >= A0 && __builtin_amdgcn_readfirstlane(threadIdx.x) < 768) {
        const int a = blockIdx.x - A0, d = 6 * ((a & 7) * 12 + (a >> 3)) + __builtin_amdgcn_readfirstlane(threadIdx.x >> 7);
        asm volatile("global_load_ushort %0, %6, %8\n\tglobal_load_ushort %1, %6, %9\n\tglobal_load_ushort %2, %6, %10\n\t"
                     "global_load_ushort %3, %6, %11\n\tglobal_load_ushort %4, %7, %12\n\tglobal_load_ushort %5, %7, %13"
                     : "=&v"(pr.l), "=&v"(pr.a), "=&v"(pr.r), "=&v"(pr.i), "=&v"(pr.t), "=&v"(pr.d)
                     : "v"((d * NS + (threadIdx.x & 127)) * 2), "v"(d * 2), "s"(lar), "s"(aimg), "s"(cre), "s"(cim), "s"(ldt), "s"(dsk) : "memory");
        asm volatile("global_load_dwordx4 %0, %4, %5\n\tglobal_load_dwordx4 %1, %4, %5 offset:1024\n\t"
                     "global_load_dwordx4 %2, %4, %5 offset:2048\n\tglobal_load_dwordx4 %3, %4, %5 offset:3072"
                     : "=&v"(pr.sel2), "=&v"(pr.sel3), "=&v"(pr.selx), "=&v"(pr.tadr)
                     : "v"((threadIdx.x & 63) * 16 + (threadIdx.x & 64) * 64), "s"((const char*)&TAB + F_END * 1024) : "memory");
    }
    if (__builtin_expect(blockIdx.x < NB2, 1)) head_b(entry, x, wr, wi, out, ws);
    else if (blockIdx.x >= A0) head_a(u, pr, out, ws);
    else warm_code(entry);
}

extern "C" void kernel_launch(void* const* d_in, const int* in_sizes, int n_in, void* d_out, int out_size, void* d_ws,
                              size_t ws_size, hipStream_t stream) {
    (void)hipFuncSetAttribute((const void*)fused_kernel, hipFuncAttributeMaxDynamicSharedMemorySize, LDS_BYTES);
    fused_kernel<<<GRID, NT, LDS_BYTES, stream>>>((const u16*)d_in[0], (const u16*)d_in[1], (const u16*)d_in[2], (const u16*)d_in[3],
                                                  (const u16*)d_in[4], (const u16*)d_in[5], (const u16*)d_in[6], (const u16*)d_in[7],
                                                  (const u16*)d_in[8], (const u16*)d_in[9], (u16*)d_out, (char*)d_ws);
}
```

```cpp
#define POLL_SLEEP 6
#include <hip/hip_runtime.h>
#define T_AT 2

typedef unsigned short u16;
typedef unsigned int u32;
typedef float f2 __attribute__((ext_vector_type(2)));
typedef float f4 __attribute__((ext_vector_type(4)));
typedef u32 u2 __attribute__((ext_vector_type(2)));
typedef u32 u4 __attribute__((ext_vector_type(4)));
typedef int i8v __attribute__((ext_vector_type(8)));
typedef short s4 __attribute__((ext_vector_type(4)));
typedef short s8 __attribute__((ext_vector_type(8)));
typedef __bf16 bf2 __attribute__((ext_vector_type(2)));
#define DI __device__ __forceinline__
#define LAS __attribute__((address_space(3)))
#define GAS __attribute__((address_space(1)))
#define CAS __attribute__((address_space(4)))
#pragma clang diagnostic ignored "-Wint-to-pointer-cast"
struct u3 { u32 v[3]; DI u32& operator[](int i) { return v[i]; } DI const u32& operator[](int i) const { return v[i]; } };

constexpr int SEQ = 2048, DM = 576, NS = 128, DF = 289;
constexpr int NB1 = 128;
constexpr int NB2 = 145;
constexpr int A0 = 152;
constexpr int NA = 96;
constexpr int GRID = A0 + NA;
constexpr int NT = 1024;

constexpr u32 WS_EPOCH = 0, WS_F1 = 4096, WS_X1 = 1u << 20, WS_Y1 = 4u << 20, WS_USED = 8u << 20;
#define STAMP(i)
#define STAMP_DECL
#define STAMP_OUT(T)

constexpr int F_SA = 0, F_EO = 4;
constexpr int F_ARE = 9, F_AIM = 10, F_BRE = 11, F_BIM = 12, F_G = 13;
constexpr int F_F16 = 21, F_GI = 23, F_AI = 31, F_M1C = 33;
constexpr int F_VAR = 37;
constexpr int F_END = F_VAR + 40;
constexpr int ET = 452, ER = 28, E2V = 580, E2U = 36, OP = 1168;
constexpr int LB_X = 0, LB_I = 0, LB_Y = 0;
constexpr int LB_E = 16 * OP;
constexpr int LB_O = LB_E, LB_E2 = LB_E;
constexpr int LB_S = LB_E + 16 * ET * 4;
constexpr int LB_W = LB_S + 1200 * 16;
constexpr int LB_T = LB_W + 2 * 256 * 36;
constexpr int LB_G = LB_T + (F_VAR + 16) * 1024;
constexpr int LB_END = LB_G + 16;
struct CosTab { double c[2048]; double c9[9], s9[9]; };
constexpr double tay_cos(double x) { double t = 1, s = 1; for (int k = 1; k <= 14; k++) { t *= -x * x / ((2 * k - 1) * (2 * k)); s += t; } return s; }
constexpr double tay_sin(double x) { double t = x, s = x; for (int k = 1; k <= 14; k++) { t *= -x * x / ((2 * k + 1) * (2 * k)); s += t; } return s; }
constexpr CosTab make_cos() {
    CosTab t = {};
    const double PI2 = 6.283185307179586476925;
    for (int k = 0; k <= 512; k++) {
        const double v = k == 512 ? 0.0 : (k <= 256 ? tay_cos(PI2 * k / 2048) : tay_sin(PI2 * (512 - k) / 2048));
        t.c[k] = v; t.c[(2048 - k) & 2047] = v; t.c[1024 - k] = -v; t.c[(1024 + k) & 2047] = -v;
    }
    t.c[512] = 0.0; t.c[1536] = 0.0;
    for (int k = 0; k < 9; k++) { const double a = PI2 * (k <= 4 ? k : k - 9) / 9; t.c9[k] = tay_cos(a); t.s9[k] = tay_sin(a); }
    return t;
}
constexpr CosTab CT = make_cos();
constexpr double cs(int n) { return CT.c[n & 2047]; }
constexpr double sn(int n) { return CT.c[(n - 512) & 2047]; }
constexpr u32 bfr(double x) { const u32 b = __builtin_bit_cast(u32, (float)x); return (b + 0x7fffu + ((b >> 16) & 1u)) >> 16; }
constexpr u32 pkc(double lo, double hi) { return bfr(lo) | (bfr(hi) << 16); }
constexpr u32 f32c(double x) { return __builtin_bit_cast(u32, (float)x); }
template <int N> struct Frags { u32 w[N * 256]; };
constexpr u32 w_re(double amp, int n) { return pkc(amp * cs(n), amp * sn(n)); }
constexpr u32 w_im(double amp, int n) { return pkc(-amp * sn(n), amp * cs(n)); }
constexpr Frags<F_VAR> make_t0() {
    Frags<F_VAR> t = {};
    const double R2 = 1.4142135623730951;
    for (int l = 0; l < 64; l++)
        for (int i = 0; i < 4; i++) {
            const int c = l & 15, g = l >> 4, r = 4 * g + i, o = l * 4 + i;
            {
                const int b = c >> 2, tq = c & 3;
                const double amp = b ? 1.0 : R2;
                const int n0 = 512 * tq * g + (b ? 256 * b * (2 * i) : -256), n1 = 512 * tq * g + (b ? 256 * b * (2 * i + 1) : 256);
                t.w[F_ARE * 256 + o] = pkc(amp * cs(n0), amp * cs(n1));
                t.w[F_AIM * 256 + o] = pkc(-amp * sn(n0), -amp * sn(n1));
            }
            for (int w = 0; w < 9; w++) {
                const int sh = 16 * (i & 1), oo = w * 128 + l * 2 + (i >> 1);
                t.w[F_EO * 256 + oo] |= (u32)((c * ET + r * ER + 2 * ((7 * w + r) % 9)) * 4) << sh;
                if (w < 8) {
                    const int k = (9 * (w & 3) + 36 * r + 64 * (c % 9)) % DM, slot = w < 4 ? (k <= 288 ? 2 * k : 2 * (DM - k) + 1) : k;
                    t.w[F_SA * 256 + oo] |= (u32)(slot * 8 + (slot >> 6) * 16) << sh;
                }
            }
            if (r < 9) { const int k = (r * c) % 9; t.w[F_BRE * 256 + o] = pkc(CT.c9[k], CT.s9[k]); t.w[F_BIM * 256 + o] = pkc(-CT.s9[k], CT.c9[k]); }
            for (int q = 0; q < 4; q++) {
                t.w[(F_G + 2 * q) * 256 + o] = w_re(1.0, 32 * r * (q + 4 * c)); t.w[(F_G + 2 * q + 1) * 256 + o] = w_im(1.0, 32 * r * (q + 4 * c));
                t.w[(F_GI + 2 * q) * 256 + o] = w_re(1.0, 32 * c * (q + 4 * r)); t.w[(F_GI + 2 * q + 1) * 256 + o] = w_im(1.0, 32 * c * (q + 4 * r));
            }
            t.w[F_F16 * 256 + o] = w_re(1.0, 128 * r * c); t.w[(F_F16 + 1) * 256 + o] = w_im(1.0, 128 * r * c);
            for (int h = 0; h < 2; h++) {
                const int j = 4 * h + (c >> 2), n = 512 * i * (c & 3) + (g ? 256 * g * j : ((j & 1) ? 256 : -256));
                t.w[(F_AI + h) * 256 + o] = w_re(g ? 2.0 : R2, n);
                const int m = 16 * r * (4 * h + 8 * c) - 512 * h;
                t.w[(F_M1C + 2 * h) * 256 + o] = pkc(0.5 * cs(m), -0.5 * sn(m));
                t.w[(F_M1C + 2 * h + 1) * 256 + o] = pkc(-0.5 * sn(m), -0.5 * cs(m));
            }
        }
    return t;
}
constexpr Frags<40> make_t1() {
    Frags<40> t = {};
    for (int v = 0; v < 5; v++)
        for (int l = 0; l < 64; l++)
            for (int i = 0; i < 4; i++) {
                const int be = v < 3 ? v + 1 : 4 * (v - 3), c = l & 15, r = 4 * (l >> 4) + i, o = v * 2048 + l * 4 + i;
                const double amp = v < 3 ? 1.0 : 0.5;
                const int n1 = 16 * r * (be + 8 * c) + (v == 4 ? 512 : 0), n4 = 16 * c * (be + 8 * r);
                t.w[o] = w_re(amp, n1); t.w[o + 256] = w_im(amp, n1);
                t.w[o + 512] = w_re(1.0, n4); t.w[o + 768] = w_im(1.0, n4);
                t.w[o + 1024] = f32c(cs(r * (be + 8 * c))); t.w[o + 1280] = f32c(-sn(r * (be + 8 * c)));
                t.w[o + 1536] = f32c(cs(c * (be + 8 * r))); t.w[o + 1792] = f32c(-sn(c * (be + 8 * r)));
            }
    return t;
}
constexpr int qidx(int l) {
    if (l <= 256) return l;
    const int sh = l < 512 ? 1 : (l < 1024 ? 2 : 3), h = 1 << (sh - 1), m = l >> sh, r = l & ((1 << sh) - 1);
    return 128 * sh + m + ((r > h || (r == h && (m & 1))) ? 1 : 0);
}
constexpr Frags<8> make_t2() {
    Frags<8> t = {};
    for (int hf = 0; hf < 2; hf++)
        for (int l = 0; l < 64; l++) {
            const int sg[3] = {17 + 4 * hf, 33 + 4 * hf, 32 + 4 * hf}, back[3] = {8, 4, 0};
            for (int k = 0; k < 3; k++) {
                const int L = 32 * sg[k] + 16 + (l & 15) - 8 * (l >> 4), q0 = qidx(L - 7);
                for (int i = 0; i < 4; i++) {
                    const u32 a = 2 * (qidx(L - 2 * i) - q0), b = 2 * (qidx(L - 2 * i - 1) - q0);
                    t.w[(4 * hf + k) * 256 + l * 4 + i] = a | ((a + 1) << 8) | (b << 16) | ((b + 1) << 24);
                }
                t.w[(4 * hf + 3) * 256 + l * 4 + k] = (u32)((q0 - back[k] - 384) * 8);
            }
            const int e = 543 - 2 * (l & 15) - (l >> 4);
            t.w[(4 * hf + 3) * 256 + l * 4 + 3] = (u32)(qidx(e) * 4) | ((u32)(qidx(e - 1) * 4) << 16);
        }
    return t;
}
struct Tables { Frags<F_VAR> a; Frags<40> b; Frags<8> c; };
constexpr Frags<8> T2 = make_t2();
constexpr Frags<F_VAR> T0 = make_t0();
constexpr Frags<40> T1 = make_t1();
static __device__ const __attribute__((aligned(128))) Tables TAB = {T0, T1, T2};
constexpr int LA_G = 0;
constexpr int LA_K = LA_G + 12 * 4608;
constexpr int KI = 1344, KC = 4 * KI + 2048;
constexpr int LA_U = LA_K + 6 * KC;
constexpr int LA_Q = LA_U + 6 * 4608;
constexpr int LA_F = LA_Q + 12 * 2576;
constexpr int LA_D = LA_F + 16;
constexpr int LA_END = LA_D + 256;
constexpr int FU0 = 0, FU2 = 1;
constexpr int LDS_BYTES = (LA_END > LB_END ? LA_END : LB_END);
static_assert(LDS_BYTES <= 160 * 1024 && LDS_BYTES > 80 * 1024, "one workgroup per CU");

DI u32 pk(float a, float b) { f2 v = {a, b}; return __builtin_bit_cast(u32, __builtin_convertvector(v, bf2)); }
DI float blo(u32 v) { return __uint_as_float(v << 16); }
DI float bhi(u32 v) { return __uint_as_float(v & 0xffff0000u); }
DI float bf2f(u16 v) { return __uint_as_float((u32)v << 16); }
DI float rbf(float x) { return blo(pk(x, x)); }
DI f2 cmul(f2 a, f2 b) { f2 t = a.xx * b; f2 s = {-b.y, b.x}; return __builtin_elementwise_fma(a.yy, s, t); }
DI f4 mfma(u4 a, u4 b, f4 c) { return __builtin_amdgcn_mfma_f32_16x16x32_bf16(__builtin_bit_cast(s8, a), __builtin_bit_cast(s8, b), c, 0, 0, 0); }
DI u4 pack4(f4 re, f4 im) { u4 r = {pk(re[0], im[0]), pk(re[1], im[1]), pk(re[2], im[2]), pk(re[3], im[3])}; return r; }
DI u4 twid(f4 re, f4 im, f4 wr, f4 wi) { return pack4(__builtin_elementwise_fma(re, wr, -(im * wi)), __builtin_elementwise_fma(re, wi, im * wr)); }
DI int mod9(int x) { return x - 9 * ((x * 57) >> 9); }
template <class T> DI void pin(T& v) { asm volatile("" : "+v"(v)); }

template <class T> DI T lds_ld(int off) { return *(const LAS T*)(u32)off; }
template <class T> DI void lds_st(int off, T v) { *(LAS T*)(u32)off = v; }
DI void bar() { __syncthreads(); }
#define PRIO(n) __builtin_amdgcn_s_setprio(n)
#define PRIOB(n)
DI void glds16(const void* g, u32 off, int lds) {
    u32 keep;
    asm volatile("s_mov_b32 %0, m0\n\ts_mov_b32 m0, %3\n\ts_nop 4\n\tglobal_load_lds_dwordx4 %1, %2\n\ts_mov_b32 m0, %0" : "=&s"(keep) : "v"(off), "s"(g), "s"(lds) : "memory");
}

DI void publish_tag(u32 epoch) {
    u32 t;
    asm volatile("v_add_u32 %0, 0x9E3779B1, %1\n\tds_write_b32 %2, %0\n\ts_waitcnt lgkmcnt(0)" : "=&v"(t) : "v"(epoch), "v"(LB_G) : "memory");
}
DI void wait_flag(__amdgpu_buffer_rsrc_t r, u32 off, u32 tag, bool on) {
    if (on) {
        const unsigned long long t0 = __builtin_amdgcn_s_memrealtime();
        while (!__all(__builtin_amdgcn_raw_buffer_load_b32(r, off, 0, 16) == tag)) {
            if (__builtin_amdgcn_s_memrealtime() - t0 > 5000000ull) break;
            __builtin_amdgcn_s_sleep(POLL_SLEEP);
            asm volatile("" ::: "memory");
        }
    }
    asm volatile("" ::: "memory");
}

constexpr u32 POISON = 0xFFFFFFFFu;
DI bool unset(u4 v) { return max(max(v[0], v[1]), max(v[2], v[3])) == POISON; }
#define DPOLL_SLEEP 1
template <bool TWO, int SLEEP = DPOLL_SLEEP> DI void load_set(__amdgpu_buffer_rsrc_t r, u32 off, bool on, u4& a, bool on2, u4& b) {
    const unsigned long long t0 = __builtin_amdgcn_s_memrealtime();
    bool ma = on, mb = TWO && on2;
    for (;;) {
        if (ma) a = __builtin_amdgcn_raw_buffer_load_b128(r, off, 0, 16);
        if (TWO && mb) b = __builtin_amdgcn_raw_buffer_load_b128(r, off, 16384, 16);
        ma = ma && unset(a);
        if (TWO) mb = mb && unset(b);
        if (!__any(ma || mb) || __builtin_amdgcn_s_memrealtime() - t0 > 5000000ull) break;
        __builtin_amdgcn_s_sleep(SLEEP);
        asm volatile("" ::: "memory");
    }
}
constexpr int CODE_LINES = 128;
DI void warm_code(const char* entry) {
    if (threadIdx.x < CODE_LINES) { const u32 t = *(const GAS u32*)(entry + threadIdx.x * 128); asm volatile("" :: "v"(t)); }
}

DI void head_b(const char* entry, const u16* __restrict__ x, const u16* __restrict__ wr, const u16* __restrict__ wi,
               u16* __restrict__ out, char* ws) {
    const int tid = threadIdx.x, lane = tid & 63, c16 = lane & 15, g = lane >> 4;
    const int wv = __builtin_amdgcn_readfirstlane(tid >> 6);
    const int id = blockIdx.x;
    STAMP_DECL;
    const bool rows = id < NB1;
    const int s00 = 32 * (id >> 4) + (id & 15);
    const char* tab = (const char*)&TAB;
    const int R = (tid * 911) >> 16;
    const u32 xoff = (s00 + 16 * (R >> 3) + 256 * (R & 7)) * (2 * DM) + (tid - R * 72) * 16;
    asm volatile("" :: "s"(x), "s"(wr), "s"(wi), "s"(out), "s"(ws), "v"(xoff));
    const __amdgpu_buffer_rsrc_t rws = __builtin_amdgcn_make_buffer_rsrc(ws, 0, WS_USED, 0x00020000);
    const int u2i = (id & 7) ? (id & 7) * 18 + 1 + (id >> 3) : (id == 136 ? 0 : (id >> 3) + (id < 136 ? 1 : 0));
    STAMP(13);
    int gi = WS_X1 + u2i * 16384 + tid * 16;
    int gy = WS_Y1 + id * (NB2 * 128) + tid * 16;
    u32 epoch = 0;
    if (wv == 0) asm volatile("global_load_dword %0, %0, %1 sc1" : "+v"(epoch) : "s"(ws) : "memory");
    if (rows) {
        glds16(x, xoff, LB_X + wv * 1024);
        if (wv < 2) glds16(x, (s00 + 16 + 1536) * (2 * DM) + (tid < 56 ? (tid + 16) * 16 : 256 * 2 * DM + (tid - 56) * 16), LB_X + 16384 + wv * 1024);
        else glds16(tab, tid * 16 - 2048, LB_T + (wv - 2) * 1024);
        if (wv >= 9) glds16(tab, tid * 16 + 5 * 1024, LB_T + (wv + 5) * 1024);
    }
    {
        const u4 P = {POISON, POISON, POISON, POISON};
        __builtin_amdgcn_raw_buffer_store_b128(P, rws, gi, 0, 16);
        if (rows) {
            __builtin_amdgcn_raw_buffer_store_b128(P, rws, gy, 0, 16);
            if (tid <= 128) __builtin_amdgcn_raw_buffer_store_b128(P, rws, gy, 16384, 16);
        }
    }
    const bool star = u2i >= 108;
    const int q36 = min((u2i * 57) >> 11, 3), kg = u2i - 36 * q36, bq = (q36 + 1) & 3;
    const bool mir = !star && kg >= 18;
    const int w0 = star ? min(16 * kg, 562) : (mir ? (2048 - bq) * (2 * DF) + 1122 - 32 * kg : bq * (2 * DF) + 32 * kg);
    __builtin_amdgcn_s_barrier();
    STAMP(14);
    const u32 woff = w0 + ((tid >> 1) & 255) * (mir ? -16 * DF : 16 * DF) + (tid & 1) * (star ? 8 * DF : 16);
#define W_LOAD u4 wra, wrb; if (wv >= 8) { __builtin_memcpy(&wra, (const char*)wr + woff, 16); __builtin_memcpy(&wrb, (const char*)wi + woff, 16); } \
    asm volatile("" ::: "memory")
    const f4 Z = {0.f, 0.f, 0.f, 0.f};
    const int tau = wv & 3, bw = wv >> 2;
    int tl = LB_T + lane * 16;
    int lin = LB_S + (tid + (tid >> 5)) * 16;
    pin(tl); pin(lin);
    int sadr[4];
    u32 tag = 0;
    u4 sv0 = {}, sv1 = {};
#define W_TO_LDS do { const int o = LB_W + ((tid >> 1) & 255) * 36 + (tid & 1) * 16; \
        _Pragma("unroll") for (int i = 0; i < 4; i++) { lds_st<u32>(o + 4 * i, wra[i]); lds_st<u32>(o + 9216 + 4 * i, wrb[i]); } } while (0)
#define TABLES23 do { glds16(tab, F_F16 * 1024 + tid * 16, LB_T + F_F16 * 1024 + wv * 1024); \
        if (star || wv < 8) glds16(tab, (F_VAR + 8 * q36) * 1024 + tid * 16, LB_T + F_VAR * 1024 + wv * 1024); } while (0)

    if (rows) {
        W_LOAD;
        if (tid < 256) {
            const int o = LB_E + 72 + (tid & 15) * (ER * 4) + (tid >> 4) * (ET * 4);
            lds_st<u2>(o, u2{0u, 0u}); lds_st<u4>(o + 8, u4{0u, 0u, 0u, 0u});
        }
        int xo = LB_X + (c16 >> 2) * 1152 + 2 * (144 * ((g - wv) & 3) + 16 * wv + 4 * (c16 & 3));
        int bo = LB_E + (wv * ET + c16 * ER + min(8 * g, 16)) * 4;
        int go = WS_X1 + (tid >> 3) * 16384 + id * 128 + (tid & 7) * 16;
        int ta = LB_T + lane * 8;
        const int cu = 8 * wv + (lane & 7) + ((lane & 8) ? 128 : 0);
        u32 fo = WS_F1 + ((id >> 3) * 19 + (cu < NB2 ? cu >> 3 : wv)) * 128 + (lane & 7) * 4;
        pin(xo); pin(bo); pin(go); pin(ta); pin(fo);
        STAMP(1);
        asm volatile("s_cmp_lt_u32 %0, 8\n\ts_cbranch_scc0 .Lw%=\n\ts_waitcnt vmcnt(0)\n.Lw%=:\n\ts_waitcnt vmcnt(2)" :: "s"(wv) : "memory", "scc");
        if (wv == 0) publish_tag(epoch);
        bar();
        STAMP(2);
        if (wv == 15 && lane < 16) __builtin_amdgcn_raw_buffer_store_b32(lds_ld<u32>(LB_G), rws, WS_F1 + (lane * 19 + (u2i >> 3)) * 128 + (u2i & 7) * 4, 0, 16);
        if (wv < 9) {
            u4 d[2];
#pragma unroll
            for (int s = 0; s < 2; s++) {
                const s4 lo = __builtin_amdgcn_ds_read_tr16_b64_v4i16((LAS s4*)(u32)(xo + s * 9216));
                const s4 hi = __builtin_amdgcn_ds_read_tr16_b64_v4i16((LAS s4*)(u32)(xo + s * 9216 + 4608));
                d[s] = __builtin_bit_cast(u4, __builtin_shufflevector(lo, hi, 0, 1, 2, 3, 4, 5, 6, 7));
            }
            const u4 are = lds_ld<u4>(tl + F_ARE * 1024), aim = lds_ld<u4>(tl + F_AIM * 1024);
            const u2 eo = lds_ld<u2>(ta + F_EO * 1024 + wv * 512);
#pragma unroll
            for (int s = 0; s < 2; s++) d[s] = pack4(mfma(d[s], are, Z), mfma(d[s], aim, Z));
#pragma unroll
            for (int i = 0; i < 4; i++) lds_st<u2>(LB_E + ((i & 1) ? eo[i >> 1] >> 16 : eo[i >> 1] & 0xffff), u2{d[0][i], d[1][i]});
        }
        u4 bre = lds_ld<u4>(tl + F_BRE * 1024), bim = lds_ld<u4>(tl + F_BIM * 1024);
        u4 gre = lds_ld<u4>(tl + (F_G + 2 * tau) * 1024), gim = lds_ld<u4>(tl + (F_G + 2 * tau + 1) * 1024);
        {
            const u2 sa = lds_ld<u2>(ta + F_SA * 1024 + (tau + (bw ? 4 : 0)) * 512);
            const int base = LB_S + ((bw + 3) & 3) * (DM * 8 + 144);
#pragma unroll
            for (int i = 0; i < 4; i++) { sadr[i] = base + ((i & 1) ? sa[i >> 1] >> 16 : sa[i >> 1] & 0xffff); pin(sadr[i]); }
        }
        tag = lds_ld<u32>(LB_G);
        pin(bre); pin(bim); pin(gre); pin(gim); pin(tag);
        bar();
        TABLES23;
        {
            PRIOB(2);
            const u4 p0 = lds_ld<u4>(bo), p1 = lds_ld<u4>(bo + 16);
            u4 d[2];
            f4 re[2], im[2];
#pragma unroll
            for (int s = 0; s < 2; s++) { const u4 a = {p0[s], p0[2 + s], p1[s], p1[2 + s]}; re[s] = mfma(a, bre, Z); im[s] = mfma(a, bim, Z); }
            PRIOB(1);
#pragma unroll
            for (int s = 0; s < 2; s++) { const u4 t = pack4(re[s], im[s]); re[s] = mfma(gre, t, Z); im[s] = mfma(gim, t, Z); }
            PRIOB(0);
#pragma unroll
            for (int s = 0; s < 2; s++) d[s] = pack4(re[s], im[s]);
            if (c16 < 9) {
#pragma unroll
                for (int i = 0; i < 4; i++) lds_st<u2>(sadr[i], u2{d[0][i], d[1][i]});
                if (wv == 0 && c16 == 0 && !(g & 1)) lds_st<u2>(sadr[0] + 8, u2{d[0][0], d[1][0]});
            }
        }
        if (wv >= 8) W_TO_LDS;
        const u32 rdy = __builtin_amdgcn_raw_buffer_load_b32(rws, fo, 0, 16);
        bar();
        STAMP(3);
        sv0 = lds_ld<u4>(lin);
        sv1 = lds_ld<u4>(lin + (wv < 3 ? 1056 * 16 : 0));
        if (!__all(rdy == tag)) wait_flag(rws, fo, tag, true);
        __builtin_amdgcn_raw_buffer_store_b128(sv0, rws, go, 0, 16);
        if (tid <= 128) __builtin_amdgcn_raw_buffer_store_b128(sv1, rws, go, 128 * 16384, 16);
        bar();
    } else {
        W_LOAD;
        if (wv < 8 && (kg == 0 || kg == (star ? 36 : 18))) {
            const int m = tid & 255;
            lds_st<u16>(LB_W + tid * 36 + 32, ((wv >> 2) ? wi : wr)[((mir ? bq + 8 * m : 2048 - bq - 8 * m) & 2047) * DF + (kg ? 288 : 0)]);
        }
        TABLES23;
        warm_code(entry);
        if (wv >= 8) W_TO_LDS;
        __builtin_amdgcn_s_waitcnt(0x0F70);
        asm volatile("s_waitcnt vmcnt(0)" ::: "memory");
        if (wv == 0) publish_tag(epoch);
        bar();
        if (wv == 15 && lane < 16) __builtin_amdgcn_raw_buffer_store_b32(lds_ld<u32>(LB_G), rws, WS_F1 + (lane * 19 + (u2i >> 3)) * 128 + (u2i & 7) * 4, 0, 16);
    }
    STAMP(4);
    STAMP(12);

    const int typ = star ? (wv & 1) : 0;
    const int tv = tl + (F_VAR + 8 * typ) * 1024;
    u4 fre = lds_ld<u4>(tl + F_F16 * 1024), fim = lds_ld<u4>(tl + (F_F16 + 1) * 1024);
    u4 m1r = lds_ld<u4>(tv), m1i = lds_ld<u4>(tv + 1024), m4r = lds_ld<u4>(tv + 2048), m4i = lds_ld<u4>(tv + 3072);
    f4 twr = lds_ld<f4>(tv + 4096), twi = lds_ld<f4>(tv + 5120), tur = lds_ld<f4>(tv + 6144), tui = lds_ld<f4>(tv + 7168);
    u4 mcr = lds_ld<u4>(tl + (F_M1C + 2 * typ) * 1024), mci = lds_ld<u4>(tl + (F_M1C + 2 * typ + 1) * 1024);
    f4 wfr, wfi;
    {
        const int kcol = star ? 8 * kg + (wv >> 1) : 16 * kg + wv;
        const int cc = star ? 8 * typ + (u2i < NB2 - 1 ? (wv >> 1) : 7) : (mir ? 15 - wv : wv);
        const int wo = LB_W + (c16 + 64 * g) * 36 + cc * 2;
#pragma unroll
        for (int i = 0; i < 4; i++) {
            wfr[i] = bf2f(lds_ld<u16>(wo + i * 576));
            wfi[i] = bf2f(lds_ld<u16>(wo + i * 576 + 9216));
        }
        float sc = 1.f / (float)(SEQ * DM);
        if (kcol == 0 || kcol == 288) {
            sc *= 0.5f;
#pragma unroll
            for (int i = 0; i < 4; i++) {
                const int m = c16 + 64 * g + 16 * i;
                const int o = LB_W + (typ ? (255 - m) * 36 + cc * 2 : m * 36 + 32);
                wfr[i] += bf2f(lds_ld<u16>(o));
                wfi[i] -= bf2f(lds_ld<u16>(o + 9216));
            }
        }
        wfr *= sc; wfi *= (mir ? -sc : sc);
    }
    f4 wnr = -wfr, wni = -wfi;
    int io = (32 * g + c16) * 128 + (((wv >> 1) ^ (c16 >> 1)) * 16);
    int so = (tid >> 3) * 128 + (((tid & 7) ^ ((tid >> 4) & 7)) * 16);
    pin(fre); pin(fim); pin(m1r); pin(m1i); pin(m4r); pin(m4i); pin(twr); pin(twi); pin(tur); pin(tui); pin(mcr); pin(mci);
    int gq = WS_Y1 + (tid >> 3) * (NB2 * 128) + u2i * 128 + (tid & 7) * 16;
    int i1 = LB_I + io + (star ? 0 : (wv & 1) * 8);
    io += LB_O + (wv & 1) * 8;
    pin(wfr); pin(wnr); pin(wni); pin(io); pin(so); pin(gi); pin(gq); pin(i1);
    asm volatile("" :: "v"(sv0), "v"(sv1));
    STAMP(5);
    {
        u4 v = {0u, 0u, 0u, 0u}, w;
        if (!rows) load_set<false, 16>(rws, gi, lane == 0, v, false, w);
        load_set<false>(rws, gi, u2i < NB2 - 1 || (tid & 7) == 0, v, false, w);
        lds_st<u4>(LB_I + so, v);
    }
    bar();
    STAMP(6);
    {
        const int o1 = i1;
        PRIOB(3);
        const u2 a0 = lds_ld<u2>(o1), a1 = lds_ld<u2>(o1 + 2048);
        const u4 a = {a0[0], a0[1], a1[0], a1[1]};
        f4 re = mfma(a, m1r, Z), im = mfma(a, m1i, Z);
        if (star) {
            const u2 b0 = lds_ld<u2>(o1 + 8), b1 = lds_ld<u2>(o1 + 8 + 2048);
            const u4 b = {b0[0], b0[1], b1[0], b1[1]};
            re = mfma(b, mcr, re); im = mfma(b, mci, im);
        }
        u4 t = twid(re, im, twr, twi);
        re = mfma(fre, t, Z); im = mfma(fim, t, Z);
        PRIOB(2);
        t = pack4(__builtin_elementwise_fma(re, wfr, im * wni), __builtin_elementwise_fma(re, wni, im * wnr));
        re = mfma(t, fre, Z); im = mfma(t, fim, Z);
        PRIOB(1);
        t = twid(re, im, tur, tui);
        re = mfma(m4r, t, Z); im = mfma(m4i, t, Z);
        PRIOB(0);
        t = pack4(re, im);
        lds_st<u2>(io, u2{t[0], t[1]}); lds_st<u2>(io + 2048, u2{t[2], t[3]});
    }
    bar();
    STAMP(7);
    {
        u4 v = lds_ld<u4>(LB_O + so);
        if (star) {
            u4 w;
#pragma unroll
            for (int s = 0; s < 2; s++) {
                const float ar = blo(v[s]), ai = bhi(v[s]), cr = blo(v[2 + s]), ci = bhi(v[2 + s]);
                w[s] = pk(ar + ci, ai - cr); w[2 + s] = pk(ar - ci, -ai - cr);
            }
            v = w;
        }
        if (u2i < NB2 - 1 || (tid & 7) == 0)
            __builtin_amdgcn_raw_buffer_store_b128(v, rws, gq, 0, 16);
        sv0 = v;
    }
    STAMP(8);
    if (!rows) { STAMP_OUT(0); return; }

    u4 gre = lds_ld<u4>(tl + (F_GI + 2 * tau) * 1024), gim = lds_ld<u4>(tl + (F_GI + 2 * tau + 1) * 1024);
    u4 bre = lds_ld<u4>(tl + F_BRE * 1024), bim = lds_ld<u4>(tl + F_BIM * 1024);
    u4 ai0 = lds_ld<u4>(tl + F_AI * 1024), ai1 = lds_ld<u4>(tl + (F_AI + 1) * 1024);
    int eadr[4];
#pragma unroll
    for (int i = 0; i < 4; i++) {
        const int r = 4 * g + i;
        eadr[i] = LB_E2 + (mod9(4 * (c16 + 18 - r)) * E2V + r * E2U + 2 * wv) * 4;
        pin(eadr[i]);
    }
    int ao = LB_E2 + (wv * E2V + c16 * E2U + 8 * g) * 4;
    int yo = LB_Y + (c16 >> 2) * OP + 2 * (144 * (((c16 & 3) - wv) & 3) + 16 * wv + 4 * g);
    int yl = LB_Y + tid * 16 + R * (OP - 1152);
    u32 oo = 2 * xoff - (tid - R * 72) * 16 + 2 * DM;
    pin(gre); pin(gim); pin(bre); pin(bim); pin(ai0); pin(ai1); pin(ao); pin(gy); pin(yo); pin(yl); pin(oo);
    asm volatile("" :: "v"(sv0));
    STAMP(9);
    {
        u4 v, w;
        if (wv < 3) { load_set<true>(rws, gy, true, v, tid <= 128, w); if (tid <= 128) lds_st<u4>(lin + 1056 * 16, w); }
        else load_set<false>(rws, gy, true, v, false, w);
        lds_st<u4>(lin, v);
    }
    bar();
    if (id == 0 && tid == 0) __builtin_amdgcn_raw_buffer_store_b32(tag, rws, WS_EPOCH, 0, 16);
    STAMP(10);
    {
        PRIOB(2);
        u4 d[2];
        u2 p[4];
#pragma unroll
        for (int i = 0; i < 4; i++) p[i] = lds_ld<u2>(sadr[i]);
        f4 re[2], im[2];
#pragma unroll
        for (int s = 0; s < 2; s++) { const u4 a = {p[0][s], p[1][s], p[2][s], p[3][s]}; re[s] = mfma(a, gre, Z); im[s] = mfma(a, gim, Z); }
        PRIOB(1);
#pragma unroll
        for (int s = 0; s < 2; s++) { const u4 t = pack4(re[s], im[s]); re[s] = mfma(t, bre, Z); im[s] = mfma(t, bim, Z); }
        PRIOB(0);
#pragma unroll
        for (int s = 0; s < 2; s++) d[s] = pack4(re[s], im[s]);
        if (c16 < 9) {
#pragma unroll
            for (int i = 0; i < 4; i++) lds_st<u2>(eadr[i], u2{d[0][i], d[1][i]});
        }
    }
    STAMP(15);
    bar();
    if (wv < 9) {
        const u4 p0 = lds_ld<u4>(ao), p1 = lds_ld<u4>(ao + 16);
#pragma unroll
        for (int s = 0; s < 2; s++) {
            const u4 a = {p0[s], p0[2 + s], p1[s], p1[2 + s]};
            const f4 y0 = mfma(a, ai0, Z), y1 = mfma(a, ai1, Z);
            lds_st<u2>(yo + s * (8 * OP), u2{pk(y0[0], y0[1]), pk(y0[2], y0[3])});
            lds_st<u2>(yo + s * (8 * OP) + 4 * OP, u2{pk(y1[0], y1[1]), pk(y1[2], y1[3])});
        }
    }
    bar();
    *(GAS u4*)((char*)out + oo) = lds_ld<u4>(yl);
    if (wv < 2) {
        const int r2 = tid < 56 ? 14 : 15, c2 = tid + 1024 - 72 * r2;
        *(GAS u4*)((char*)out + (u32)((s00 + 16 + 256 * (r2 - 8)) * (4 * DM) + 2 * DM + c2 * 16)) = lds_ld<u4>(LB_Y + r2 * OP + c2 * 16);
    }
    STAMP(11);
    STAMP_OUT(0);
}

DI f2 cexpa(float ar2, float air, float p) {
    const float e = __builtin_amdgcn_exp2f(ar2 * p), t = __builtin_amdgcn_fractf(air * p);
    return f2{e * __builtin_amdgcn_cosf(t), e * __builtin_amdgcn_sinf(t)};
}
DI u4 ld_k(int o) {
    const u2 a = *(const volatile LAS u2*)(u32)o, b = *(const volatile LAS u2*)(u32)(o + 8);
    return u4{a[0], a[1], b[0], b[1]};
}
DI void a_rows_out(int yb, int ystr, char* o) {
    u2 y[6];
#pragma unroll
    for (int c = 0; c < 6; c++) y[c] = lds_ld<u2>(yb + c * ystr);
#pragma unroll
    for (int r = 0; r < 4; r++) {
        u3 v;
#pragma unroll
        for (int c = 0; c < 3; c++) v[c] = __builtin_amdgcn_perm(y[2 * c + 1][r >> 1], y[2 * c][r >> 1], (r & 1) ? 0x07060302u : 0x05040100u);
        *(u3*)(o + r * (4 * DM)) = v;
    }
}
template <int R> DI void stage(u4 v) {
    asm volatile("ds_write_addtid_b32 %0 offset:%4\n\tds_write_addtid_b32 %1 offset:%5\n\tds_write_addtid_b32 %2 offset:%6\n\tds_write_addtid_b32 %3 offset:%7"
                 :: "v"(v[0]), "v"(v[1]), "v"(v[2]), "v"(v[3]), "i"(288 * R), "i"(288 * R + 288), "i"(288 * R + 576), "i"(288 * R + 864) : "memory");
}
#define CM_MUL(d, a, b) "v_pk_mul_f32 " d ", " a ", " b " op_sel_hi:[0,1]\n\t"
#define CM_FMA(d, a, b) "v_pk_fma_f32 " d ", " a ", " b ", " d " op_sel:[1,1,0] op_sel_hi:[1,0,1] neg_lo:[1,0,0]\n\t"
struct C4 { f2 a, b, c, d; };
DI C4 cmul4(f2 a0, f2 b0, f2 a1, f2 b1, f2 a2, f2 b2, f2 a3, f2 b3) {
    C4 r;
    asm("s_nop 0\n\t" CM_MUL("%0", "%4", "%5") CM_MUL("%1", "%6", "%7") CM_MUL("%2", "%8", "%9") CM_MUL("%3", "%10", "%11")
        CM_FMA("%0", "%4", "%5") CM_FMA("%1", "%6", "%7") CM_FMA("%2", "%8", "%9") CM_FMA("%3", "%10", "%11") "s_nop 0"
        : "=&v"(r.a), "=&v"(r.b), "=&v"(r.c), "=&v"(r.d) : "v"(a0), "v"(b0), "v"(a1), "v"(b1), "v"(a2), "v"(b2), "v"(a3), "v"(b3));
    return r;
}
DI C4 cmul4(const C4& a, f2 b) { return cmul4(a.a, b, a.b, b, a.c, b, a.d, b); }
DI u4 pk4(const C4& v) { return u4{pk(v.a.x, v.a.y), pk(v.b.x, v.b.y), pk(v.c.x, v.c.y), pk(v.d.x, v.d.y)}; }

DI void sig(int f) {
    asm volatile("s_waitcnt lgkmcnt(0)" ::: "memory");
    if ((threadIdx.x & 63) == 0) __hip_atomic_fetch_add((LAS u32*)(u32)(LA_F + 4 * f), 1u, __ATOMIC_RELAXED, __HIP_MEMORY_SCOPE_WORKGROUP);
}
template <int SLEEP = 0> DI void await(int f, u32 n) {
    while ((u32)__builtin_amdgcn_readfirstlane(*(const volatile LAS u32*)(u32)(LA_F + 4 * f)) < n) if (SLEEP) __builtin_amdgcn_s_sleep(SLEEP);
    asm volatile("" ::: "memory");
}
DI float exp_bf(float x) {
    const float t = x * 1.44269504f, r = __builtin_fmaf(x, 1.92596299e-8f, __builtin_fmaf(x, 1.44269504f, -t));
    const float e = __builtin_amdgcn_exp2f(t);
    return __builtin_fmaf(e * 0.693147181f, r, e);
}

struct APar { u32 l, a, r, i, t, d; u4 sel2, sel3, selx, tadr; };
DI void head_a(const u16* __restrict__ u, APar pr, u16* __restrict__ out, char* ws) {
    const int tid = threadIdx.x, lane = tid & 63, c16 = lane & 15, g = lane >> 4;
    const int wv = __builtin_amdgcn_readfirstlane(tid >> 6);
    STAMP_DECL;
    const int ai_ = blockIdx.x - A0;
    const int d0 = 6 * ((ai_ & 7) * 12 + (ai_ >> 3));
    const f4 Z = {0.f, 0.f, 0.f, 0.f};

    if (wv >= 12) {
        __builtin_amdgcn_s_setprio(3);
        const int t = tid - 768;
        if (t == 0) lds_st<u2>(LA_F, u2{0u, 0u});
        if (t < 180) lds_st<u4>(LA_U + (t / 30) * 4608 + (t % 30) * 16, u4{0u, 0u, 0u, 0u});
        bar();
#pragma unroll
        for (int i = 0; i < 2; i++)
            if (i == 0 || wv < 14) {
                const int r = min((ai_ >> 3) + 12 * (128 * i + (t >> 1)), SEQ - 1);
                u32 keep;
                asm volatile("s_mov_b32 %0, m0\n\ts_mov_b32 m0, %3\n\ts_nop 4\n\tglobal_load_lds_dword %1, %2\n\ts_mov_b32 m0, %0"
                             : "=&s"(keep) : "v"(r * (2 * DM) + (((ai_ & 7) * 144) & ~127) + (t & 1) * 128), "s"(u), "s"(LA_D) : "memory");
            }
        u3 ua[4], ub[4];
#pragma unroll
        for (int r = 0; r < 4; r++) ua[r] = *(const u3*)(u + (4 * t + r) * DM + d0);
        sig(FU0);
        await<1>(FU0, 4);
#pragma unroll
        for (int r = 0; r < 4; r++) ub[r] = *(const u3*)(u + (1024 + 4 * t + r) * DM + d0);
        asm volatile("" ::: "memory");
#pragma unroll
        for (int c = 0; c < 6; c++) {
            const u32 sel = (c & 1) ? 0x07060302u : 0x05040100u;
            lds_st<u2>(LA_U + c * 4608 + 480 + t * 8, u2{__builtin_amdgcn_perm(ua[1][c >> 1], ua[0][c >> 1], sel), __builtin_amdgcn_perm(ua[3][c >> 1], ua[2][c >> 1], sel)});
        }
        STAMP(7);
        bar();
        bar();
#pragma unroll
        for (int c = 0; c < 6; c++) {
            const u32 sel = (c & 1) ? 0x07060302u : 0x05040100u;
            lds_st<u2>(LA_U + c * 4608 + 480 + 2048 + t * 8, u2{__builtin_amdgcn_perm(ub[1][c >> 1], ub[0][c >> 1], sel), __builtin_amdgcn_perm(ub[3][c >> 1], ub[2][c >> 1], sel)});
        }
        sig(FU2);
        STAMP(8);
        const int tile = wv - 12, yl = ((lane >> 2) + 16 * (lane & 3)) * 8;
        char* o = (char*)(out + 4 * t * (2 * DM) + d0);
        bar();
        bar();
        STAMP(11);
        a_rows_out(LA_G + ((tile >> 1) ^ 1) * 4608 + (tile & 1) * 1024 + yl, 9216, o);
        STAMP(9);
        bar();
        bar();
        STAMP(12);
        a_rows_out(LA_G + ((tile >> 1) ^ 1) * 4608 + (tile & 1) * 1024 + 2048 + yl, 9216, o + 1024 * (4 * DM));
        STAMP(10);
        STAMP_OUT(768);
        return;
    }
    const int ch = wv >> 1, hf = wv & 1;
    {
        __builtin_amdgcn_s_barrier();
        asm volatile("s_waitcnt vmcnt(4)" : "+v"(pr.l), "+v"(pr.a), "+v"(pr.r), "+v"(pr.i), "+v"(pr.t), "+v"(pr.d) :: "memory");
        PRIO(3);
        const float dt = rbf(exp_bf(blo(pr.t)));
        const float ar2 = -rbf(exp_bf(blo(pr.l))) * dt * 1.44269504f, air = blo(pr.a) * dt * 0.159154943f;
        const f2 C = {2.f * dt * blo(pr.r), 2.f * dt * blo(pr.i)};
        STAMP(1);
        const f2 z1 = cexpa(ar2, air, 1.f), z8c = cexpa(ar2, -air, 8.f), z128 = cexpa(ar2, air, 128.f);
        const int gr = LA_G + wv * 4608 + c16 * 288 + g * 16, q = LA_Q + wv * 2576;
        asm volatile("s_mov_b32 m0, %0\n\ts_nop 0" :: "s"(LA_G + wv * 4608) : "memory");
#define SB __builtin_amdgcn_sched_barrier(0)
#define STAGE16(v) do { stage<0>(v[0]); stage<4>(v[1]); stage<8>(v[2]); stage<12>(v[3]); } while (0)
#define READ4(f) do { _Pragma("unroll") for (int s = 0; s < 4; s++) f[s] = lds_ld<u4>(gr + 64 * s); SB; } while (0)
        u4 v[4], bfr[4], afr[4];
        const C4 s1 = cmul4(z8c, z8c, z1, z1, z128, z128, C, z1);
        const C4 s2 = cmul4(s1.a, s1.a, z8c, s1.a, s1.c, s1.c, C, s1.b);
        {
            C4 w = {f2{1.f, 0.f}, z8c, s1.a, s2.b};
            v[0] = pk4(w);
#pragma unroll
            for (int i = 1; i < 4; i++) { w = cmul4(w, s2.a); v[i] = pk4(w); }
        }
        STAGE16(v); READ4(bfr);
        PRIO(2);
        const C4 s3 = cmul4(s1.d, s1.b, s1.b, s1.b, s2.c, s2.c, s2.c, z128);
        const C4 e0 = {C, s1.d, s2.d, s3.a}, e1 = cmul4(e0, s3.b);
        v[0] = pk4(e0); v[1] = pk4(e1); v[2] = pk4(cmul4(e0, z128)); v[3] = pk4(cmul4(e1, z128));
        STAGE16(v); READ4(afr);
        PRIO(1);
        {
            const C4 f = cmul4(C4{e0.a, e0.c, e1.a, e1.c}, s1.c), h = cmul4(e0.a, s2.c, e1.a, s2.c, e0.a, s3.d, e1.a, s3.d);
            v[0] = pk4(f); v[1] = pk4(cmul4(f, z128)); v[2] = pk4(h); v[3] = pk4(cmul4(h, s1.c));
        }
        SB;
        f4 acc = Z;
#pragma unroll
        for (int s = 0; s < 4; s++) acc = mfma(afr[s], bfr[s], acc);
        STAGE16(v); READ4(afr);
        PRIO(0);
        lds_st<f4>(q + (4 * (g & 1) + 128 * (g >> 1) + 8 * c16) * 4, acc);
        {
            const f2 f0 = cmul(C, s3.c), f1 = cmul(f0, z128);
            const C4 f = cmul4(f0, s1.c, f1, s1.c, f0, s3.c, f0, s3.c), h = cmul4(C4{f0, f1, f.a, f.b}, s2.c);
            v[0] = pk4(C4{f0, f1, f.a, f.b}); v[1] = pk4(h); v[2][0] = pk(f.c.x, f.c.y);
        }
        SB;
        acc = Z;
#pragma unroll
        for (int s = 0; s < 4; s++) acc = mfma(afr[s], bfr[s], acc);
        stage<0>(v[0]); stage<4>(v[1]);
        asm volatile("ds_write_addtid_b32 %0 offset:%1" :: "v"(v[2][0]), "i"(288 * 8) : "memory");
        READ4(afr);
        if (g < 2) lds_st<f4>(q + (256 + 64 * g + 4 * c16) * 4, acc);
        else {
            const int o = q + (384 + 64 * (g - 2) + 2 * c16) * 4;
            lds_st<f2>(o, f2{acc[0], acc[1]}); lds_st<f2>(o + 128, f2{acc[2], acc[3]});
        }
        acc = Z;
#pragma unroll
        for (int s = 0; s < 4; s++) acc = mfma(afr[s], bfr[s], acc);
        if (g < 2) {
#pragma unroll
            for (int i = 0; i < 4; i++) lds_st<float>(q + (512 + 64 * g + 16 * i + c16) * 4, acc[i]);
        } else if (lane == 32) lds_st<float>(q + 640 * 4, acc[0]);
    }
    STAMP(2);
    asm volatile("s_waitcnt vmcnt(0) lgkmcnt(0)" : "+v"(pr.sel2), "+v"(pr.sel3), "+v"(pr.selx), "+v"(pr.tadr) :: "memory");
    const u4 sel2 = pr.sel2, sel3 = pr.sel3, selx = pr.selx, tadr = pr.tadr;
    bar();
    STAMP(9);
    const int k0 = LA_K + ch * KC;
    {
        const LAS float* qa = (const LAS float*)(u32)(LA_Q + ch * 5152);
        const LAS float* qb = qa + 644;
        u2 w[4];
        int y;
        if (hf == 0) {
            const int j = 384 + 4 * lane;
            const f4 a = *(const LAS f4*)(qa + j) + *(const LAS f4*)(qb + j), b = *(const LAS f4*)(qa + j + 4) + *(const LAS f4*)(qb + j + 4);
            const u32 ix = tadr[3];
            const float e0 = qa[(ix & 0xffff) >> 2] + qb[(ix & 0xffff) >> 2], e1 = qa[ix >> 18] + qb[ix >> 18];
            f4 k = *(const LAS f4*)(qa + 4 * lane) + *(const LAS f4*)(qb + 4 * lane);
            f4 p = *(const LAS f4*)(qa + 4 * lane - 4) + *(const LAS f4*)(qb + 4 * lane - 4);
            const u32 p23 = pk(a[2], a[3]), p34 = pk(a[3], b[0]);
            lds_st<u4>(k0 + 4 * KI + lane * 32, u4{pk(a[0], a[1]), p23, pk(a[1], a[2]), p34});
            lds_st<u4>(k0 + 4 * KI + lane * 32 + 16, u4{p23, pk(b[0], b[1]), p34, pk(b[1], b[2])});
            lds_st<u32>(k0 + g * KI + c16 * 4, pk(e0, e1));
            if (lane == 0) { k[0] += blo(pr.d); p = Z; }
            const u32 c = pk(k[1], k[0]), d = pk(k[0], p[3]);
            w[0] = u2{pk(k[3], k[2]), c}; w[1] = u2{pk(k[2], k[1]), d}; w[2] = u2{c, pk(p[3], p[2])}; w[3] = u2{d, pk(p[2], p[1])};
            y = k0 + (270 - 2 * lane) * 4;
        } else {
            const int j = 256 + 2 * lane;
            const float q0 = qa[j] + qb[j], q1 = qa[j + 1] + qb[j + 1], q2 = qa[j + 2] + qb[j + 2];
            const float p1 = qa[j - 1] + qb[j - 1], p2 = qa[j - 2] + qb[j - 2], p3 = qa[j - 3] + qb[j - 3];
            const u32 c = pk(q0, q0), d = pk(q0, p1), e = pk(p1, p2), f = lane ? c : d;
            w[0] = u2{pk(q2, q1), c}; w[1] = u2{pk(q1, q0), f}; w[2] = u2{c, lane ? d : e}; w[3] = u2{f, lane ? e : pk(p2, p3)};
            y = k0 + (142 - 2 * lane) * 4;
            if (lane < 32) lds_st<u32>(k0 + (lane >> 3) * KI + (272 + (lane & 7)) * 4, 0u);
        }
#pragma unroll
        for (int i = 0; i < 4; i++) lds_st<u2>(y + i * KI, w[i]);
    }
    const int x0 = 2031 - c16 + 8 * g;
    int ka = k0 + (x0 & 3) * KI + (x0 >> 2) * 8 - 3008 - 256 * hf - 192 - 512;
    int ubs = LA_U + ch * 4608 + 480 + (16 * c16 + 8 * g - 16) * 2 - 256 * hf - 192;
    int q2a = k0 + 4 * KI + tadr[0], q3a = k0 + 4 * KI + tadr[1], qxa = k0 + 4 * KI + tadr[2];
    pin(ka); pin(ubs); pin(q2a); pin(q3a); pin(qxa);
    STAMP(13);
    bar();
    STAMP(3);
#define KFR(rho, p) ld_k(ka + 192 - 64 * (rho) + 512 - 512 * (p))
#define UFR(rho, q) lds_ld<u4>(ubs + 192 - 64 * (rho) + 512 * (q))
#define QFR(rho, p) ((p) < 4 ? lds_ld<u2>(q2a + 64 * (8 * ((p) - 2) + (rho))) : ((p) == 4 && (rho) == 0 ? lds_ld<u2>(qxa) : lds_ld<u2>(q3a + 32 * (8 * ((p) - 4) + (rho)))))
#define QSEL(rho, p) ((p) < 4 ? sel2 : ((p) == 4 && (rho) == 0 ? selx : sel3))
#define QBUILD(r, s) u4{__builtin_amdgcn_perm(r[1], r[0], s[0]), __builtin_amdgcn_perm(r[1], r[0], s[1]), __builtin_amdgcn_perm(r[1], r[0], s[2]), __builtin_amdgcn_perm(r[1], r[0], s[3])}
#define FIRST2(dst) do { if (hf) { const u2 r_ = QFR(0, 2); dst = QBUILD(r_, sel2); } else dst = ld_k(ka - 320); } while (0)
    f4 acc[8];
#pragma unroll
    for (int i = 0; i < 8; i++) acc[i] = Z;
    {
        u4 A[2], T[2], B[2][4];
        u2 R[8];
#pragma unroll
        for (int p = 0; p < 4; p++) B[0][p] = UFR(0, p);
        A[0] = KFR(0, 0); A[1] = KFR(0, 1);
#pragma unroll
        for (int p = 3; p < 8; p++) R[p] = QFR(0, p);
        FIRST2(T[0]);
#pragma unroll
        for (int rho = 0; rho < 4; rho++)
#pragma unroll
            for (int p = 0; p < 8; p++) {
                SB;
                if (p == 0) { if (rho == 0) PRIO(3); else if (rho == 1) PRIO(2); else if (rho == 2) PRIO(1); else PRIO(0); }
                if (rho < 3 && p < 4) B[(rho + 1) & 1][p] = UFR(rho + 1, p);
                if (p >= 1 && p < 7 && !(p == 1 && rho == 0)) T[(p + 1) & 1] = QBUILD(R[p + 1], QSEL(rho, p + 1));
#pragma unroll
                for (int q = 0; q < 4; q++)
                    if (p + q < 8) acc[p + q] = mfma(p < 2 ? A[p] : T[p & 1], B[rho & 1][q], acc[p + q]);
                SB;
                if (rho < 3) {
                    if (p < 2) A[p] = KFR(rho + 1, p);
                    if (p >= 1 && p < 7) R[p + 1] = QFR(rho + 1, p + 1);
                }
            }
    }
#pragma unroll
    for (int i = 0; i < 8; i++) pin(acc[i]);
    STAMP(4);
#define EXCHANGE(t0, off)                                                                     \
    {                                                                                         \
        const int mine = LA_G + wv * 4608 + (off), other = LA_G + (wv ^ 1) * 4608 + (off);     \
        if (hf) { lds_st<f4>(mine + lane * 16, acc[t0]); lds_st<f4>(mine + 1024 + lane * 16, acc[t0 + 1]); acc[t0] = acc[t0 + 2]; acc[t0 + 1] = acc[t0 + 3]; } \
        else { lds_st<f4>(mine + lane * 16, acc[t0 + 2]); lds_st<f4>(mine + 1024 + lane * 16, acc[t0 + 3]); } \
        bar();                                                                                \
        const f4 y0 = lds_ld<f4>(other + lane * 16) + acc[t0], y1 = lds_ld<f4>(other + 1024 + lane * 16) + acc[t0 + 1]; \
        lds_st<u2>(other + lane * 8, u2{pk(y0[0], y0[1]), pk(y0[2], y0[3])});                 \
        lds_st<u2>(other + 1024 + lane * 8, u2{pk(y1[0], y1[1]), pk(y1[2], y1[3])});          \
    }
    await<1>(FU2, 4);
    EXCHANGE(0, 0);
    {
        u4 A[2][2], T[2], B[2][4];
        u2 R[4];
#pragma unroll
        for (int p = 0; p < 4; p++) B[0][p] = UFR(0, 4 + p);
        A[0][0] = KFR(0, 0); A[0][1] = KFR(0, 1); R[3] = QFR(0, 3);
        FIRST2(T[0]);
        bar();
        STAMP(14);
#pragma unroll
        for (int rho = 0; rho < 4; rho++)
#pragma unroll
            for (int p = 0; p < 4; p++) {
                SB;
                if (p == 0) { if (rho == 0) PRIO(3); else if (rho == 1) PRIO(2); else if (rho == 2) PRIO(1); else PRIO(0); }
                if (rho < 3) { B[(rho + 1) & 1][p] = UFR(rho + 1, 4 + p); if (p < 2) A[(rho + 1) & 1][p] = KFR(rho + 1, p); }
                if (p == 0 && rho > 0) T[0] = QBUILD(R[2], sel2);
                if (p == 1) T[1] = QBUILD(R[3], sel2);
#pragma unroll
                for (int q = 0; p + q < 4; q++) acc[4 + p + q] = mfma(p < 2 ? A[rho & 1][p] : T[p & 1], B[rho & 1][q], acc[4 + p + q]);
                SB;
                if (rho < 3 && p < 2) R[2 + p] = QFR(rho + 1, 2 + p);
            }
        SB;
    }
#pragma unroll
    for (int i = 4; i < 8; i++) pin(acc[i]);
    STAMP(5);
    EXCHANGE(4, 2048);
    bar();
    STAMP(6);
    STAMP_OUT(0);
}

__global__ void __launch_bounds__(NT) __attribute__((aligned(4096))) fused_kernel(const u16* __restrict__ u, const u16* __restrict__ lar, const u16* __restrict__ aimg,
                                                   const u16* __restrict__ cre, const u16* __restrict__ cim, const u16* __restrict__ ldt,
                                                   const u16* __restrict__ dsk, const u16* __restrict__ x, const u16* __restrict__ wr,
                                                   const u16* __restrict__ wi, u16* __restrict__ out, char* ws) {
    const char* entry;
    asm volatile("s_getpc_b64 %0" : "=s"(entry));
    APar pr;
    if (blockIdx.x >= A0 && __builtin_amdgcn_readfirstlane(threadIdx.x) < 768) {
        const int a = blockIdx.x - A0, d = 6 * ((a & 7) * 12 + (a >> 3)) + __builtin_amdgcn_readfirstlane(threadIdx.x >> 7);
        asm volatile("global_load_ushort %0, %6, %8\n\tglobal_load_ushort %1, %6, %9\n\tglobal_load_ushort %2, %6, %10\n\t"
                     "global_load_ushort %3, %6, %11\n\tglobal_load_ushort %4, %7, %12\n\tglobal_load_ushort %5, %7, %13"
                     : "=&v"(pr.l), "=&v"(pr.a), "=&v"(pr.r), "=&v"(pr.i), "=&v"(pr.t), "=&v"(pr.d)
                     : "v"((d * NS + (threadIdx.x & 127)) * 2), "v"(d * 2), "s"(lar), "s"(aimg), "s"(cre), "s"(cim), "s"(ldt), "s"(dsk) : "memory");
        asm volatile("global_load_dwordx4 %0, %4, %5\n\tglobal_load_dwordx4 %1, %4, %5 offset:1024\n\t"
                     "global_load_dwordx4 %2, %4, %5 offset:2048\n\tglobal_load_dwordx4 %3, %4, %5 offset:3072"
                     : "=&v"(pr.sel2), "=&v"(pr.sel3), "=&v"(pr.selx), "=&v"(pr.tadr)
                     : "v"((threadIdx.x & 63) * 16 + (threadIdx.x & 64) * 64), "s"((const char*)&TAB + F_END * 1024) : "memory");
    }
    if (__builtin_expect(blockIdx.x < NB2, 1)) head_b(entry, x, wr, wi, out, ws);
    else if (blockIdx.x >= A0) head_a(u, pr, out, ws);
    else warm_code(entry);
}

extern "C" void kernel_launch(void* const* d_in, const int* in_sizes, int n_in, void* d_out, int out_size, void* d_ws,
                              size_t ws_size, hipStream_t stream) {
    (void)hipFuncSetAttribute((const void*)fused_kernel, hipFuncAttributeMaxDynamicSharedMemorySize, LDS_BYTES);
    fused_kernel<<<GRID, NT, LDS_BYTES, stream>>>((const u16*)d_in[0], (const u16*)d_in[1], (const u16*)d_in[2], (const u16*)d_in[3],
                                                  (const u16*)d_in[4], (const u16*)d_in[5], (const u16*)d_in[6], (const u16*)d_in[7],
                                                  (const u16*)d_in[8], (const u16*)d_in[9], (u16*)d_out, (char*)d_ws);
}
```

```cpp
#define NO_UPF
#define POLL_SLEEP 6
#include <hip/hip_runtime.h>

typedef unsigned short u16;
typedef unsigned int u32;
typedef float f2 __attribute__((ext_vector_type(2)));
typedef float f4 __attribute__((ext_vector_type(4)));
typedef u32 u2 __attribute__((ext_vector_type(2)));
typedef u32 u4 __attribute__((ext_vector_type(4)));
typedef int i8v __attribute__((ext_vector_type(8)));
typedef short s4 __attribute__((ext_vector_type(4)));
typedef short s8 __attribute__((ext_vector_type(8)));
typedef __bf16 bf2 __attribute__((ext_vector_type(2)));
#define DI __device__ __forceinline__
#define LAS __attribute__((address_space(3)))
#define GAS __attribute__((address_space(1)))
#define CAS __attribute__((address_space(4)))
#pragma clang diagnostic ignored "-Wint-to-pointer-cast"
struct u3 { u32 v[3]; DI u32& operator[](int i) { return v[i]; } DI const u32& operator[](int i) const { return v[i]; } };

constexpr int SEQ = 2048, DM = 576, NS = 128, DF = 289;
constexpr int NB1 = 128;
constexpr int NB2 = 145;
constexpr int A0 = 152;
constexpr int NA = 96;
constexpr int GRID = A0 + NA;
constexpr int NT = 1024;

constexpr u32 WS_EPOCH = 0, WS_F1 = 4096, WS_F2 = WS_F1 + 16 * 16 * 128, WS_X1 = 1u << 20, WS_Y1 = 4u << 20, WS_USED = 8u << 20;
#define STAMP(i)
#define STAMP_DECL
#define STAMP_OUT(T)

constexpr int F_SA = 0, F_EO = 4;
constexpr int F_ARE = 9, F_AIM = 10, F_BRE = 11, F_BIM = 12, F_G = 13;
constexpr int F_F16 = 21, F_GI = 23, F_AI = 31, F_M1C = 33;
constexpr int F_VAR = 37;
constexpr int F_END = F_VAR + 40;
constexpr int ET = 452, ER = 28, E2V = 580, E2U = 36, OP = 1168;
constexpr int LB_X = 0, LB_I = 0, LB_Y = 0;
constexpr int LB_E = 16 * OP;
constexpr int LB_O = LB_E, LB_E2 = LB_E;
constexpr int LB_S = LB_E + 16 * ET * 4;
constexpr int LB_W = LB_S + 1200 * 16;
constexpr int LB_T = LB_W + 2 * 256 * 36;
constexpr int LB_G = LB_T + (F_VAR + 16) * 1024;
constexpr int LB_END = LB_G + 16;
struct CosTab { double c[2048]; double c9[9], s9[9]; };
constexpr double tay_cos(double x) { double t = 1, s = 1; for (int k = 1; k <= 14; k++) { t *= -x * x / ((2 * k - 1) * (2 * k)); s += t; } return s; }
constexpr double tay_sin(double x) { double t = x, s = x; for (int k = 1; k <= 14; k++) { t *= -x * x / ((2 * k + 1) * (2 * k)); s += t; } return s; }
constexpr CosTab make_cos() {
    CosTab t = {};
    const double PI2 = 6.283185307179586476925;
    for (int k = 0; k <= 512; k++) {
        const double v = k == 512 ? 0.0 : (k <= 256 ? tay_cos(PI2 * k / 2048) : tay_sin(PI2 * (512 - k) / 2048));
        t.c[k] = v; t.c[(2048 - k) & 2047] = v; t.c[1024 - k] = -v; t.c[(1024 + k) & 2047] = -v;
    }
    t.c[512] = 0.0; t.c[1536] = 0.0;
    for (int k = 0; k < 9; k++) { const double a = PI2 * (k <= 4 ? k : k - 9) / 9; t.c9[k] = tay_cos(a); t.s9[k] = tay_sin(a); }
    return t;
}
constexpr CosTab CT = make_cos();
constexpr double cs(int n) { return CT.c[n & 2047]; }
constexpr double sn(int n) { return CT.c[(n - 512) & 2047]; }
constexpr u32 bfr(double x) { const u32 b = __builtin_bit_cast(u32, (float)x); return (b + 0x7fffu + ((b >> 16) & 1u)) >> 16; }
constexpr u32 pkc(double lo, double hi) { return bfr(lo) | (bfr(hi) << 16); }
constexpr u32 f32c(double x) { return __builtin_bit_cast(u32, (float)x); }
template <int N> struct Frags { u32 w[N * 256]; };
constexpr u32 w_re(double amp, int n) { return pkc(amp * cs(n), amp * sn(n)); }
constexpr u32 w_im(double amp, int n) { return pkc(-amp * sn(n), amp * cs(n)); }
constexpr Frags<F_VAR> make_t0() {
    Frags<F_VAR> t = {};
    const double R2 = 1.4142135623730951;
    for (int l = 0; l < 64; l++)
        for (int i = 0; i < 4; i++) {
            const int c = l & 15, g = l >> 4, r = 4 * g + i, o = l * 4 + i;
            {
                const int b = c >> 2, tq = c & 3;
                const double amp = b ? 1.0 : R2;
                const int n0 = 512 * tq * g + (b ? 256 * b * (2 * i) : -256), n1 = 512 * tq * g + (b ? 256 * b * (2 * i + 1) : 256);
                t.w[F_ARE * 256 + o] = pkc(amp * cs(n0), amp * cs(n1));
                t.w[F_AIM * 256 + o] = pkc(-amp * sn(n0), -amp * sn(n1));
            }
            for (int w = 0; w < 9; w++) {
                const int sh = 16 * (i & 1), oo = w * 128 + l * 2 + (i >> 1);
                t.w[F_EO * 256 + oo] |= (u32)((c * ET + r * ER + 2 * ((7 * w + r) % 9)) * 4) << sh;
                if (w < 8) {
                    const int k = (9 * (w & 3) + 36 * r + 64 * (c % 9)) % DM, slot = w < 4 ? (k <= 288 ? 2 * k : 2 * (DM - k) + 1) : k;
                    t.w[F_SA * 256 + oo] |= (u32)(slot * 8 + (slot >> 6) * 16) << sh;
                }
            }
            if (r < 9) { const int k = (r * c) % 9; t.w[F_BRE * 256 + o] = pkc(CT.c9[k], CT.s9[k]); t.w[F_BIM * 256 + o] = pkc(-CT.s9[k], CT.c9[k]); }
            for (int q = 0; q < 4; q++) {
                t.w[(F_G + 2 * q) * 256 + o] = w_re(1.0, 32 * r * (q + 4 * c)); t.w[(F_G + 2 * q + 1) * 256 + o] = w_im(1.0, 32 * r * (q + 4 * c));
                t.w[(F_GI + 2 * q) * 256 + o] = w_re(1.0, 32 * c * (q + 4 * r)); t.w[(F_GI + 2 * q + 1) * 256 + o] = w_im(1.0, 32 * c * (q + 4 * r));
            }
            t.w[F_F16 * 256 + o] = w_re(1.0, 128 * r * c); t.w[(F_F16 + 1) * 256 + o] = w_im(1.0, 128 * r * c);
            for (int h = 0; h < 2; h++) {
                const int j = 4 * h + (c >> 2), n = 512 * i * (c & 3) + (g ? 256 * g * j : ((j & 1) ? 256 : -256));
                t.w[(F_AI + h) * 256 + o] = w_re(g ? 2.0 : R2, n);
                const int m = 16 * r * (4 * h + 8 * c) - 512 * h;
                t.w[(F_M1C + 2 * h) * 256 + o] = pkc(0.5 * cs(m), -0.5 * sn(m));
                t.w[(F_M1C + 2 * h + 1) * 256 + o] = pkc(-0.5 * sn(m), -0.5 * cs(m));
            }
        }
    return t;
}
constexpr Frags<40> make_t1() {
    Frags<40> t = {};
    for (int v = 0; v < 5; v++)
        for (int l = 0; l < 64; l++)
            for (int i = 0; i < 4; i++) {
                const int be = v < 3 ? v + 1 : 4 * (v - 3), c = l & 15, r = 4 * (l >> 4) + i, o = v * 2048 + l * 4 + i;
                const double amp = v < 3 ? 1.0 : 0.5;
                const int n1 = 16 * r * (be + 8 * c) + (v == 4 ? 512 : 0), n4 = 16 * c * (be + 8 * r);
                t.w[o] = w_re(amp, n1); t.w[o + 256] = w_im(amp, n1);
                t.w[o + 512] = w_re(1.0, n4); t.w[o + 768] = w_im(1.0, n4);
                t.w[o + 1024] = f32c(cs(r * (be + 8 * c))); t.w[o + 1280] = f32c(-sn(r * (be + 8 * c)));
                t.w[o + 1536] = f32c(cs(c * (be + 8 * r))); t.w[o + 1792] = f32c(-sn(c * (be + 8 * r)));
            }
    return t;
}
constexpr int qidx(int l) {
    if (l <= 256) return l;
    const int sh = l < 512 ? 1 : (l < 1024 ? 2 : 3), h = 1 << (sh - 1), m = l >> sh, r = l & ((1 << sh) - 1);
    return 128 * sh + m + ((r > h || (r == h && (m & 1))) ? 1 : 0);
}
constexpr Frags<6> make_t2() {
    Frags<6> t = {};
    for (int l = 0; l < 64; l++) {
        const int c = l & 15, g = l >> 4, e = 543 - 2 * c - g;
        t.w[256 + l * 4] = (u32)((e < 518 ? qidx(e) : 643) * 4) | ((u32)((e - 1 < 518 ? qidx(e - 1) : 643) * 4) << 16);
        for (int i = 0; i < 4; i++)
            for (int h = 0; h < 2; h++) {
                const int lag = 1024 + 16 + c - 8 * g - 2 * i - h, x = 8 * g + 2 * i + h;
                t.w[l * 4 + i] |= (lag < 1027 || lag > 1035 ? 0x0c0cu : (lag < 1029 ? 0x0100u : 0x0302u)) << (16 * h);
                if (x >= c + 8 && x <= c + 16) t.w[2 * 256 + l * 4 + i] |= 0x3f80u << (16 * h);
                if (x >= c + 1 && x <= c + 7) t.w[3 * 256 + l * 4 + i] |= 0x3f80u << (16 * h);
                if (x >= c + 6 && x <= c + 10) t.w[4 * 256 + l * 4 + i] |= 0x3f80u << (16 * h);
                if (x >= c + 3 && x <= c + 5) t.w[5 * 256 + l * 4 + i] |= 0x3f80u << (16 * h);
            }
    }
    return t;
}
struct Tables { Frags<F_VAR> a; Frags<40> b; Frags<6> c; };
constexpr Frags<6> T2 = make_t2();
constexpr Frags<F_VAR> T0 = make_t0();
constexpr Frags<40> T1 = make_t1();
static __device__ const __attribute__((aligned(128))) Tables TAB = {T0, T1, T2};
constexpr int LA_G = 0;
constexpr int LA_K = LA_G + 12 * 4608;
constexpr int KI = 1344, KQ = 4 * KI, KP = KQ + 512, KX = KP + 320, KC = KX + 16;
constexpr int LA_U = LA_K + 6 * KC;
constexpr int LA_Q = LA_U + 6 * 4608;
constexpr int LA_F = LA_Q + 12 * 2576;
constexpr int LA_D = LA_F + 16;
constexpr int LA_S = LA_D + 256;
constexpr int LA_END = LA_S + 6144;
constexpr int FU0 = 0, FU2 = 1;
constexpr int LDS_BYTES = (LA_END > LB_END ? LA_END : LB_END);
static_assert(LDS_BYTES <= 160 * 1024 && LDS_BYTES > 80 * 1024, "one workgroup per CU");

DI u32 pk(float a, float b) { f2 v = {a, b}; return __builtin_bit_cast(u32, __builtin_convertvector(v, bf2)); }
DI float blo(u32 v) { return __uint_as_float(v << 16); }
DI float bhi(u32 v) { return __uint_as_float(v & 0xffff0000u); }
DI float bf2f(u16 v) { return __uint_as_float((u32)v << 16); }
DI float rbf(float x) { return blo(pk(x, x)); }
DI f2 cmul(f2 a, f2 b) { f2 t = a.xx * b; f2 s = {-b.y, b.x}; return __builtin_elementwise_fma(a.yy, s, t); }
DI f4 mfma(u4 a, u4 b, f4 c) { return __builtin_amdgcn_mfma_f32_16x16x32_bf16(__builtin_bit_cast(s8, a), __builtin_bit_cast(s8, b), c, 0, 0, 0); }
DI u4 pack4(f4 re, f4 im) { u4 r = {pk(re[0], im[0]), pk(re[1], im[1]), pk(re[2], im[2]), pk(re[3], im[3])}; return r; }
DI u4 twid(f4 re, f4 im, f4 wr, f4 wi) { return pack4(__builtin_elementwise_fma(re, wr, -(im * wi)), __builtin_elementwise_fma(re, wi, im * wr)); }
DI int mod9(int x) { return x - 9 * ((x * 57) >> 9); }
template <class T> DI void pin(T& v) { asm volatile("" : "+v"(v)); }

template <class T> DI T lds_ld(int off) { return *(const LAS T*)(u32)off; }
template <class T> DI void lds_st(int off, T v) { *(LAS T*)(u32)off = v; }
DI void bar() { __syncthreads(); }
#define PRIO(n) __builtin_amdgcn_s_setprio(n)
#define PRIOB(n)
DI void glds16(const void* g, u32 off, int lds) {
    u32 keep;
    asm volatile("s_mov_b32 %0, m0\n\ts_mov_b32 m0, %3\n\ts_nop 4\n\tglobal_load_lds_dwordx4 %1, %2\n\ts_mov_b32 m0, %0" : "=&s"(keep) : "v"(off), "s"(g), "s"(lds) : "memory");
}

DI void publish_tag(u32 epoch) {
    u32 t;
    asm volatile("v_add_u32 %0, 0x9E3779B1, %1\n\tds_write_b32 %2, %0\n\ts_waitcnt lgkmcnt(0)" : "=&v"(t) : "v"(epoch), "v"(LB_G) : "memory");
}
DI void wait_flag(__amdgpu_buffer_rsrc_t r, u32 off, u32 tag, bool on) {
    if (on) {
        const unsigned long long t0 = __builtin_amdgcn_s_memrealtime();
        while (!__all(__builtin_amdgcn_raw_buffer_load_b32(r, off, 0, 16) == tag)) {
            if (__builtin_amdgcn_s_memrealtime() - t0 > 5000000ull) break;
            __builtin_amdgcn_s_sleep(POLL_SLEEP);
            asm volatile("" ::: "memory");
        }
    }
    asm volatile("" ::: "memory");
}

constexpr u32 POISON = 0xFFFFFFFFu;
DI bool unset(u4 v) { return max(max(v[0], v[1]), max(v[2], v[3])) == POISON; }
#define DPOLL_SLEEP 1
template <bool TWO> DI void load_set(__amdgpu_buffer_rsrc_t r, u32 off, bool on, u4& a, bool on2, u4& b) {
    const unsigned long long t0 = __builtin_amdgcn_s_memrealtime();
    bool ma = on, mb = TWO && on2;
    for (;;) {
        if (ma) a = __builtin_amdgcn_raw_buffer_load_b128(r, off, 0, 16);
        if (TWO && mb) b = __builtin_amdgcn_raw_buffer_load_b128(r, off, 16384, 16);
        ma = ma && unset(a);
        if (TWO) mb = mb && unset(b);
        if (!__any(ma || mb) || __builtin_amdgcn_s_memrealtime() - t0 > 5000000ull) break;
        __builtin_amdgcn_s_sleep(DPOLL_SLEEP);
        asm volatile("" ::: "memory");
    }
}
constexpr int CODE_LINES = 128;
DI void warm_code(const char* entry) {
    if (threadIdx.x < CODE_LINES) { const u32 t = *(const GAS u32*)(entry + threadIdx.x * 128); asm volatile("" :: "v"(t)); }
}

DI void head_b(const char* entry, const u16* __restrict__ x, const u16* __restrict__ wr, const u16* __restrict__ wi,
               u16* __restrict__ out, char* ws) {
    const int tid = threadIdx.x, lane = tid & 63, c16 = lane & 15, g = lane >> 4;
    const int wv = __builtin_amdgcn_readfirstlane(tid >> 6);
    const int id = blockIdx.x;
    STAMP_DECL;
    const bool rows = id < NB1;
    const int s00 = 32 * (id >> 4) + (id & 15);
    const char* tab = (const char*)&TAB;
    const int R = (tid * 911) >> 16;
    const u32 xoff = (s00 + 16 * (R >> 3) + 256 * (R & 7)) * (2 * DM) + (tid - R * 72) * 16;
    asm volatile("" :: "s"(x), "s"(wr), "s"(wi), "s"(out), "s"(ws), "v"(xoff));
    const __amdgpu_buffer_rsrc_t rws = __builtin_amdgcn_make_buffer_rsrc(ws, 0, WS_USED, 0x00020000);
    STAMP(13);
    int gy = WS_Y1 + id * (NB2 * 128) + tid * 16;
    u32 epoch = 0;
    if (wv == 0) asm volatile("global_load_dword %0, %0, %1 sc1" : "+v"(epoch) : "s"(ws) : "memory");
    if (rows) {
        glds16(x, xoff, LB_X + wv * 1024);
        if (wv < 2) glds16(x, (s00 + 16 + 1536) * (2 * DM) + (tid < 56 ? (tid + 16) * 16 : 256 * 2 * DM + (tid - 56) * 16), LB_X + 16384 + wv * 1024);
        else glds16(tab, tid * 16 - 2048, LB_T + (wv - 2) * 1024);
        if (wv >= 9) glds16(tab, tid * 16 + 5 * 1024, LB_T + (wv + 5) * 1024);
        const u4 P = {POISON, POISON, POISON, POISON};
        __builtin_amdgcn_raw_buffer_store_b128(P, rws, gy, 0, 16);
        if (tid <= 128) __builtin_amdgcn_raw_buffer_store_b128(P, rws, gy, 16384, 16);
    }
    const int u2i = (id & 7) ? (id & 7) * 18 + 1 + (id >> 3) : (id == 136 ? 0 : (id >> 3) + (id < 136 ? 1 : 0));
    const bool star = u2i >= 108;
    const int q36 = min((u2i * 57) >> 11, 3), kg = u2i - 36 * q36, bq = (q36 + 1) & 3;
    const bool mir = !star && kg >= 18;
    const int w0 = star ? min(16 * kg, 562) : (mir ? (2048 - bq) * (2 * DF) + 1122 - 32 * kg : bq * (2 * DF) + 32 * kg);
    __builtin_amdgcn_s_barrier();
    STAMP(14);
    const u32 woff = w0 + ((tid >> 1) & 255) * (mir ? -16 * DF : 16 * DF) + (tid & 1) * (star ? 8 * DF : 16);
    u4 wra, wrb;
    if (wv >= 8) {
        __builtin_memcpy(&wra, (const char*)wr + woff, 16);
        __builtin_memcpy(&wrb, (const char*)wi + woff, 16);
    }
    __builtin_amdgcn_sched_barrier(0);
    const f4 Z = {0.f, 0.f, 0.f, 0.f};
    const int tau = wv & 3, bw = wv >> 2;
    int tl = LB_T + lane * 16;
    int lin = LB_S + (tid + (tid >> 5)) * 16;
    pin(tl); pin(lin);
    int sadr[4];

    if (rows) {
        if (tid < 256) {
            const int o = LB_E + 72 + (tid & 15) * (ER * 4) + (tid >> 4) * (ET * 4);
            lds_st<u2>(o, u2{0u, 0u}); lds_st<u4>(o + 8, u4{0u, 0u, 0u, 0u});
        }
        int xo = LB_X + (c16 >> 2) * 1152 + 2 * (144 * ((g - wv) & 3) + 16 * wv + 4 * (c16 & 3));
        int bo = LB_E + (wv * ET + c16 * ER + min(8 * g, 16)) * 4;
        int go = WS_X1 + (tid >> 3) * 16384 + id * 128 + (tid & 7) * 16;
        int ta = LB_T + lane * 8;
        pin(xo); pin(bo); pin(go); pin(ta);
        STAMP(1);
        asm volatile("s_cmp_lt_u32 %0, 8\n\ts_cbranch_scc0 .Lw%=\n\ts_waitcnt vmcnt(0)\n.Lw%=:\n\ts_waitcnt vmcnt(2)" :: "s"(wv) : "memory", "scc");
        if (wv == 0) publish_tag(epoch);
        bar();
        STAMP(2);
        if (wv < 9) {
            u4 d[2];
#pragma unroll
            for (int s = 0; s < 2; s++) {
                const s4 lo = __builtin_amdgcn_ds_read_tr16_b64_v4i16((LAS s4*)(u32)(xo + s * 9216));
                const s4 hi = __builtin_amdgcn_ds_read_tr16_b64_v4i16((LAS s4*)(u32)(xo + s * 9216 + 4608));
                d[s] = __builtin_bit_cast(u4, __builtin_shufflevector(lo, hi, 0, 1, 2, 3, 4, 5, 6, 7));
            }
            const u4 are = lds_ld<u4>(tl + F_ARE * 1024), aim = lds_ld<u4>(tl + F_AIM * 1024);
            const u2 eo = lds_ld<u2>(ta + F_EO * 1024 + wv * 512);
#pragma unroll
            for (int s = 0; s < 2; s++) d[s] = pack4(mfma(d[s], are, Z), mfma(d[s], aim, Z));
#pragma unroll
            for (int i = 0; i < 4; i++) lds_st<u2>(LB_E + ((i & 1) ? eo[i >> 1] >> 16 : eo[i >> 1] & 0xffff), u2{d[0][i], d[1][i]});
        }
        u4 bre = lds_ld<u4>(tl + F_BRE * 1024), bim = lds_ld<u4>(tl + F_BIM * 1024);
        u4 gre = lds_ld<u4>(tl + (F_G + 2 * tau) * 1024), gim = lds_ld<u4>(tl + (F_G + 2 * tau + 1) * 1024);
        {
            const u2 sa = lds_ld<u2>(ta + F_SA * 1024 + (tau + (bw ? 4 : 0)) * 512);
            const int base = LB_S + ((bw + 3) & 3) * (DM * 8 + 144);
#pragma unroll
            for (int i = 0; i < 4; i++) { sadr[i] = base + ((i & 1) ? sa[i >> 1] >> 16 : sa[i >> 1] & 0xffff); pin(sadr[i]); }
        }
        pin(bre); pin(bim); pin(gre); pin(gim);
        bar();
        {
            PRIOB(2);
            const u4 p0 = lds_ld<u4>(bo), p1 = lds_ld<u4>(bo + 16);
            u4 d[2];
            f4 re[2], im[2];
#pragma unroll
            for (int s = 0; s < 2; s++) { const u4 a = {p0[s], p0[2 + s], p1[s], p1[2 + s]}; re[s] = mfma(a, bre, Z); im[s] = mfma(a, bim, Z); }
            PRIOB(1);
#pragma unroll
            for (int s = 0; s < 2; s++) { const u4 t = pack4(re[s], im[s]); re[s] = mfma(gre, t, Z); im[s] = mfma(gim, t, Z); }
            PRIOB(0);
#pragma unroll
            for (int s = 0; s < 2; s++) d[s] = pack4(re[s], im[s]);
            if (c16 < 9) {
#pragma unroll
                for (int i = 0; i < 4; i++) lds_st<u2>(sadr[i], u2{d[0][i], d[1][i]});
                if (wv == 0 && c16 == 0 && !(g & 1)) lds_st<u2>(sadr[0] + 8, u2{d[0][0], d[1][0]});
            }
        }
        bar();
        STAMP(3);
        __builtin_amdgcn_raw_buffer_store_b128(lds_ld<u4>(lin), rws, go, 0, 16);
        if (tid <= 128) __builtin_amdgcn_raw_buffer_store_b128(lds_ld<u4>(lin + 1056 * 16), rws, go, 128 * 16384, 16);
    } else {
        if (wv < 8 && (kg == 0 || kg == (star ? 36 : 18))) {
            const int m = tid & 255;
            lds_st<u16>(LB_W + tid * 36 + 32, ((wv >> 2) ? wi : wr)[((mir ? bq + 8 * m : 2048 - bq - 8 * m) & 2047) * DF + (kg ? 288 : 0)]);
        }
        warm_code(entry);
        asm volatile("s_waitcnt vmcnt(0)" ::: "memory");
        if (wv == 0) publish_tag(epoch);
        bar();
    }
    const u32 tag = lds_ld<u32>(LB_G);
    if (wv >= 8) {
        const int o = LB_W + ((tid >> 1) & 255) * 36 + (tid & 1) * 16;
#pragma unroll
        for (int i = 0; i < 4; i++) { lds_st<u32>(o + 4 * i, wra[i]); lds_st<u32>(o + 9216 + 4 * i, wrb[i]); }
    }
    asm volatile("s_waitcnt vmcnt(0)" ::: "memory");
    if (rows && lane == 0) __builtin_amdgcn_raw_buffer_store_b32(tag, rws, WS_F1 + (wv * 16 + (id >> 3)) * 128 + (id & 7) * 4, 0, 16);
    STAMP(4);
    glds16(tab, F_F16 * 1024 + tid * 16, LB_T + F_F16 * 1024 + wv * 1024);
    if (star || wv < 8) glds16(tab, (F_VAR + 8 * q36) * 1024 + tid * 16, LB_T + F_VAR * 1024 + wv * 1024);
    asm volatile("s_waitcnt vmcnt(0)" ::: "memory");
    bar();
    STAMP(12);

    const int typ = star ? (wv & 1) : 0;
    const int tv = tl + (F_VAR + 8 * typ) * 1024;
    u4 fre = lds_ld<u4>(tl + F_F16 * 1024), fim = lds_ld<u4>(tl + (F_F16 + 1) * 1024);
    u4 m1r = lds_ld<u4>(tv), m1i = lds_ld<u4>(tv + 1024), m4r = lds_ld<u4>(tv + 2048), m4i = lds_ld<u4>(tv + 3072);
    f4 twr = lds_ld<f4>(tv + 4096), twi = lds_ld<f4>(tv + 5120), tur = lds_ld<f4>(tv + 6144), tui = lds_ld<f4>(tv + 7168);
    u4 mcr = lds_ld<u4>(tl + (F_M1C + 2 * typ) * 1024), mci = lds_ld<u4>(tl + (F_M1C + 2 * typ + 1) * 1024);
    f4 wfr, wfi;
    {
        const int kcol = star ? 8 * kg + (wv >> 1) : 16 * kg + wv;
        const int cc = star ? 8 * typ + (u2i < NB2 - 1 ? (wv >> 1) : 7) : (mir ? 15 - wv : wv);
        const int wo = LB_W + (c16 + 64 * g) * 36 + cc * 2;
#pragma unroll
        for (int i = 0; i < 4; i++) {
            wfr[i] = bf2f(lds_ld<u16>(wo + i * 576));
            wfi[i] = bf2f(lds_ld<u16>(wo + i * 576 + 9216));
        }
        float sc = 1.f / (float)(SEQ * DM);
        if (kcol == 0 || kcol == 288) {
            sc *= 0.5f;
#pragma unroll
            for (int i = 0; i < 4; i++) {
                const int m = c16 + 64 * g + 16 * i;
                const int o = LB_W + (typ ? (255 - m) * 36 + cc * 2 : m * 36 + 32);
                wfr[i] += bf2f(lds_ld<u16>(o));
                wfi[i] -= bf2f(lds_ld<u16>(o + 9216));
            }
        }
        wfr *= sc; wfi *= (mir ? -sc : sc);
    }
    f4 wnr = -wfr, wni = -wfi;
    int io = (32 * g + c16) * 128 + (((wv >> 1) ^ (c16 >> 1)) * 16);
    int so = (tid >> 3) * 128 + (((tid & 7) ^ ((tid >> 4) & 7)) * 16);
    pin(fre); pin(fim); pin(m1r); pin(m1i); pin(m4r); pin(m4i); pin(twr); pin(twi); pin(tur); pin(tui); pin(mcr); pin(mci);
    int gi = WS_X1 + u2i * 16384 + tid * 16, gq = WS_Y1 + (tid >> 3) * (NB2 * 128) + u2i * 128 + (tid & 7) * 16;
    int i1 = LB_I + io + (star ? 0 : (wv & 1) * 8);
    io += LB_O + (wv & 1) * 8;
    pin(wfr); pin(wnr); pin(wni); pin(io); pin(so); pin(gi); pin(gq); pin(i1);
    wait_flag(rws, WS_F1 + (((u2i >> 3) & 15) * 16 + wv) * 128 + lane * 4, tag, lane < 8);
    STAMP(5);
    lds_st<u4>(LB_I + so, __builtin_amdgcn_raw_buffer_load_b128(rws, gi, 0, 16));
    bar();
    STAMP(6);
    {
        const int o1 = i1;
        PRIOB(3);
        const u2 a0 = lds_ld<u2>(o1), a1 = lds_ld<u2>(o1 + 2048);
        const u4 a = {a0[0], a0[1], a1[0], a1[1]};
        f4 re = mfma(a, m1r, Z), im = mfma(a, m1i, Z);
        if (star) {
            const u2 b0 = lds_ld<u2>(o1 + 8), b1 = lds_ld<u2>(o1 + 8 + 2048);
            const u4 b = {b0[0], b0[1], b1[0], b1[1]};
            re = mfma(b, mcr, re); im = mfma(b, mci, im);
        }
        u4 t = twid(re, im, twr, twi);
        re = mfma(fre, t, Z); im = mfma(fim, t, Z);
        PRIOB(2);
        t = pack4(__builtin_elementwise_fma(re, wfr, im * wni), __builtin_elementwise_fma(re, wni, im * wnr));
        re = mfma(t, fre, Z); im = mfma(t, fim, Z);
        PRIOB(1);
        t = twid(re, im, tur, tui);
        re = mfma(m4r, t, Z); im = mfma(m4i, t, Z);
        PRIOB(0);
        t = pack4(re, im);
        lds_st<u2>(io, u2{t[0], t[1]}); lds_st<u2>(io + 2048, u2{t[2], t[3]});
    }
    bar();
    STAMP(7);
    {
        u4 v = lds_ld<u4>(LB_O + so);
        if (star) {
            u4 w;
#pragma unroll
            for (int s = 0; s < 2; s++) {
                const float ar = blo(v[s]), ai = bhi(v[s]), cr = blo(v[2 + s]), ci = bhi(v[2 + s]);
                w[s] = pk(ar + ci, ai - cr); w[2 + s] = pk(ar - ci, -ai - cr);
            }
            v = w;
        }
        if (u2i < NB2 - 1 || (tid & 7) == 0)
            __builtin_amdgcn_raw_buffer_store_b128(v, rws, gq, 0, 16);
    }
    STAMP(8);
    if (!rows) { STAMP_OUT(0); return; }

    u4 gre = lds_ld<u4>(tl + (F_GI + 2 * tau) * 1024), gim = lds_ld<u4>(tl + (F_GI + 2 * tau + 1) * 1024);
    u4 bre = lds_ld<u4>(tl + F_BRE * 1024), bim = lds_ld<u4>(tl + F_BIM * 1024);
    u4 ai0 = lds_ld<u4>(tl + F_AI * 1024), ai1 = lds_ld<u4>(tl + (F_AI + 1) * 1024);
    int eadr[4];
#pragma unroll
    for (int i = 0; i < 4; i++) {
        const int r = 4 * g + i;
        eadr[i] = LB_E2 + (mod9(4 * (c16 + 18 - r)) * E2V + r * E2U + 2 * wv) * 4;
        pin(eadr[i]);
    }
    int ao = LB_E2 + (wv * E2V + c16 * E2U + 8 * g) * 4;
    int yo = LB_Y + (c16 >> 2) * OP + 2 * (144 * (((c16 & 3) - wv) & 3) + 16 * wv + 4 * g);
    int yl = LB_Y + tid * 16 + R * (OP - 1152);
    u32 oo = 2 * xoff - (tid - R * 72) * 16 + 2 * DM;
    pin(gre); pin(gim); pin(bre); pin(bim); pin(ai0); pin(ai1); pin(ao); pin(gy); pin(yo); pin(yl); pin(oo);
    STAMP(9);
    {
        u4 v, w;
        if (wv < 3) { load_set<true>(rws, gy, true, v, tid <= 128, w); if (tid <= 128) lds_st<u4>(lin + 1056 * 16, w); }
        else load_set<false>(rws, gy, true, v, false, w);
        lds_st<u4>(lin, v);
    }
    bar();
    if (id == 0 && tid == 0) __builtin_amdgcn_raw_buffer_store_b32(tag, rws, WS_EPOCH, 0, 16);
    STAMP(10);
    {
        PRIOB(2);
        u4 d[2];
        u2 p[4];
#pragma unroll
        for (int i = 0; i < 4; i++) p[i] = lds_ld<u2>(sadr[i]);
        f4 re[2], im[2];
#pragma unroll
        for (int s = 0; s < 2; s++) { const u4 a = {p[0][s], p[1][s], p[2][s], p[3][s]}; re[s] = mfma(a, gre, Z); im[s] = mfma(a, gim, Z); }
        PRIOB(1);
#pragma unroll
        for (int s = 0; s < 2; s++) { const u4 t = pack4(re[s], im[s]); re[s] = mfma(t, bre, Z); im[s] = mfma(t, bim, Z); }
        PRIOB(0);
#pragma unroll
        for (int s = 0; s < 2; s++) d[s] = pack4(re[s], im[s]);
        if (c16 < 9) {
#pragma unroll
            for (int i = 0; i < 4; i++) lds_st<u2>(eadr[i], u2{d[0][i], d[1][i]});
        }
    }
    STAMP(15);
    bar();
    if (wv < 9) {
        const u4 p0 = lds_ld<u4>(ao), p1 = lds_ld<u4>(ao + 16);
#pragma unroll
        for (int s = 0; s < 2; s++) {
            const u4 a = {p0[s], p0[2 + s], p1[s], p1[2 + s]};
            const f4 y0 = mfma(a, ai0, Z), y1 = mfma(a, ai1, Z);
            lds_st<u2>(yo + s * (8 * OP), u2{pk(y0[0], y0[1]), pk(y0[2], y0[3])});
            lds_st<u2>(yo + s * (8 * OP) + 4 * OP, u2{pk(y1[0], y1[1]), pk(y1[2], y1[3])});
        }
    }
    bar();
    *(GAS u4*)((char*)out + oo) = lds_ld<u4>(yl);
    if (wv < 2) {
        const int r2 = tid < 56 ? 14 : 15, c2 = tid + 1024 - 72 * r2;
        *(GAS u4*)((char*)out + (u32)((s00 + 16 + 256 * (r2 - 8)) * (4 * DM) + 2 * DM + c2 * 16)) = lds_ld<u4>(LB_Y + r2 * OP + c2 * 16);
    }
    STAMP(11);
    STAMP_OUT(0);
}

DI f2 cexpa(float ar2, float air, float p) {
    const float e = __builtin_amdgcn_exp2f(ar2 * p), t = __builtin_amdgcn_fractf(air * p);
    return f2{e * __builtin_amdgcn_cosf(t), e * __builtin_amdgcn_sinf(t)};
}
DI u4 ld_k(int o) {
    const u2 a = *(const volatile LAS u2*)(u32)o, b = *(const volatile LAS u2*)(u32)(o + 8);
    return u4{a[0], a[1], b[0], b[1]};
}
DI void a_rows_out(int yb, int ystr, char* o) {
    u2 y[6];
#pragma unroll
    for (int c = 0; c < 6; c++) y[c] = lds_ld<u2>(yb + c * ystr);
#pragma unroll
    for (int r = 0; r < 4; r++) {
        u3 v;
#pragma unroll
        for (int c = 0; c < 3; c++) v[c] = __builtin_amdgcn_perm(y[2 * c + 1][r >> 1], y[2 * c][r >> 1], (r & 1) ? 0x07060302u : 0x05040100u);
        *(u3*)(o + r * (4 * DM)) = v;
    }
}
template <int R> DI void stage(u4 v) {
    asm volatile("ds_write_addtid_b32 %0 offset:%4\n\tds_write_addtid_b32 %1 offset:%5\n\tds_write_addtid_b32 %2 offset:%6\n\tds_write_addtid_b32 %3 offset:%7"
                 :: "v"(v[0]), "v"(v[1]), "v"(v[2]), "v"(v[3]), "i"(288 * R), "i"(288 * R + 288), "i"(288 * R + 576), "i"(288 * R + 864) : "memory");
}
#define CM_MUL(d, a, b) "v_pk_mul_f32 " d ", " a ", " b " op_sel_hi:[0,1]\n\t"
#define CM_FMA(d, a, b) "v_pk_fma_f32 " d ", " a ", " b ", " d " op_sel:[1,1,0] op_sel_hi:[1,0,1] neg_lo:[1,0,0]\n\t"
struct C4 { f2 a, b, c, d; };
DI C4 cmul4(f2 a0, f2 b0, f2 a1, f2 b1, f2 a2, f2 b2, f2 a3, f2 b3) {
    C4 r;
    asm("s_nop 0\n\t" CM_MUL("%0", "%4", "%5") CM_MUL("%1", "%6", "%7") CM_MUL("%2", "%8", "%9") CM_MUL("%3", "%10", "%11")
        CM_FMA("%0", "%4", "%5") CM_FMA("%1", "%6", "%7") CM_FMA("%2", "%8", "%9") CM_FMA("%3", "%10", "%11") "s_nop 0"
        : "=&v"(r.a), "=&v"(r.b), "=&v"(r.c), "=&v"(r.d) : "v"(a0), "v"(b0), "v"(a1), "v"(b1), "v"(a2), "v"(b2), "v"(a3), "v"(b3));
    return r;
}
DI C4 cmul4(const C4& a, f2 b) { return cmul4(a.a, b, a.b, b, a.c, b, a.d, b); }
DI u4 pk4(const C4& v) { return u4{pk(v.a.x, v.a.y), pk(v.b.x, v.b.y), pk(v.c.x, v.c.y), pk(v.d.x, v.d.y)}; }

DI void sig(int f) {
    asm volatile("s_waitcnt lgkmcnt(0)" ::: "memory");
    if ((threadIdx.x & 63) == 0) __hip_atomic_fetch_add((LAS u32*)(u32)(LA_F + 4 * f), 1u, __ATOMIC_RELAXED, __HIP_MEMORY_SCOPE_WORKGROUP);
}
template <int SLEEP = 0> DI void await(int f, u32 n) {
    while ((u32)__builtin_amdgcn_readfirstlane(*(const volatile LAS u32*)(u32)(LA_F + 4 * f)) < n) if (SLEEP) __builtin_amdgcn_s_sleep(SLEEP);
    asm volatile("" ::: "memory");
}
DI float exp_bf(float x) {
    const float t = x * 1.44269504f, r = __builtin_fmaf(x, 1.92596299e-8f, __builtin_fmaf(x, 1.44269504f, -t));
    const float e = __builtin_amdgcn_exp2f(t);
    return __builtin_fmaf(e * 0.693147181f, r, e);
}

struct APar { u32 l, a, r, i, t, d; };
DI void head_a(const u16* __restrict__ u, APar pr, u16* __restrict__ out, char* ws) {
    const int tid = threadIdx.x, lane = tid & 63, c16 = lane & 15, g = lane >> 4;
    const int wv = __builtin_amdgcn_readfirstlane(tid >> 6);
    STAMP_DECL;
    const int ai_ = blockIdx.x - A0;
    const int d0 = 6 * ((ai_ & 7) * 12 + (ai_ >> 3));
    const f4 Z = {0.f, 0.f, 0.f, 0.f};

    if (wv >= 12) {
        __builtin_amdgcn_s_setprio(3);
        const int t = tid - 768;
        if (t == 0) lds_st<u2>(LA_F, u2{0u, 0u});
        if (t < 180) lds_st<u4>(LA_U + (t / 30) * 4608 + (t % 30) * 16, u4{0u, 0u, 0u, 0u});
        bar();
        u3 ua[4], ub[4];
#pragma unroll
        for (int r = 0; r < 4; r++) ua[r] = *(const u3*)(u + (4 * t + r) * DM + d0);
        glds16((const char*)&TAB + F_END * 1024, t * 16, LA_S + (wv - 12) * 1024);
        if (wv < 14) glds16((const char*)&TAB + (F_END + 4) * 1024, t * 16, LA_S + (wv - 8) * 1024);
        sig(FU0);
        await<1>(FU0, 4);
#pragma unroll
        for (int r = 0; r < 4; r++) ub[r] = *(const u3*)(u + (1024 + 4 * t + r) * DM + d0);
        asm volatile("" ::: "memory");
#pragma unroll
        for (int c = 0; c < 6; c++) {
            const u32 sel = (c & 1) ? 0x07060302u : 0x05040100u;
            lds_st<u2>(LA_U + c * 4608 + 480 + t * 8, u2{__builtin_amdgcn_perm(ua[1][c >> 1], ua[0][c >> 1], sel), __builtin_amdgcn_perm(ua[3][c >> 1], ua[2][c >> 1], sel)});
        }
        STAMP(7);
        asm volatile("s_waitcnt vmcnt(4)" ::: "memory");
        bar();
        bar();
#pragma unroll
        for (int c = 0; c < 6; c++) {
            const u32 sel = (c & 1) ? 0x07060302u : 0x05040100u;
            lds_st<u2>(LA_U + c * 4608 + 480 + 2048 + t * 8, u2{__builtin_amdgcn_perm(ub[1][c >> 1], ub[0][c >> 1], sel), __builtin_amdgcn_perm(ub[3][c >> 1], ub[2][c >> 1], sel)});
        }
        sig(FU2);
        STAMP(8);
        const int tile = wv - 12, yl = ((lane >> 2) + 16 * (lane & 3)) * 8;
        char* o = (char*)(out + 4 * t * (2 * DM) + d0);
        bar();
        bar();
        STAMP(11);
        a_rows_out(LA_G + ((tile >> 1) ^ 1) * 4608 + (tile & 1) * 1024 + yl, 9216, o);
        STAMP(9);
        bar();
        bar();
        STAMP(12);
        a_rows_out(LA_G + ((tile >> 1) ^ 1) * 4608 + (tile & 1) * 1024 + 2048 + yl, 9216, o + 1024 * (4 * DM));
        STAMP(10);
        STAMP_OUT(768);
        return;
    }
    const int ch = wv >> 1, hf = wv & 1;
    {
        __builtin_amdgcn_s_barrier();
        asm volatile("s_waitcnt vmcnt(0)" : "+v"(pr.l), "+v"(pr.a), "+v"(pr.r), "+v"(pr.i), "+v"(pr.t), "+v"(pr.d) :: "memory");
        PRIO(3);
        const float dt = rbf(exp_bf(blo(pr.t)));
        const float ar2 = -rbf(exp_bf(blo(pr.l))) * dt * 1.44269504f, air = blo(pr.a) * dt * 0.159154943f;
        const f2 C = {2.f * dt * blo(pr.r), 2.f * dt * blo(pr.i)};
        STAMP(1);
        const f2 z1 = cexpa(ar2, air, 1.f), z8c = cexpa(ar2, -air, 8.f), z128 = cexpa(ar2, air, 128.f);
        const int gr = LA_G + wv * 4608 + c16 * 288 + g * 16, q = LA_Q + wv * 2576;
        asm volatile("s_mov_b32 m0, %0\n\ts_nop 0" :: "s"(LA_G + wv * 4608) : "memory");
#define SB __builtin_amdgcn_sched_barrier(0)
#define STAGE16(v) do { stage<0>(v[0]); stage<4>(v[1]); stage<8>(v[2]); stage<12>(v[3]); } while (0)
#define READ4(f) do { _Pragma("unroll") for (int s = 0; s < 4; s++) f[s] = lds_ld<u4>(gr + 64 * s); SB; } while (0)
        u4 v[4], bfr[4], afr[4];
        const C4 s1 = cmul4(z8c, z8c, z1, z1, z128, z128, C, z1);
        const C4 s2 = cmul4(s1.a, s1.a, z8c, s1.a, s1.c, s1.c, C, s1.b);
        {
            C4 w = {f2{1.f, 0.f}, z8c, s1.a, s2.b};
            v[0] = pk4(w);
#pragma unroll
            for (int i = 1; i < 4; i++) { w = cmul4(w, s2.a); v[i] = pk4(w); }
        }
        STAGE16(v); READ4(bfr);
        PRIO(2);
        const C4 s3 = cmul4(s1.d, s1.b, s1.b, s1.b, s2.c, s2.c, s2.c, z128);
        const C4 e0 = {C, s1.d, s2.d, s3.a}, e1 = cmul4(e0, s3.b);
        v[0] = pk4(e0); v[1] = pk4(e1); v[2] = pk4(cmul4(e0, z128)); v[3] = pk4(cmul4(e1, z128));
        STAGE16(v); READ4(afr);
        PRIO(1);
        {
            const C4 f = cmul4(C4{e0.a, e0.c, e1.a, e1.c}, s1.c), h = cmul4(e0.a, s2.c, e1.a, s2.c, e0.a, s3.d, e1.a, s3.d);
            v[0] = pk4(f); v[1] = pk4(cmul4(f, z128)); v[2] = pk4(h); v[3] = pk4(cmul4(h, s1.c));
        }
        SB;
        f4 acc = Z;
#pragma unroll
        for (int s = 0; s < 4; s++) acc = mfma(afr[s], bfr[s], acc);
        STAGE16(v); READ4(afr);
        PRIO(0);
        lds_st<f4>(q + (4 * (g & 1) + 128 * (g >> 1) + 8 * c16) * 4, acc);
        {
            const f2 f0 = cmul(C, s3.c), f1 = cmul(f0, z128);
            const C4 f = cmul4(f0, s1.c, f1, s1.c, f0, s3.c, f0, s3.c), h = cmul4(C4{f0, f1, f.a, f.b}, s2.c);
            v[0] = pk4(C4{f0, f1, f.a, f.b}); v[1] = pk4(h); v[2][0] = pk(f.c.x, f.c.y);
        }
        SB;
        acc = Z;
#pragma unroll
        for (int s = 0; s < 4; s++) acc = mfma(afr[s], bfr[s], acc);
        stage<0>(v[0]); stage<4>(v[1]);
        asm volatile("ds_write_addtid_b32 %0 offset:%1" :: "v"(v[2][0]), "i"(288 * 8) : "memory");
        READ4(afr);
        if (g < 2) lds_st<f4>(q + (256 + 64 * g + 4 * c16) * 4, acc);
        else {
            const int o = q + (384 + 64 * (g - 2) + 2 * c16) * 4;
            lds_st<f2>(o, f2{acc[0], acc[1]}); lds_st<f2>(o + 128, f2{acc[2], acc[3]});
        }
        acc = Z;
#pragma unroll
        for (int s = 0; s < 4; s++) acc = mfma(afr[s], bfr[s], acc);
        if (g < 2) {
#pragma unroll
            for (int i = 0; i < 4; i++) lds_st<float>(q + (512 + 64 * g + 16 * i + c16) * 4, acc[i]);
        } else if (lane < 34) lds_st<float>(q + (lane == 32 ? 640 : 643) * 4, lane == 32 ? acc[0] : 0.f);
    }
    STAMP(2);
    asm volatile("s_waitcnt lgkmcnt(0)" ::: "memory");
    bar();
    STAMP(9);

    const int k0 = LA_K + ch * KC;
    {
        const LAS float* qa = (const LAS float*)(u32)(LA_Q + ch * 5152);
        const LAS float* qb = qa + 644;
        u2 w[4];
        int y;
        if (hf == 0) {
            const u32 ix = lds_ld<u32>(LA_S + 1024 + lane * 16);
            f4 k = *(const LAS f4*)(qa + 4 * lane) + *(const LAS f4*)(qb + 4 * lane);
            f4 p = *(const LAS f4*)(qa + 4 * lane - 4) + *(const LAS f4*)(qb + 4 * lane - 4);
            const f2 s = *(const LAS f2*)(qa + 512 - 2 * lane) + *(const LAS f2*)(qb + 512 - 2 * lane);
            const float e0 = qa[(ix & 0xffff) >> 2] + qb[(ix & 0xffff) >> 2], e1 = qa[ix >> 18] + qb[ix >> 18];
            lds_st<u32>(k0 + KQ + (32 + lane) * 4, pk(s.x, lane ? s.y : 0.f));
            if (lane == 0) lds_st<u32>(k0 + KX, pk(s.x, s.y));
            lds_st<u32>(k0 + KQ + (lane + (lane & 32) * 2) * 4, 0u);
            lds_st<u32>(k0 + g * KI + c16 * 4, pk(e0, e1));
            if (lane == 0) { k[0] += blo(pr.d); p = Z; }
            const u32 c = pk(k[1], k[0]), d = pk(k[0], p[3]);
            w[0] = u2{pk(k[3], k[2]), c}; w[1] = u2{pk(k[2], k[1]), d}; w[2] = u2{c, pk(p[3], p[2])}; w[3] = u2{d, pk(p[2], p[1])};
            y = k0 + (270 - 2 * lane) * 4;
        } else {
            const int j = 256 + 2 * lane;
            const float q0 = qa[j] + qb[j], q1 = qa[j + 1] + qb[j + 1], q2 = qa[j + 2] + qb[j + 2];
            const float p1 = qa[j - 1] + qb[j - 1], p2 = qa[j - 2] + qb[j - 2], p3 = qa[j - 3] + qb[j - 3];
            const u32 c = pk(q0, q0), d = pk(q0, p1), e = pk(p1, p2), f = lane ? c : d;
            w[0] = u2{pk(q2, q1), c}; w[1] = u2{pk(q1, q0), f}; w[2] = u2{c, lane ? d : e}; w[3] = u2{f, lane ? e : pk(p2, p3)};
            y = k0 + (142 - 2 * lane) * 4;
            if (lane < 48) lds_st<u32>(k0 + (lane < 32 ? (lane >> 3) * KI + (272 + (lane & 7)) * 4 : KP + (32 + lane) * 4), 0u);
            const f2 s = *(const LAS f2*)(qa + 640 - 2 * lane) + *(const LAS f2*)(qb + 640 - 2 * lane);
            lds_st<u32>(k0 + KP + lane * 4, pk(s.x, lane ? s.y : 0.f));
        }
#pragma unroll
        for (int i = 0; i < 4; i++) lds_st<u2>(y + i * KI, w[i]);
    }
    const int x0 = 2031 - c16 + 8 * g;
    int ka = k0 + (x0 & 3) * KI + (x0 >> 2) * 8 - 3008 - 256 * hf - 192 - 512;
    int ubs = LA_U + ch * 4608 + 480 + (16 * c16 + 8 * g - 16) * 2 - 256 * hf - 192;
    pin(ka); pin(ubs);
    const int lt = LA_S + lane * 16;
    const u4 bnd5 = lds_ld<u4>(lt + 4096), bnd3 = lds_ld<u4>(lt + 5120);
    int uv = LA_U + ch * 4608 + 480 + (8 * c16 + 8 * g - 24) * 2 + 256 * hf;
    pin(uv);
#define VSUM(n) ({ const u4 a_ = lds_ld<u4>(uv + 512 * (n)); pack4(mfma(a_, bnd5, Z), mfma(a_, bnd3, Z)); })
    u4 vs[4], ws0, ws1, ws2;
    {
        const u4 bnd9 = lds_ld<u4>(lt + 2048), bnd7 = lds_ld<u4>(lt + 3072);
        const int uw = LA_U + ch * 4608 + 480 + (16 * c16 + 8 * g - 28) * 2;
#define WSUM(I) ({ const u4 a_ = ld_k(uw + 512 * (I)); pack4(mfma(a_, bnd9, Z), mfma(a_, bnd7, Z)); })
#pragma unroll
        for (int n = 0; n < 4; n++) vs[n] = VSUM(n);
        ws0 = WSUM(hf);
        if (hf) { ws1 = WSUM(2); ws2 = WSUM(3); }
    }
    STAMP(13);
    bar();
    STAMP(3);
#define KFR(rho, p) ld_k(ka + 192 - 64 * (rho) + 512 - 512 * (p))
#define UFR(rho, q) lds_ld<u4>(ubs + 192 - 64 * (rho) + 512 * (q))
    f4 acc[4];
#pragma unroll
    for (int i = 0; i < 4; i++) acc[i] = Z;
    u4 gv[3];
    {
        u4 A[2][2], B[2][4];
        PRIO(3);
#pragma unroll
        for (int p = 0; p < 4; p++) B[0][p] = UFR(0, p);
        A[0][0] = KFR(0, 0); A[0][1] = KFR(0, 1);
#pragma unroll
        for (int k = 0; k < 3; k++) gv[k] = ld_k(k0 + KQ + (94 + 16 * hf - 32 * k - 2 * c16 + 4 * g) * 4);
        if (!hf) { const u4 t = ld_k(ka - 320); acc[2] = mfma(t, B[0][0], acc[2]); acc[3] = mfma(t, B[0][1], acc[3]); }
        acc[2] = mfma(vs[0], gv[0], acc[2]); acc[3] = mfma(vs[0], gv[1], acc[3]); acc[3] = mfma(vs[1], gv[0], acc[3]);
#pragma unroll
        for (int rho = 0; rho < 4; rho++)
#pragma unroll
            for (int p = 0; p < 2; p++) {
                SB;
                if (p == 0) { if (rho == 1) PRIO(2); else if (rho == 2) PRIO(1); else if (rho == 3) PRIO(0); }
                if (rho < 3) { B[(rho + 1) & 1][2 * p] = UFR(rho + 1, 2 * p); B[(rho + 1) & 1][2 * p + 1] = UFR(rho + 1, 2 * p + 1); A[(rho + 1) & 1][p] = KFR(rho + 1, p); }
#pragma unroll
                for (int q = 0; p + q < 4; q++) acc[p + q] = mfma(A[rho & 1][p], B[rho & 1][q], acc[p + q]);
            }
        SB;
    }
#pragma unroll
    for (int i = 0; i < 4; i++) pin(acc[i]);
    STAMP(4);
#define EXCHANGE(off)                                                                         \
    {                                                                                         \
        const int mine = LA_G + wv * 4608 + (off), other = LA_G + (wv ^ 1) * 4608 + (off);     \
        if (hf) { lds_st<f4>(mine + lane * 16, acc[0]); lds_st<f4>(mine + 1024 + lane * 16, acc[1]); acc[0] = acc[2]; acc[1] = acc[3]; } \
        else { lds_st<f4>(mine + lane * 16, acc[2]); lds_st<f4>(mine + 1024 + lane * 16, acc[3]); } \
        bar();                                                                                \
        const f4 y0 = lds_ld<f4>(other + lane * 16) + acc[0], y1 = lds_ld<f4>(other + 1024 + lane * 16) + acc[1]; \
        lds_st<u2>(other + lane * 8, u2{pk(y0[0], y0[1]), pk(y0[2], y0[3])});                 \
        lds_st<u2>(other + 1024 + lane * 8, u2{pk(y1[0], y1[1]), pk(y1[2], y1[3])});          \
    }
    EXCHANGE(0);
    {
        const int po = k0 + KP + (15 - c16 + 4 * g) * 4;
#define PFR(d) u4{lds_ld<u32>(po + 64 * (7 - (d))), lds_ld<u32>(po + 64 * (7 - (d)) + 4), lds_ld<u32>(po + 64 * (7 - (d)) + 8), lds_ld<u32>(po + 64 * (7 - (d)) + 12)}
        const u4 g4 = PFR(4), g5 = PFR(5), g6 = PFR(6);
        bar();
        STAMP(14);
        PRIO(3);
        acc[0] = mfma(vs[0], gv[2], Z); acc[1] = mfma(vs[1], gv[2], Z); acc[2] = mfma(vs[2], gv[2], Z); acc[3] = mfma(vs[3], gv[2], Z);
        acc[0] = mfma(vs[1], gv[1], acc[0]); acc[1] = mfma(vs[2], gv[1], acc[1]); acc[2] = mfma(vs[3], gv[1], acc[2]);
        acc[0] = mfma(vs[2], gv[0], acc[0]); acc[1] = mfma(vs[3], gv[0], acc[1]);
        if (hf) {
            acc[3] = mfma(ws0, g6, acc[3]); acc[2] = mfma(ws0, g5, acc[2]); acc[1] = mfma(ws0, g4, acc[1]);
            acc[3] = mfma(ws1, g5, acc[3]); acc[2] = mfma(ws1, g4, acc[2]); acc[3] = mfma(ws2, g4, acc[3]);
        } else {
            const u4 g7 = PFR(7), sel = lds_ld<u4>(lt);
            const u32 r = lds_ld<u32>(k0 + KX);
            acc[3] = mfma(ws0, g7, acc[3]); acc[2] = mfma(ws0, g6, acc[2]); acc[0] = mfma(ws0, g4, acc[0]); acc[1] = mfma(ws0, g5, acc[1]);
            const u4 t = {__builtin_amdgcn_perm(r, r, sel[0]), __builtin_amdgcn_perm(r, r, sel[1]), __builtin_amdgcn_perm(r, r, sel[2]), __builtin_amdgcn_perm(r, r, sel[3])};
#pragma unroll
            for (int q = 0; q < 4; q++) acc[q] = mfma(t, UFR(0, q), acc[q]);
        }
#pragma unroll
        for (int i = 0; i < 4; i++) pin(acc[i]);
        await<1>(FU2, 4);
        u4 A[2][2], B[2][5];
#pragma unroll
        for (int p = 0; p < 5; p++) B[0][p] = UFR(0, 3 + p);
        A[0][0] = KFR(0, 0); A[0][1] = KFR(0, 1);
        vs[0] = VSUM(4); vs[1] = VSUM(5);
        if (!hf) {
            const u4 t = ld_k(ka - 320);
            acc[0] = mfma(t, UFR(0, 2), acc[0]);
#pragma unroll
            for (int q = 1; q < 4; q++) acc[q] = mfma(t, B[0][q - 1], acc[q]);
        }
        acc[2] = mfma(vs[0], gv[0], acc[2]); acc[3] = mfma(vs[0], gv[1], acc[3]); acc[3] = mfma(vs[1], gv[0], acc[3]);
#pragma unroll
        for (int rho = 0; rho < 4; rho++)
#pragma unroll
            for (int p = 0; p < 2; p++) {
                SB;
                if (p == 0) { if (rho == 1) PRIO(2); else if (rho == 2) PRIO(1); else if (rho == 3) PRIO(0); }
                if (rho < 3) {
#pragma unroll
                    for (int q = 3 * p; q < 3 + 2 * p; q++) B[(rho + 1) & 1][q] = UFR(rho + 1, 3 + q);
                    A[(rho + 1) & 1][p] = KFR(rho + 1, p);
                }
#pragma unroll
                for (int j = 0; j < 4; j++) acc[j] = mfma(A[rho & 1][p], B[rho & 1][j + 1 - p], acc[j]);
            }
        SB;
    }
#pragma unroll
    for (int i = 0; i < 4; i++) pin(acc[i]);
    STAMP(5);
    EXCHANGE(2048);
    bar();
    STAMP(6);
    STAMP_OUT(0);
}

__global__ void __launch_bounds__(NT) __attribute__((aligned(4096))) fused_kernel(const u16* __restrict__ u, const u16* __restrict__ lar, const u16* __restrict__ aimg,
                                                   const u16* __restrict__ cre, const u16* __restrict__ cim, const u16* __restrict__ ldt,
                                                   const u16* __restrict__ dsk, const u16* __restrict__ x, const u16* __restrict__ wr,
                                                   const u16* __restrict__ wi, u16* __restrict__ out, char* ws) {
    const char* entry;
    asm volatile("s_getpc_b64 %0" : "=s"(entry));
    APar pr;
    if (blockIdx.x >= A0 && __builtin_amdgcn_readfirstlane(threadIdx.x) < 768) {
        const int a = blockIdx.x - A0, d = 6 * ((a & 7) * 12 + (a >> 3)) + __builtin_amdgcn_readfirstlane(threadIdx.x >> 7);
        asm volatile("global_load_ushort %0, %6, %8\n\tglobal_load_ushort %1, %6, %9\n\tglobal_load_ushort %2, %6, %10\n\t"
                     "global_load_ushort %3, %6, %11\n\tglobal_load_ushort %4, %7, %12\n\tglobal_load_ushort %5, %7, %13"
                     : "=&v"(pr.l), "=&v"(pr.a), "=&v"(pr.r), "=&v"(pr.i), "=&v"(pr.t), "=&v"(pr.d)
                     : "v"((d * NS + (threadIdx.x & 127)) * 2), "v"(d * 2), "s"(lar), "s"(aimg), "s"(cre), "s"(cim), "s"(ldt), "s"(dsk) : "memory");
    }
    if (__builtin_expect(blockIdx.x < NB2, 1)) head_b(entry, x, wr, wi, out, ws);
    else if (blockIdx.x >= A0) head_a(u, pr, out, ws);
    else warm_code(entry);
}

extern "C" void kernel_launch(void* const* d_in, const int* in_sizes, int n_in, void* d_out, int out_size, void* d_ws,
                              size_t ws_size, hipStream_t stream) {
    (void)hipFuncSetAttribute((const void*)fused_kernel, hipFuncAttributeMaxDynamicSharedMemorySize, LDS_BYTES);
    fused_kernel<<<GRID, NT, LDS_BYTES, stream>>>((const u16*)d_in[0], (const u16*)d_in[1], (const u16*)d_in[2], (const u16*)d_in[3],
                                                  (const u16*)d_in[4], (const u16*)d_in[5], (const u16*)d_in[6], (const u16*)d_in[7],
                                                  (const u16*)d_in[8], (const u16*)d_in[9], (u16*)d_out, (char*)d_ws);
}
```

```cpp
#define NO_UPF
#define POLL_SLEEP 6
#include <hip/hip_runtime.h>

typedef unsigned short u16;
typedef unsigned int u32;
typedef float f2 __attribute__((ext_vector_type(2)));
typedef float f4 __attribute__((ext_vector_type(4)));
typedef u32 u2 __attribute__((ext_vector_type(2)));
typedef u32 u4 __attribute__((ext_vector_type(4)));
typedef int i8v __attribute__((ext_vector_type(8)));
typedef short s4 __attribute__((ext_vector_type(4)));
typedef short s8 __attribute__((ext_vector_type(8)));
typedef __bf16 bf2 __attribute__((ext_vector_type(2)));
#define DI __device__ __forceinline__
#define LAS __attribute__((address_space(3)))
#define GAS __attribute__((address_space(1)))
#define CAS __attribute__((address_space(4)))
#pragma clang diagnostic ignored "-Wint-to-pointer-cast"
struct u3 { u32 v[3]; DI u32& operator[](int i) { return v[i]; } DI const u32& operator[](int i) const { return v[i]; } };

constexpr int SEQ = 2048, DM = 576, NS = 128, DF = 289;
constexpr int NB1 = 128;
constexpr int NB2 = 145;
constexpr int A0 = 152;
constexpr int NA = 96;
constexpr int GRID = A0 + NA;
constexpr int NT = 1024;

constexpr u32 WS_EPOCH = 0, WS_F1 = 4096, WS_F2 = WS_F1 + 16 * 16 * 128, WS_X1 = 1u << 20, WS_Y1 = 4u << 20, WS_USED = 8u << 20;
#define STAMP(i) do { if ((i) == PK && HEADSEL) { __builtin_amdgcn_sched_barrier(0); tk_ = __builtin_amdgcn_s_memrealtime(); __builtin_amdgcn_sched_barrier(0); } } while (0)
#define STAMP_DECL const unsigned long long t0_ = __builtin_amdgcn_s_memrealtime(); unsigned long long tk_ = t0_
#define STAMP_OUT(T) do { if (HEADSEL && wv == PW) while (__builtin_amdgcn_s_memrealtime() - t0_ < 3000ull + 20ull * (tk_ - t0_)) __builtin_amdgcn_s_sleep(2); } while (0)
#define PK 2
#define PW 0
#define HEADSEL (blockIdx.x < NB2)

constexpr int F_SA = 0, F_EO = 4;
constexpr int F_ARE = 9, F_AIM = 10, F_BRE = 11, F_BIM = 12, F_G = 13;
constexpr int F_F16 = 21, F_GI = 23, F_AI = 31, F_M1C = 33;
constexpr int F_VAR = 37;
constexpr int F_END = F_VAR + 40;
constexpr int ET = 452, ER = 28, E2V = 580, E2U = 36, OP = 1168;
constexpr int LB_X = 0, LB_I = 0, LB_Y = 0;
constexpr int LB_E = 16 * OP;
constexpr int LB_O = LB_E, LB_E2 = LB_E;
constexpr int LB_S = LB_E + 16 * ET * 4;
constexpr int LB_W = LB_S + 1200 * 16;
constexpr int LB_T = LB_W + 2 * 256 * 36;
constexpr int LB_G = LB_T + (F_VAR + 16) * 1024;
constexpr int LB_END = LB_G + 16;
struct CosTab { double c[2048]; double c9[9], s9[9]; };
constexpr double tay_cos(double x) { double t = 1, s = 1; for (int k = 1; k <= 14; k++) { t *= -x * x / ((2 * k - 1) * (2 * k)); s += t; } return s; }
constexpr double tay_sin(double x) { double t = x, s = x; for (int k = 1; k <= 14; k++) { t *= -x * x / ((2 * k + 1) * (2 * k)); s += t; } return s; }
constexpr CosTab make_cos() {
    CosTab t = {};
    const double PI2 = 6.283185307179586476925;
    for (int k = 0; k <= 512; k++) {
        const double v = k == 512 ? 0.0 : (k <= 256 ? tay_cos(PI2 * k / 2048) : tay_sin(PI2 * (512 - k) / 2048));
        t.c[k] = v; t.c[(2048 - k) & 2047] = v; t.c[1024 - k] = -v; t.c[(1024 + k) & 2047] = -v;
    }
    t.c[512] = 0.0; t.c[1536] = 0.0;
    for (int k = 0; k < 9; k++) { const double a = PI2 * (k <= 4 ? k : k - 9) / 9; t.c9[k] = tay_cos(a); t.s9[k] = tay_sin(a); }
    return t;
}
constexpr CosTab CT = make_cos();
constexpr double cs(int n) { return CT.c[n & 2047]; }
constexpr double sn(int n) { return CT.c[(n - 512) & 2047]; }
constexpr u32 bfr(double x) { const u32 b = __builtin_bit_cast(u32, (float)x); return (b + 0x7fffu + ((b >> 16) & 1u)) >> 16; }
constexpr u32 pkc(double lo, double hi) { return bfr(lo) | (bfr(hi) << 16); }
constexpr u32 f32c(double x) { return __builtin_bit_cast(u32, (float)x); }
template <int N> struct Frags { u32 w[N * 256]; };
constexpr u32 w_re(double amp, int n) { return pkc(amp * cs(n), amp * sn(n)); }
constexpr u32 w_im(double amp, int n) { return pkc(-amp * sn(n), amp * cs(n)); }
constexpr Frags<F_VAR> make_t0() {
    Frags<F_VAR> t = {};
    const double R2 = 1.4142135623730951;
    for (int l = 0; l < 64; l++)
        for (int i = 0; i < 4; i++) {
            const int c = l & 15, g = l >> 4, r = 4 * g + i, o = l * 4 + i;
            {
                const int b = c >> 2, tq = c & 3;
                const double amp = b ? 1.0 : R2;
                const int n0 = 512 * tq * g + (b ? 256 * b * (2 * i) : -256), n1 = 512 * tq * g + (b ? 256 * b * (2 * i + 1) : 256);
                t.w[F_ARE * 256 + o] = pkc(amp * cs(n0), amp * cs(n1));
                t.w[F_AIM * 256 + o] = pkc(-amp * sn(n0), -amp * sn(n1));
            }
            for (int w = 0; w < 9; w++) {
                const int sh = 16 * (i & 1), oo = w * 128 + l * 2 + (i >> 1);
                t.w[F_EO * 256 + oo] |= (u32)((c * ET + r * ER + 2 * ((7 * w + r) % 9)) * 4) << sh;
                if (w < 8) {
                    const int k = (9 * (w & 3) + 36 * r + 64 * (c % 9)) % DM, slot = w < 4 ? (k <= 288 ? 2 * k : 2 * (DM - k) + 1) : k;
                    t.w[F_SA * 256 + oo] |= (u32)(slot * 8 + (slot >> 6) * 16) << sh;
                }
            }
            if (r < 9) { const int k = (r * c) % 9; t.w[F_BRE * 256 + o] = pkc(CT.c9[k], CT.s9[k]); t.w[F_BIM * 256 + o] = pkc(-CT.s9[k], CT.c9[k]); }
            for (int q = 0; q < 4; q++) {
                t.w[(F_G + 2 * q) * 256 + o] = w_re(1.0, 32 * r * (q + 4 * c)); t.w[(F_G + 2 * q + 1) * 256 + o] = w_im(1.0, 32 * r * (q + 4 * c));
                t.w[(F_GI + 2 * q) * 256 + o] = w_re(1.0, 32 * c * (q + 4 * r)); t.w[(F_GI + 2 * q + 1) * 256 + o] = w_im(1.0, 32 * c * (q + 4 * r));
            }
            t.w[F_F16 * 256 + o] = w_re(1.0, 128 * r * c); t.w[(F_F16 + 1) * 256 + o] = w_im(1.0, 128 * r * c);
            for (int h = 0; h < 2; h++) {
                const int j = 4 * h + (c >> 2), n = 512 * i * (c & 3) + (g ? 256 * g * j : ((j & 1) ? 256 : -256));
                t.w[(F_AI + h) * 256 + o] = w_re(g ? 2.0 : R2, n);
                const int m = 16 * r * (4 * h + 8 * c) - 512 * h;
                t.w[(F_M1C + 2 * h) * 256 + o] = pkc(0.5 * cs(m), -0.5 * sn(m));
                t.w[(F_M1C + 2 * h + 1) * 256 + o] = pkc(-0.5 * sn(m), -0.5 * cs(m));
            }
        }
    return t;
}
constexpr Frags<40> make_t1() {
    Frags<40> t = {};
    for (int v = 0; v < 5; v++)
        for (int l = 0; l < 64; l++)
            for (int i = 0; i < 4; i++) {
                const int be = v < 3 ? v + 1 : 4 * (v - 3), c = l & 15, r = 4 * (l >> 4) + i, o = v * 2048 + l * 4 + i;
                const double amp = v < 3 ? 1.0 : 0.5;
                const int n1 = 16 * r * (be + 8 * c) + (v == 4 ? 512 : 0), n4 = 16 * c * (be + 8 * r);
                t.w[o] = w_re(amp, n1); t.w[o + 256] = w_im(amp, n1);
                t.w[o + 512] = w_re(1.0, n4); t.w[o + 768] = w_im(1.0, n4);
                t.w[o + 1024] = f32c(cs(r * (be + 8 * c))); t.w[o + 1280] = f32c(-sn(r * (be + 8 * c)));
                t.w[o + 1536] = f32c(cs(c * (be + 8 * r))); t.w[o + 1792] = f32c(-sn(c * (be + 8 * r)));
            }
    return t;
}
constexpr int qidx(int l) {
    if (l <= 256) return l;
    const int sh = l < 512 ? 1 : (l < 1024 ? 2 : 3), h = 1 << (sh - 1), m = l >> sh, r = l & ((1 << sh) - 1);
    return 128 * sh + m + ((r > h || (r == h && (m & 1))) ? 1 : 0);
}
constexpr Frags<6> make_t2() {
    Frags<6> t = {};
    for (int l = 0; l < 64; l++) {
        const int c = l & 15, g = l >> 4, e = 543 - 2 * c - g;
        t.w[256 + l * 4] = (u32)((e < 518 ? qidx(e) : 643) * 4) | ((u32)((e - 1 < 518 ? qidx(e - 1) : 643) * 4) << 16);
        for (int i = 0; i < 4; i++)
            for (int h = 0; h < 2; h++) {
                const int lag = 1024 + 16 + c - 8 * g - 2 * i - h, x = 8 * g + 2 * i + h;
                t.w[l * 4 + i] |= (lag < 1027 || lag > 1035 ? 0x0c0cu : (lag < 1029 ? 0x0100u : 0x0302u)) << (16 * h);
                if (x >= c + 8 && x <= c + 16) t.w[2 * 256 + l * 4 + i] |= 0x3f80u << (16 * h);
                if (x >= c + 1 && x <= c + 7) t.w[3 * 256 + l * 4 + i] |= 0x3f80u << (16 * h);
                if (x >= c + 6 && x <= c + 10) t.w[4 * 256 + l * 4 + i] |= 0x3f80u << (16 * h);
                if (x >= c + 3 && x <= c + 5) t.w[5 * 256 + l * 4 + i] |= 0x3f80u << (16 * h);
            }
    }
    return t;
}
struct Tables { Frags<F_VAR> a; Frags<40> b; Frags<6> c; };
constexpr Frags<6> T2 = make_t2();
constexpr Frags<F_VAR> T0 = make_t0();
constexpr Frags<40> T1 = make_t1();
static __device__ const __attribute__((aligned(128))) Tables TAB = {T0, T1, T2};
constexpr int LA_G = 0;
constexpr int LA_K = LA_G + 12 * 4608;
constexpr int KI = 1344, KQ = 4 * KI, KP = KQ + 512, KX = KP + 320, KC = KX + 16;
constexpr int LA_U = LA_K + 6 * KC;
constexpr int LA_Q = LA_U + 6 * 4608;
constexpr int LA_F = LA_Q + 12 * 2576;
constexpr int LA_D = LA_F + 16;
constexpr int LA_S = LA_D + 256;
constexpr int LA_END = LA_S + 6144;
constexpr int FU0 = 0, FU2 = 1;
constexpr int LDS_BYTES = (LA_END > LB_END ? LA_END : LB_END);
static_assert(LDS_BYTES <= 160 * 1024 && LDS_BYTES > 80 * 1024, "one workgroup per CU");

DI u32 pk(float a, float b) { f2 v = {a, b}; return __builtin_bit_cast(u32, __builtin_convertvector(v, bf2)); }
DI float blo(u32 v) { return __uint_as_float(v << 16); }
DI float bhi(u32 v) { return __uint_as_float(v & 0xffff0000u); }
DI float bf2f(u16 v) { return __uint_as_float((u32)v << 16); }
DI float rbf(float x) { return blo(pk(x, x)); }
DI f2 cmul(f2 a, f2 b) { f2 t = a.xx * b; f2 s = {-b.y, b.x}; return __builtin_elementwise_fma(a.yy, s, t); }
DI f4 mfma(u4 a, u4 b, f4 c) { return __builtin_amdgcn_mfma_f32_16x16x32_bf16(__builtin_bit_cast(s8, a), __builtin_bit_cast(s8, b), c, 0, 0, 0); }
DI u4 pack4(f4 re, f4 im) { u4 r = {pk(re[0], im[0]), pk(re[1], im[1]), pk(re[2], im[2]), pk(re[3], im[3])}; return r; }
DI u4 twid(f4 re, f4 im, f4 wr, f4 wi) { return pack4(__builtin_elementwise_fma(re, wr, -(im * wi)), __builtin_elementwise_fma(re, wi, im * wr)); }
DI int mod9(int x) { return x - 9 * ((x * 57) >> 9); }
template <class T> DI void pin(T& v) { asm volatile("" : "+v"(v)); }

template <class T> DI T lds_ld(int off) { return *(const LAS T*)(u32)off; }
template <class T> DI void lds_st(int off, T v) { *(LAS T*)(u32)off = v; }
DI void bar() { __syncthreads(); }
#define PRIO(n) __builtin_amdgcn_s_setprio(n)
#define PRIOB(n)
DI void glds16(const void* g, u32 off, int lds) {
    u32 keep;
    asm volatile("s_mov_b32 %0, m0\n\ts_mov_b32 m0, %3\n\ts_nop 4\n\tglobal_load_lds_dwordx4 %1, %2\n\ts_mov_b32 m0, %0" : "=&s"(keep) : "v"(off), "s"(g), "s"(lds) : "memory");
}

DI void publish_tag(u32 epoch) {
    u32 t;
    asm volatile("v_add_u32 %0, 0x9E3779B1, %1\n\tds_write_b32 %2, %0\n\ts_waitcnt lgkmcnt(0)" : "=&v"(t) : "v"(epoch), "v"(LB_G) : "memory");
}
DI void wait_flag(__amdgpu_buffer_rsrc_t r, u32 off, u32 tag, bool on) {
    if (on) {
        const unsigned long long t0 = __builtin_amdgcn_s_memrealtime();
        while (!__all(__builtin_amdgcn_raw_buffer_load_b32(r, off, 0, 16) == tag)) {
            if (__builtin_amdgcn_s_memrealtime() - t0 > 5000000ull) break;
            __builtin_amdgcn_s_sleep(POLL_SLEEP);
            asm volatile("" ::: "memory");
        }
    }
    asm volatile("" ::: "memory");
}

constexpr u32 POISON = 0xFFFFFFFFu;
DI bool unset(u4 v) { return max(max(v[0], v[1]), max(v[2], v[3])) == POISON; }
#define DPOLL_SLEEP 1
template <bool TWO> DI void load_set(__amdgpu_buffer_rsrc_t r, u32 off, bool on, u4& a, bool on2, u4& b) {
    const unsigned long long t0 = __builtin_amdgcn_s_memrealtime();
    bool ma = on, mb = TWO && on2;
    for (;;) {
        if (ma) a = __builtin_amdgcn_raw_buffer_load_b128(r, off, 0, 16);
        if (TWO && mb) b = __builtin_amdgcn_raw_buffer_load_b128(r, off, 16384, 16);
        ma = ma && unset(a);
        if (TWO) mb = mb && unset(b);
        if (!__any(ma || mb) || __builtin_amdgcn_s_memrealtime() - t0 > 5000000ull) break;
        __builtin_amdgcn_s_sleep(DPOLL_SLEEP);
        asm volatile("" ::: "memory");
    }
}
constexpr int CODE_LINES = 128;
DI void warm_code(const char* entry) {
    if (threadIdx.x < CODE_LINES) { const u32 t = *(const GAS u32*)(entry + threadIdx.x * 128); asm volatile("" :: "v"(t)); }
}

DI void head_b(const char* entry, const u16* __restrict__ x, const u16* __restrict__ wr, const u16* __restrict__ wi,
               u16* __restrict__ out, char* ws) {
    const int tid = threadIdx.x, lane = tid & 63, c16 = lane & 15, g = lane >> 4;
    const int wv = __builtin_amdgcn_readfirstlane(tid >> 6);
    const int id = blockIdx.x;
    STAMP_DECL;
    const bool rows = id < NB1;
    const int s00 = 32 * (id >> 4) + (id & 15);
    const char* tab = (const char*)&TAB;
    const int R = (tid * 911) >> 16;
    const u32 xoff = (s00 + 16 * (R >> 3) + 256 * (R & 7)) * (2 * DM) + (tid - R * 72) * 16;
    asm volatile("" :: "s"(x), "s"(wr), "s"(wi), "s"(out), "s"(ws), "v"(xoff));
    const __amdgpu_buffer_rsrc_t rws = __builtin_amdgcn_make_buffer_rsrc(ws, 0, WS_USED, 0x00020000);
    STAMP(13);
    int gy = WS_Y1 + id * (NB2 * 128) + tid * 16;
    u32 epoch = 0;
    if (wv == 0) asm volatile("global_load_dword %0, %0, %1 sc1" : "+v"(epoch) : "s"(ws) : "memory");
    if (rows) {
        glds16(x, xoff, LB_X + wv * 1024);
        if (wv < 2) glds16(x, (s00 + 16 + 1536) * (2 * DM) + (tid < 56 ? (tid + 16) * 16 : 256 * 2 * DM + (tid - 56) * 16), LB_X + 16384 + wv * 1024);
        else glds16(tab, tid * 16 - 2048, LB_T + (wv - 2) * 1024);
        if (wv >= 9) glds16(tab, tid * 16 + 5 * 1024, LB_T + (wv + 5) * 1024);
        const u4 P = {POISON, POISON, POISON, POISON};
        __builtin_amdgcn_raw_buffer_store_b128(P, rws, gy, 0, 16);
        if (tid <= 128) __builtin_amdgcn_raw_buffer_store_b128(P, rws, gy, 16384, 16);
    }
    const int u2i = (id & 7) ? (id & 7) * 18 + 1 + (id >> 3) : (id == 136 ? 0 : (id >> 3) + (id < 136 ? 1 : 0));
    const bool star = u2i >= 108;
    const int q36 = min((u2i * 57) >> 11, 3), kg = u2i - 36 * q36, bq = (q36 + 1) & 3;
    const bool mir = !star && kg >= 18;
    const int w0 = star ? min(16 * kg, 562) : (mir ? (2048 - bq) * (2 * DF) + 1122 - 32 * kg : bq * (2 * DF) + 32 * kg);
    __builtin_amdgcn_s_barrier();
    STAMP(14);
    const u32 woff = w0 + ((tid >> 1) & 255) * (mir ? -16 * DF : 16 * DF) + (tid & 1) * (star ? 8 * DF : 16);
    u4 wra, wrb;
    if (wv >= 8) {
        __builtin_memcpy(&wra, (const char*)wr + woff, 16);
        __builtin_memcpy(&wrb, (const char*)wi + woff, 16);
    }
    __builtin_amdgcn_sched_barrier(0);
    const f4 Z = {0.f, 0.f, 0.f, 0.f};
    const int tau = wv & 3, bw = wv >> 2;
    int tl = LB_T + lane * 16;
    int lin = LB_S + (tid + (tid >> 5)) * 16;
    pin(tl); pin(lin);
    int sadr[4];

    if (rows) {
        if (tid < 256) {
            const int o = LB_E + 72 + (tid & 15) * (ER * 4) + (tid >> 4) * (ET * 4);
            lds_st<u2>(o, u2{0u, 0u}); lds_st<u4>(o + 8, u4{0u, 0u, 0u, 0u});
        }
        int xo = LB_X + (c16 >> 2) * 1152 + 2 * (144 * ((g - wv) & 3) + 16 * wv + 4 * (c16 & 3));
        int bo = LB_E + (wv * ET + c16 * ER + min(8 * g, 16)) * 4;
        int go = WS_X1 + (tid >> 3) * 16384 + id * 128 + (tid & 7) * 16;
        int ta = LB_T + lane * 8;
        pin(xo); pin(bo); pin(go); pin(ta);
        STAMP(1);
        asm volatile("s_cmp_lt_u32 %0, 8\n\ts_cbranch_scc0 .Lw%=\n\ts_waitcnt vmcnt(0)\n.Lw%=:\n\ts_waitcnt vmcnt(2)" :: "s"(wv) : "memory", "scc");
        if (wv == 0) publish_tag(epoch);
        bar();
        STAMP(2);
        if (wv < 9) {
            u4 d[2];
#pragma unroll
            for (int s = 0; s < 2; s++) {
                const s4 lo = __builtin_amdgcn_ds_read_tr16_b64_v4i16((LAS s4*)(u32)(xo + s * 9216));
                const s4 hi = __builtin_amdgcn_ds_read_tr16_b64_v4i16((LAS s4*)(u32)(xo + s * 9216 + 4608));
                d[s] = __builtin_bit_cast(u4, __builtin_shufflevector(lo, hi, 0, 1, 2, 3, 4, 5, 6, 7));
            }
            const u4 are = lds_ld<u4>(tl + F_ARE * 1024), aim = lds_ld<u4>(tl + F_AIM * 1024);
            const u2 eo = lds_ld<u2>(ta + F_EO * 1024 + wv * 512);
#pragma unroll
            for (int s = 0; s < 2; s++) d[s] = pack4(mfma(d[s], are, Z), mfma(d[s], aim, Z));
#pragma unroll
            for (int i = 0; i < 4; i++) lds_st<u2>(LB_E + ((i & 1) ? eo[i >> 1] >> 16 : eo[i >> 1] & 0xffff), u2{d[0][i], d[1][i]});
        }
        u4 bre = lds_ld<u4>(tl + F_BRE * 1024), bim = lds_ld<u4>(tl + F_BIM * 1024);
        u4 gre = lds_ld<u4>(tl + (F_G + 2 * tau) * 1024), gim = lds_ld<u4>(tl + (F_G + 2 * tau + 1) * 1024);
        {
            const u2 sa = lds_ld<u2>(ta + F_SA * 1024 + (tau + (bw ? 4 : 0)) * 512);
            const int base = LB_S + ((bw + 3) & 3) * (DM * 8 + 144);
#pragma unroll
            for (int i = 0; i < 4; i++) { sadr[i] = base + ((i & 1) ? sa[i >> 1] >> 16 : sa[i >> 1] & 0xffff); pin(sadr[i]); }
        }
        pin(bre); pin(bim); pin(gre); pin(gim);
        bar();
        {
            PRIOB(2);
            const u4 p0 = lds_ld<u4>(bo), p1 = lds_ld<u4>(bo + 16);
            u4 d[2];
            f4 re[2], im[2];
#pragma unroll
            for (int s = 0; s < 2; s++) { const u4 a = {p0[s], p0[2 + s], p1[s], p1[2 + s]}; re[s] = mfma(a, bre, Z); im[s] = mfma(a, bim, Z); }
            PRIOB(1);
#pragma unroll
            for (int s = 0; s < 2; s++) { const u4 t = pack4(re[s], im[s]); re[s] = mfma(gre, t, Z); im[s] = mfma(gim, t, Z); }
            PRIOB(0);
#pragma unroll
            for (int s = 0; s < 2; s++) d[s] = pack4(re[s], im[s]);
            if (c16 < 9) {
#pragma unroll
                for (int i = 0; i < 4; i++) lds_st<u2>(sadr[i], u2{d[0][i], d[1][i]});
                if (wv == 0 && c16 == 0 && !(g & 1)) lds_st<u2>(sadr[0] + 8, u2{d[0][0], d[1][0]});
            }
        }
        bar();
        STAMP(3);
        __builtin_amdgcn_raw_buffer_store_b128(lds_ld<u4>(lin), rws, go, 0, 16);
        if (tid <= 128) __builtin_amdgcn_raw_buffer_store_b128(lds_ld<u4>(lin + 1056 * 16), rws, go, 128 * 16384, 16);
    } else {
        if (wv < 8 && (kg == 0 || kg == (star ? 36 : 18))) {
            const int m = tid & 255;
            lds_st<u16>(LB_W + tid * 36 + 32, ((wv >> 2) ? wi : wr)[((mir ? bq + 8 * m : 2048 - bq - 8 * m) & 2047) * DF + (kg ? 288 : 0)]);
        }
        warm_code(entry);
        asm volatile("s_waitcnt vmcnt(0)" ::: "memory");
        if (wv == 0) publish_tag(epoch);
        bar();
    }
    const u32 tag = lds_ld<u32>(LB_G);
    if (wv >= 8) {
        const int o = LB_W + ((tid >> 1) & 255) * 36 + (tid & 1) * 16;
#pragma unroll
        for (int i = 0; i < 4; i++) { lds_st<u32>(o + 4 * i, wra[i]); lds_st<u32>(o + 9216 + 4 * i, wrb[i]); }
    }
    asm volatile("s_waitcnt vmcnt(0)" ::: "memory");
    if (rows && lane == 0) __builtin_amdgcn_raw_buffer_store_b32(tag, rws, WS_F1 + (wv * 16 + (id >> 3)) * 128 + (id & 7) * 4, 0, 16);
    STAMP(4);
    glds16(tab, F_F16 * 1024 + tid * 16, LB_T + F_F16 * 1024 + wv * 1024);
    if (star || wv < 8) glds16(tab, (F_VAR + 8 * q36) * 1024 + tid * 16, LB_T + F_VAR * 1024 + wv * 1024);
    asm volatile("s_waitcnt vmcnt(0)" ::: "memory");
    bar();
    STAMP(12);

    const int typ = star ? (wv & 1) : 0;
    const int tv = tl + (F_VAR + 8 * typ) * 1024;
    u4 fre = lds_ld<u4>(tl + F_F16 * 1024), fim = lds_ld<u4>(tl + (F_F16 + 1) * 1024);
    u4 m1r = lds_ld<u4>(tv), m1i = lds_ld<u4>(tv + 1024), m4r = lds_ld<u4>(tv + 2048), m4i = lds_ld<u4>(tv + 3072);
    f4 twr = lds_ld<f4>(tv + 4096), twi = lds_ld<f4>(tv + 5120), tur = lds_ld<f4>(tv + 6144), tui = lds_ld<f4>(tv + 7168);
    u4 mcr = lds_ld<u4>(tl + (F_M1C + 2 * typ) * 1024), mci = lds_ld<u4>(tl + (F_M1C + 2 * typ + 1) * 1024);
    f4 wfr, wfi;
    {
        const int kcol = star ? 8 * kg + (wv >> 1) : 16 * kg + wv;
        const int cc = star ? 8 * typ + (u2i < NB2 - 1 ? (wv >> 1) : 7) : (mir ? 15 - wv : wv);
        const int wo = LB_W + (c16 + 64 * g) * 36 + cc * 2;
#pragma unroll
        for (int i = 0; i < 4; i++) {
            wfr[i] = bf2f(lds_ld<u16>(wo + i * 576));
            wfi[i] = bf2f(lds_ld<u16>(wo + i * 576 + 9216));
        }
        float sc = 1.f / (float)(SEQ * DM);
        if (kcol == 0 || kcol == 288) {
            sc *= 0.5f;
#pragma unroll
            for (int i = 0; i < 4; i++) {
                const int m = c16 + 64 * g + 16 * i;
                const int o = LB_W + (typ ? (255 - m) * 36 + cc * 2 : m * 36 + 32);
                wfr[i] += bf2f(lds_ld<u16>(o));
                wfi[i] -= bf2f(lds_ld<u16>(o + 9216));
            }
        }
        wfr *= sc; wfi *= (mir ? -sc : sc);
    }
    f4 wnr = -wfr, wni = -wfi;
    int io = (32 * g + c16) * 128 + (((wv >> 1) ^ (c16 >> 1)) * 16);
    int so = (tid >> 3) * 128 + (((tid & 7) ^ ((tid >> 4) & 7)) * 16);
    pin(fre); pin(fim); pin(m1r); pin(m1i); pin(m4r); pin(m4i); pin(twr); pin(twi); pin(tur); pin(tui); pin(mcr); pin(mci);
    int gi = WS_X1 + u2i * 16384 + tid * 16, gq = WS_Y1 + (tid >> 3) * (NB2 * 128) + u2i * 128 + (tid & 7) * 16;
    int i1 = LB_I + io + (star ? 0 : (wv & 1) * 8);
    io += LB_O + (wv & 1) * 8;
    pin(wfr); pin(wnr); pin(wni); pin(io); pin(so); pin(gi); pin(gq); pin(i1);
    wait_flag(rws, WS_F1 + (((u2i >> 3) & 15) * 16 + wv) * 128 + lane * 4, tag, lane < 8);
    STAMP(5);
    lds_st<u4>(LB_I + so, __builtin_amdgcn_raw_buffer_load_b128(rws, gi, 0, 16));
    bar();
    STAMP(6);
    {
        const int o1 = i1;
        PRIOB(3);
        const u2 a0 = lds_ld<u2>(o1), a1 = lds_ld<u2>(o1 + 2048);
        const u4 a = {a0[0], a0[1], a1[0], a1[1]};
        f4 re = mfma(a, m1r, Z), im = mfma(a, m1i, Z);
        if (star) {
            const u2 b0 = lds_ld<u2>(o1 + 8), b1 = lds_ld<u2>(o1 + 8 + 2048);
            const u4 b = {b0[0], b0[1], b1[0], b1[1]};
            re = mfma(b, mcr, re); im = mfma(b, mci, im);
        }
        u4 t = twid(re, im, twr, twi);
        re = mfma(fre, t, Z); im = mfma(fim, t, Z);
        PRIOB(2);
        t = pack4(__builtin_elementwise_fma(re, wfr, im * wni), __builtin_elementwise_fma(re, wni, im * wnr));
        re = mfma(t, fre, Z); im = mfma(t, fim, Z);
        PRIOB(1);
        t = twid(re, im, tur, tui);
        re = mfma(m4r, t, Z); im = mfma(m4i, t, Z);
        PRIOB(0);
        t = pack4(re, im);
        lds_st<u2>(io, u2{t[0], t[1]}); lds_st<u2>(io + 2048, u2{t[2], t[3]});
    }
    bar();
    STAMP(7);
    {
        u4 v = lds_ld<u4>(LB_O + so);
        if (star) {
            u4 w;
#pragma unroll
            for (int s = 0; s < 2; s++) {
                const float ar = blo(v[s]), ai = bhi(v[s]), cr = blo(v[2 + s]), ci = bhi(v[2 + s]);
                w[s] = pk(ar + ci, ai - cr); w[2 + s] = pk(ar - ci, -ai - cr);
            }
            v = w;
        }
        if (u2i < NB2 - 1 || (tid & 7) == 0)
            __builtin_amdgcn_raw_buffer_store_b128(v, rws, gq, 0, 16);
    }
    STAMP(8);
    if (!rows) { STAMP_OUT(0); return; }

    u4 gre = lds_ld<u4>(tl + (F_GI + 2 * tau) * 1024), gim = lds_ld<u4>(tl + (F_GI + 2 * tau + 1) * 1024);
    u4 bre = lds_ld<u4>(tl + F_BRE * 1024), bim = lds_ld<u4>(tl + F_BIM * 1024);
    u4 ai0 = lds_ld<u4>(tl + F_AI * 1024), ai1 = lds_ld<u4>(tl + (F_AI + 1) * 1024);
    int eadr[4];
#pragma unroll
    for (int i = 0; i < 4; i++) {
        const int r = 4 * g + i;
        eadr[i] = LB_E2 + (mod9(4 * (c16 + 18 - r)) * E2V + r * E2U + 2 * wv) * 4;
        pin(eadr[i]);
    }
    int ao = LB_E2 + (wv * E2V + c16 * E2U + 8 * g) * 4;
    int yo = LB_Y + (c16 >> 2) * OP + 2 * (144 * (((c16 & 3) - wv) & 3) + 16 * wv + 4 * g);
    int yl = LB_Y + tid * 16 + R * (OP - 1152);
    u32 oo = 2 * xoff - (tid - R * 72) * 16 + 2 * DM;
    pin(gre); pin(gim); pin(bre); pin(bim); pin(ai0); pin(ai1); pin(ao); pin(gy); pin(yo); pin(yl); pin(oo);
    STAMP(9);
    {
        u4 v, w;
        if (wv < 3) { load_set<true>(rws, gy, true, v, tid <= 128, w); if (tid <= 128) lds_st<u4>(lin + 1056 * 16, w); }
        else load_set<false>(rws, gy, true, v, false, w);
        lds_st<u4>(lin, v);
    }
    bar();
    if (id == 0 && tid == 0) __builtin_amdgcn_raw_buffer_store_b32(tag, rws, WS_EPOCH, 0, 16);
    STAMP(10);
    {
        PRIOB(2);
        u4 d[2];
        u2 p[4];
#pragma unroll
        for (int i = 0; i < 4; i++) p[i] = lds_ld<u2>(sadr[i]);
        f4 re[2], im[2];
#pragma unroll
        for (int s = 0; s < 2; s++) { const u4 a = {p[0][s], p[1][s], p[2][s], p[3][s]}; re[s] = mfma(a, gre, Z); im[s] = mfma(a, gim, Z); }
        PRIOB(1);
#pragma unroll
        for (int s = 0; s < 2; s++) { const u4 t = pack4(re[s], im[s]); re[s] = mfma(t, bre, Z); im[s] = mfma(t, bim, Z); }
        PRIOB(0);
#pragma unroll
        for (int s = 0; s < 2; s++) d[s] = pack4(re[s], im[s]);
        if (c16 < 9) {
#pragma unroll
            for (int i = 0; i < 4; i++) lds_st<u2>(eadr[i], u2{d[0][i], d[1][i]});
        }
    }
    STAMP(15);
    bar();
    if (wv < 9) {
        const u4 p0 = lds_ld<u4>(ao), p1 = lds_ld<u4>(ao + 16);
#pragma unroll
        for (int s = 0; s < 2; s++) {
            const u4 a = {p0[s], p0[2 + s], p1[s], p1[2 + s]};
            const f4 y0 = mfma(a, ai0, Z), y1 = mfma(a, ai1, Z);
            lds_st<u2>(yo + s * (8 * OP), u2{pk(y0[0], y0[1]), pk(y0[2], y0[3])});
            lds_st<u2>(yo + s * (8 * OP) + 4 * OP, u2{pk(y1[0], y1[1]), pk(y1[2], y1[3])});
        }
    }
    bar();
    *(GAS u4*)((char*)out + oo) = lds_ld<u4>(yl);
    if (wv < 2) {
        const int r2 = tid < 56 ? 14 : 15, c2 = tid + 1024 - 72 * r2;
        *(GAS u4*)((char*)out + (u32)((s00 + 16 + 256 * (r2 - 8)) * (4 * DM) + 2 * DM + c2 * 16)) = lds_ld<u4>(LB_Y + r2 * OP + c2 * 16);
    }
    STAMP(11);
    STAMP_OUT(0);
}

DI f2 cexpa(float ar2, float air, float p) {
    const float e = __builtin_amdgcn_exp2f(ar2 * p), t = __builtin_amdgcn_fractf(air * p);
    return f2{e * __builtin_amdgcn_cosf(t), e * __builtin_amdgcn_sinf(t)};
}
DI u4 ld_k(int o) {
    const u2 a = *(const volatile LAS u2*)(u32)o, b = *(const volatile LAS u2*)(u32)(o + 8);
    return u4{a[0], a[1], b[0], b[1]};
}
DI void a_rows_out(int yb, int ystr, char* o) {
    u2 y[6];
#pragma unroll
    for (int c = 0; c < 6; c++) y[c] = lds_ld<u2>(yb + c * ystr);
#pragma unroll
    for (int r = 0; r < 4; r++) {
        u3 v;
#pragma unroll
        for (int c = 0; c < 3; c++) v[c] = __builtin_amdgcn_perm(y[2 * c + 1][r >> 1], y[2 * c][r >> 1], (r & 1) ? 0x07060302u : 0x05040100u);
        *(u3*)(o + r * (4 * DM)) = v;
    }
}
template <int R> DI void stage(u4 v) {
    asm volatile("ds_write_addtid_b32 %0 offset:%4\n\tds_write_addtid_b32 %1 offset:%5\n\tds_write_addtid_b32 %2 offset:%6\n\tds_write_addtid_b32 %3 offset:%7"
                 :: "v"(v[0]), "v"(v[1]), "v"(v[2]), "v"(v[3]), "i"(288 * R), "i"(288 * R + 288), "i"(288 * R + 576), "i"(288 * R + 864) : "memory");
}
#define CM_MUL(d, a, b) "v_pk_mul_f32 " d ", " a ", " b " op_sel_hi:[0,1]\n\t"
#define CM_FMA(d, a, b) "v_pk_fma_f32 " d ", " a ", " b ", " d " op_sel:[1,1,0] op_sel_hi:[1,0,1] neg_lo:[1,0,0]\n\t"
struct C4 { f2 a, b, c, d; };
DI C4 cmul4(f2 a0, f2 b0, f2 a1, f2 b1, f2 a2, f2 b2, f2 a3, f2 b3) {
    C4 r;
    asm("s_nop 0\n\t" CM_MUL("%0", "%4", "%5") CM_MUL("%1", "%6", "%7") CM_MUL("%2", "%8", "%9") CM_MUL("%3", "%10", "%11")
        CM_FMA("%0", "%4", "%5") CM_FMA("%1", "%6", "%7") CM_FMA("%2", "%8", "%9") CM_FMA("%3", "%10", "%11") "s_nop 0"
        : "=&v"(r.a), "=&v"(r.b), "=&v"(r.c), "=&v"(r.d) : "v"(a0), "v"(b0), "v"(a1), "v"(b1), "v"(a2), "v"(b2), "v"(a3), "v"(b3));
    return r;
}
DI C4 cmul4(const C4& a, f2 b) { return cmul4(a.a, b, a.b, b, a.c, b, a.d, b); }
DI u4 pk4(const C4& v) { return u4{pk(v.a.x, v.a.y), pk(v.b.x, v.b.y), pk(v.c.x, v.c.y), pk(v.d.x, v.d.y)}; }

DI void sig(int f) {
    asm volatile("s_waitcnt lgkmcnt(0)" ::: "memory");
    if ((threadIdx.x & 63) == 0) __hip_atomic_fetch_add((LAS u32*)(u32)(LA_F + 4 * f), 1u, __ATOMIC_RELAXED, __HIP_MEMORY_SCOPE_WORKGROUP);
}
template <int SLEEP = 0> DI void await(int f, u32 n) {
    while ((u32)__builtin_amdgcn_readfirstlane(*(const volatile LAS u32*)(u32)(LA_F + 4 * f)) < n) if (SLEEP) __builtin_amdgcn_s_sleep(SLEEP);
    asm volatile("" ::: "memory");
}
DI float exp_bf(float x) {
    const float t = x * 1.44269504f, r = __builtin_fmaf(x, 1.92596299e-8f, __builtin_fmaf(x, 1.44269504f, -t));
    const float e = __builtin_amdgcn_exp2f(t);
    return __builtin_fmaf(e * 0.693147181f, r, e);
}

struct APar { u32 l, a, r, i, t, d; };
DI void head_a(const u16* __restrict__ u, APar pr, u16* __restrict__ out, char* ws) {
    const int tid = threadIdx.x, lane = tid & 63, c16 = lane & 15, g = lane >> 4;
    const int wv = __builtin_amdgcn_readfirstlane(tid >> 6);
    STAMP_DECL;
    const int ai_ = blockIdx.x - A0;
    const int d0 = 6 * ((ai_ & 7) * 12 + (ai_ >> 3));
    const f4 Z = {0.f, 0.f, 0.f, 0.f};

    if (wv >= 12) {
        __builtin_amdgcn_s_setprio(3);
        const int t = tid - 768;
        if (t == 0) lds_st<u2>(LA_F, u2{0u, 0u});
        if (t < 180) lds_st<u4>(LA_U + (t / 30) * 4608 + (t % 30) * 16, u4{0u, 0u, 0u, 0u});
        bar();
        u3 ua[4], ub[4];
#pragma unroll
        for (int r = 0; r < 4; r++) ua[r] = *(const u3*)(u + (4 * t + r) * DM + d0);
        glds16((const char*)&TAB + F_END * 1024, t * 16, LA_S + (wv - 12) * 1024);
        if (wv < 14) glds16((const char*)&TAB + (F_END + 4) * 1024, t * 16, LA_S + (wv - 8) * 1024);
        sig(FU0);
        await<1>(FU0, 4);
#pragma unroll
        for (int r = 0; r < 4; r++) ub[r] = *(const u3*)(u + (1024 + 4 * t + r) * DM + d0);
        asm volatile("" ::: "memory");
#pragma unroll
        for (int c = 0; c < 6; c++) {
            const u32 sel = (c & 1) ? 0x07060302u : 0x05040100u;
            lds_st<u2>(LA_U + c * 4608 + 480 + t * 8, u2{__builtin_amdgcn_perm(ua[1][c >> 1], ua[0][c >> 1], sel), __builtin_amdgcn_perm(ua[3][c >> 1], ua[2][c >> 1], sel)});
        }
        STAMP(7);
        asm volatile("s_waitcnt vmcnt(4)" ::: "memory");
        bar();
        bar();
#pragma unroll
        for (int c = 0; c < 6; c++) {
            const u32 sel = (c & 1) ? 0x07060302u : 0x05040100u;
            lds_st<u2>(LA_U + c * 4608 + 480 + 2048 + t * 8, u2{__builtin_amdgcn_perm(ub[1][c >> 1], ub[0][c >> 1], sel), __builtin_amdgcn_perm(ub[3][c >> 1], ub[2][c >> 1], sel)});
        }
        sig(FU2);
        STAMP(8);
        const int tile = wv - 12, yl = ((lane >> 2) + 16 * (lane & 3)) * 8;
        char* o = (char*)(out + 4 * t * (2 * DM) + d0);
        bar();
        bar();
        STAMP(11);
        a_rows_out(LA_G + ((tile >> 1) ^ 1) * 4608 + (tile & 1) * 1024 + yl, 9216, o);
        STAMP(9);
        bar();
        bar();
        STAMP(12);
        a_rows_out(LA_G + ((tile >> 1) ^ 1) * 4608 + (tile & 1) * 1024 + 2048 + yl, 9216, o + 1024 * (4 * DM));
        STAMP(10);
        STAMP_OUT(768);
        return;
    }
    const int ch = wv >> 1, hf = wv & 1;
    {
        __builtin_amdgcn_s_barrier();
        asm volatile("s_waitcnt vmcnt(0)" : "+v"(pr.l), "+v"(pr.a), "+v"(pr.r), "+v"(pr.i), "+v"(pr.t), "+v"(pr.d) :: "memory");
        PRIO(3);
        const float dt = rbf(exp_bf(blo(pr.t)));
        const float ar2 = -rbf(exp_bf(blo(pr.l))) * dt * 1.44269504f, air = blo(pr.a) * dt * 0.159154943f;
        const f2 C = {2.f * dt * blo(pr.r), 2.f * dt * blo(pr.i)};
        STAMP(1);
        const f2 z1 = cexpa(ar2, air, 1.f), z8c = cexpa(ar2, -air, 8.f), z128 = cexpa(ar2, air, 128.f);
        const int gr = LA_G + wv * 4608 + c16 * 288 + g * 16, q = LA_Q + wv * 2576;
        asm volatile("s_mov_b32 m0, %0\n\ts_nop 0" :: "s"(LA_G + wv * 4608) : "memory");
#define SB __builtin_amdgcn_sched_barrier(0)
#define STAGE16(v) do { stage<0>(v[0]); stage<4>(v[1]); stage<8>(v[2]); stage<12>(v[3]); } while (0)
#define READ4(f) do { _Pragma("unroll") for (int s = 0; s < 4; s++) f[s] = lds_ld<u4>(gr + 64 * s); SB; } while (0)
        u4 v[4], bfr[4], afr[4];
        const C4 s1 = cmul4(z8c, z8c, z1, z1, z128, z128, C, z1);
        const C4 s2 = cmul4(s1.a, s1.a, z8c, s1.a, s1.c, s1.c, C, s1.b);
        {
            C4 w = {f2{1.f, 0.f}, z8c, s1.a, s2.b};
            v[0] = pk4(w);
#pragma unroll
            for (int i = 1; i < 4; i++) { w = cmul4(w, s2.a); v[i] = pk4(w); }
        }
        STAGE16(v); READ4(bfr);
        PRIO(2);
        const C4 s3 = cmul4(s1.d, s1.b, s1.b, s1.b, s2.c, s2.c, s2.c, z128);
        const C4 e0 = {C, s1.d, s2.d, s3.a}, e1 = cmul4(e0, s3.b);
        v[0] = pk4(e0); v[1] = pk4(e1); v[2] = pk4(cmul4(e0, z128)); v[3] = pk4(cmul4(e1, z128));
        STAGE16(v); READ4(afr);
        PRIO(1);
        {
            const C4 f = cmul4(C4{e0.a, e0.c, e1.a, e1.c}, s1.c), h = cmul4(e0.a, s2.c, e1.a, s2.c, e0.a, s3.d, e1.a, s3.d);
            v[0] = pk4(f); v[1] = pk4(cmul4(f, z128)); v[2] = pk4(h); v[3] = pk4(cmul4(h, s1.c));
        }
        SB;
        f4 acc = Z;
#pragma unroll
        for (int s = 0; s < 4; s++) acc = mfma(afr[s], bfr[s], acc);
        STAGE16(v); READ4(afr);
        PRIO(0);
        lds_st<f4>(q + (4 * (g & 1) + 128 * (g >> 1) + 8 * c16) * 4, acc);
        {
            const f2 f0 = cmul(C, s3.c), f1 = cmul(f0, z128);
            const C4 f = cmul4(f0, s1.c, f1, s1.c, f0, s3.c, f0, s3.c), h = cmul4(C4{f0, f1, f.a, f.b}, s2.c);
            v[0] = pk4(C4{f0, f1, f.a, f.b}); v[1] = pk4(h); v[2][0] = pk(f.c.x, f.c.y);
        }
        SB;
        acc = Z;
#pragma unroll
        for (int s = 0; s < 4; s++) acc = mfma(afr[s], bfr[s], acc);
        stage<0>(v[0]); stage<4>(v[1]);
        asm volatile("ds_write_addtid_b32 %0 offset:%1" :: "v"(v[2][0]), "i"(288 * 8) : "memory");
        READ4(afr);
        if (g < 2) lds_st<f4>(q + (256 + 64 * g + 4 * c16) * 4, acc);
        else {
            const int o = q + (384 + 64 * (g - 2) + 2 * c16) * 4;
            lds_st<f2>(o, f2{acc[0], acc[1]}); lds_st<f2>(o + 128, f2{acc[2], acc[3]});
        }
        acc = Z;
#pragma unroll
        for (int s = 0; s < 4; s++) acc = mfma(afr[s], bfr[s], acc);
        if (g < 2) {
#pragma unroll
            for (int i = 0; i < 4; i++) lds_st<float>(q + (512 + 64 * g + 16 * i + c16) * 4, acc[i]);
        } else if (lane < 34) lds_st<float>(q + (lane == 32 ? 640 : 643) * 4, lane == 32 ? acc[0] : 0.f);
    }
    STAMP(2);
    asm volatile("s_waitcnt lgkmcnt(0)" ::: "memory");
    bar();
    STAMP(9);

    const int k0 = LA_K + ch * KC;
    {
        const LAS float* qa = (const LAS float*)(u32)(LA_Q + ch * 5152);
        const LAS float* qb = qa + 644;
        u2 w[4];
        int y;
        if (hf == 0) {
            const u32 ix = lds_ld<u32>(LA_S + 1024 + lane * 16);
            f4 k = *(const LAS f4*)(qa + 4 * lane) + *(const LAS f4*)(qb + 4 * lane);
            f4 p = *(const LAS f4*)(qa + 4 * lane - 4) + *(const LAS f4*)(qb + 4 * lane - 4);
            const f2 s = *(const LAS f2*)(qa + 512 - 2 * lane) + *(const LAS f2*)(qb + 512 - 2 * lane);
            const float e0 = qa[(ix & 0xffff) >> 2] + qb[(ix & 0xffff) >> 2], e1 = qa[ix >> 18] + qb[ix >> 18];
            lds_st<u32>(k0 + KQ + (32 + lane) * 4, pk(s.x, lane ? s.y : 0.f));
            if (lane == 0) lds_st<u32>(k0 + KX, pk(s.x, s.y));
            lds_st<u32>(k0 + KQ + (lane + (lane & 32) * 2) * 4, 0u);
            lds_st<u32>(k0 + g * KI + c16 * 4, pk(e0, e1));
            if (lane == 0) { k[0] += blo(pr.d); p = Z; }
            const u32 c = pk(k[1], k[0]), d = pk(k[0], p[3]);
            w[0] = u2{pk(k[3], k[2]), c}; w[1] = u2{pk(k[2], k[1]), d}; w[2] = u2{c, pk(p[3], p[2])}; w[3] = u2{d, pk(p[2], p[1])};
            y = k0 + (270 - 2 * lane) * 4;
        } else {
            const int j = 256 + 2 * lane;
            const float q0 = qa[j] + qb[j], q1 = qa[j + 1] + qb[j + 1], q2 = qa[j + 2] + qb[j + 2];
            const float p1 = qa[j - 1] + qb[j - 1], p2 = qa[j - 2] + qb[j - 2], p3 = qa[j - 3] + qb[j - 3];
            const u32 c = pk(q0, q0), d = pk(q0, p1), e = pk(p1, p2), f = lane ? c : d;
            w[0] = u2{pk(q2, q1), c}; w[1] = u2{pk(q1, q0), f}; w[2] = u2{c, lane ? d : e}; w[3] = u2{f, lane ? e : pk(p2, p3)};
            y = k0 + (142 - 2 * lane) * 4;
            if (lane < 48) lds_st<u32>(k0 + (lane < 32 ? (lane >> 3) * KI + (272 + (lane & 7)) * 4 : KP + (32 + lane) * 4), 0u);
            const f2 s = *(const LAS f2*)(qa + 640 - 2 * lane) + *(const LAS f2*)(qb + 640 - 2 * lane);
            lds_st<u32>(k0 + KP + lane * 4, pk(s.x, lane ? s.y : 0.f));
        }
#pragma unroll
        for (int i = 0; i < 4; i++) lds_st<u2>(y + i * KI, w[i]);
    }
    const int x0 = 2031 - c16 + 8 * g;
    int ka = k0 + (x0 & 3) * KI + (x0 >> 2) * 8 - 3008 - 256 * hf - 192 - 512;
    int ubs = LA_U + ch * 4608 + 480 + (16 * c16 + 8 * g - 16) * 2 - 256 * hf - 192;
    pin(ka); pin(ubs);
    const int lt = LA_S + lane * 16;
    const u4 bnd5 = lds_ld<u4>(lt + 4096), bnd3 = lds_ld<u4>(lt + 5120);
    int uv = LA_U + ch * 4608 + 480 + (8 * c16 + 8 * g - 24) * 2 + 256 * hf;
    pin(uv);
#define VSUM(n) ({ const u4 a_ = lds_ld<u4>(uv + 512 * (n)); pack4(mfma(a_, bnd5, Z), mfma(a_, bnd3, Z)); })
    u4 vs[4], ws0, ws1, ws2;
    {
        const u4 bnd9 = lds_ld<u4>(lt + 2048), bnd7 = lds_ld<u4>(lt + 3072);
        const int uw = LA_U + ch * 4608 + 480 + (16 * c16 + 8 * g - 28) * 2;
#define WSUM(I) ({ const u4 a_ = ld_k(uw + 512 * (I)); pack4(mfma(a_, bnd9, Z), mfma(a_, bnd7, Z)); })
#pragma unroll
        for (int n = 0; n < 4; n++) vs[n] = VSUM(n);
        ws0 = WSUM(hf);
        if (hf) { ws1 = WSUM(2); ws2 = WSUM(3); }
    }
    STAMP(13);
    bar();
    STAMP(3);
#define KFR(rho, p) ld_k(ka + 192 - 64 * (rho) + 512 - 512 * (p))
#define UFR(rho, q) lds_ld<u4>(ubs + 192 - 64 * (rho) + 512 * (q))
    f4 acc[4];
#pragma unroll
    for (int i = 0; i < 4; i++) acc[i] = Z;
    u4 gv[3];
    {
        u4 A[2][2], B[2][4];
        PRIO(3);
#pragma unroll
        for (int p = 0; p < 4; p++) B[0][p] = UFR(0, p);
        A[0][0] = KFR(0, 0); A[0][1] = KFR(0, 1);
#pragma unroll
        for (int k = 0; k < 3; k++) gv[k] = ld_k(k0 + KQ + (94 + 16 * hf - 32 * k - 2 * c16 + 4 * g) * 4);
        if (!hf) { const u4 t = ld_k(ka - 320); acc[2] = mfma(t, B[0][0], acc[2]); acc[3] = mfma(t, B[0][1], acc[3]); }
        acc[2] = mfma(vs[0], gv[0], acc[2]); acc[3] = mfma(vs[0], gv[1], acc[3]); acc[3] = mfma(vs[1], gv[0], acc[3]);
#pragma unroll
        for (int rho = 0; rho < 4; rho++)
#pragma unroll
            for (int p = 0; p < 2; p++) {
                SB;
                if (p == 0) { if (rho == 1) PRIO(2); else if (rho == 2) PRIO(1); else if (rho == 3) PRIO(0); }
                if (rho < 3) { B[(rho + 1) & 1][2 * p] = UFR(rho + 1, 2 * p); B[(rho + 1) & 1][2 * p + 1] = UFR(rho + 1, 2 * p + 1); A[(rho + 1) & 1][p] = KFR(rho + 1, p); }
#pragma unroll
                for (int q = 0; p + q < 4; q++) acc[p + q] = mfma(A[rho & 1][p], B[rho & 1][q], acc[p + q]);
            }
        SB;
    }
#pragma unroll
    for (int i = 0; i < 4; i++) pin(acc[i]);
    STAMP(4);
#define EXCHANGE(off)                                                                         \
    {                                                                                         \
        const int mine = LA_G + wv * 4608 + (off), other = LA_G + (wv ^ 1) * 4608 + (off);     \
        if (hf) { lds_st<f4>(mine + lane * 16, acc[0]); lds_st<f4>(mine + 1024 + lane * 16, acc[1]); acc[0] = acc[2]; acc[1] = acc[3]; } \
        else { lds_st<f4>(mine + lane * 16, acc[2]); lds_st<f4>(mine + 1024 + lane * 16, acc[3]); } \
        bar();                                                                                \
        const f4 y0 = lds_ld<f4>(other + lane * 16) + acc[0], y1 = lds_ld<f4>(other + 1024 + lane * 16) + acc[1]; \
        lds_st<u2>(other + lane * 8, u2{pk(y0[0], y0[1]), pk(y0[2], y0[3])});                 \
        lds_st<u2>(other + 1024 + lane * 8, u2{pk(y1[0], y1[1]), pk(y1[2], y1[3])});          \
    }
    EXCHANGE(0);
    {
        const int po = k0 + KP + (15 - c16 + 4 * g) * 4;
#define PFR(d) u4{lds_ld<u32>(po + 64 * (7 - (d))), lds_ld<u32>(po + 64 * (7 - (d)) + 4), lds_ld<u32>(po + 64 * (7 - (d)) + 8), lds_ld<u32>(po + 64 * (7 - (d)) + 12)}
        const u4 g4 = PFR(4), g5 = PFR(5), g6 = PFR(6);
        bar();
        STAMP(14);
        PRIO(3);
        acc[0] = mfma(vs[0], gv[2], Z); acc[1] = mfma(vs[1], gv[2], Z); acc[2] = mfma(vs[2], gv[2], Z); acc[3] = mfma(vs[3], gv[2], Z);
        acc[0] = mfma(vs[1], gv[1], acc[0]); acc[1] = mfma(vs[2], gv[1], acc[1]); acc[2] = mfma(vs[3], gv[1], acc[2]);
        acc[0] = mfma(vs[2], gv[0], acc[0]); acc[1] = mfma(vs[3], gv[0], acc[1]);
        if (hf) {
            acc[3] = mfma(ws0, g6, acc[3]); acc[2] = mfma(ws0, g5, acc[2]); acc[1] = mfma(ws0, g4, acc[1]);
            acc[3] = mfma(ws1, g5, acc[3]); acc[2] = mfma(ws1, g4, acc[2]); acc[3] = mfma(ws2, g4, acc[3]);
        } else {
            const u4 g7 = PFR(7), sel = lds_ld<u4>(lt);
            const u32 r = lds_ld<u32>(k0 + KX);
            acc[3] = mfma(ws0, g7, acc[3]); acc[2] = mfma(ws0, g6, acc[2]); acc[0] = mfma(ws0, g4, acc[0]); acc[1] = mfma(ws0, g5, acc[1]);
            const u4 t = {__builtin_amdgcn_perm(r, r, sel[0]), __builtin_amdgcn_perm(r, r, sel[1]), __builtin_amdgcn_perm(r, r, sel[2]), __builtin_amdgcn_perm(r, r, sel[3])};
#pragma unroll
            for (int q = 0; q < 4; q++) acc[q] = mfma(t, UFR(0, q), acc[q]);
        }
#pragma unroll
        for (int i = 0; i < 4; i++) pin(acc[i]);
        await<1>(FU2, 4);
        u4 A[2][2], B[2][5];
#pragma unroll
        for (int p = 0; p < 5; p++) B[0][p] = UFR(0, 3 + p);
        A[0][0] = KFR(0, 0); A[0][1] = KFR(0, 1);
        vs[0] = VSUM(4); vs[1] = VSUM(5);
        if (!hf) {
            const u4 t = ld_k(ka - 320);
            acc[0] = mfma(t, UFR(0, 2), acc[0]);
#pragma unroll
            for (int q = 1; q < 4; q++) acc[q] = mfma(t, B[0][q - 1], acc[q]);
        }
        acc[2] = mfma(vs[0], gv[0], acc[2]); acc[3] = mfma(vs[0], gv[1], acc[3]); acc[3] = mfma(vs[1], gv[0], acc[3]);
#pragma unroll
        for (int rho = 0; rho < 4; rho++)
#pragma unroll
            for (int p = 0; p < 2; p++) {
                SB;
                if (p == 0) { if (rho == 1) PRIO(2); else if (rho == 2) PRIO(1); else if (rho == 3) PRIO(0); }
                if (rho < 3) {
#pragma unroll
                    for (int q = 3 * p; q < 3 + 2 * p; q++) B[(rho + 1) & 1][q] = UFR(rho + 1, 3 + q);
                    A[(rho + 1) & 1][p] = KFR(rho + 1, p);
                }
#pragma unroll
                for (int j = 0; j < 4; j++) acc[j] = mfma(A[rho & 1][p], B[rho & 1][j + 1 - p], acc[j]);
            }
        SB;
    }
#pragma unroll
    for (int i = 0; i < 4; i++) pin(acc[i]);
    STAMP(5);
    EXCHANGE(2048);
    bar();
    STAMP(6);
    STAMP_OUT(0);
}

__global__ void __launch_bounds__(NT) __attribute__((aligned(4096))) fused_kernel(const u16* __restrict__ u, const u16* __restrict__ lar, const u16* __restrict__ aimg,
                                                   const u16* __restrict__ cre, const u16* __restrict__ cim, const u16* __restrict__ ldt,
                                                   const u16* __restrict__ dsk, const u16* __restrict__ x, const u16* __restrict__ wr,
                                                   const u16* __restrict__ wi, u16* __restrict__ out, char* ws) {
    const char* entry;
    asm volatile("s_getpc_b64 %0" : "=s"(entry));
    APar pr;
    if (blockIdx.x >= A0 && __builtin_amdgcn_readfirstlane(threadIdx.x) < 768) {
        const int a = blockIdx.x - A0, d = 6 * ((a & 7) * 12 + (a >> 3)) + __builtin_amdgcn_readfirstlane(threadIdx.x >> 7);
        asm volatile("global_load_ushort %0, %6, %8\n\tglobal_load_ushort %1, %6, %9\n\tglobal_load_ushort %2, %6, %10\n\t"
                     "global_load_ushort %3, %6, %11\n\tglobal_load_ushort %4, %7, %12\n\tglobal_load_ushort %5, %7, %13"
                     : "=&v"(pr.l), "=&v"(pr.a), "=&v"(pr.r), "=&v"(pr.i), "=&v"(pr.t), "=&v"(pr.d)
                     : "v"((d * NS + (threadIdx.x & 127)) * 2), "v"(d * 2), "s"(lar), "s"(aimg), "s"(cre), "s"(cim), "s"(ldt), "s"(dsk) : "memory");
    }
    if (__builtin_expect(blockIdx.x < NB2, 1)) head_b(entry, x, wr, wi, out, ws);
    else if (blockIdx.x >= A0) head_a(u, pr, out, ws);
    else warm_code(entry);
}

extern "C" void kernel_launch(void* const* d_in, const int* in_sizes, int n_in, void* d_out, int out_size, void* d_ws,
                              size_t ws_size, hipStream_t stream) {
    (void)hipFuncSetAttribute((const void*)fused_kernel, hipFuncAttributeMaxDynamicSharedMemorySize, LDS_BYTES);
    fused_kernel<<<GRID, NT, LDS_BYTES, stream>>>((const u16*)d_in[0], (const u16*)d_in[1], (const u16*)d_in[2], (const u16*)d_in[3],
                                                  (const u16*)d_in[4], (const u16*)d_in[5], (const u16*)d_in[6], (const u16*)d_in[7],
                                                  (const u16*)d_in[8], (const u16*)d_in[9], (u16*)d_out, (char*)d_ws);
}
```

```cpp
#define POLL_SLEEP 6
#include <hip/hip_runtime.h>

typedef unsigned short u16;
typedef unsigned int u32;
typedef float f2 __attribute__((ext_vector_type(2)));
typedef float f4 __attribute__((ext_vector_type(4)));
typedef u32 u2 __attribute__((ext_vector_type(2)));
typedef u32 u4 __attribute__((ext_vector_type(4)));
typedef int i8v __attribute__((ext_vector_type(8)));
typedef short s4 __attribute__((ext_vector_type(4)));
typedef short s8 __attribute__((ext_vector_type(8)));
typedef __bf16 bf2 __attribute__((ext_vector_type(2)));
#define DI __device__ __forceinline__
#define LAS __attribute__((address_space(3)))
#define GAS __attribute__((address_space(1)))
#define CAS __attribute__((address_space(4)))
#pragma clang diagnostic ignored "-Wint-to-pointer-cast"
struct u3 { u32 v[3]; DI u32& operator[](int i) { return v[i]; } DI const u32& operator[](int i) const { return v[i]; } };

constexpr int SEQ = 2048, DM = 576, NS = 128, DF = 289;
constexpr int NB1 = 128;
constexpr int NB2 = 145;
constexpr int A0 = 152;
constexpr int NA = 96;
constexpr int GRID = A0 + NA;
constexpr int NT = 1024;

constexpr u32 WS_EPOCH = 0, WS_F1 = 4096, WS_X1 = 1u << 20, WS_Y1 = 4u << 20, WS_USED = 8u << 20;
#define STAMP(i)
#define STAMP_DECL
#define STAMP_OUT(T)

constexpr int F_SA = 0, F_EO = 4;
constexpr int F_ARE = 9, F_AIM = 10, F_BRE = 11, F_BIM = 12, F_G = 13;
constexpr int F_F16 = 21, F_GI = 23, F_AI = 31, F_M1C = 33;
constexpr int F_VAR = 37;
constexpr int F_END = F_VAR + 40;
constexpr int ET = 452, ER = 28, E2V = 580, E2U = 36, OP = 1168;
constexpr int LB_X = 0, LB_I = 0, LB_Y = 0;
constexpr int LB_E = 16 * OP;
constexpr int LB_O = LB_E, LB_E2 = LB_E;
constexpr int LB_S = LB_E + 16 * ET * 4;
constexpr int LB_W = LB_S + 1200 * 16;
constexpr int LB_T = LB_W + 2 * 256 * 36;
constexpr int LB_G = LB_T + (F_VAR + 16) * 1024;
constexpr int LB_END = LB_G + 16;
struct CosTab { double c[2048]; double c9[9], s9[9]; };
constexpr double tay_cos(double x) { double t = 1, s = 1; for (int k = 1; k <= 14; k++) { t *= -x * x / ((2 * k - 1) * (2 * k)); s += t; } return s; }
constexpr double tay_sin(double x) { double t = x, s = x; for (int k = 1; k <= 14; k++) { t *= -x * x / ((2 * k + 1) * (2 * k)); s += t; } return s; }
constexpr CosTab make_cos() {
    CosTab t = {};
    const double PI2 = 6.283185307179586476925;
    for (int k = 0; k <= 512; k++) {
        const double v = k == 512 ? 0.0 : (k <= 256 ? tay_cos(PI2 * k / 2048) : tay_sin(PI2 * (512 - k) / 2048));
        t.c[k] = v; t.c[(2048 - k) & 2047] = v; t.c[1024 - k] = -v; t.c[(1024 + k) & 2047] = -v;
    }
    t.c[512] = 0.0; t.c[1536] = 0.0;
    for (int k = 0; k < 9; k++) { const double a = PI2 * (k <= 4 ? k : k - 9) / 9; t.c9[k] = tay_cos(a); t.s9[k] = tay_sin(a); }
    return t;
}
constexpr CosTab CT = make_cos();
constexpr double cs(int n) { return CT.c[n & 2047]; }
constexpr double sn(int n) { return CT.c[(n - 512) & 2047]; }
constexpr u32 bfr(double x) { const u32 b = __builtin_bit_cast(u32, (float)x); return (b + 0x7fffu + ((b >> 16) & 1u)) >> 16; }
constexpr u32 pkc(double lo, double hi) { return bfr(lo) | (bfr(hi) << 16); }
constexpr u32 f32c(double x) { return __builtin_bit_cast(u32, (float)x); }
template <int N> struct Frags { u32 w[N * 256]; };
constexpr u32 w_re(double amp, int n) { return pkc(amp * cs(n), amp * sn(n)); }
constexpr u32 w_im(double amp, int n) { return pkc(-amp * sn(n), amp * cs(n)); }
constexpr Frags<F_VAR> make_t0() {
    Frags<F_VAR> t = {};
    const double R2 = 1.4142135623730951;
    for (int l = 0; l < 64; l++)
        for (int i = 0; i < 4; i++) {
            const int c = l & 15, g = l >> 4, r = 4 * g + i, o = l * 4 + i;
            {
                const int b = c >> 2, tq = c & 3;
                const double amp = b ? 1.0 : R2;
                const int n0 = 512 * tq * g + (b ? 256 * b * (2 * i) : -256), n1 = 512 * tq * g + (b ? 256 * b * (2 * i + 1) : 256);
                t.w[F_ARE * 256 + o] = pkc(amp * cs(n0), amp * cs(n1));
                t.w[F_AIM * 256 + o] = pkc(-amp * sn(n0), -amp * sn(n1));
            }
            for (int w = 0; w < 9; w++) {
                const int sh = 16 * (i & 1), oo = w * 128 + l * 2 + (i >> 1);
                t.w[F_EO * 256 + oo] |= (u32)((c * ET + r * ER + 2 * ((7 * w + r) % 9)) * 4) << sh;
                if (w < 8) {
                    const int k = (9 * (w & 3) + 36 * r + 64 * (c % 9)) % DM, slot = w < 4 ? (k <= 288 ? 2 * k : 2 * (DM - k) + 1) : k;
                    t.w[F_SA * 256 + oo] |= (u32)(slot * 8 + (slot >> 6) * 16) << sh;
                }
            }
            if (r < 9) { const int k = (r * c) % 9; t.w[F_BRE * 256 + o] = pkc(CT.c9[k], CT.s9[k]); t.w[F_BIM * 256 + o] = pkc(-CT.s9[k], CT.c9[k]); }
            for (int q = 0; q < 4; q++) {
                t.w[(F_G + 2 * q) * 256 + o] = w_re(1.0, 32 * r * (q + 4 * c)); t.w[(F_G + 2 * q + 1) * 256 + o] = w_im(1.0, 32 * r * (q + 4 * c));
                t.w[(F_GI + 2 * q) * 256 + o] = w_re(1.0, 32 * c * (q + 4 * r)); t.w[(F_GI + 2 * q + 1) * 256 + o] = w_im(1.0, 32 * c * (q + 4 * r));
            }
            t.w[F_F16 * 256 + o] = w_re(1.0, 128 * r * c); t.w[(F_F16 + 1) * 256 + o] = w_im(1.0, 128 * r * c);
            for (int h = 0; h < 2; h++) {
                const int j = 4 * h + (c >> 2), n = 512 * i * (c & 3) + (g ? 256 * g * j : ((j & 1) ? 256 : -256));
                t.w[(F_AI + h) * 256 + o] = w_re(g ? 2.0 : R2, n);
                const int m = 16 * r * (4 * h + 8 * c) - 512 * h;
                t.w[(F_M1C + 2 * h) * 256 + o] = pkc(0.5 * cs(m), -0.5 * sn(m));
                t.w[(F_M1C + 2 * h + 1) * 256 + o] = pkc(-0.5 * sn(m), -0.5 * cs(m));
            }
        }
    return t;
}
constexpr Frags<40> make_t1() {
    Frags<40> t = {};
    for (int v = 0; v < 5; v++)
        for (int l = 0; l < 64; l++)
            for (int i = 0; i < 4; i++) {
                const int be = v < 3 ? v + 1 : 4 * (v - 3), c = l & 15, r = 4 * (l >> 4) + i, o = v * 2048 + l * 4 + i;
                const double amp = v < 3 ? 1.0 : 0.5;
                const int n1 = 16 * r * (be + 8 * c) + (v == 4 ? 512 : 0), n4 = 16 * c * (be + 8 * r);
                t.w[o] = w_re(amp, n1); t.w[o + 256] = w_im(amp, n1);
                t.w[o + 512] = w_re(1.0, n4); t.w[o + 768] = w_im(1.0, n4);
                t.w[o + 1024] = f32c(cs(r * (be + 8 * c))); t.w[o + 1280] = f32c(-sn(r * (be + 8 * c)));
                t.w[o + 1536] = f32c(cs(c * (be + 8 * r))); t.w[o + 1792] = f32c(-sn(c * (be + 8 * r)));
            }
    return t;
}
constexpr int qidx(int l) {
    if (l <= 256) return l;
    const int sh = l < 512 ? 1 : (l < 1024 ? 2 : 3), h = 1 << (sh - 1), m = l >> sh, r = l & ((1 << sh) - 1);
    return 128 * sh + m + ((r > h || (r == h && (m & 1))) ? 1 : 0);
}
constexpr Frags<6> make_t2() {
    Frags<6> t = {};
    for (int l = 0; l < 64; l++) {
        const int c = l & 15, g = l >> 4, e = 543 - 2 * c - g;
        t.w[256 + l * 4] = (u32)((e < 518 ? qidx(e) : 643) * 4) | ((u32)((e - 1 < 518 ? qidx(e - 1) : 643) * 4) << 16);
        for (int i = 0; i < 4; i++)
            for (int h = 0; h < 2; h++) {
                const int lag = 1024 + 16 + c - 8 * g - 2 * i - h, x = 8 * g + 2 * i + h;
                t.w[l * 4 + i] |= (lag < 1027 || lag > 1035 ? 0x0c0cu : (lag < 1029 ? 0x0100u : 0x0302u)) << (16 * h);
                if (x >= c + 8 && x <= c + 16) t.w[2 * 256 + l * 4 + i] |= 0x3f80u << (16 * h);
                if (x >= c + 1 && x <= c + 7) t.w[3 * 256 + l * 4 + i] |= 0x3f80u << (16 * h);
                if (x >= c + 6 && x <= c + 10) t.w[4 * 256 + l * 4 + i] |= 0x3f80u << (16 * h);
                if (x >= c + 3 && x <= c + 5) t.w[5 * 256 + l * 4 + i] |= 0x3f80u << (16 * h);
            }
    }
    return t;
}
struct Tables { Frags<F_VAR> a; Frags<40> b; Frags<6> c; };
constexpr Frags<6> T2 = make_t2();
constexpr Frags<F_VAR> T0 = make_t0();
constexpr Frags<40> T1 = make_t1();
static __device__ const __attribute__((aligned(128))) Tables TAB = {T0, T1, T2};
constexpr int LA_G = 0;
constexpr int LA_K = LA_G + 12 * 4608;
constexpr int KI = 1344, KQ = 4 * KI, KP = KQ + 512, KX = KP + 320, KC = KX + 16;
constexpr int LA_U = LA_K + 6 * KC;
constexpr int LA_Q = LA_U + 6 * 4608;
constexpr int LA_F = LA_Q + 12 * 2576;
constexpr int LA_S = LA_F + 16;
constexpr int LA_END = LA_S + 6144;
constexpr int FU0 = 0, FU2 = 1;
constexpr int LDS_BYTES = (LA_END > LB_END ? LA_END : LB_END);
static_assert(LDS_BYTES <= 160 * 1024 && LDS_BYTES > 80 * 1024, "one workgroup per CU");

DI u32 pk(float a, float b) { f2 v = {a, b}; return __builtin_bit_cast(u32, __builtin_convertvector(v, bf2)); }
DI float blo(u32 v) { return __uint_as_float(v << 16); }
DI float bhi(u32 v) { return __uint_as_float(v & 0xffff0000u); }
DI float bf2f(u16 v) { return __uint_as_float((u32)v << 16); }
DI float rbf(float x) { return blo(pk(x, x)); }
DI f2 cmul(f2 a, f2 b) { f2 t = a.xx * b; f2 s = {-b.y, b.x}; return __builtin_elementwise_fma(a.yy, s, t); }
DI f4 mfma(u4 a, u4 b, f4 c) { return __builtin_amdgcn_mfma_f32_16x16x32_bf16(__builtin_bit_cast(s8, a), __builtin_bit_cast(s8, b), c, 0, 0, 0); }
DI u4 pack4(f4 re, f4 im) { u4 r = {pk(re[0], im[0]), pk(re[1], im[1]), pk(re[2], im[2]), pk(re[3], im[3])}; return r; }
DI u4 twid(f4 re, f4 im, f4 wr, f4 wi) { return pack4(__builtin_elementwise_fma(re, wr, -(im * wi)), __builtin_elementwise_fma(re, wi, im * wr)); }
DI int mod9(int x) { return x - 9 * ((x * 57) >> 9); }
template <class T> DI void pin(T& v) { asm volatile("" : "+v"(v)); }

template <class T> DI T lds_ld(int off) { return *(const LAS T*)(u32)off; }
template <class T> DI void lds_st(int off, T v) { *(LAS T*)(u32)off = v; }
DI void bar() { __syncthreads(); }
#define PRIO(n) __builtin_amdgcn_s_setprio(n)
#define PRIOB(n)
DI void glds16(const void* g, u32 off, int lds) {
    u32 keep;
    asm volatile("s_mov_b32 %0, m0\n\ts_mov_b32 m0, %3\n\ts_nop 4\n\tglobal_load_lds_dwordx4 %1, %2\n\ts_mov_b32 m0, %0" : "=&s"(keep) : "v"(off), "s"(g), "s"(lds) : "memory");
}

DI void publish_tag(u32 epoch) {
    u32 t;
    asm volatile("v_add_u32 %0, 0x9E3779B1, %1\n\tds_write_b32 %2, %0\n\ts_waitcnt lgkmcnt(0)" : "=&v"(t) : "v"(epoch), "v"(LB_G) : "memory");
}
DI void wait_flag(__amdgpu_buffer_rsrc_t r, u32 off, u32 tag, bool on) {
    if (on) {
        const unsigned long long t0 = __builtin_amdgcn_s_memrealtime();
        while (!__all(__builtin_amdgcn_raw_buffer_load_b32(r, off, 0, 16) == tag)) {
            if (__builtin_amdgcn_s_memrealtime() - t0 > 5000000ull) break;
            __builtin_amdgcn_s_sleep(POLL_SLEEP);
            asm volatile("" ::: "memory");
        }
    }
    asm volatile("" ::: "memory");
}

constexpr u32 POISON = 0xFFFFFFFFu;
DI bool unset(u4 v) { return max(max(v[0], v[1]), max(v[2], v[3])) == POISON; }
#define DPOLL_SLEEP 1
template <bool TWO> DI void load_set(__amdgpu_buffer_rsrc_t r, u32 off, bool on, u4& a, bool on2, u4& b) {
    const unsigned long long t0 = __builtin_amdgcn_s_memrealtime();
    bool ma = on, mb = TWO && on2;
    for (;;) {
        if (ma) a = __builtin_amdgcn_raw_buffer_load_b128(r, off, 0, 16);
        if (TWO && mb) b = __builtin_amdgcn_raw_buffer_load_b128(r, off, 16384, 16);
        ma = ma && unset(a);
        if (TWO) mb = mb && unset(b);
        if (!__any(ma || mb) || __builtin_amdgcn_s_memrealtime() - t0 > 5000000ull) break;
        __builtin_amdgcn_s_sleep(DPOLL_SLEEP);
        asm volatile("" ::: "memory");
    }
}
constexpr int CODE_LINES = 128;
DI void warm_code(const char* entry) {
    if (threadIdx.x < CODE_LINES) { const u32 t = *(const GAS u32*)(entry + threadIdx.x * 128); asm volatile("" :: "v"(t)); }
}

DI void head_b(const char* entry, const u16* __restrict__ x, const u16* __restrict__ wr, const u16* __restrict__ wi,
               u16* __restrict__ out, char* ws) {
    const int tid = threadIdx.x, lane = tid & 63, c16 = lane & 15, g = lane >> 4;
    const int wv = __builtin_amdgcn_readfirstlane(tid >> 6);
    const int id = blockIdx.x;
    STAMP_DECL;
    const bool rows = id < NB1;
    const int s00 = 32 * (id >> 4) + (id & 15);
    const char* tab = (const char*)&TAB;
    const int R = (tid * 911) >> 16;
    const u32 xoff = (s00 + 16 * (R >> 3) + 256 * (R & 7)) * (2 * DM) + (tid - R * 72) * 16;
    asm volatile("" :: "s"(x), "s"(wr), "s"(wi), "s"(out), "s"(ws), "v"(xoff));
    const __amdgpu_buffer_rsrc_t rws = __builtin_amdgcn_make_buffer_rsrc(ws, 0, WS_USED, 0x00020000);
    STAMP(13);
    int gy = WS_Y1 + id * (NB2 * 128) + tid * 16;
    u32 epoch = 0;
    if (wv == 0) asm volatile("global_load_dword %0, %0, %1 sc1" : "+v"(epoch) : "s"(ws) : "memory");
    if (rows) {
        glds16(x, xoff, LB_X + wv * 1024);
        if (wv < 2) glds16(x, (s00 + 16 + 1536) * (2 * DM) + (tid < 56 ? (tid + 16) * 16 : 256 * 2 * DM + (tid - 56) * 16), LB_X + 16384 + wv * 1024);
        else glds16(tab, tid * 16 - 2048, LB_T + (wv - 2) * 1024);
        if (wv >= 9) glds16(tab, tid * 16 + 5 * 1024, LB_T + (wv + 5) * 1024);
        const u4 P = {POISON, POISON, POISON, POISON};
        __builtin_amdgcn_raw_buffer_store_b128(P, rws, gy, 0, 16);
        if (tid <= 128) __builtin_amdgcn_raw_buffer_store_b128(P, rws, gy, 16384, 16);
    }
    const int u2i = (id & 7) ? (id & 7) * 18 + 1 + (id >> 3) : (id == 136 ? 0 : (id >> 3) + (id < 136 ? 1 : 0));
    const bool star = u2i >= 108;
    const int q36 = min((u2i * 57) >> 11, 3), kg = u2i - 36 * q36, bq = (q36 + 1) & 3;
    const bool mir = !star && kg >= 18;
    const int w0 = star ? min(16 * kg, 562) : (mir ? (2048 - bq) * (2 * DF) + 1122 - 32 * kg : bq * (2 * DF) + 32 * kg);
    __builtin_amdgcn_s_barrier();
    STAMP(14);
    const u32 woff = w0 + ((tid >> 1) & 255) * (mir ? -16 * DF : 16 * DF) + (tid & 1) * (star ? 8 * DF : 16);
    u4 wra, wrb;
    if (wv >= 8) asm volatile("global_load_dwordx4 %0, %2, %3\n\tglobal_load_dwordx4 %1, %2, %4" : "=&v"(wra), "=&v"(wrb) : "v"(woff), "s"(wr), "s"(wi) : "memory");
#define STAGE23 do { const int o = LB_W + ((tid >> 1) & 255) * 36 + (tid & 1) * 16, t16 = (tid - 512) * 16, lw = LB_T + (wv - 8) * 1024; \
        glds16(tab, F_F16 * 1024 + t16, lw + F_F16 * 1024); glds16(tab, (F_F16 + 8) * 1024 + t16, lw + (F_F16 + 8) * 1024); \
        glds16(tab, (F_VAR + 8 * q36) * 1024 + t16, lw + F_VAR * 1024); if (star) glds16(tab, (F_VAR + 32) * 1024 + t16, lw + (F_VAR + 8) * 1024); \
        asm volatile("s_waitcnt vmcnt(0)\n\tds_write2_b32 %0, %2, %3 offset1:1\n\tds_write2_b32 %0, %4, %5 offset0:2 offset1:3\n\t" \
                     "ds_write2_b32 %1, %6, %7 offset1:1\n\tds_write2_b32 %1, %8, %9 offset0:2 offset1:3\n\ts_waitcnt lgkmcnt(0)" \
                     :: "v"(o), "v"(o + 9216), "v"(wra[0]), "v"(wra[1]), "v"(wra[2]), "v"(wra[3]), "v"(wrb[0]), "v"(wrb[1]), "v"(wrb[2]), "v"(wrb[3]) : "memory"); } while (0)
    const f4 Z = {0.f, 0.f, 0.f, 0.f};
    const int tau = wv & 3, bw = wv >> 2;
    int tl = LB_T + lane * 16;
    int lin = LB_S + (tid + (tid >> 5)) * 16;
    pin(tl); pin(lin);
    int sadr[4];

    u32 tag;
    if (rows) {
        if (tid < 256) {
            const int o = LB_E + 72 + (tid & 15) * (ER * 4) + (tid >> 4) * (ET * 4);
            lds_st<u2>(o, u2{0u, 0u}); lds_st<u4>(o + 8, u4{0u, 0u, 0u, 0u});
        }
        int xo = LB_X + (c16 >> 2) * 1152 + 2 * (144 * ((g - wv) & 3) + 16 * wv + 4 * (c16 & 3));
        int bo = LB_E + (wv * ET + c16 * ER + min(8 * g, 16)) * 4;
        int go = WS_X1 + (tid >> 3) * 16384 + id * 128 + (tid & 7) * 16;
        int ta = LB_T + lane * 8;
        pin(xo); pin(bo); pin(go); pin(ta);
        STAMP(1);
        asm volatile("s_cmp_lt_u32 %0, 8\n\ts_cbranch_scc0 .Lw%=\n\ts_waitcnt vmcnt(0)\n.Lw%=:\n\ts_waitcnt vmcnt(2)" :: "s"(wv) : "memory", "scc");
        if (wv == 0) publish_tag(epoch);
        bar();
        STAMP(2);
        if (wv < 9) {
            u4 d[2];
#pragma unroll
            for (int s = 0; s < 2; s++) {
                const s4 lo = __builtin_amdgcn_ds_read_tr16_b64_v4i16((LAS s4*)(u32)(xo + s * 9216));
                const s4 hi = __builtin_amdgcn_ds_read_tr16_b64_v4i16((LAS s4*)(u32)(xo + s * 9216 + 4608));
                d[s] = __builtin_bit_cast(u4, __builtin_shufflevector(lo, hi, 0, 1, 2, 3, 4, 5, 6, 7));
            }
            const u4 are = lds_ld<u4>(tl + F_ARE * 1024), aim = lds_ld<u4>(tl + F_AIM * 1024);
            const u2 eo = lds_ld<u2>(ta + F_EO * 1024 + wv * 512);
#pragma unroll
            for (int s = 0; s < 2; s++) d[s] = pack4(mfma(d[s], are, Z), mfma(d[s], aim, Z));
#pragma unroll
            for (int i = 0; i < 4; i++) lds_st<u2>(LB_E + ((i & 1) ? eo[i >> 1] >> 16 : eo[i >> 1] & 0xffff), u2{d[0][i], d[1][i]});
        }
        u4 bre = lds_ld<u4>(tl + F_BRE * 1024), bim = lds_ld<u4>(tl + F_BIM * 1024);
        u4 gre = lds_ld<u4>(tl + (F_G + 2 * tau) * 1024), gim = lds_ld<u4>(tl + (F_G + 2 * tau + 1) * 1024);
        {
            const u2 sa = lds_ld<u2>(ta + F_SA * 1024 + (tau + (bw ? 4 : 0)) * 512);
            const int base = LB_S + ((bw + 3) & 3) * (DM * 8 + 144);
#pragma unroll
            for (int i = 0; i < 4; i++) { sadr[i] = base + ((i & 1) ? sa[i >> 1] >> 16 : sa[i >> 1] & 0xffff); pin(sadr[i]); }
        }
        tag = lds_ld<u32>(LB_G);
        pin(bre); pin(bim); pin(gre); pin(gim); pin(tag);
        bar();
        {
            PRIOB(2);
            const u4 p0 = lds_ld<u4>(bo), p1 = lds_ld<u4>(bo + 16);
            u4 d[2];
            f4 re[2], im[2];
#pragma unroll
            for (int s = 0; s < 2; s++) { const u4 a = {p0[s], p0[2 + s], p1[s], p1[2 + s]}; re[s] = mfma(a, bre, Z); im[s] = mfma(a, bim, Z); }
            PRIOB(1);
#pragma unroll
            for (int s = 0; s < 2; s++) { const u4 t = pack4(re[s], im[s]); re[s] = mfma(gre, t, Z); im[s] = mfma(gim, t, Z); }
            PRIOB(0);
#pragma unroll
            for (int s = 0; s < 2; s++) d[s] = pack4(re[s], im[s]);
            if (c16 < 9) {
#pragma unroll
                for (int i = 0; i < 4; i++) lds_st<u2>(sadr[i], u2{d[0][i], d[1][i]});
                if (wv == 0 && c16 == 0 && !(g & 1)) lds_st<u2>(sadr[0] + 8, u2{d[0][0], d[1][0]});
            }
        }
        bar();
        STAMP(3);
        if (wv < 8) {
            u4 v0 = lds_ld<u4>(lin), v1 = lds_ld<u4>(lin + 528 * 16), v2 = lds_ld<u4>(lin + (wv < 3 ? 1056 * 16 : 0));
            pin(v2);
            __builtin_amdgcn_raw_buffer_store_b128(v0, rws, go, 0, 16);
            __builtin_amdgcn_raw_buffer_store_b128(v1, rws, go, 64 * 16384, 16);
            if (tid <= 128) __builtin_amdgcn_raw_buffer_store_b128(v2, rws, go, 128 * 16384, 16);
            asm volatile("s_waitcnt vmcnt(0)" ::: "memory");
            if (lane == 0) __builtin_amdgcn_raw_buffer_store_b32(tag, rws, WS_F1 + (wv * 16 + (id >> 3)) * 128 + (id & 7) * 4, 0, 16);
            STAMP(4);
        } else {
            STAGE23;
        }
        bar();
    } else {
        if (wv < 8 && (kg == 0 || kg == (star ? 36 : 18))) {
            const int m = tid & 255;
            lds_st<u16>(LB_W + tid * 36 + 32, ((wv >> 2) ? wi : wr)[((mir ? bq + 8 * m : 2048 - bq - 8 * m) & 2047) * DF + (kg ? 288 : 0)]);
        }
        warm_code(entry);
        if (wv >= 8) STAGE23;
        asm volatile("s_waitcnt vmcnt(0)" ::: "memory");
        if (wv == 0) publish_tag(epoch);
        bar();
        tag = lds_ld<u32>(LB_G);
    }
    STAMP(12);

    const int typ = star ? (wv & 1) : 0;
    const int tv = tl + (F_VAR + 8 * typ) * 1024;
    u4 fre = lds_ld<u4>(tl + F_F16 * 1024), fim = lds_ld<u4>(tl + (F_F16 + 1) * 1024);
    u4 m1r = lds_ld<u4>(tv), m1i = lds_ld<u4>(tv + 1024), m4r = lds_ld<u4>(tv + 2048), m4i = lds_ld<u4>(tv + 3072);
    f4 twr = lds_ld<f4>(tv + 4096), twi = lds_ld<f4>(tv + 5120), tur = lds_ld<f4>(tv + 6144), tui = lds_ld<f4>(tv + 7168);
    u4 mcr = lds_ld<u4>(tl + (F_M1C + 2 * typ) * 1024), mci = lds_ld<u4>(tl + (F_M1C + 2 * typ + 1) * 1024);
    f4 wfr, wfi;
    {
        const int kcol = star ? 8 * kg + (wv >> 1) : 16 * kg + wv;
        const int cc = star ? 8 * typ + (u2i < NB2 - 1 ? (wv >> 1) : 7) : (mir ? 15 - wv : wv);
        const int wo = LB_W + (c16 + 64 * g) * 36 + cc * 2;
#pragma unroll
        for (int i = 0; i < 4; i++) {
            wfr[i] = bf2f(lds_ld<u16>(wo + i * 576));
            wfi[i] = bf2f(lds_ld<u16>(wo + i * 576 + 9216));
        }
        float sc = 1.f / (float)(SEQ * DM);
        if (kcol == 0 || kcol == 288) {
            sc *= 0.5f;
#pragma unroll
            for (int i = 0; i < 4; i++) {
                const int m = c16 + 64 * g + 16 * i;
                const int o = LB_W + (typ ? (255 - m) * 36 + cc * 2 : m * 36 + 32);
                wfr[i] += bf2f(lds_ld<u16>(o));
                wfi[i] -= bf2f(lds_ld<u16>(o + 9216));
            }
        }
        wfr *= sc; wfi *= (mir ? -sc : sc);
    }
    f4 wnr = -wfr, wni = -wfi;
    int io = (32 * g + c16) * 128 + (((wv >> 1) ^ (c16 >> 1)) * 16);
    int so = (tid >> 3) * 128 + (((tid & 7) ^ ((tid >> 4) & 7)) * 16);
    pin(fre); pin(fim); pin(m1r); pin(m1i); pin(m4r); pin(m4i); pin(twr); pin(twi); pin(tur); pin(tui); pin(mcr); pin(mci);
    int gi = WS_X1 + u2i * 16384 + tid * 16, gq = WS_Y1 + (tid >> 3) * (NB2 * 128) + u2i * 128 + (tid & 7) * 16;
    int i1 = LB_I + io + (star ? 0 : (wv & 1) * 8);
    io += LB_O + (wv & 1) * 8;
    pin(wfr); pin(wnr); pin(wni); pin(io); pin(so); pin(gi); pin(gq); pin(i1);
    wait_flag(rws, WS_F1 + (((u2i >> 3) & 7) * 16 + wv) * 128 + lane * 4, tag, lane < 8);
    STAMP(5);
    lds_st<u4>(LB_I + so, __builtin_amdgcn_raw_buffer_load_b128(rws, gi, 0, 16));
    bar();
    STAMP(6);
    {
        const int o1 = i1;
        PRIOB(3);
        const u2 a0 = lds_ld<u2>(o1), a1 = lds_ld<u2>(o1 + 2048);
        const u4 a = {a0[0], a0[1], a1[0], a1[1]};
        f4 re = mfma(a, m1r, Z), im = mfma(a, m1i, Z);
        if (star) {
            const u2 b0 = lds_ld<u2>(o1 + 8), b1 = lds_ld<u2>(o1 + 8 + 2048);
            const u4 b = {b0[0], b0[1], b1[0], b1[1]};
            re = mfma(b, mcr, re); im = mfma(b, mci, im);
        }
        u4 t = twid(re, im, twr, twi);
        re = mfma(fre, t, Z); im = mfma(fim, t, Z);
        PRIOB(2);
        t = pack4(__builtin_elementwise_fma(re, wfr, im * wni), __builtin_elementwise_fma(re, wni, im * wnr));
        re = mfma(t, fre, Z); im = mfma(t, fim, Z);
        PRIOB(1);
        t = twid(re, im, tur, tui);
        re = mfma(m4r, t, Z); im = mfma(m4i, t, Z);
        PRIOB(0);
        t = pack4(re, im);
        lds_st<u2>(io, u2{t[0], t[1]}); lds_st<u2>(io + 2048, u2{t[2], t[3]});
    }
    bar();
    STAMP(7);
    u4 sv;
    {
        u4 v = lds_ld<u4>(LB_O + so);
        if (star) {
            u4 w;
#pragma unroll
            for (int s = 0; s < 2; s++) {
                const float ar = blo(v[s]), ai = bhi(v[s]), cr = blo(v[2 + s]), ci = bhi(v[2 + s]);
                w[s] = pk(ar + ci, ai - cr); w[2 + s] = pk(ar - ci, -ai - cr);
            }
            v = w;
        }
        if (u2i < NB2 - 1 || (tid & 7) == 0)
            __builtin_amdgcn_raw_buffer_store_b128(v, rws, gq, 0, 16);
        sv = v;
    }
    STAMP(8);
    if (!rows) { STAMP_OUT(0); return; }

    u4 gre = lds_ld<u4>(tl + (F_GI + 2 * tau) * 1024), gim = lds_ld<u4>(tl + (F_GI + 2 * tau + 1) * 1024);
    u4 bre = lds_ld<u4>(tl + F_BRE * 1024), bim = lds_ld<u4>(tl + F_BIM * 1024);
    u4 ai0 = lds_ld<u4>(tl + F_AI * 1024), ai1 = lds_ld<u4>(tl + (F_AI + 1) * 1024);
    int eadr[4];
#pragma unroll
    for (int i = 0; i < 4; i++) {
        const int r = 4 * g + i;
        eadr[i] = LB_E2 + (mod9(4 * (c16 + 18 - r)) * E2V + r * E2U + 2 * wv) * 4;
        pin(eadr[i]);
    }
    int ao = LB_E2 + (wv * E2V + c16 * E2U + 8 * g) * 4;
    int yo = LB_Y + (c16 >> 2) * OP + 2 * (144 * (((c16 & 3) - wv) & 3) + 16 * wv + 4 * g);
    int yl = LB_Y + tid * 16 + R * (OP - 1152);
    u32 oo = 2 * xoff - (tid - R * 72) * 16 + 2 * DM;
    pin(gre); pin(gim); pin(bre); pin(bim); pin(ai0); pin(ai1); pin(ao); pin(gy); pin(yo); pin(yl); pin(oo);
    asm volatile("" :: "v"(sv));
    STAMP(9);
    {
        u4 v, w;
        if (wv < 3) { load_set<true>(rws, gy, true, v, tid <= 128, w); if (tid <= 128) lds_st<u4>(lin + 1056 * 16, w); }
        else load_set<false>(rws, gy, true, v, false, w);
        lds_st<u4>(lin, v);
    }
    bar();
    if (id == 0 && tid == 0) __builtin_amdgcn_raw_buffer_store_b32(tag, rws, WS_EPOCH, 0, 16);
    STAMP(10);
    {
        PRIOB(2);
        u4 d[2];
        u2 p[4];
#pragma unroll
        for (int i = 0; i < 4; i++) p[i] = lds_ld<u2>(sadr[i]);
        f4 re[2], im[2];
#pragma unroll
        for (int s = 0; s < 2; s++) { const u4 a = {p[0][s], p[1][s], p[2][s], p[3][s]}; re[s] = mfma(a, gre, Z); im[s] = mfma(a, gim, Z); }
        PRIOB(1);
#pragma unroll
        for (int s = 0; s < 2; s++) { const u4 t = pack4(re[s], im[s]); re[s] = mfma(t, bre, Z); im[s] = mfma(t, bim, Z); }
        PRIOB(0);
#pragma unroll
        for (int s = 0; s < 2; s++) d[s] = pack4(re[s], im[s]);
        if (c16 < 9) {
#pragma unroll
            for (int i = 0; i < 4; i++) lds_st<u2>(eadr[i], u2{d[0][i], d[1][i]});
        }
    }
    STAMP(15);
    bar();
    if (wv < 9) {
        const u4 p0 = lds_ld<u4>(ao), p1 = lds_ld<u4>(ao + 16);
#pragma unroll
        for (int s = 0; s < 2; s++) {
            const u4 a = {p0[s], p0[2 + s], p1[s], p1[2 + s]};
            const f4 y0 = mfma(a, ai0, Z), y1 = mfma(a, ai1, Z);
            lds_st<u2>(yo + s * (8 * OP), u2{pk(y0[0], y0[1]), pk(y0[2], y0[3])});
            lds_st<u2>(yo + s * (8 * OP) + 4 * OP, u2{pk(y1[0], y1[1]), pk(y1[2], y1[3])});
        }
    }
    bar();
    *(GAS u4*)((char*)out + oo) = lds_ld<u4>(yl);
    if (wv < 2) {
        const int r2 = tid < 56 ? 14 : 15, c2 = tid + 1024 - 72 * r2;
        *(GAS u4*)((char*)out + (u32)((s00 + 16 + 256 * (r2 - 8)) * (4 * DM) + 2 * DM + c2 * 16)) = lds_ld<u4>(LB_Y + r2 * OP + c2 * 16);
    }
    STAMP(11);
    STAMP_OUT(0);
}

DI f2 cexpa(float ar2, float air, float p) {
    const float e = __builtin_amdgcn_exp2f(ar2 * p), t = __builtin_amdgcn_fractf(air * p);
    return f2{e * __builtin_amdgcn_cosf(t), e * __builtin_amdgcn_sinf(t)};
}
DI u4 ld_k(int o) {
    const u2 a = *(const volatile LAS u2*)(u32)o, b = *(const volatile LAS u2*)(u32)(o + 8);
    return u4{a[0], a[1], b[0], b[1]};
}
DI void a_rows_out(int yb, int ystr, char* o) {
    u2 y[6];
#pragma unroll
    for (int c = 0; c < 6; c++) y[c] = lds_ld<u2>(yb + c * ystr);
#pragma unroll
    for (int r = 0; r < 4; r++) {
        u3 v;
#pragma unroll
        for (int c = 0; c < 3; c++) v[c] = __builtin_amdgcn_perm(y[2 * c + 1][r >> 1], y[2 * c][r >> 1], (r & 1) ? 0x07060302u : 0x05040100u);
        *(u3*)(o + r * (4 * DM)) = v;
    }
}
template <int R> DI void stage(u4 v) {
    asm volatile("ds_write_addtid_b32 %0 offset:%4\n\tds_write_addtid_b32 %1 offset:%5\n\tds_write_addtid_b32 %2 offset:%6\n\tds_write_addtid_b32 %3 offset:%7"
                 :: "v"(v[0]), "v"(v[1]), "v"(v[2]), "v"(v[3]), "i"(288 * R), "i"(288 * R + 288), "i"(288 * R + 576), "i"(288 * R + 864) : "memory");
}
#define CM_MUL(d, a, b) "v_pk_mul_f32 " d ", " a ", " b " op_sel_hi:[0,1]\n\t"
#define CM_FMA(d, a, b) "v_pk_fma_f32 " d ", " a ", " b ", " d " op_sel:[1,1,0] op_sel_hi:[1,0,1] neg_lo:[1,0,0]\n\t"
struct C4 { f2 a, b, c, d; };
DI C4 cmul4(f2 a0, f2 b0, f2 a1, f2 b1, f2 a2, f2 b2, f2 a3, f2 b3) {
    C4 r;
    asm("s_nop 0\n\t" CM_MUL("%0", "%4", "%5") CM_MUL("%1", "%6", "%7") CM_MUL("%2", "%8", "%9") CM_MUL("%3", "%10", "%11")
        CM_FMA("%0", "%4", "%5") CM_FMA("%1", "%6", "%7") CM_FMA("%2", "%8", "%9") CM_FMA("%3", "%10", "%11") "s_nop 0"
        : "=&v"(r.a), "=&v"(r.b), "=&v"(r.c), "=&v"(r.d) : "v"(a0), "v"(b0), "v"(a1), "v"(b1), "v"(a2), "v"(b2), "v"(a3), "v"(b3));
    return r;
}
DI C4 cmul4(const C4& a, f2 b) { return cmul4(a.a, b, a.b, b, a.c, b, a.d, b); }
DI u4 pk4(const C4& v) { return u4{pk(v.a.x, v.a.y), pk(v.b.x, v.b.y), pk(v.c.x, v.c.y), pk(v.d.x, v.d.y)}; }

DI void sig(int f) {
    asm volatile("s_waitcnt lgkmcnt(0)" ::: "memory");
    if ((threadIdx.x & 63) == 0) __hip_atomic_fetch_add((LAS u32*)(u32)(LA_F + 4 * f), 1u, __ATOMIC_RELAXED, __HIP_MEMORY_SCOPE_WORKGROUP);
}
template <int SLEEP = 0> DI void await(int f, u32 n) {
    while ((u32)__builtin_amdgcn_readfirstlane(*(const volatile LAS u32*)(u32)(LA_F + 4 * f)) < n) if (SLEEP) __builtin_amdgcn_s_sleep(SLEEP);
    asm volatile("" ::: "memory");
}
DI float exp_bf(float x) {
    const float t = x * 1.44269504f, r = __builtin_fmaf(x, 1.92596299e-8f, __builtin_fmaf(x, 1.44269504f, -t));
    const float e = __builtin_amdgcn_exp2f(t);
    return __builtin_fmaf(e * 0.693147181f, r, e);
}

struct APar { u32 l, a, r, i, t, d; };
DI void head_a(const u16* __restrict__ u, APar pr, u16* __restrict__ out, char* ws) {
    const int tid = threadIdx.x, lane = tid & 63, c16 = lane & 15, g = lane >> 4;
    const int wv = __builtin_amdgcn_readfirstlane(tid >> 6);
    STAMP_DECL;
    const int ai_ = blockIdx.x - A0;
    const int d0 = 6 * ((ai_ & 7) * 12 + (ai_ >> 3));
    const f4 Z = {0.f, 0.f, 0.f, 0.f};

    if (wv >= 12) {
        __builtin_amdgcn_s_setprio(3);
        const int t = tid - 768;
        if (t == 0) lds_st<u2>(LA_F, u2{0u, 0u});
        if (t < 180) lds_st<u4>(LA_U + (t / 30) * 4608 + (t % 30) * 16, u4{0u, 0u, 0u, 0u});
        bar();
        u3 ua[4], ub[4];
#pragma unroll
        for (int r = 0; r < 4; r++) ua[r] = *(const u3*)(u + (4 * t + r) * DM + d0);
        glds16((const char*)&TAB + F_END * 1024, t * 16, LA_S + (wv - 12) * 1024);
        if (wv < 14) glds16((const char*)&TAB + (F_END + 4) * 1024, t * 16, LA_S + (wv - 8) * 1024);
#pragma unroll
        for (int c = 0; c < 6; c++) {
            const u32 sel = (c & 1) ? 0x07060302u : 0x05040100u;
            lds_st<u2>(LA_U + c * 4608 + 480 + t * 8, u2{__builtin_amdgcn_perm(ua[1][c >> 1], ua[0][c >> 1], sel), __builtin_amdgcn_perm(ua[3][c >> 1], ua[2][c >> 1], sel)});
        }
        asm volatile("" ::: "memory");
#pragma unroll
        for (int r = 0; r < 4; r++) ub[r] = *(const u3*)(u + (1024 + 4 * t + r) * DM + d0);
        STAMP(7);
        asm volatile("s_waitcnt vmcnt(4)" ::: "memory");
        bar();
        bar();
#pragma unroll
        for (int c = 0; c < 6; c++) {
            const u32 sel = (c & 1) ? 0x07060302u : 0x05040100u;
            lds_st<u2>(LA_U + c * 4608 + 480 + 2048 + t * 8, u2{__builtin_amdgcn_perm(ub[1][c >> 1], ub[0][c >> 1], sel), __builtin_amdgcn_perm(ub[3][c >> 1], ub[2][c >> 1], sel)});
        }
        sig(FU2);
        STAMP(8);
        const int tile = wv - 12, yl = ((lane >> 2) + 16 * (lane & 3)) * 8;
        char* o = (char*)(out + 4 * t * (2 * DM) + d0);
        bar();
        bar();
        STAMP(11);
        a_rows_out(LA_G + ((tile >> 1) ^ 1) * 4608 + (tile & 1) * 1024 + yl, 9216, o);
        STAMP(9);
        bar();
        bar();
        STAMP(12);
        a_rows_out(LA_G + ((tile >> 1) ^ 1) * 4608 + (tile & 1) * 1024 + 2048 + yl, 9216, o + 1024 * (4 * DM));
        STAMP(10);
        STAMP_OUT(768);
        return;
    }
    const int ch = wv >> 1, hf = wv & 1;
    {
        __builtin_amdgcn_s_barrier();
        asm volatile("s_waitcnt vmcnt(0)" : "+v"(pr.l), "+v"(pr.a), "+v"(pr.r), "+v"(pr.i), "+v"(pr.t), "+v"(pr.d) :: "memory");
        PRIO(3);
        const float dt = rbf(exp_bf(blo(pr.t)));
        const float ar2 = -rbf(exp_bf(blo(pr.l))) * dt * 1.44269504f, air = blo(pr.a) * dt * 0.159154943f;
        const f2 C = {2.f * dt * blo(pr.r), 2.f * dt * blo(pr.i)};
        STAMP(1);
        const f2 z1 = cexpa(ar2, air, 1.f), z8c = cexpa(ar2, -air, 8.f), z128 = cexpa(ar2, air, 128.f);
        const int gr = LA_G + wv * 4608 + c16 * 288 + g * 16, q = LA_Q + wv * 2576;
        asm volatile("s_mov_b32 m0, %0\n\ts_nop 0" :: "s"(LA_G + wv * 4608) : "memory");
#define SB __builtin_amdgcn_sched_barrier(0)
#define STAGE16(v) do { stage<0>(v[0]); stage<4>(v[1]); stage<8>(v[2]); stage<12>(v[3]); } while (0)
#define READ4(f) do { _Pragma("unroll") for (int s = 0; s < 4; s++) f[s] = lds_ld<u4>(gr + 64 * s); SB; } while (0)
        u4 v[4], bfr[4], afr[4];
        const C4 s1 = cmul4(z8c, z8c, z1, z1, z128, z128, C, z1);
        const C4 s2 = cmul4(s1.a, s1.a, z8c, s1.a, s1.c, s1.c, C, s1.b);
        {
            C4 w = {f2{1.f, 0.f}, z8c, s1.a, s2.b};
            v[0] = pk4(w);
#pragma unroll
            for (int i = 1; i < 4; i++) { w = cmul4(w, s2.a); v[i] = pk4(w); }
        }
        STAGE16(v); READ4(bfr);
        PRIO(2);
        const C4 s3 = cmul4(s1.d, s1.b, s1.b, s1.b, s2.c, s2.c, s2.c, z128);
        const C4 e0 = {C, s1.d, s2.d, s3.a}, e1 = cmul4(e0, s3.b);
        v[0] = pk4(e0); v[1] = pk4(e1); v[2] = pk4(cmul4(e0, z128)); v[3] = pk4(cmul4(e1, z128));
        STAGE16(v); READ4(afr);
        PRIO(1);
        {
            const C4 f = cmul4(C4{e0.a, e0.c, e1.a, e1.c}, s1.c), h = cmul4(e0.a, s2.c, e1.a, s2.c, e0.a, s3.d, e1.a, s3.d);
            v[0] = pk4(f); v[1] = pk4(cmul4(f, z128)); v[2] = pk4(h); v[3] = pk4(cmul4(h, s1.c));
        }
        SB;
        f4 acc = Z;
#pragma unroll
        for (int s = 0; s < 4; s++) acc = mfma(afr[s], bfr[s], acc);
        STAGE16(v); READ4(afr);
        PRIO(0);
        lds_st<f4>(q + (4 * (g & 1) + 128 * (g >> 1) + 8 * c16) * 4, acc);
        {
            const f2 f0 = cmul(C, s3.c), f1 = cmul(f0, z128);
            const C4 f = cmul4(f0, s1.c, f1, s1.c, f0, s3.c, f0, s3.c), h = cmul4(C4{f0, f1, f.a, f.b}, s2.c);
            v[0] = pk4(C4{f0, f1, f.a, f.b}); v[1] = pk4(h); v[2][0] = pk(f.c.x, f.c.y);
        }
        SB;
        acc = Z;
#pragma unroll
        for (int s = 0; s < 4; s++) acc = mfma(afr[s], bfr[s], acc);
        stage<0>(v[0]); stage<4>(v[1]);
        asm volatile("ds_write_addtid_b32 %0 offset:%1" :: "v"(v[2][0]), "i"(288 * 8) : "memory");
        READ4(afr);
        if (g < 2) lds_st<f4>(q + (256 + 64 * g + 4 * c16) * 4, acc);
        else {
            const int o = q + (384 + 64 * (g - 2) + 2 * c16) * 4;
            lds_st<f2>(o, f2{acc[0], acc[1]}); lds_st<f2>(o + 128, f2{acc[2], acc[3]});
        }
        acc = Z;
#pragma unroll
        for (int s = 0; s < 4; s++) acc = mfma(afr[s], bfr[s], acc);
        if (g < 2) {
#pragma unroll
            for (int i = 0; i < 4; i++) lds_st<float>(q + (512 + 64 * g + 16 * i + c16) * 4, acc[i]);
        } else if (lane < 34) lds_st<float>(q + (lane == 32 ? 640 : 643) * 4, lane == 32 ? acc[0] : 0.f);
    }
    STAMP(2);
    asm volatile("s_waitcnt lgkmcnt(0)" ::: "memory");
    bar();
    STAMP(9);

    const int k0 = LA_K + ch * KC;
    {
        const LAS float* qa = (const LAS float*)(u32)(LA_Q + ch * 5152);
        const LAS float* qb = qa + 644;
        u2 w[4];
        int y;
        if (hf == 0) {
            const u32 ix = lds_ld<u32>(LA_S + 1024 + lane * 16);
            f4 k = *(const LAS f4*)(qa + 4 * lane) + *(const LAS f4*)(qb + 4 * lane);
            f4 p = *(const LAS f4*)(qa + 4 * lane - 4) + *(const LAS f4*)(qb + 4 * lane - 4);
            const f2 s = *(const LAS f2*)(qa + 512 - 2 * lane) + *(const LAS f2*)(qb + 512 - 2 * lane);
            const float e0 = qa[(ix & 0xffff) >> 2] + qb[(ix & 0xffff) >> 2], e1 = qa[ix >> 18] + qb[ix >> 18];
            lds_st<u32>(k0 + KQ + (32 + lane) * 4, pk(s.x, lane ? s.y : 0.f));
            if (lane == 0) lds_st<u32>(k0 + KX, pk(s.x, s.y));
            lds_st<u32>(k0 + KQ + (lane + (lane & 32) * 2) * 4, 0u);
            lds_st<u32>(k0 + g * KI + c16 * 4, pk(e0, e1));
            if (lane == 0) { k[0] += blo(pr.d); p = Z; }
            const u32 c = pk(k[1], k[0]), d = pk(k[0], p[3]);
            w[0] = u2{pk(k[3], k[2]), c}; w[1] = u2{pk(k[2], k[1]), d}; w[2] = u2{c, pk(p[3], p[2])}; w[3] = u2{d, pk(p[2], p[1])};
            y = k0 + (270 - 2 * lane) * 4;
        } else {
            const int j = 256 + 2 * lane;
            const float q0 = qa[j] + qb[j], q1 = qa[j + 1] + qb[j + 1], q2 = qa[j + 2] + qb[j + 2];
            const float p1 = qa[j - 1] + qb[j - 1], p2 = qa[j - 2] + qb[j - 2], p3 = qa[j - 3] + qb[j - 3];
            const u32 c = pk(q0, q0), d = pk(q0, p1), e = pk(p1, p2), f = lane ? c : d;
            w[0] = u2{pk(q2, q1), c}; w[1] = u2{pk(q1, q0), f}; w[2] = u2{c, lane ? d : e}; w[3] = u2{f, lane ? e : pk(p2, p3)};
            y = k0 + (142 - 2 * lane) * 4;
            if (lane < 48) lds_st<u32>(k0 + (lane < 32 ? (lane >> 3) * KI + (272 + (lane & 7)) * 4 : KP + (32 + lane) * 4), 0u);
            const f2 s = *(const LAS f2*)(qa + 640 - 2 * lane) + *(const LAS f2*)(qb + 640 - 2 * lane);
            lds_st<u32>(k0 + KP + lane * 4, pk(s.x, lane ? s.y : 0.f));
        }
#pragma unroll
        for (int i = 0; i < 4; i++) lds_st<u2>(y + i * KI, w[i]);
    }
    const int x0 = 2031 - c16 + 8 * g;
    int ka = k0 + (x0 & 3) * KI + (x0 >> 2) * 8 - 3008 - 256 * hf - 192 - 512;
    int ubs = LA_U + ch * 4608 + 480 + (16 * c16 + 8 * g - 16) * 2 - 256 * hf - 192;
    pin(ka); pin(ubs);
    const int lt = LA_S + lane * 16;
    const u4 bnd5 = lds_ld<u4>(lt + 4096), bnd3 = lds_ld<u4>(lt + 5120);
    int uv = LA_U + ch * 4608 + 480 + (8 * c16 + 8 * g - 24) * 2 + 256 * hf;
    pin(uv);
#define VSUM(n) ({ const u4 a_ = lds_ld<u4>(uv + 512 * (n)); pack4(mfma(a_, bnd5, Z), mfma(a_, bnd3, Z)); })
    u4 vs[4], ws0, ws1, ws2;
    {
        const u4 bnd9 = lds_ld<u4>(lt + 2048), bnd7 = lds_ld<u4>(lt + 3072);
        const int uw = LA_U + ch * 4608 + 480 + (16 * c16 + 8 * g - 28) * 2;
#define WSUM(I) ({ const u4 a_ = ld_k(uw + 512 * (I)); pack4(mfma(a_, bnd9, Z), mfma(a_, bnd7, Z)); })
#pragma unroll
        for (int n = 0; n < 4; n++) vs[n] = VSUM(n);
        ws0 = WSUM(hf);
        if (hf) { ws1 = WSUM(2); ws2 = WSUM(3); }
    }
    STAMP(13);
    bar();
    STAMP(3);
#define KFR(rho, p) ld_k(ka + 192 - 64 * (rho) + 512 - 512 * (p))
#define UFR(rho, q) lds_ld<u4>(ubs + 192 - 64 * (rho) + 512 * (q))
    f4 acc[4];
#pragma unroll
    for (int i = 0; i < 4; i++) acc[i] = Z;
    u4 gv[3];
    {
        u4 A[2][2], B[2][4];
        PRIO(3);
#pragma unroll
        for (int p = 0; p < 4; p++) B[0][p] = UFR(0, p);
        A[0][0] = KFR(0, 0); A[0][1] = KFR(0, 1);
#pragma unroll
        for (int k = 0; k < 3; k++) gv[k] = ld_k(k0 + KQ + (94 + 16 * hf - 32 * k - 2 * c16 + 4 * g) * 4);
        if (!hf) { const u4 t = ld_k(ka - 320); acc[2] = mfma(t, B[0][0], acc[2]); acc[3] = mfma(t, B[0][1], acc[3]); }
        acc[2] = mfma(vs[0], gv[0], acc[2]); acc[3] = mfma(vs[0], gv[1], acc[3]); acc[3] = mfma(vs[1], gv[0], acc[3]);
#pragma unroll
        for (int rho = 0; rho < 4; rho++)
#pragma unroll
            for (int p = 0; p < 2; p++) {
                SB;
                if (p == 0) { if (rho == 1) PRIO(2); else if (rho == 2) PRIO(1); else if (rho == 3) PRIO(0); }
                if (rho < 3) { B[(rho + 1) & 1][2 * p] = UFR(rho + 1, 2 * p); B[(rho + 1) & 1][2 * p + 1] = UFR(rho + 1, 2 * p + 1); A[(rho + 1) & 1][p] = KFR(rho + 1, p); }
#pragma unroll
                for (int q = 0; p + q < 4; q++) acc[p + q] = mfma(A[rho & 1][p], B[rho & 1][q], acc[p + q]);
            }
        SB;
    }
#pragma unroll
    for (int i = 0; i < 4; i++) pin(acc[i]);
    STAMP(4);
#define EXCHANGE(off)                                                                         \
    {                                                                                         \
        const int mine = LA_G + wv * 4608 + (off), other = LA_G + (wv ^ 1) * 4608 + (off);     \
        if (hf) { lds_st<f4>(mine + lane * 16, acc[0]); lds_st<f4>(mine + 1024 + lane * 16, acc[1]); acc[0] = acc[2]; acc[1] = acc[3]; } \
        else { lds_st<f4>(mine + lane * 16, acc[2]); lds_st<f4>(mine + 1024 + lane * 16, acc[3]); } \
        bar();                                                                                \
        const f4 y0 = lds_ld<f4>(other + lane * 16) + acc[0], y1 = lds_ld<f4>(other + 1024 + lane * 16) + acc[1]; \
        lds_st<u2>(other + lane * 8, u2{pk(y0[0], y0[1]), pk(y0[2], y0[3])});                 \
        lds_st<u2>(other + 1024 + lane * 8, u2{pk(y1[0], y1[1]), pk(y1[2], y1[3])});          \
    }
    EXCHANGE(0);
    {
        const int po = k0 + KP + (15 - c16 + 4 * g) * 4;
#define PFR(d) u4{lds_ld<u32>(po + 64 * (7 - (d))), lds_ld<u32>(po + 64 * (7 - (d)) + 4), lds_ld<u32>(po + 64 * (7 - (d)) + 8), lds_ld<u32>(po + 64 * (7 - (d)) + 12)}
        const u4 g4 = PFR(4), g5 = PFR(5), g6 = PFR(6);
        bar();
        STAMP(14);
        PRIO(3);
        acc[0] = mfma(vs[0], gv[2], Z); acc[1] = mfma(vs[1], gv[2], Z); acc[2] = mfma(vs[2], gv[2], Z); acc[3] = mfma(vs[3], gv[2], Z);
        acc[0] = mfma(vs[1], gv[1], acc[0]); acc[1] = mfma(vs[2], gv[1], acc[1]); acc[2] = mfma(vs[3], gv[1], acc[2]);
        acc[0] = mfma(vs[2], gv[0], acc[0]); acc[1] = mfma(vs[3], gv[0], acc[1]);
        if (hf) {
            acc[3] = mfma(ws0, g6, acc[3]); acc[2] = mfma(ws0, g5, acc[2]); acc[1] = mfma(ws0, g4, acc[1]);
            acc[3] = mfma(ws1, g5, acc[3]); acc[2] = mfma(ws1, g4, acc[2]); acc[3] = mfma(ws2, g4, acc[3]);
        } else {
            const u4 g7 = PFR(7), sel = lds_ld<u4>(lt);
            const u32 r = lds_ld<u32>(k0 + KX);
            acc[3] = mfma(ws0, g7, acc[3]); acc[2] = mfma(ws0, g6, acc[2]); acc[0] = mfma(ws0, g4, acc[0]); acc[1] = mfma(ws0, g5, acc[1]);
            const u4 t = {__builtin_amdgcn_perm(r, r, sel[0]), __builtin_amdgcn_perm(r, r, sel[1]), __builtin_amdgcn_perm(r, r, sel[2]), __builtin_amdgcn_perm(r, r, sel[3])};
#pragma unroll
            for (int q = 0; q < 4; q++) acc[q] = mfma(t, UFR(0, q), acc[q]);
        }
#pragma unroll
        for (int i = 0; i < 4; i++) pin(acc[i]);
        await<1>(FU2, 4);
        u4 A[2][2], B[2][5];
#pragma unroll
        for (int p = 0; p < 5; p++) B[0][p] = UFR(0, 3 + p);
        A[0][0] = KFR(0, 0); A[0][1] = KFR(0, 1);
        vs[0] = VSUM(4); vs[1] = VSUM(5);
        if (!hf) {
            const u4 t = ld_k(ka - 320);
            acc[0] = mfma(t, UFR(0, 2), acc[0]);
#pragma unroll
            for (int q = 1; q < 4; q++) acc[q] = mfma(t, B[0][q - 1], acc[q]);
        }
        acc[2] = mfma(vs[0], gv[0], acc[2]); acc[3] = mfma(vs[0], gv[1], acc[3]); acc[3] = mfma(vs[1], gv[0], acc[3]);
#pragma unroll
        for (int rho = 0; rho < 4; rho++)
#pragma unroll
            for (int p = 0; p < 2; p++) {
                SB;
                if (p == 0) { if (rho == 1) PRIO(2); else if (rho == 2) PRIO(1); else if (rho == 3) PRIO(0); }
                if (rho < 3) {
#pragma unroll
                    for (int q = 3 * p; q < 3 + 2 * p; q++) B[(rho + 1) & 1][q] = UFR(rho + 1, 3 + q);
                    A[(rho + 1) & 1][p] = KFR(rho + 1, p);
                }
#pragma unroll
                for (int j = 0; j < 4; j++) acc[j] = mfma(A[rho & 1][p], B[rho & 1][j + 1 - p], acc[j]);
            }
        SB;
    }
#pragma unroll
    for (int i = 0; i < 4; i++) pin(acc[i]);
    STAMP(5);
    EXCHANGE(2048);
    bar();
    STAMP(6);
    STAMP_OUT(0);
}

__global__ void __launch_bounds__(NT) __attribute__((aligned(4096))) fused_kernel(const u16* __restrict__ u, const u16* __restrict__ lar, const u16* __restrict__ aimg,
                                                   const u16* __restrict__ cre, const u16* __restrict__ cim, const u16* __restrict__ ldt,
                                                   const u16* __restrict__ dsk, const u16* __restrict__ x, const u16* __restrict__ wr,
                                                   const u16* __restrict__ wi, u16* __restrict__ out, char* ws) {
    const char* entry;
    asm volatile("s_getpc_b64 %0" : "=s"(entry));
    APar pr;
    if (blockIdx.x >= A0 && __builtin_amdgcn_readfirstlane(threadIdx.x) < 768) {
        const int a = blockIdx.x - A0, d = 6 * ((a & 7) * 12 + (a >> 3)) + __builtin_amdgcn_readfirstlane(threadIdx.x >> 7);
        asm volatile("global_load_ushort %0, %6, %8\n\tglobal_load_ushort %1, %6, %9\n\tglobal_load_ushort %2, %6, %10\n\t"
                     "global_load_ushort %3, %6, %11\n\tglobal_load_ushort %4, %7, %12\n\tglobal_load_ushort %5, %7, %13"
                     : "=&v"(pr.l), "=&v"(pr.a), "=&v"(pr.r), "=&v"(pr.i), "=&v"(pr.t), "=&v"(pr.d)
                     : "v"((d * NS + (threadIdx.x & 127)) * 2), "v"(d * 2), "s"(lar), "s"(aimg), "s"(cre), "s"(cim), "s"(ldt), "s"(dsk) : "memory");
    }
    if (__builtin_expect(blockIdx.x < NB2, 1)) head_b(entry, x, wr, wi, out, ws);
    else if (blockIdx.x >= A0) head_a(u, pr, out, ws);
    else warm_code(entry);
}

extern "C" void kernel_launch(void* const* d_in, const int* in_sizes, int n_in, void* d_out, int out_size, void* d_ws,
                              size_t ws_size, hipStream_t stream) {
    (void)hipFuncSetAttribute((const void*)fused_kernel, hipFuncAttributeMaxDynamicSharedMemorySize, LDS_BYTES);
    fused_kernel<<<GRID, NT, LDS_BYTES, stream>>>((const u16*)d_in[0], (const u16*)d_in[1], (const u16*)d_in[2], (const u16*)d_in[3],
                                                  (const u16*)d_in[4], (const u16*)d_in[5], (const u16*)d_in[6], (const u16*)d_in[7],
                                                  (const u16*)d_in[8], (const u16*)d_in[9], (u16*)d_out, (char*)d_ws);
}
```

```cpp
#define POLL_SLEEP 6
#include <hip/hip_runtime.h>

typedef unsigned short u16;
typedef unsigned int u32;
typedef float f2 __attribute__((ext_vector_type(2)));
typedef float f4 __attribute__((ext_vector_type(4)));
typedef u32 u2 __attribute__((ext_vector_type(2)));
typedef u32 u4 __attribute__((ext_vector_type(4)));
typedef int i8v __attribute__((ext_vector_type(8)));
typedef short s4 __attribute__((ext_vector_type(4)));
typedef short s8 __attribute__((ext_vector_type(8)));
typedef __bf16 bf2 __attribute__((ext_vector_type(2)));
#define DI __device__ __forceinline__
#define LAS __attribute__((address_space(3)))
#define GAS __attribute__((address_space(1)))
#define CAS __attribute__((address_space(4)))
#pragma clang diagnostic ignored "-Wint-to-pointer-cast"
struct u3 { u32 v[3]; DI u32& operator[](int i) { return v[i]; } DI const u32& operator[](int i) const { return v[i]; } };

constexpr int SEQ = 2048, DM = 576, NS = 128, DF = 289;
constexpr int NB1 = 128;
constexpr int NB2 = 145;
constexpr int A0 = 152;
constexpr int NA = 96;
constexpr int GRID = A0 + NA;
constexpr int NT = 1024;

constexpr u32 WS_EPOCH = 0, WS_F1 = 4096, WS_X1 = 1u << 20, WS_Y1 = 4u << 20, WS_USED = 8u << 20;
#define STAMP(i)
#define STAMP_DECL
#define STAMP_OUT(T)

constexpr int F_SA = 0, F_EO = 4;
constexpr int F_ARE = 9, F_AIM = 10, F_BRE = 11, F_BIM = 12, F_G = 13;
constexpr int F_F16 = 21, F_GI = 23, F_AI = 31, F_M1C = 33;
constexpr int F_VAR = 37;
constexpr int F_END = F_VAR + 40;
constexpr int ET = 452, ER = 28, E2V = 580, E2U = 36, OP = 1168;
constexpr int LB_X = 0, LB_I = 0, LB_Y = 0;
constexpr int LB_E = 16 * OP;
constexpr int LB_O = LB_E, LB_E2 = LB_E;
constexpr int LB_S = LB_E + 16 * ET * 4;
constexpr int LB_W = LB_S + 1200 * 16;
constexpr int LB_T = LB_W + 2 * 256 * 36;
constexpr int LB_G = LB_T + (F_VAR + 16) * 1024;
constexpr int LB_END = LB_G + 16;
struct CosTab { double c[2048]; double c9[9], s9[9]; };
constexpr double tay_cos(double x) { double t = 1, s = 1; for (int k = 1; k <= 14; k++) { t *= -x * x / ((2 * k - 1) * (2 * k)); s += t; } return s; }
constexpr double tay_sin(double x) { double t = x, s = x; for (int k = 1; k <= 14; k++) { t *= -x * x / ((2 * k + 1) * (2 * k)); s += t; } return s; }
constexpr CosTab make_cos() {
    CosTab t = {};
    const double PI2 = 6.283185307179586476925;
    for (int k = 0; k <= 512; k++) {
        const double v = k == 512 ? 0.0 : (k <= 256 ? tay_cos(PI2 * k / 2048) : tay_sin(PI2 * (512 - k) / 2048));
        t.c[k] = v; t.c[(2048 - k) & 2047] = v; t.c[1024 - k] = -v; t.c[(1024 + k) & 2047] = -v;
    }
    t.c[512] = 0.0; t.c[1536] = 0.0;
    for (int k = 0; k < 9; k++) { const double a = PI2 * (k <= 4 ? k : k - 9) / 9; t.c9[k] = tay_cos(a); t.s9[k] = tay_sin(a); }
    return t;
}
constexpr CosTab CT = make_cos();
constexpr double cs(int n) { return CT.c[n & 2047]; }
constexpr double sn(int n) { return CT.c[(n - 512) & 2047]; }
constexpr u32 bfr(double x) { const u32 b = __builtin_bit_cast(u32, (float)x); return (b + 0x7fffu + ((b >> 16) & 1u)) >> 16; }
constexpr u32 pkc(double lo, double hi) { return bfr(lo) | (bfr(hi) << 16); }
constexpr u32 f32c(double x) { return __builtin_bit_cast(u32, (float)x); }
template <int N> struct Frags { u32 w[N * 256]; };
constexpr u32 w_re(double amp, int n) { return pkc(amp * cs(n), amp * sn(n)); }
constexpr u32 w_im(double amp, int n) { return pkc(-amp * sn(n), amp * cs(n)); }
constexpr Frags<F_VAR> make_t0() {
    Frags<F_VAR> t = {};
    const double R2 = 1.4142135623730951;
    for (int l = 0; l < 64; l++)
        for (int i = 0; i < 4; i++) {
            const int c = l & 15, g = l >> 4, r = 4 * g + i, o = l * 4 + i;
            {
                const int b = c >> 2, tq = c & 3;
                const double amp = b ? 1.0 : R2;
                const int n0 = 512 * tq * g + (b ? 256 * b * (2 * i) : -256), n1 = 512 * tq * g + (b ? 256 * b * (2 * i + 1) : 256);
                t.w[F_ARE * 256 + o] = pkc(amp * cs(n0), amp * cs(n1));
                t.w[F_AIM * 256 + o] = pkc(-amp * sn(n0), -amp * sn(n1));
            }
            for (int w = 0; w < 9; w++) {
                const int sh = 16 * (i & 1), oo = w * 128 + l * 2 + (i >> 1);
                t.w[F_EO * 256 + oo] |= (u32)((c * ET + r * ER + 2 * ((7 * w + r) % 9)) * 4) << sh;
                if (w < 8) {
                    const int k = (9 * (w & 3) + 36 * r + 64 * (c % 9)) % DM, slot = w < 4 ? (k <= 288 ? 2 * k : 2 * (DM - k) + 1) : k;
                    t.w[F_SA * 256 + oo] |= (u32)(slot * 8 + (slot >> 6) * 16) << sh;
                }
            }
            if (r < 9) { const int k = (r * c) % 9; t.w[F_BRE * 256 + o] = pkc(CT.c9[k], CT.s9[k]); t.w[F_BIM * 256 + o] = pkc(-CT.s9[k], CT.c9[k]); }
            for (int q = 0; q < 4; q++) {
                t.w[(F_G + 2 * q) * 256 + o] = w_re(1.0, 32 * r * (q + 4 * c)); t.w[(F_G + 2 * q + 1) * 256 + o] = w_im(1.0, 32 * r * (q + 4 * c));
                t.w[(F_GI + 2 * q) * 256 + o] = w_re(1.0, 32 * c * (q + 4 * r)); t.w[(F_GI + 2 * q + 1) * 256 + o] = w_im(1.0, 32 * c * (q + 4 * r));
            }
            t.w[F_F16 * 256 + o] = w_re(1.0, 128 * r * c); t.w[(F_F16 + 1) * 256 + o] = w_im(1.0, 128 * r * c);
            for (int h = 0; h < 2; h++) {
                const int j = 4 * h + (c >> 2), n = 512 * i * (c & 3) + (g ? 256 * g * j : ((j & 1) ? 256 : -256));
                t.w[(F_AI + h) * 256 + o] = w_re(g ? 2.0 : R2, n);
                const int m = 16 * r * (4 * h + 8 * c) - 512 * h;
                t.w[(F_M1C + 2 * h) * 256 + o] = pkc(0.5 * cs(m), -0.5 * sn(m));
                t.w[(F_M1C + 2 * h + 1) * 256 + o] = pkc(-0.5 * sn(m), -0.5 * cs(m));
            }
        }
    return t;
}
constexpr Frags<40> make_t1() {
    Frags<40> t = {};
    for (int v = 0; v < 5; v++)
        for (int l = 0; l < 64; l++)
            for (int i = 0; i < 4; i++) {
                const int be = v < 3 ? v + 1 : 4 * (v - 3), c = l & 15, r = 4 * (l >> 4) + i, o = v * 2048 + l * 4 + i;
                const double amp = v < 3 ? 1.0 : 0.5;
                const int n1 = 16 * r * (be + 8 * c) + (v == 4 ? 512 : 0), n4 = 16 * c * (be + 8 * r);
                t.w[o] = w_re(amp, n1); t.w[o + 256] = w_im(amp, n1);
                t.w[o + 512] = w_re(1.0, n4); t.w[o + 768] = w_im(1.0, n4);
                t.w[o + 1024] = f32c(cs(r * (be + 8 * c))); t.w[o + 1280] = f32c(-sn(r * (be + 8 * c)));
                t.w[o + 1536] = f32c(cs(c * (be + 8 * r))); t.w[o + 1792] = f32c(-sn(c * (be + 8 * r)));
            }
    return t;
}
constexpr int qidx(int l) {
    if (l <= 256) return l;
    const int sh = l < 512 ? 1 : (l < 1024 ? 2 : 3), h = 1 << (sh - 1), m = l >> sh, r = l & ((1 << sh) - 1);
    return 128 * sh + m + ((r > h || (r == h && (m & 1))) ? 1 : 0);
}
constexpr Frags<6> make_t2() {
    Frags<6> t = {};
    for (int l = 0; l < 64; l++) {
        const int c = l & 15, g = l >> 4, e = 543 - 2 * c - g;
        t.w[256 + l * 4] = (u32)((e < 518 ? qidx(e) : 643) * 4) | ((u32)((e - 1 < 518 ? qidx(e - 1) : 643) * 4) << 16);
        for (int i = 0; i < 4; i++)
            for (int h = 0; h < 2; h++) {
                const int lag = 1024 + 16 + c - 8 * g - 2 * i - h, x = 8 * g + 2 * i + h;
                t.w[l * 4 + i] |= (lag < 1027 || lag > 1035 ? 0x0c0cu : (lag < 1029 ? 0x0100u : 0x0302u)) << (16 * h);
                if (x >= c + 8 && x <= c + 16) t.w[2 * 256 + l * 4 + i] |= 0x3f80u << (16 * h);
                if (x >= c + 1 && x <= c + 7) t.w[3 * 256 + l * 4 + i] |= 0x3f80u << (16 * h);
                if (x >= c + 6 && x <= c + 10) t.w[4 * 256 + l * 4 + i] |= 0x3f80u << (16 * h);
                if (x >= c + 3 && x <= c + 5) t.w[5 * 256 + l * 4 + i] |= 0x3f80u << (16 * h);
            }
    }
    return t;
}
struct Tables { Frags<F_VAR> a; Frags<40> b; Frags<6> c; };
constexpr Frags<6> T2 = make_t2();
constexpr Frags<F_VAR> T0 = make_t0();
constexpr Frags<40> T1 = make_t1();
static __device__ const __attribute__((aligned(128))) Tables TAB = {T0, T1, T2};
constexpr int LA_G = 0;
constexpr int LA_K = LA_G + 12 * 4608;
constexpr int KI = 1344, KQ = 4 * KI, KP = KQ + 512, KX = KP + 320, KC = KX + 16;
constexpr int LA_U = LA_K + 6 * KC;
constexpr int LA_Q = LA_U + 6 * 4608;
constexpr int LA_F = LA_Q + 12 * 2576;
constexpr int LA_S = LA_F + 16;
constexpr int LA_END = LA_S + 6144;
constexpr int FU0 = 0, FU2 = 1;
constexpr int LDS_BYTES = (LA_END > LB_END ? LA_END : LB_END);
static_assert(LDS_BYTES <= 160 * 1024 && LDS_BYTES > 80 * 1024, "one workgroup per CU");

DI u32 pk(float a, float b) { f2 v = {a, b}; return __builtin_bit_cast(u32, __builtin_convertvector(v, bf2)); }
DI float blo(u32 v) { return __uint_as_float(v << 16); }
DI float bhi(u32 v) { return __uint_as_float(v & 0xffff0000u); }
DI float bf2f(u16 v) { return __uint_as_float((u32)v << 16); }
DI float rbf(float x) { return blo(pk(x, x)); }
DI f2 cmul(f2 a, f2 b) { f2 t = a.xx * b; f2 s = {-b.y, b.x}; return __builtin_elementwise_fma(a.yy, s, t); }
DI f4 mfma(u4 a, u4 b, f4 c) { return __builtin_amdgcn_mfma_f32_16x16x32_bf16(__builtin_bit_cast(s8, a), __builtin_bit_cast(s8, b), c, 0, 0, 0); }
DI u4 pack4(f4 re, f4 im) { u4 r = {pk(re[0], im[0]), pk(re[1], im[1]), pk(re[2], im[2]), pk(re[3], im[3])}; return r; }
DI u4 twid(f4 re, f4 im, f4 wr, f4 wi) { return pack4(__builtin_elementwise_fma(re, wr, -(im * wi)), __builtin_elementwise_fma(re, wi, im * wr)); }
DI int mod9(int x) { return x - 9 * ((x * 57) >> 9); }
template <class T> DI void pin(T& v) { asm volatile("" : "+v"(v)); }

template <class T> DI T lds_ld(int off) { return *(const LAS T*)(u32)off; }
template <class T> DI void lds_st(int off, T v) { *(LAS T*)(u32)off = v; }
DI void bar() { __syncthreads(); }
#define PRIO(n) __builtin_amdgcn_s_setprio(n)
#define PRIOB(n)
DI void glds16(const void* g, u32 off, int lds) {
    u32 keep;
    asm volatile("s_mov_b32 %0, m0\n\ts_mov_b32 m0, %3\n\ts_nop 4\n\tglobal_load_lds_dwordx4 %1, %2\n\ts_mov_b32 m0, %0" : "=&s"(keep) : "v"(off), "s"(g), "s"(lds) : "memory");
}

DI void publish_tag(u32 epoch) {
    u32 t;
    asm volatile("v_add_u32 %0, 0x9E3779B1, %1\n\tds_write_b32 %2, %0\n\ts_waitcnt lgkmcnt(0)" : "=&v"(t) : "v"(epoch), "v"(LB_G) : "memory");
}
DI void wait_flag(__amdgpu_buffer_rsrc_t r, u32 off, u32 tag, bool on) {
    if (on) {
        const unsigned long long t0 = __builtin_amdgcn_s_memrealtime();
        while (!__all(__builtin_amdgcn_raw_buffer_load_b32(r, off, 0, 16) == tag)) {
            if (__builtin_amdgcn_s_memrealtime() - t0 > 5000000ull) break;
            __builtin_amdgcn_s_sleep(POLL_SLEEP);
            asm volatile("" ::: "memory");
        }
    }
    asm volatile("" ::: "memory");
}

constexpr u32 POISON = 0xFFFFFFFFu;
DI bool unset(u4 v) { return max(max(v[0], v[1]), max(v[2], v[3])) == POISON; }
#define DPOLL_SLEEP 4
template <bool TWO> DI void load_set(__amdgpu_buffer_rsrc_t r, u32 off, bool on, u4& a, bool on2, u4& b) {
    const unsigned long long t0 = __builtin_amdgcn_s_memrealtime();
    bool ma = on, mb = TWO && on2;
    for (;;) {
        if (ma) a = __builtin_amdgcn_raw_buffer_load_b128(r, off, 0, 16);
        if (TWO && mb) b = __builtin_amdgcn_raw_buffer_load_b128(r, off, 16384, 16);
        ma = ma && unset(a);
        if (TWO) mb = mb && unset(b);
        if (!__any(ma || mb) || __builtin_amdgcn_s_memrealtime() - t0 > 5000000ull) break;
        __builtin_amdgcn_s_sleep(DPOLL_SLEEP);
        asm volatile("" ::: "memory");
    }
}
constexpr int CODE_LINES = 128;
DI void warm_code(const char* entry) {
    if (threadIdx.x < CODE_LINES) { const u32 t = *(const GAS u32*)(entry + threadIdx.x * 128); asm volatile("" :: "v"(t)); }
}

DI void head_b(const char* entry, const u16* __restrict__ x, const u16* __restrict__ wr, const u16* __restrict__ wi,
               u16* __restrict__ out, char* ws) {
    const int tid = threadIdx.x, lane = tid & 63, c16 = lane & 15, g = lane >> 4;
    const int wv = __builtin_amdgcn_readfirstlane(tid >> 6);
    const int id = blockIdx.x;
    STAMP_DECL;
    const bool rows = id < NB1;
    const int s00 = 32 * (id >> 4) + (id & 15);
    const char* tab = (const char*)&TAB;
    const int R = (tid * 911) >> 16;
    const u32 xoff = (s00 + 16 * (R >> 3) + 256 * (R & 7)) * (2 * DM) + (tid - R * 72) * 16;
    asm volatile("" :: "s"(x), "s"(wr), "s"(wi), "s"(out), "s"(ws), "v"(xoff));
    const __amdgpu_buffer_rsrc_t rws = __builtin_amdgcn_make_buffer_rsrc(ws, 0, WS_USED, 0x00020000);
    STAMP(13);
    int gy = WS_Y1 + id * (NB2 * 128) + tid * 16;
    u32 epoch = 0;
    if (wv == 0) asm volatile("global_load_dword %0, %0, %1 sc1" : "+v"(epoch) : "s"(ws) : "memory");
    if (rows) {
        glds16(x, xoff, LB_X + wv * 1024);
        if (wv < 2) glds16(x, (s00 + 16 + 1536) * (2 * DM) + (tid < 56 ? (tid + 16) * 16 : 256 * 2 * DM + (tid - 56) * 16), LB_X + 16384 + wv * 1024);
        else glds16(tab, tid * 16 - 2048, LB_T + (wv - 2) * 1024);
        if (wv >= 9) glds16(tab, tid * 16 + 5 * 1024, LB_T + (wv + 5) * 1024);
        const u4 P = {POISON, POISON, POISON, POISON};
        __builtin_amdgcn_raw_buffer_store_b128(P, rws, gy, 0, 16);
        if (tid <= 128) __builtin_amdgcn_raw_buffer_store_b128(P, rws, gy, 16384, 16);
    }
    const int u2i = (id & 7) ? (id & 7) * 18 + 1 + (id >> 3) : (id == 136 ? 0 : (id >> 3) + (id < 136 ? 1 : 0));
    const bool star = u2i >= 108;
    const int q36 = min((u2i * 57) >> 11, 3), kg = u2i - 36 * q36, bq = (q36 + 1) & 3;
    const bool mir = !star && kg >= 18;
    const int w0 = star ? min(16 * kg, 562) : (mir ? (2048 - bq) * (2 * DF) + 1122 - 32 * kg : bq * (2 * DF) + 32 * kg);
    __builtin_amdgcn_s_barrier();
    STAMP(14);
    const u32 woff = w0 + ((tid >> 1) & 255) * (mir ? -16 * DF : 16 * DF) + (tid & 1) * (star ? 8 * DF : 16);
    u4 wra, wrb;
    if (wv >= 8) asm volatile("global_load_dwordx4 %0, %2, %3\n\tglobal_load_dwordx4 %1, %2, %4" : "=&v"(wra), "=&v"(wrb) : "v"(woff), "s"(wr), "s"(wi) : "memory");
#define STAGE23 do { const int o = LB_W + ((tid >> 1) & 255) * 36 + (tid & 1) * 16, t16 = (tid - 512) * 16, lw = LB_T + (wv - 8) * 1024; \
        glds16(tab, F_F16 * 1024 + t16, lw + F_F16 * 1024); glds16(tab, (F_F16 + 8) * 1024 + t16, lw + (F_F16 + 8) * 1024); \
        glds16(tab, (F_VAR + 8 * q36) * 1024 + t16, lw + F_VAR * 1024); if (star) glds16(tab, (F_VAR + 32) * 1024 + t16, lw + (F_VAR + 8) * 1024); \
        asm volatile("s_waitcnt vmcnt(0)\n\tds_write2_b32 %0, %2, %3 offset1:1\n\tds_write2_b32 %0, %4, %5 offset0:2 offset1:3\n\t" \
                     "ds_write2_b32 %1, %6, %7 offset1:1\n\tds_write2_b32 %1, %8, %9 offset0:2 offset1:3\n\ts_waitcnt lgkmcnt(0)" \
                     :: "v"(o), "v"(o + 9216), "v"(wra[0]), "v"(wra[1]), "v"(wra[2]), "v"(wra[3]), "v"(wrb[0]), "v"(wrb[1]), "v"(wrb[2]), "v"(wrb[3]) : "memory"); } while (0)
    const f4 Z = {0.f, 0.f, 0.f, 0.f};
    const int tau = wv & 3, bw = wv >> 2;
    int tl = LB_T + lane * 16;
    int lin = LB_S + (tid + (tid >> 5)) * 16;
    pin(tl); pin(lin);
    int sadr[4];

    u32 tag;
    if (rows) {
        if (tid < 256) {
            const int o = LB_E + 72 + (tid & 15) * (ER * 4) + (tid >> 4) * (ET * 4);
            lds_st<u2>(o, u2{0u, 0u}); lds_st<u4>(o + 8, u4{0u, 0u, 0u, 0u});
        }
        int xo = LB_X + (c16 >> 2) * 1152 + 2 * (144 * ((g - wv) & 3) + 16 * wv + 4 * (c16 & 3));
        int bo = LB_E + (wv * ET + c16 * ER + min(8 * g, 16)) * 4;
        int go = WS_X1 + (tid >> 3) * 16384 + id * 128 + (tid & 7) * 16;
        int ta = LB_T + lane * 8;
        pin(xo); pin(bo); pin(go); pin(ta);
        STAMP(1);
        asm volatile("s_cmp_lt_u32 %0, 8\n\ts_cbranch_scc0 .Lw%=\n\ts_waitcnt vmcnt(0)\n.Lw%=:\n\ts_waitcnt vmcnt(2)" :: "s"(wv) : "memory", "scc");
        if (wv == 0) publish_tag(epoch);
        bar();
        STAMP(2);
        if (wv < 9) {
            u4 d[2];
#pragma unroll
            for (int s = 0; s < 2; s++) {
                const s4 lo = __builtin_amdgcn_ds_read_tr16_b64_v4i16((LAS s4*)(u32)(xo + s * 9216));
                const s4 hi = __builtin_amdgcn_ds_read_tr16_b64_v4i16((LAS s4*)(u32)(xo + s * 9216 + 4608));
                d[s] = __builtin_bit_cast(u4, __builtin_shufflevector(lo, hi, 0, 1, 2, 3, 4, 5, 6, 7));
            }
            const u4 are = lds_ld<u4>(tl + F_ARE * 1024), aim = lds_ld<u4>(tl + F_AIM * 1024);
            const u2 eo = lds_ld<u2>(ta + F_EO * 1024 + wv * 512);
#pragma unroll
            for (int s = 0; s < 2; s++) d[s] = pack4(mfma(d[s], are, Z), mfma(d[s], aim, Z));
#pragma unroll
            for (int i = 0; i < 4; i++) lds_st<u2>(LB_E + ((i & 1) ? eo[i >> 1] >> 16 : eo[i >> 1] & 0xffff), u2{d[0][i], d[1][i]});
        }
        u4 bre = lds_ld<u4>(tl + F_BRE * 1024), bim = lds_ld<u4>(tl + F_BIM * 1024);
        u4 gre = lds_ld<u4>(tl + (F_G + 2 * tau) * 1024), gim = lds_ld<u4>(tl + (F_G + 2 * tau + 1) * 1024);
        {
            const u2 sa = lds_ld<u2>(ta + F_SA * 1024 + (tau + (bw ? 4 : 0)) * 512);
            const int base = LB_S + ((bw + 3) & 3) * (DM * 8 + 144);
#pragma unroll
            for (int i = 0; i < 4; i++) { sadr[i] = base + ((i & 1) ? sa[i >> 1] >> 16 : sa[i >> 1] & 0xffff); pin(sadr[i]); }
        }
        tag = lds_ld<u32>(LB_G);
        pin(bre); pin(bim); pin(gre); pin(gim); pin(tag);
        bar();
        {
            PRIOB(2);
            const u4 p0 = lds_ld<u4>(bo), p1 = lds_ld<u4>(bo + 16);
            u4 d[2];
            f4 re[2], im[2];
#pragma unroll
            for (int s = 0; s < 2; s++) { const u4 a = {p0[s], p0[2 + s], p1[s], p1[2 + s]}; re[s] = mfma(a, bre, Z); im[s] = mfma(a, bim, Z); }
            PRIOB(1);
#pragma unroll
            for (int s = 0; s < 2; s++) { const u4 t = pack4(re[s], im[s]); re[s] = mfma(gre, t, Z); im[s] = mfma(gim, t, Z); }
            PRIOB(0);
#pragma unroll
            for (int s = 0; s < 2; s++) d[s] = pack4(re[s], im[s]);
            if (c16 < 9) {
#pragma unroll
                for (int i = 0; i < 4; i++) lds_st<u2>(sadr[i], u2{d[0][i], d[1][i]});
                if (wv == 0 && c16 == 0 && !(g & 1)) lds_st<u2>(sadr[0] + 8, u2{d[0][0], d[1][0]});
            }
        }
        bar();
        STAMP(3);
        if (wv < 8) {
            u4 v0 = lds_ld<u4>(lin), v1 = lds_ld<u4>(lin + 528 * 16), v2 = lds_ld<u4>(lin + (wv < 3 ? 1056 * 16 : 0));
            pin(v2);
            __builtin_amdgcn_raw_buffer_store_b128(v0, rws, go, 0, 16);
            __builtin_amdgcn_raw_buffer_store_b128(v1, rws, go, 64 * 16384, 16);
            if (tid <= 128) __builtin_amdgcn_raw_buffer_store_b128(v2, rws, go, 128 * 16384, 16);
            asm volatile("s_waitcnt vmcnt(0)" ::: "memory");
            if (lane == 0) __builtin_amdgcn_raw_buffer_store_b32(tag, rws, WS_F1 + (wv * 16 + (id >> 3)) * 128 + (id & 7) * 4, 0, 16);
            STAMP(4);
        } else {
            STAGE23;
        }
        bar();
    } else {
        if (wv < 8 && (kg == 0 || kg == (star ? 36 : 18))) {
            const int m = tid & 255;
            lds_st<u16>(LB_W + tid * 36 + 32, ((wv >> 2) ? wi : wr)[((mir ? bq + 8 * m : 2048 - bq - 8 * m) & 2047) * DF + (kg ? 288 : 0)]);
        }
        warm_code(entry);
        if (wv >= 8) STAGE23;
        asm volatile("s_waitcnt vmcnt(0)" ::: "memory");
        if (wv == 0) publish_tag(epoch);
        bar();
        tag = lds_ld<u32>(LB_G);
    }
    STAMP(12);

    const int typ = star ? (wv & 1) : 0;
    const int tv = tl + (F_VAR + 8 * typ) * 1024;
    u4 fre = lds_ld<u4>(tl + F_F16 * 1024), fim = lds_ld<u4>(tl + (F_F16 + 1) * 1024);
    u4 m1r = lds_ld<u4>(tv), m1i = lds_ld<u4>(tv + 1024), m4r = lds_ld<u4>(tv + 2048), m4i = lds_ld<u4>(tv + 3072);
    f4 twr = lds_ld<f4>(tv + 4096), twi = lds_ld<f4>(tv + 5120), tur = lds_ld<f4>(tv + 6144), tui = lds_ld<f4>(tv + 7168);
    u4 mcr = lds_ld<u4>(tl + (F_M1C + 2 * typ) * 1024), mci = lds_ld<u4>(tl + (F_M1C + 2 * typ + 1) * 1024);
    f4 wfr, wfi;
    {
        const int kcol = star ? 8 * kg + (wv >> 1) : 16 * kg + wv;
        const int cc = star ? 8 * typ + (u2i < NB2 - 1 ? (wv >> 1) : 7) : (mir ? 15 - wv : wv);
        const int wo = LB_W + (c16 + 64 * g) * 36 + cc * 2;
#pragma unroll
        for (int i = 0; i < 4; i++) {
            wfr[i] = bf2f(lds_ld<u16>(wo + i * 576));
            wfi[i] = bf2f(lds_ld<u16>(wo + i * 576 + 9216));
        }
        float sc = 1.f / (float)(SEQ * DM);
        if (kcol == 0 || kcol == 288) {
            sc *= 0.5f;
#pragma unroll
            for (int i = 0; i < 4; i++) {
                const int m = c16 + 64 * g + 16 * i;
                const int o = LB_W + (typ ? (255 - m) * 36 + cc * 2 : m * 36 + 32);
                wfr[i] += bf2f(lds_ld<u16>(o));
                wfi[i] -= bf2f(lds_ld<u16>(o + 9216));
            }
        }
        wfr *= sc; wfi *= (mir ? -sc : sc);
    }
    f4 wnr = -wfr, wni = -wfi;
    int io = (32 * g + c16) * 128 + (((wv >> 1) ^ (c16 >> 1)) * 16);
    int so = (tid >> 3) * 128 + (((tid & 7) ^ ((tid >> 4) & 7)) * 16);
    pin(fre); pin(fim); pin(m1r); pin(m1i); pin(m4r); pin(m4i); pin(twr); pin(twi); pin(tur); pin(tui); pin(mcr); pin(mci);
    int gi = WS_X1 + u2i * 16384 + tid * 16, gq = WS_Y1 + (tid >> 3) * (NB2 * 128) + u2i * 128 + (tid & 7) * 16;
    int i1 = LB_I + io + (star ? 0 : (wv & 1) * 8);
    io += LB_O + (wv & 1) * 8;
    pin(wfr); pin(wnr); pin(wni); pin(io); pin(so); pin(gi); pin(gq); pin(i1);
    wait_flag(rws, WS_F1 + (((u2i >> 3) & 7) * 16 + wv) * 128 + lane * 4, tag, lane < 8);
    STAMP(5);
    lds_st<u4>(LB_I + so, __builtin_amdgcn_raw_buffer_load_b128(rws, gi, 0, 16));
    bar();
    STAMP(6);
    {
        const int o1 = i1;
        PRIOB(3);
        const u2 a0 = lds_ld<u2>(o1), a1 = lds_ld<u2>(o1 + 2048);
        const u4 a = {a0[0], a0[1], a1[0], a1[1]};
        f4 re = mfma(a, m1r, Z), im = mfma(a, m1i, Z);
        if (star) {
            const u2 b0 = lds_ld<u2>(o1 + 8), b1 = lds_ld<u2>(o1 + 8 + 2048);
            const u4 b = {b0[0], b0[1], b1[0], b1[1]};
            re = mfma(b, mcr, re); im = mfma(b, mci, im);
        }
        u4 t = twid(re, im, twr, twi);
        re = mfma(fre, t, Z); im = mfma(fim, t, Z);
        PRIOB(2);
        t = pack4(__builtin_elementwise_fma(re, wfr, im * wni), __builtin_elementwise_fma(re, wni, im * wnr));
        re = mfma(t, fre, Z); im = mfma(t, fim, Z);
        PRIOB(1);
        t = twid(re, im, tur, tui);
        re = mfma(m4r, t, Z); im = mfma(m4i, t, Z);
        PRIOB(0);
        t = pack4(re, im);
        lds_st<u2>(io, u2{t[0], t[1]}); lds_st<u2>(io + 2048, u2{t[2], t[3]});
    }
    bar();
    STAMP(7);
    u4 sv;
    {
        u4 v = lds_ld<u4>(LB_O + so);
        if (star) {
            u4 w;
#pragma unroll
            for (int s = 0; s < 2; s++) {
                const float ar = blo(v[s]), ai = bhi(v[s]), cr = blo(v[2 + s]), ci = bhi(v[2 + s]);
                w[s] = pk(ar + ci, ai - cr); w[2 + s] = pk(ar - ci, -ai - cr);
            }
            v = w;
        }
        if (u2i < NB2 - 1 || (tid & 7) == 0)
            __builtin_amdgcn_raw_buffer_store_b128(v, rws, gq, 0, 16);
        sv = v;
    }
    STAMP(8);
    if (!rows) { STAMP_OUT(0); return; }

    u4 gre = lds_ld<u4>(tl + (F_GI + 2 * tau) * 1024), gim = lds_ld<u4>(tl + (F_GI + 2 * tau + 1) * 1024);
    u4 bre = lds_ld<u4>(tl + F_BRE * 1024), bim = lds_ld<u4>(tl + F_BIM * 1024);
    u4 ai0 = lds_ld<u4>(tl + F_AI * 1024), ai1 = lds_ld<u4>(tl + (F_AI + 1) * 1024);
    int eadr[4];
#pragma unroll
    for (int i = 0; i < 4; i++) {
        const int r = 4 * g + i;
        eadr[i] = LB_E2 + (mod9(4 * (c16 + 18 - r)) * E2V + r * E2U + 2 * wv) * 4;
        pin(eadr[i]);
    }
    int ao = LB_E2 + (wv * E2V + c16 * E2U + 8 * g) * 4;
    int yo = LB_Y + (c16 >> 2) * OP + 2 * (144 * (((c16 & 3) - wv) & 3) + 16 * wv + 4 * g);
    int yl = LB_Y + tid * 16 + R * (OP - 1152);
    u32 oo = 2 * xoff - (tid - R * 72) * 16 + 2 * DM;
    pin(gre); pin(gim); pin(bre); pin(bim); pin(ai0); pin(ai1); pin(ao); pin(gy); pin(yo); pin(yl); pin(oo);
    asm volatile("" :: "v"(sv));
    STAMP(9);
    {
        u4 v, w;
        if (wv < 3) { load_set<true>(rws, gy, true, v, tid <= 128, w); if (tid <= 128) lds_st<u4>(lin + 1056 * 16, w); }
        else load_set<false>(rws, gy, true, v, false, w);
        lds_st<u4>(lin, v);
    }
    bar();
    if (id == 0 && tid == 0) __builtin_amdgcn_raw_buffer_store_b32(tag, rws, WS_EPOCH, 0, 16);
    STAMP(10);
    {
        PRIOB(2);
        u4 d[2];
        u2 p[4];
#pragma unroll
        for (int i = 0; i < 4; i++) p[i] = lds_ld<u2>(sadr[i]);
        f4 re[2], im[2];
#pragma unroll
        for (int s = 0; s < 2; s++) { const u4 a = {p[0][s], p[1][s], p[2][s], p[3][s]}; re[s] = mfma(a, gre, Z); im[s] = mfma(a, gim, Z); }
        PRIOB(1);
#pragma unroll
        for (int s = 0; s < 2; s++) { const u4 t = pack4(re[s], im[s]); re[s] = mfma(t, bre, Z); im[s] = mfma(t, bim, Z); }
        PRIOB(0);
#pragma unroll
        for (int s = 0; s < 2; s++) d[s] = pack4(re[s], im[s]);
        if (c16 < 9) {
#pragma unroll
            for (int i = 0; i < 4; i++) lds_st<u2>(eadr[i], u2{d[0][i], d[1][i]});
        }
    }
    STAMP(15);
    bar();
    if (wv < 9) {
        const u4 p0 = lds_ld<u4>(ao), p1 = lds_ld<u4>(ao + 16);
#pragma unroll
        for (int s = 0; s < 2; s++) {
            const u4 a = {p0[s], p0[2 + s], p1[s], p1[2 + s]};
            const f4 y0 = mfma(a, ai0, Z), y1 = mfma(a, ai1, Z);
            lds_st<u2>(yo + s * (8 * OP), u2{pk(y0[0], y0[1]), pk(y0[2], y0[3])});
            lds_st<u2>(yo + s * (8 * OP) + 4 * OP, u2{pk(y1[0], y1[1]), pk(y1[2], y1[3])});
        }
    }
    bar();
    const __amdgpu_buffer_rsrc_t rout = __builtin_amdgcn_make_buffer_rsrc(out, 0, SEQ * DM * 4, 0x00020000);
    __builtin_amdgcn_raw_buffer_store_b128(lds_ld<u4>(yl), rout, oo, 0, 2);
    if (wv < 2) {
        const int r2 = tid < 56 ? 14 : 15, c2 = tid + 1024 - 72 * r2;
        __builtin_amdgcn_raw_buffer_store_b128(lds_ld<u4>(LB_Y + r2 * OP + c2 * 16), rout, (s00 + 16 + 256 * (r2 - 8)) * (4 * DM) + 2 * DM + c2 * 16, 0, 2);
    }
    STAMP(11);
    STAMP_OUT(0);
}

DI f2 cexpa(float ar2, float air, float p) {
    const float e = __builtin_amdgcn_exp2f(ar2 * p), t = __builtin_amdgcn_fractf(air * p);
    return f2{e * __builtin_amdgcn_cosf(t), e * __builtin_amdgcn_sinf(t)};
}
DI u4 ld_k(int o) {
    const u2 a = *(const volatile LAS u2*)(u32)o, b = *(const volatile LAS u2*)(u32)(o + 8);
    return u4{a[0], a[1], b[0], b[1]};
}
DI void a_rows_out(int yb, int ystr, char* o) {
    u2 y[6];
#pragma unroll
    for (int c = 0; c < 6; c++) y[c] = lds_ld<u2>(yb + c * ystr);
#pragma unroll
    for (int r = 0; r < 4; r++) {
        u3 v;
#pragma unroll
        for (int c = 0; c < 3; c++) v[c] = __builtin_amdgcn_perm(y[2 * c + 1][r >> 1], y[2 * c][r >> 1], (r & 1) ? 0x07060302u : 0x05040100u);
        *(u3*)(o + r * (4 * DM)) = v;
    }
}
template <int R> DI void stage(u4 v) {
    asm volatile("ds_write_addtid_b32 %0 offset:%4\n\tds_write_addtid_b32 %1 offset:%5\n\tds_write_addtid_b32 %2 offset:%6\n\tds_write_addtid_b32 %3 offset:%7"
                 :: "v"(v[0]), "v"(v[1]), "v"(v[2]), "v"(v[3]), "i"(288 * R), "i"(288 * R + 288), "i"(288 * R + 576), "i"(288 * R + 864) : "memory");
}
#define CM_MUL(d, a, b) "v_pk_mul_f32 " d ", " a ", " b " op_sel_hi:[0,1]\n\t"
#define CM_FMA(d, a, b) "v_pk_fma_f32 " d ", " a ", " b ", " d " op_sel:[1,1,0] op_sel_hi:[1,0,1] neg_lo:[1,0,0]\n\t"
struct C4 { f2 a, b, c, d; };
DI C4 cmul4(f2 a0, f2 b0, f2 a1, f2 b1, f2 a2, f2 b2, f2 a3, f2 b3) {
    C4 r;
    asm("s_nop 0\n\t" CM_MUL("%0", "%4", "%5") CM_MUL("%1", "%6", "%7") CM_MUL("%2", "%8", "%9") CM_MUL("%3", "%10", "%11")
        CM_FMA("%0", "%4", "%5") CM_FMA("%1", "%6", "%7") CM_FMA("%2", "%8", "%9") CM_FMA("%3", "%10", "%11") "s_nop 0"
        : "=&v"(r.a), "=&v"(r.b), "=&v"(r.c), "=&v"(r.d) : "v"(a0), "v"(b0), "v"(a1), "v"(b1), "v"(a2), "v"(b2), "v"(a3), "v"(b3));
    return r;
}
DI C4 cmul4(const C4& a, f2 b) { return cmul4(a.a, b, a.b, b, a.c, b, a.d, b); }
DI u4 pk4(const C4& v) { return u4{pk(v.a.x, v.a.y), pk(v.b.x, v.b.y), pk(v.c.x, v.c.y), pk(v.d.x, v.d.y)}; }

DI void sig(int f) {
    asm volatile("s_waitcnt lgkmcnt(0)" ::: "memory");
    if ((threadIdx.x & 63) == 0) __hip_atomic_fetch_add((LAS u32*)(u32)(LA_F + 4 * f), 1u, __ATOMIC_RELAXED, __HIP_MEMORY_SCOPE_WORKGROUP);
}
template <int SLEEP = 0> DI void await(int f, u32 n) {
    while ((u32)__builtin_amdgcn_readfirstlane(*(const volatile LAS u32*)(u32)(LA_F + 4 * f)) < n) if (SLEEP) __builtin_amdgcn_s_sleep(SLEEP);
    asm volatile("" ::: "memory");
}
DI float exp_bf(float x) {
    const float t = x * 1.44269504f, r = __builtin_fmaf(x, 1.92596299e-8f, __builtin_fmaf(x, 1.44269504f, -t));
    const float e = __builtin_amdgcn_exp2f(t);
    return __builtin_fmaf(e * 0.693147181f, r, e);
}

struct APar { u32 l, a, r, i, t, d; };
DI void head_a(const u16* __restrict__ u, APar pr, u16* __restrict__ out, char* ws) {
    const int tid = threadIdx.x, lane = tid & 63, c16 = lane & 15, g = lane >> 4;
    const int wv = __builtin_amdgcn_readfirstlane(tid >> 6);
    STAMP_DECL;
    const int ai_ = blockIdx.x - A0;
    const int d0 = 6 * ((ai_ & 7) * 12 + (ai_ >> 3));
    const f4 Z = {0.f, 0.f, 0.f, 0.f};

    if (wv >= 12) {
        __builtin_amdgcn_s_setprio(3);
        const int t = tid - 768;
        if (t == 0) lds_st<u2>(LA_F, u2{0u, 0u});
        if (t < 180) lds_st<u4>(LA_U + (t / 30) * 4608 + (t % 30) * 16, u4{0u, 0u, 0u, 0u});
        bar();
        u3 ua[4], ub[4];
#pragma unroll
        for (int r = 0; r < 4; r++) ua[r] = *(const u3*)(u + (4 * t + r) * DM + d0);
        glds16((const char*)&TAB + F_END * 1024, t * 16, LA_S + (wv - 12) * 1024);
        if (wv < 14) glds16((const char*)&TAB + (F_END + 4) * 1024, t * 16, LA_S + (wv - 8) * 1024);
#pragma unroll
        for (int c = 0; c < 6; c++) {
            const u32 sel = (c & 1) ? 0x07060302u : 0x05040100u;
            lds_st<u2>(LA_U + c * 4608 + 480 + t * 8, u2{__builtin_amdgcn_perm(ua[1][c >> 1], ua[0][c >> 1], sel), __builtin_amdgcn_perm(ua[3][c >> 1], ua[2][c >> 1], sel)});
        }
        STAMP(7);
        asm volatile("s_waitcnt vmcnt(0)" ::: "memory");
        bar();
        bar();
        asm volatile("" ::: "memory");
#pragma unroll
        for (int r = 0; r < 4; r++) ub[r] = *(const u3*)(u + (1024 + 4 * t + r) * DM + d0);
#pragma unroll
        for (int c = 0; c < 6; c++) {
            const u32 sel = (c & 1) ? 0x07060302u : 0x05040100u;
            lds_st<u2>(LA_U + c * 4608 + 480 + 2048 + t * 8, u2{__builtin_amdgcn_perm(ub[1][c >> 1], ub[0][c >> 1], sel), __builtin_amdgcn_perm(ub[3][c >> 1], ub[2][c >> 1], sel)});
        }
        sig(FU2);
        STAMP(8);
        const int tile = wv - 12, yl = ((lane >> 2) + 16 * (lane & 3)) * 8;
        char* o = (char*)(out + 4 * t * (2 * DM) + d0);
        bar();
        bar();
        STAMP(11);
        a_rows_out(LA_G + ((tile >> 1) ^ 1) * 4608 + (tile & 1) * 1024 + yl, 9216, o);
        STAMP(9);
        bar();
        bar();
        STAMP(12);
        a_rows_out(LA_G + ((tile >> 1) ^ 1) * 4608 + (tile & 1) * 1024 + 2048 + yl, 9216, o + 1024 * (4 * DM));
        STAMP(10);
        STAMP_OUT(768);
        return;
    }
    const int ch = wv >> 1, hf = wv & 1;
    {
        __builtin_amdgcn_s_barrier();
        asm volatile("s_waitcnt vmcnt(0)" : "+v"(pr.l), "+v"(pr.a), "+v"(pr.r), "+v"(pr.i), "+v"(pr.t), "+v"(pr.d) :: "memory");
        PRIO(3);
        const float dt = rbf(exp_bf(blo(pr.t)));
        const float ar2 = -rbf(exp_bf(blo(pr.l))) * dt * 1.44269504f, air = blo(pr.a) * dt * 0.159154943f;
        const f2 C = {2.f * dt * blo(pr.r), 2.f * dt * blo(pr.i)};
        STAMP(1);
        const f2 z1 = cexpa(ar2, air, 1.f), z8c = cexpa(ar2, -air, 8.f), z128 = cexpa(ar2, air, 128.f);
        const int gr = LA_G + wv * 4608 + c16 * 288 + g * 16, q = LA_Q + wv * 2576;
        asm volatile("s_mov_b32 m0, %0\n\ts_nop 0" :: "s"(LA_G + wv * 4608) : "memory");
#define SB __builtin_amdgcn_sched_barrier(0)
#define STAGE16(v) do { stage<0>(v[0]); stage<4>(v[1]); stage<8>(v[2]); stage<12>(v[3]); } while (0)
#define READ4(f) do { _Pragma("unroll") for (int s = 0; s < 4; s++) f[s] = lds_ld<u4>(gr + 64 * s); SB; } while (0)
        u4 v[4], bfr[4], afr[4];
        const C4 s1 = cmul4(z8c, z8c, z1, z1, z128, z128, C, z1);
        const C4 s2 = cmul4(s1.a, s1.a, z8c, s1.a, s1.c, s1.c, C, s1.b);
        {
            C4 w = {f2{1.f, 0.f}, z8c, s1.a, s2.b};
            v[0] = pk4(w);
#pragma unroll
            for (int i = 1; i < 4; i++) { w = cmul4(w, s2.a); v[i] = pk4(w); }
        }
        STAGE16(v); READ4(bfr);
        PRIO(2);
        const C4 s3 = cmul4(s1.d, s1.b, s1.b, s1.b, s2.c, s2.c, s2.c, z128);
        const C4 e0 = {C, s1.d, s2.d, s3.a}, e1 = cmul4(e0, s3.b);
        v[0] = pk4(e0); v[1] = pk4(e1); v[2] = pk4(cmul4(e0, z128)); v[3] = pk4(cmul4(e1, z128));
        STAGE16(v); READ4(afr);
        PRIO(1);
        {
            const C4 f = cmul4(C4{e0.a, e0.c, e1.a, e1.c}, s1.c), h = cmul4(e0.a, s2.c, e1.a, s2.c, e0.a, s3.d, e1.a, s3.d);
            v[0] = pk4(f); v[1] = pk4(cmul4(f, z128)); v[2] = pk4(h); v[3] = pk4(cmul4(h, s1.c));
        }
        SB;
        f4 acc = Z;
#pragma unroll
        for (int s = 0; s < 4; s++) acc = mfma(afr[s], bfr[s], acc);
        STAGE16(v); READ4(afr);
        PRIO(0);
        lds_st<f4>(q + (4 * (g & 1) + 128 * (g >> 1) + 8 * c16) * 4, acc);
        {
            const f2 f0 = cmul(C, s3.c), f1 = cmul(f0, z128);
            const C4 f = cmul4(f0, s1.c, f1, s1.c, f0, s3.c, f0, s3.c), h = cmul4(C4{f0, f1, f.a, f.b}, s2.c);
            v[0] = pk4(C4{f0, f1, f.a, f.b}); v[1] = pk4(h); v[2][0] = pk(f.c.x, f.c.y);
        }
        SB;
        acc = Z;
#pragma unroll
        for (int s = 0; s < 4; s++) acc = mfma(afr[s], bfr[s], acc);
        stage<0>(v[0]); stage<4>(v[1]);
        asm volatile("ds_write_addtid_b32 %0 offset:%1" :: "v"(v[2][0]), "i"(288 * 8) : "memory");
        READ4(afr);
        if (g < 2) lds_st<f4>(q + (256 + 64 * g + 4 * c16) * 4, acc);
        else {
            const int o = q + (384 + 64 * (g - 2) + 2 * c16) * 4;
            lds_st<f2>(o, f2{acc[0], acc[1]}); lds_st<f2>(o + 128, f2{acc[2], acc[3]});
        }
        acc = Z;
#pragma unroll
        for (int s = 0; s < 4; s++) acc = mfma(afr[s], bfr[s], acc);
        if (g < 2) {
#pragma unroll
            for (int i = 0; i < 4; i++) lds_st<float>(q + (512 + 64 * g + 16 * i + c16) * 4, acc[i]);
        } else if (lane < 34) lds_st<float>(q + (lane == 32 ? 640 : 643) * 4, lane == 32 ? acc[0] : 0.f);
    }
    STAMP(2);
    asm volatile("s_waitcnt lgkmcnt(0)" ::: "memory");
    bar();
    STAMP(9);

    const int k0 = LA_K + ch * KC;
    {
        const LAS float* qa = (const LAS float*)(u32)(LA_Q + ch * 5152);
        const LAS float* qb = qa + 644;
        u2 w[4];
        int y;
        if (hf == 0) {
            const u32 ix = lds_ld<u32>(LA_S + 1024 + lane * 16);
            f4 k = *(const LAS f4*)(qa + 4 * lane) + *(const LAS f4*)(qb + 4 * lane);
            f4 p = *(const LAS f4*)(qa + 4 * lane - 4) + *(const LAS f4*)(qb + 4 * lane - 4);
            const f2 s = *(const LAS f2*)(qa + 512 - 2 * lane) + *(const LAS f2*)(qb + 512 - 2 * lane);
            const float e0 = qa[(ix & 0xffff) >> 2] + qb[(ix & 0xffff) >> 2], e1 = qa[ix >> 18] + qb[ix >> 18];
            lds_st<u32>(k0 + KQ + (32 + lane) * 4, pk(s.x, lane ? s.y : 0.f));
            if (lane == 0) lds_st<u32>(k0 + KX, pk(s.x, s.y));
            lds_st<u32>(k0 + KQ + (lane + (lane & 32) * 2) * 4, 0u);
            lds_st<u32>(k0 + g * KI + c16 * 4, pk(e0, e1));
            if (lane == 0) { k[0] += blo(pr.d); p = Z; }
            const u32 c = pk(k[1], k[0]), d = pk(k[0], p[3]);
            w[0] = u2{pk(k[3], k[2]), c}; w[1] = u2{pk(k[2], k[1]), d}; w[2] = u2{c, pk(p[3], p[2])}; w[3] = u2{d, pk(p[2], p[1])};
            y = k0 + (270 - 2 * lane) * 4;
        } else {
            const int j = 256 + 2 * lane;
            const float q0 = qa[j] + qb[j], q1 = qa[j + 1] + qb[j + 1], q2 = qa[j + 2] + qb[j + 2];
            const float p1 = qa[j - 1] + qb[j - 1], p2 = qa[j - 2] + qb[j - 2], p3 = qa[j - 3] + qb[j - 3];
            const u32 c = pk(q0, q0), d = pk(q0, p1), e = pk(p1, p2), f = lane ? c : d;
            w[0] = u2{pk(q2, q1), c}; w[1] = u2{pk(q1, q0), f}; w[2] = u2{c, lane ? d : e}; w[3] = u2{f, lane ? e : pk(p2, p3)};
            y = k0 + (142 - 2 * lane) * 4;
            if (lane < 48) lds_st<u32>(k0 + (lane < 32 ? (lane >> 3) * KI + (272 + (lane & 7)) * 4 : KP + (32 + lane) * 4), 0u);
            const f2 s = *(const LAS f2*)(qa + 640 - 2 * lane) + *(const LAS f2*)(qb + 640 - 2 * lane);
            lds_st<u32>(k0 + KP + lane * 4, pk(s.x, lane ? s.y : 0.f));
        }
#pragma unroll
        for (int i = 0; i < 4; i++) lds_st<u2>(y + i * KI, w[i]);
    }
    const int x0 = 2031 - c16 + 8 * g;
    int ka = k0 + (x0 & 3) * KI + (x0 >> 2) * 8 - 3008 - 256 * hf - 192 - 512;
    int ubs = LA_U + ch * 4608 + 480 + (16 * c16 + 8 * g - 16) * 2 - 256 * hf - 192;
    pin(ka); pin(ubs);
    const int lt = LA_S + lane * 16;
    const u4 bnd5 = lds_ld<u4>(lt + 4096), bnd3 = lds_ld<u4>(lt + 5120);
    int uv = LA_U + ch * 4608 + 480 + (8 * c16 + 8 * g - 24) * 2 + 256 * hf;
    pin(uv);
#define VSUM(n) ({ const u4 a_ = lds_ld<u4>(uv + 512 * (n)); pack4(mfma(a_, bnd5, Z), mfma(a_, bnd3, Z)); })
    u4 vs[4], ws0, ws1, ws2;
    {
        const u4 bnd9 = lds_ld<u4>(lt + 2048), bnd7 = lds_ld<u4>(lt + 3072);
        const int uw = LA_U + ch * 4608 + 480 + (16 * c16 + 8 * g - 28) * 2;
#define WSUM(I) ({ const u4 a_ = ld_k(uw + 512 * (I)); pack4(mfma(a_, bnd9, Z), mfma(a_, bnd7, Z)); })
#pragma unroll
        for (int n = 0; n < 4; n++) vs[n] = VSUM(n);
        ws0 = WSUM(hf);
        if (hf) { ws1 = WSUM(2); ws2 = WSUM(3); }
    }
    STAMP(13);
    bar();
    STAMP(3);
#define KFR(rho, p) ld_k(ka + 192 - 64 * (rho) + 512 - 512 * (p))
#define UFR(rho, q) lds_ld<u4>(ubs + 192 - 64 * (rho) + 512 * (q))
    f4 acc[4];
#pragma unroll
    for (int i = 0; i < 4; i++) acc[i] = Z;
    u4 gv[3];
    {
        u4 A[2][2], B[2][4];
        PRIO(3);
#pragma unroll
        for (int p = 0; p < 4; p++) B[0][p] = UFR(0, p);
        A[0][0] = KFR(0, 0); A[0][1] = KFR(0, 1);
#pragma unroll
        for (int k = 0; k < 3; k++) gv[k] = ld_k(k0 + KQ + (94 + 16 * hf - 32 * k - 2 * c16 + 4 * g) * 4);
        if (!hf) { const u4 t = ld_k(ka - 320); acc[2] = mfma(t, B[0][0], acc[2]); acc[3] = mfma(t, B[0][1], acc[3]); }
        acc[2] = mfma(vs[0], gv[0], acc[2]); acc[3] = mfma(vs[0], gv[1], acc[3]); acc[3] = mfma(vs[1], gv[0], acc[3]);
#pragma unroll
        for (int rho = 0; rho < 4; rho++)
#pragma unroll
            for (int p = 0; p < 2; p++) {
                SB;
                if (p == 0) { if (rho == 1) PRIO(2); else if (rho == 2) PRIO(1); else if (rho == 3) PRIO(0); }
                if (rho < 3) { B[(rho + 1) & 1][2 * p] = UFR(rho + 1, 2 * p); B[(rho + 1) & 1][2 * p + 1] = UFR(rho + 1, 2 * p + 1); A[(rho + 1) & 1][p] = KFR(rho + 1, p); }
#pragma unroll
                for (int q = 0; p + q < 4; q++) acc[p + q] = mfma(A[rho & 1][p], B[rho & 1][q], acc[p + q]);
            }
        SB;
    }
#pragma unroll
    for (int i = 0; i < 4; i++) pin(acc[i]);
    STAMP(4);
#define EXCHANGE(off)                                                                         \
    {                                                                                         \
        const int mine = LA_G + wv * 4608 + (off), other = LA_G + (wv ^ 1) * 4608 + (off);     \
        if (hf) { lds_st<f4>(mine + lane * 16, acc[0]); lds_st<f4>(mine + 1024 + lane * 16, acc[1]); acc[0] = acc[2]; acc[1] = acc[3]; } \
        else { lds_st<f4>(mine + lane * 16, acc[2]); lds_st<f4>(mine + 1024 + lane * 16, acc[3]); } \
        bar();                                                                                \
        const f4 y0 = lds_ld<f4>(other + lane * 16) + acc[0], y1 = lds_ld<f4>(other + 1024 + lane * 16) + acc[1]; \
        lds_st<u2>(other + lane * 8, u2{pk(y0[0], y0[1]), pk(y0[2], y0[3])});                 \
        lds_st<u2>(other + 1024 + lane * 8, u2{pk(y1[0], y1[1]), pk(y1[2], y1[3])});          \
    }
    EXCHANGE(0);
    {
        const int po = k0 + KP + (15 - c16 + 4 * g) * 4;
#define PFR(d) u4{lds_ld<u32>(po + 64 * (7 - (d))), lds_ld<u32>(po + 64 * (7 - (d)) + 4), lds_ld<u32>(po + 64 * (7 - (d)) + 8), lds_ld<u32>(po + 64 * (7 - (d)) + 12)}
        const u4 g4 = PFR(4), g5 = PFR(5), g6 = PFR(6);
        bar();
        STAMP(14);
        PRIO(3);
        acc[0] = mfma(vs[0], gv[2], Z); acc[1] = mfma(vs[1], gv[2], Z); acc[2] = mfma(vs[2], gv[2], Z); acc[3] = mfma(vs[3], gv[2], Z);
        acc[0] = mfma(vs[1], gv[1], acc[0]); acc[1] = mfma(vs[2], gv[1], acc[1]); acc[2] = mfma(vs[3], gv[1], acc[2]);
        acc[0] = mfma(vs[2], gv[0], acc[0]); acc[1] = mfma(vs[3], gv[0], acc[1]);
        if (hf) {
            acc[3] = mfma(ws0, g6, acc[3]); acc[2] = mfma(ws0, g5, acc[2]); acc[1] = mfma(ws0, g4, acc[1]);
            acc[3] = mfma(ws1, g5, acc[3]); acc[2] = mfma(ws1, g4, acc[2]); acc[3] = mfma(ws2, g4, acc[3]);
        } else {
            const u4 g7 = PFR(7), sel = lds_ld<u4>(lt);
            const u32 r = lds_ld<u32>(k0 + KX);
            acc[3] = mfma(ws0, g7, acc[3]); acc[2] = mfma(ws0, g6, acc[2]); acc[0] = mfma(ws0, g4, acc[0]); acc[1] = mfma(ws0, g5, acc[1]);
            const u4 t = {__builtin_amdgcn_perm(r, r, sel[0]), __builtin_amdgcn_perm(r, r, sel[1]), __builtin_amdgcn_perm(r, r, sel[2]), __builtin_amdgcn_perm(r, r, sel[3])};
#pragma unroll
            for (int q = 0; q < 4; q++) acc[q] = mfma(t, UFR(0, q), acc[q]);
        }
#pragma unroll
        for (int i = 0; i < 4; i++) pin(acc[i]);
        await<1>(FU2, 4);
        u4 A[2][2], B[2][5];
#pragma unroll
        for (int p = 0; p < 5; p++) B[0][p] = UFR(0, 3 + p);
        A[0][0] = KFR(0, 0); A[0][1] = KFR(0, 1);
        vs[0] = VSUM(4); vs[1] = VSUM(5);
        if (!hf) {
            const u4 t = ld_k(ka - 320);
            acc[0] = mfma(t, UFR(0, 2), acc[0]);
#pragma unroll
            for (int q = 1; q < 4; q++) acc[q] = mfma(t, B[0][q - 1], acc[q]);
        }
        acc[2] = mfma(vs[0], gv[0], acc[2]); acc[3] = mfma(vs[0], gv[1], acc[3]); acc[3] = mfma(vs[1], gv[0], acc[3]);
#pragma unroll
        for (int rho = 0; rho < 4; rho++)
#pragma unroll
            for (int p = 0; p < 2; p++) {
                SB;
                if (p == 0) { if (rho == 1) PRIO(2); else if (rho == 2) PRIO(1); else if (rho == 3) PRIO(0); }
                if (rho < 3) {
#pragma unroll
                    for (int q = 3 * p; q < 3 + 2 * p; q++) B[(rho + 1) & 1][q] = UFR(rho + 1, 3 + q);
                    A[(rho + 1) & 1][p] = KFR(rho + 1, p);
                }
#pragma unroll
                for (int j = 0; j < 4; j++) acc[j] = mfma(A[rho & 1][p], B[rho & 1][j + 1 - p], acc[j]);
            }
        SB;
    }
#pragma unroll
    for (int i = 0; i < 4; i++) pin(acc[i]);
    STAMP(5);
    EXCHANGE(2048);
    bar();
    STAMP(6);
    STAMP_OUT(0);
}

__global__ void __launch_bounds__(NT) __attribute__((aligned(4096))) fused_kernel(const u16* __restrict__ u, const u16* __restrict__ lar, const u16* __restrict__ aimg,
                                                   const u16* __restrict__ cre, const u16* __restrict__ cim, const u16* __restrict__ ldt,
                                                   const u16* __restrict__ dsk, const u16* __restrict__ x, const u16* __restrict__ wr,
                                                   const u16* __restrict__ wi, u16* __restrict__ out, char* ws) {
    const char* entry;
    asm volatile("s_getpc_b64 %0" : "=s"(entry));
    APar pr;
    if (blockIdx.x >= A0 && __builtin_amdgcn_readfirstlane(threadIdx.x) < 768) {
        const int a = blockIdx.x - A0, d = 6 * ((a & 7) * 12 + (a >> 3)) + __builtin_amdgcn_readfirstlane(threadIdx.x >> 7);
        asm volatile("global_load_ushort %0, %6, %8\n\tglobal_load_ushort %1, %6, %9\n\tglobal_load_ushort %2, %6, %10\n\t"
                     "global_load_ushort %3, %6, %11\n\tglobal_load_ushort %4, %7, %12\n\tglobal_load_ushort %5, %7, %13"
                     : "=&v"(pr.l), "=&v"(pr.a), "=&v"(pr.r), "=&v"(pr.i), "=&v"(pr.t), "=&v"(pr.d)
                     : "v"((d * NS + (threadIdx.x & 127)) * 2), "v"(d * 2), "s"(lar), "s"(aimg), "s"(cre), "s"(cim), "s"(ldt), "s"(dsk) : "memory");
    }
    if (__builtin_expect(blockIdx.x < NB2, 1)) head_b(entry, x, wr, wi, out, ws);
    else if (blockIdx.x >= A0) head_a(u, pr, out, ws);
    else warm_code(entry);
}

extern "C" void kernel_launch(void* const* d_in, const int* in_sizes, int n_in, void* d_out, int out_size, void* d_ws,
                              size_t ws_size, hipStream_t stream) {
    (void)hipFuncSetAttribute((const void*)fused_kernel, hipFuncAttributeMaxDynamicSharedMemorySize, LDS_BYTES);
    fused_kernel<<<GRID, NT, LDS_BYTES, stream>>>((const u16*)d_in[0], (const u16*)d_in[1], (const u16*)d_in[2], (const u16*)d_in[3],
                                                  (const u16*)d_in[4], (const u16*)d_in[5], (const u16*)d_in[6], (const u16*)d_in[7],
                                                  (const u16*)d_in[8], (const u16*)d_in[9], (u16*)d_out, (char*)d_ws);
}
```

```cpp
#define POLL_SLEEP 6
#include <hip/hip_runtime.h>

typedef unsigned short u16;
typedef unsigned int u32;
typedef float f2 __attribute__((ext_vector_type(2)));
typedef float f4 __attribute__((ext_vector_type(4)));
typedef u32 u2 __attribute__((ext_vector_type(2)));
typedef u32 u4 __attribute__((ext_vector_type(4)));
typedef int i8v __attribute__((ext_vector_type(8)));
typedef short s4 __attribute__((ext_vector_type(4)));
typedef short s8 __attribute__((ext_vector_type(8)));
typedef __bf16 bf2 __attribute__((ext_vector_type(2)));
#define DI __device__ __forceinline__
#define LAS __attribute__((address_space(3)))
#define GAS __attribute__((address_space(1)))
#define CAS __attribute__((address_space(4)))
#pragma clang diagnostic ignored "-Wint-to-pointer-cast"
struct u3 { u32 v[3]; DI u32& operator[](int i) { return v[i]; } DI const u32& operator[](int i) const { return v[i]; } };

constexpr int SEQ = 2048, DM = 576, NS = 128, DF = 289;
constexpr int NB1 = 128;
constexpr int NB2 = 145;
constexpr int A0 = 152;
constexpr int NA = 96;
constexpr int H0 = A0 + NA;
constexpr int GRID = H0 + 8;
constexpr int NT = 1024;

constexpr u32 WS_EPOCH = 0, WS_F1 = 4096, WS_FY = WS_F1 + 8 * 16 * 128, WS_X1 = 1u << 20, WS_Y1 = 4u << 20, WS_USED = 8u << 20;
#define STAMP(i)
#define STAMP_DECL
#define STAMP_OUT(T)

constexpr int F_SA = 0, F_EO = 4;
constexpr int F_ARE = 9, F_AIM = 10, F_BRE = 11, F_BIM = 12, F_G = 13;
constexpr int F_F16 = 21, F_GI = 23, F_AI = 31, F_M1C = 33;
constexpr int F_VAR = 37;
constexpr int F_END = F_VAR + 40;
constexpr int ET = 452, ER = 28, E2V = 580, E2U = 36, OP = 1168;
constexpr int LB_X = 0, LB_I = 0, LB_Y = 0;
constexpr int LB_E = 16 * OP;
constexpr int LB_O = LB_E, LB_E2 = LB_E;
constexpr int LB_S = LB_E + 16 * ET * 4;
constexpr int LB_W = LB_S + 1200 * 16;
constexpr int LB_T = LB_W + 2 * 256 * 36;
constexpr int LB_G = LB_T + (F_VAR + 16) * 1024;
constexpr int LB_END = LB_G + 16;
struct CosTab { double c[2048]; double c9[9], s9[9]; };
constexpr double tay_cos(double x) { double t = 1, s = 1; for (int k = 1; k <= 14; k++) { t *= -x * x / ((2 * k - 1) * (2 * k)); s += t; } return s; }
constexpr double tay_sin(double x) { double t = x, s = x; for (int k = 1; k <= 14; k++) { t *= -x * x / ((2 * k + 1) * (2 * k)); s += t; } return s; }
constexpr CosTab make_cos() {
    CosTab t = {};
    const double PI2 = 6.283185307179586476925;
    for (int k = 0; k <= 512; k++) {
        const double v = k == 512 ? 0.0 : (k <= 256 ? tay_cos(PI2 * k / 2048) : tay_sin(PI2 * (512 - k) / 2048));
        t.c[k] = v; t.c[(2048 - k) & 2047] = v; t.c[1024 - k] = -v; t.c[(1024 + k) & 2047] = -v;
    }
    t.c[512] = 0.0; t.c[1536] = 0.0;
    for (int k = 0; k < 9; k++) { const double a = PI2 * (k <= 4 ? k : k - 9) / 9; t.c9[k] = tay_cos(a); t.s9[k] = tay_sin(a); }
    return t;
}
constexpr CosTab CT = make_cos();
constexpr double cs(int n) { return CT.c[n & 2047]; }
constexpr double sn(int n) { return CT.c[(n - 512) & 2047]; }
constexpr u32 bfr(double x) { const u32 b = __builtin_bit_cast(u32, (float)x); return (b + 0x7fffu + ((b >> 16) & 1u)) >> 16; }
constexpr u32 pkc(double lo, double hi) { return bfr(lo) | (bfr(hi) << 16); }
constexpr u32 f32c(double x) { return __builtin_bit_cast(u32, (float)x); }
template <int N> struct Frags { u32 w[N * 256]; };
constexpr u32 w_re(double amp, int n) { return pkc(amp * cs(n), amp * sn(n)); }
constexpr u32 w_im(double amp, int n) { return pkc(-amp * sn(n), amp * cs(n)); }
constexpr Frags<F_VAR> make_t0() {
    Frags<F_VAR> t = {};
    const double R2 = 1.4142135623730951;
    for (int l = 0; l < 64; l++)
        for (int i = 0; i < 4; i++) {
            const int c = l & 15, g = l >> 4, r = 4 * g + i, o = l * 4 + i;
            {
                const int b = c >> 2, tq = c & 3;
                const double amp = b ? 1.0 : R2;
                const int n0 = 512 * tq * g + (b ? 256 * b * (2 * i) : -256), n1 = 512 * tq * g + (b ? 256 * b * (2 * i + 1) : 256);
                t.w[F_ARE * 256 + o] = pkc(amp * cs(n0), amp * cs(n1));
                t.w[F_AIM * 256 + o] = pkc(-amp * sn(n0), -amp * sn(n1));
            }
            for (int w = 0; w < 9; w++) {
                const int sh = 16 * (i & 1), oo = w * 128 + l * 2 + (i >> 1);
                t.w[F_EO * 256 + oo] |= (u32)((c * ET + r * ER + 2 * ((7 * w + r) % 9)) * 4) << sh;
                if (w < 8) {
                    const int k = (9 * (w & 3) + 36 * r + 64 * (c % 9)) % DM, slot = w < 4 ? (k <= 288 ? 2 * k : 2 * (DM - k) + 1) : k;
                    t.w[F_SA * 256 + oo] |= (u32)(slot * 8 + (slot >> 6) * 16) << sh;
                }
            }
            if (r < 9) { const int k = (r * c) % 9; t.w[F_BRE * 256 + o] = pkc(CT.c9[k], CT.s9[k]); t.w[F_BIM * 256 + o] = pkc(-CT.s9[k], CT.c9[k]); }
            for (int q = 0; q < 4; q++) {
                t.w[(F_G + 2 * q) * 256 + o] = w_re(1.0, 32 * r * (q + 4 * c)); t.w[(F_G + 2 * q + 1) * 256 + o] = w_im(1.0, 32 * r * (q + 4 * c));
                t.w[(F_GI + 2 * q) * 256 + o] = w_re(1.0, 32 * c * (q + 4 * r)); t.w[(F_GI + 2 * q + 1) * 256 + o] = w_im(1.0, 32 * c * (q + 4 * r));
            }
            t.w[F_F16 * 256 + o] = w_re(1.0, 128 * r * c); t.w[(F_F16 + 1) * 256 + o] = w_im(1.0, 128 * r * c);
            for (int h = 0; h < 2; h++) {
                const int j = 4 * h + (c >> 2), n = 512 * i * (c & 3) + (g ? 256 * g * j : ((j & 1) ? 256 : -256));
                t.w[(F_AI + h) * 256 + o] = w_re(g ? 2.0 : R2, n);
                const int m = 16 * r * (4 * h + 8 * c) - 512 * h;
                t.w[(F_M1C + 2 * h) * 256 + o] = pkc(0.5 * cs(m), -0.5 * sn(m));
                t.w[(F_M1C + 2 * h + 1) * 256 + o] = pkc(-0.5 * sn(m), -0.5 * cs(m));
            }
        }
    return t;
}
constexpr Frags<40> make_t1() {
    Frags<40> t = {};
    for (int v = 0; v < 5; v++)
        for (int l = 0; l < 64; l++)
            for (int i = 0; i < 4; i++) {
                const int be = v < 3 ? v + 1 : 4 * (v - 3), c = l & 15, r = 4 * (l >> 4) + i, o = v * 2048 + l * 4 + i;
                const double amp = v < 3 ? 1.0 : 0.5;
                const int n1 = 16 * r * (be + 8 * c) + (v == 4 ? 512 : 0), n4 = 16 * c * (be + 8 * r);
                t.w[o] = w_re(amp, n1); t.w[o + 256] = w_im(amp, n1);
                t.w[o + 512] = w_re(1.0, n4); t.w[o + 768] = w_im(1.0, n4);
                t.w[o + 1024] = f32c(cs(r * (be + 8 * c))); t.w[o + 1280] = f32c(-sn(r * (be + 8 * c)));
                t.w[o + 1536] = f32c(cs(c * (be + 8 * r))); t.w[o + 1792] = f32c(-sn(c * (be + 8 * r)));
            }
    return t;
}
constexpr int qidx(int l) {
    if (l <= 256) return l;
    const int sh = l < 512 ? 1 : (l < 1024 ? 2 : 3), h = 1 << (sh - 1), m = l >> sh, r = l & ((1 << sh) - 1);
    return 128 * sh + m + ((r > h || (r == h && (m & 1))) ? 1 : 0);
}
constexpr Frags<6> make_t2() {
    Frags<6> t = {};
    for (int l = 0; l < 64; l++) {
        const int c = l & 15, g = l >> 4, e = 543 - 2 * c - g;
        t.w[256 + l * 4] = (u32)((e < 518 ? qidx(e) : 643) * 4) | ((u32)((e - 1 < 518 ? qidx(e - 1) : 643) * 4) << 16);
        for (int i = 0; i < 4; i++)
            for (int h = 0; h < 2; h++) {
                const int lag = 1024 + 16 + c - 8 * g - 2 * i - h, x = 8 * g + 2 * i + h;
                t.w[l * 4 + i] |= (lag < 1027 || lag > 1035 ? 0x0c0cu : (lag < 1029 ? 0x0100u : 0x0302u)) << (16 * h);
                if (x >= c + 8 && x <= c + 16) t.w[2 * 256 + l * 4 + i] |= 0x3f80u << (16 * h);
                if (x >= c + 1 && x <= c + 7) t.w[3 * 256 + l * 4 + i] |= 0x3f80u << (16 * h);
                if (x >= c + 6 && x <= c + 10) t.w[4 * 256 + l * 4 + i] |= 0x3f80u << (16 * h);
                if (x >= c + 3 && x <= c + 5) t.w[5 * 256 + l * 4 + i] |= 0x3f80u << (16 * h);
            }
    }
    return t;
}
struct Tables { Frags<F_VAR> a; Frags<40> b; Frags<6> c; };
constexpr Frags<6> T2 = make_t2();
constexpr Frags<F_VAR> T0 = make_t0();
constexpr Frags<40> T1 = make_t1();
static __device__ const __attribute__((aligned(128))) Tables TAB = {T0, T1, T2};
constexpr int LA_G = 0;
constexpr int LA_K = LA_G + 12 * 4608;
constexpr int KI = 1344, KQ = 4 * KI, KP = KQ + 512, KX = KP + 320, KC = KX + 16;
constexpr int LA_U = LA_K + 6 * KC;
constexpr int LA_Q = LA_U + 6 * 4608;
constexpr int LA_F = LA_Q + 12 * 2576;
constexpr int LA_S = LA_F + 16;
constexpr int LA_END = LA_S + 6144;
constexpr int FU2 = 0;
constexpr int LDS_BYTES = (LA_END > LB_END ? LA_END : LB_END);
static_assert(LDS_BYTES <= 160 * 1024 && LDS_BYTES > 80 * 1024, "one workgroup per CU");

DI u32 pk(float a, float b) { f2 v = {a, b}; return __builtin_bit_cast(u32, __builtin_convertvector(v, bf2)); }
DI float blo(u32 v) { return __uint_as_float(v << 16); }
DI float bhi(u32 v) { return __uint_as_float(v & 0xffff0000u); }
DI float bf2f(u16 v) { return __uint_as_float((u32)v << 16); }
DI float rbf(float x) { return blo(pk(x, x)); }
DI f2 cmul(f2 a, f2 b) { f2 t = a.xx * b; f2 s = {-b.y, b.x}; return __builtin_elementwise_fma(a.yy, s, t); }
DI f4 mfma(u4 a, u4 b, f4 c) { return __builtin_amdgcn_mfma_f32_16x16x32_bf16(__builtin_bit_cast(s8, a), __builtin_bit_cast(s8, b), c, 0, 0, 0); }
DI u4 pack4(f4 re, f4 im) { u4 r = {pk(re[0], im[0]), pk(re[1], im[1]), pk(re[2], im[2]), pk(re[3], im[3])}; return r; }
DI u4 twid(f4 re, f4 im, f4 wr, f4 wi) { return pack4(__builtin_elementwise_fma(re, wr, -(im * wi)), __builtin_elementwise_fma(re, wi, im * wr)); }
DI int mod9(int x) { return x - 9 * ((x * 57) >> 9); }
template <class T> DI void pin(T& v) { asm volatile("" : "+v"(v)); }

template <class T> DI T lds_ld(int off) { return *(const LAS T*)(u32)off; }
template <class T> DI void lds_st(int off, T v) { *(LAS T*)(u32)off = v; }
DI void bar() { __syncthreads(); }
#define PRIO(n) __builtin_amdgcn_s_setprio(n)
#define PRIOB(n)
DI void glds16(const void* g, u32 off, int lds) {
    u32 keep;
    asm volatile("s_mov_b32 %0, m0\n\ts_mov_b32 m0, %3\n\ts_nop 4\n\tglobal_load_lds_dwordx4 %1, %2\n\ts_mov_b32 m0, %0" : "=&s"(keep) : "v"(off), "s"(g), "s"(lds) : "memory");
}

DI void publish_tag(u32 epoch) {
    u32 t;
    asm volatile("v_add_u32 %0, 0x9E3779B1, %1\n\tds_write_b32 %2, %0\n\ts_waitcnt lgkmcnt(0)" : "=&v"(t) : "v"(epoch), "v"(LB_G) : "memory");
}
DI void wait_flag(__amdgpu_buffer_rsrc_t r, u32 off, u32 tag, bool on) {
    if (on) {
        const unsigned long long t0 = __builtin_amdgcn_s_memrealtime();
        while (!__all(__builtin_amdgcn_raw_buffer_load_b32(r, off, 0, 16) == tag)) {
            if (__builtin_amdgcn_s_memrealtime() - t0 > 5000000ull) break;
            __builtin_amdgcn_s_sleep(POLL_SLEEP);
            asm volatile("" ::: "memory");
        }
    }
    asm volatile("" ::: "memory");
}

constexpr u32 POISON = 0xFFFFFFFFu;
DI bool unset(u4 v) { return max(max(v[0], v[1]), max(v[2], v[3])) == POISON; }
#define DPOLL_SLEEP 1
template <bool TWO> DI void load_set(__amdgpu_buffer_rsrc_t r, u32 off, bool on, u4& a, bool on2, u4& b) {
    const unsigned long long t0 = __builtin_amdgcn_s_memrealtime();
    bool ma = on, mb = TWO && on2;
    for (;;) {
        if (ma) a = __builtin_amdgcn_raw_buffer_load_b128(r, off, 0, 16);
        if (TWO && mb) b = __builtin_amdgcn_raw_buffer_load_b128(r, off, 16384, 16);
        ma = ma && unset(a);
        if (TWO) mb = mb && unset(b);
        if (!__any(ma || mb) || __builtin_amdgcn_s_memrealtime() - t0 > 5000000ull) break;
        __builtin_amdgcn_s_sleep(DPOLL_SLEEP);
        asm volatile("" ::: "memory");
    }
}
DI int unit_w0(int u) {
    const int q = min((u * 57) >> 11, 3), kg = u - 36 * q, b = (q + 1) & 3;
    return u >= 108 ? min(16 * kg, 562) : (kg >= 18 ? (2048 - b) * (2 * DF) + 1122 - 32 * kg : b * (2 * DF) + 32 * kg);
}
constexpr int CODE_LINES = 120;
DI void warm_code(const char* entry) {
    if (threadIdx.x < CODE_LINES) { const u32 t = *(const GAS u32*)(entry + threadIdx.x * 128); asm volatile("" :: "v"(t)); }
}

DI void head_b(const char* entry, const u16* __restrict__ x, const u16* __restrict__ wr, const u16* __restrict__ wi,
               u16* __restrict__ out, char* ws) {
    const int tid = threadIdx.x, lane = tid & 63, c16 = lane & 15, g = lane >> 4;
    const int wv = __builtin_amdgcn_readfirstlane(tid >> 6);
    const int id = blockIdx.x;
    STAMP_DECL;
    const bool rows = id < NB1;
    const int s00 = 32 * (id >> 4) + (id & 15);
    const char* tab = (const char*)&TAB;
    const int R = (tid * 911) >> 16;
    const u32 xoff = (s00 + 16 * (R >> 3) + 256 * (R & 7)) * (2 * DM) + (tid - R * 72) * 16;
    asm volatile("" :: "s"(x), "s"(wr), "s"(wi), "s"(out), "s"(ws), "v"(xoff));
    const __amdgpu_buffer_rsrc_t rws = __builtin_amdgcn_make_buffer_rsrc(ws, 0, WS_USED, 0x00020000);
    STAMP(13);
    int gy = WS_Y1 + id * (NB2 * 128) + tid * 16;
    u32 epoch = 0;
    if (wv == 0) asm volatile("global_load_dword %0, %0, %1 sc1" : "+v"(epoch) : "s"(ws) : "memory");
    if (rows) {
        glds16(x, xoff, LB_X + wv * 1024);
        if (wv < 2) glds16(x, (s00 + 16 + 1536) * (2 * DM) + (tid < 56 ? (tid + 16) * 16 : 256 * 2 * DM + (tid - 56) * 16), LB_X + 16384 + wv * 1024);
        else glds16(tab, tid * 16 - 2048, LB_T + (wv - 2) * 1024);
        if (wv >= 9) glds16(tab, tid * 16 + 5 * 1024, LB_T + (wv + 5) * 1024);
    }
    const int u2i = (id & 7) ? (id & 7) * 18 + 1 + (id >> 3) : (id == 136 ? 0 : (id >> 3) + (id < 136 ? 1 : 0));
    const bool star = u2i >= 108;
    const int q36 = min((u2i * 57) >> 11, 3), kg = u2i - 36 * q36, bq = (q36 + 1) & 3;
    const bool mir = !star && kg >= 18;
    const int w0 = unit_w0(u2i);
    __builtin_amdgcn_s_barrier();
    STAMP(14);
    const u32 woff = w0 + ((tid >> 1) & 255) * (mir ? -16 * DF : 16 * DF) + (tid & 1) * (star ? 8 * DF : 16);
    u4 wra, wrb;
    if (wv >= 8) asm volatile("global_load_dwordx4 %0, %2, %3\n\tglobal_load_dwordx4 %1, %2, %4" : "=&v"(wra), "=&v"(wrb) : "v"(woff), "s"(wr), "s"(wi) : "memory");
#define STAGE23 do { const int o = LB_W + ((tid >> 1) & 255) * 36 + (tid & 1) * 16, t16 = (tid - 512) * 16, lw = LB_T + (wv - 8) * 1024; \
        glds16(tab, F_F16 * 1024 + t16, lw + F_F16 * 1024); glds16(tab, (F_F16 + 8) * 1024 + t16, lw + (F_F16 + 8) * 1024); \
        glds16(tab, (F_VAR + 8 * q36) * 1024 + t16, lw + F_VAR * 1024); if (star) glds16(tab, (F_VAR + 32) * 1024 + t16, lw + (F_VAR + 8) * 1024); \
        asm volatile("s_waitcnt vmcnt(0)\n\tds_write2_b32 %0, %2, %3 offset1:1\n\tds_write2_b32 %0, %4, %5 offset0:2 offset1:3\n\t" \
                     "ds_write2_b32 %1, %6, %7 offset1:1\n\tds_write2_b32 %1, %8, %9 offset0:2 offset1:3\n\ts_waitcnt lgkmcnt(0)" \
                     :: "v"(o), "v"(o + 9216), "v"(wra[0]), "v"(wra[1]), "v"(wra[2]), "v"(wra[3]), "v"(wrb[0]), "v"(wrb[1]), "v"(wrb[2]), "v"(wrb[3]) : "memory"); } while (0)
    const f4 Z = {0.f, 0.f, 0.f, 0.f};
    const int tau = wv & 3, bw = wv >> 2;
    int tl = LB_T + lane * 16;
    int lin = LB_S + (tid + (tid >> 5)) * 16;
    pin(tl); pin(lin);
    int sadr[4];

    u32 tag;
    if (rows) {
        if (tid < 256) {
            const int o = LB_E + 72 + (tid & 15) * (ER * 4) + (tid >> 4) * (ET * 4);
            lds_st<u2>(o, u2{0u, 0u}); lds_st<u4>(o + 8, u4{0u, 0u, 0u, 0u});
        }
        int xo = LB_X + (c16 >> 2) * 1152 + 2 * (144 * ((g - wv) & 3) + 16 * wv + 4 * (c16 & 3));
        int bo = LB_E + (wv * ET + c16 * ER + min(8 * g, 16)) * 4;
        int go = WS_X1 + (tid >> 3) * 16384 + id * 128 + (tid & 7) * 16;
        int ta = LB_T + lane * 8;
        pin(xo); pin(bo); pin(go); pin(ta);
        STAMP(1);
        asm volatile("s_cmp_lt_u32 %0, 8\n\ts_cbranch_scc0 .Lw%=\n\ts_waitcnt vmcnt(0)\n.Lw%=:\n\ts_waitcnt vmcnt(2)" :: "s"(wv) : "memory", "scc");
        if (wv == 0) publish_tag(epoch);
        bar();
        STAMP(2);
        if (wv < 9) {
            u4 d[2];
#pragma unroll
            for (int s = 0; s < 2; s++) {
                const s4 lo = __builtin_amdgcn_ds_read_tr16_b64_v4i16((LAS s4*)(u32)(xo + s * 9216));
                const s4 hi = __builtin_amdgcn_ds_read_tr16_b64_v4i16((LAS s4*)(u32)(xo + s * 9216 + 4608));
                d[s] = __builtin_bit_cast(u4, __builtin_shufflevector(lo, hi, 0, 1, 2, 3, 4, 5, 6, 7));
            }
            const u4 are = lds_ld<u4>(tl + F_ARE * 1024), aim = lds_ld<u4>(tl + F_AIM * 1024);
            const u2 eo = lds_ld<u2>(ta + F_EO * 1024 + wv * 512);
#pragma unroll
            for (int s = 0; s < 2; s++) d[s] = pack4(mfma(d[s], are, Z), mfma(d[s], aim, Z));
#pragma unroll
            for (int i = 0; i < 4; i++) lds_st<u2>(LB_E + ((i & 1) ? eo[i >> 1] >> 16 : eo[i >> 1] & 0xffff), u2{d[0][i], d[1][i]});
        }
        u4 bre = lds_ld<u4>(tl + F_BRE * 1024), bim = lds_ld<u4>(tl + F_BIM * 1024);
        u4 gre = lds_ld<u4>(tl + (F_G + 2 * tau) * 1024), gim = lds_ld<u4>(tl + (F_G + 2 * tau + 1) * 1024);
        {
            const u2 sa = lds_ld<u2>(ta + F_SA * 1024 + (tau + (bw ? 4 : 0)) * 512);
            const int base = LB_S + ((bw + 3) & 3) * (DM * 8 + 144);
#pragma unroll
            for (int i = 0; i < 4; i++) { sadr[i] = base + ((i & 1) ? sa[i >> 1] >> 16 : sa[i >> 1] & 0xffff); pin(sadr[i]); }
        }
        tag = lds_ld<u32>(LB_G);
        pin(bre); pin(bim); pin(gre); pin(gim); pin(tag);
        bar();
        {
            PRIOB(2);
            const u4 p0 = lds_ld<u4>(bo), p1 = lds_ld<u4>(bo + 16);
            u4 d[2];
            f4 re[2], im[2];
#pragma unroll
            for (int s = 0; s < 2; s++) { const u4 a = {p0[s], p0[2 + s], p1[s], p1[2 + s]}; re[s] = mfma(a, bre, Z); im[s] = mfma(a, bim, Z); }
            PRIOB(1);
#pragma unroll
            for (int s = 0; s < 2; s++) { const u4 t = pack4(re[s], im[s]); re[s] = mfma(gre, t, Z); im[s] = mfma(gim, t, Z); }
            PRIOB(0);
#pragma unroll
            for (int s = 0; s < 2; s++) d[s] = pack4(re[s], im[s]);
            if (c16 < 9) {
#pragma unroll
                for (int i = 0; i < 4; i++) lds_st<u2>(sadr[i], u2{d[0][i], d[1][i]});
                if (wv == 0 && c16 == 0 && !(g & 1)) lds_st<u2>(sadr[0] + 8, u2{d[0][0], d[1][0]});
            }
        }
        bar();
        STAMP(3);
        if (wv < 8) {
            u4 v0 = lds_ld<u4>(lin), v1 = lds_ld<u4>(lin + 528 * 16), v2 = lds_ld<u4>(lin + (wv < 3 ? 1056 * 16 : 0));
            pin(v2);
            __builtin_amdgcn_raw_buffer_store_b128(v0, rws, go, 0, 16);
            __builtin_amdgcn_raw_buffer_store_b128(v1, rws, go, 64 * 16384, 16);
            if (tid <= 128) __builtin_amdgcn_raw_buffer_store_b128(v2, rws, go, 128 * 16384, 16);
            asm volatile("s_waitcnt vmcnt(0)" ::: "memory");
            if (lane == 0) __builtin_amdgcn_raw_buffer_store_b32(tag, rws, WS_F1 + (wv * 16 + (id >> 3)) * 128 + (id & 7) * 4, 0, 16);
            STAMP(4);
        } else {
            STAGE23;
            const u4 P = {POISON, POISON, POISON, POISON};
            __builtin_amdgcn_raw_buffer_store_b128(P, rws, gy - 8192, 0, 16);
            __builtin_amdgcn_raw_buffer_store_b128(P, rws, gy, 0, 16);
            if (tid <= 640) __builtin_amdgcn_raw_buffer_store_b128(P, rws, gy, 8192, 16);
        }
        bar();
    } else {
        if (wv < 8 && (kg == 0 || kg == (star ? 36 : 18))) {
            const int m = tid & 255;
            lds_st<u16>(LB_W + tid * 36 + 32, ((wv >> 2) ? wi : wr)[((mir ? bq + 8 * m : 2048 - bq - 8 * m) & 2047) * DF + (kg ? 288 : 0)]);
        }
        warm_code(entry);
        if (wv >= 8) STAGE23;
        asm volatile("s_waitcnt vmcnt(0)" ::: "memory");
        if (wv == 0) publish_tag(epoch);
        bar();
        tag = lds_ld<u32>(LB_G);
    }
    STAMP(12);

    const int typ = star ? (wv & 1) : 0;
    const int tv = tl + (F_VAR + 8 * typ) * 1024;
    u4 fre = lds_ld<u4>(tl + F_F16 * 1024), fim = lds_ld<u4>(tl + (F_F16 + 1) * 1024);
    u4 m1r = lds_ld<u4>(tv), m1i = lds_ld<u4>(tv + 1024), m4r = lds_ld<u4>(tv + 2048), m4i = lds_ld<u4>(tv + 3072);
    f4 twr = lds_ld<f4>(tv + 4096), twi = lds_ld<f4>(tv + 5120), tur = lds_ld<f4>(tv + 6144), tui = lds_ld<f4>(tv + 7168);
    u4 mcr = lds_ld<u4>(tl + (F_M1C + 2 * typ) * 1024), mci = lds_ld<u4>(tl + (F_M1C + 2 * typ + 1) * 1024);
    f4 wfr, wfi;
    {
        const int kcol = star ? 8 * kg + (wv >> 1) : 16 * kg + wv;
        const int cc = star ? 8 * typ + (u2i < NB2 - 1 ? (wv >> 1) : 7) : (mir ? 15 - wv : wv);
        const int wo = LB_W + (c16 + 64 * g) * 36 + cc * 2;
#pragma unroll
        for (int i = 0; i < 4; i++) {
            wfr[i] = bf2f(lds_ld<u16>(wo + i * 576));
            wfi[i] = bf2f(lds_ld<u16>(wo + i * 576 + 9216));
        }
        float sc = 1.f / (float)(SEQ * DM);
        if (kcol == 0 || kcol == 288) {
            sc *= 0.5f;
#pragma unroll
            for (int i = 0; i < 4; i++) {
                const int m = c16 + 64 * g + 16 * i;
                const int o = LB_W + (typ ? (255 - m) * 36 + cc * 2 : m * 36 + 32);
                wfr[i] += bf2f(lds_ld<u16>(o));
                wfi[i] -= bf2f(lds_ld<u16>(o + 9216));
            }
        }
        wfr *= sc; wfi *= (mir ? -sc : sc);
    }
    f4 wnr = -wfr, wni = -wfi;
    int io = (32 * g + c16) * 128 + (((wv >> 1) ^ (c16 >> 1)) * 16);
    int so = (tid >> 3) * 128 + (((tid & 7) ^ ((tid >> 4) & 7)) * 16);
    pin(fre); pin(fim); pin(m1r); pin(m1i); pin(m4r); pin(m4i); pin(twr); pin(twi); pin(tur); pin(tui); pin(mcr); pin(mci);
    int gi = WS_X1 + u2i * 16384 + tid * 16, gq = WS_Y1 + (tid >> 3) * (NB2 * 128) + u2i * 128 + (tid & 7) * 16;
    int i1 = LB_I + io + (star ? 0 : (wv & 1) * 8);
    io += LB_O + (wv & 1) * 8;
    pin(wfr); pin(wnr); pin(wni); pin(io); pin(so); pin(gi); pin(gq); pin(i1);
    u32 fy = WS_FY + (((u2i >> 3) & 7) * 16 + wv) * 128 + (lane & 7) * 4;
    pin(fy);
    if (rows && wv >= 8) {
        asm volatile("s_waitcnt vmcnt(0)" ::: "memory");
        if (lane == 0) __builtin_amdgcn_raw_buffer_store_b32(tag, rws, WS_FY + ((wv - 8) * 16 + (id >> 3)) * 128 + (id & 7) * 4, 0, 16);
    }
    wait_flag(rws, WS_F1 + (((u2i >> 3) & 7) * 16 + wv) * 128 + lane * 4, tag, lane < 8);
    STAMP(5);
    lds_st<u4>(LB_I + so, __builtin_amdgcn_raw_buffer_load_b128(rws, gi, 0, 16));
    bar();
    STAMP(6);
    u32 rdy;
    {
        const int o1 = i1;
        PRIOB(3);
        const u2 a0 = lds_ld<u2>(o1), a1 = lds_ld<u2>(o1 + 2048);
        const u4 a = {a0[0], a0[1], a1[0], a1[1]};
        f4 re = mfma(a, m1r, Z), im = mfma(a, m1i, Z);
        if (star) {
            const u2 b0 = lds_ld<u2>(o1 + 8), b1 = lds_ld<u2>(o1 + 8 + 2048);
            const u4 b = {b0[0], b0[1], b1[0], b1[1]};
            re = mfma(b, mcr, re); im = mfma(b, mci, im);
        }
        u4 t = twid(re, im, twr, twi);
        re = mfma(fre, t, Z); im = mfma(fim, t, Z);
        PRIOB(2);
        __builtin_amdgcn_sched_barrier(0);
        asm volatile("buffer_load_dword %0, %1, %2, 0 offen sc1" : "=&v"(rdy) : "v"(fy), "s"(rws), "v"(t) : "memory");
        __builtin_amdgcn_sched_barrier(0);
        t = pack4(__builtin_elementwise_fma(re, wfr, im * wni), __builtin_elementwise_fma(re, wni, im * wnr));
        re = mfma(t, fre, Z); im = mfma(t, fim, Z);
        PRIOB(1);
        t = twid(re, im, tur, tui);
        re = mfma(m4r, t, Z); im = mfma(m4i, t, Z);
        PRIOB(0);
        t = pack4(re, im);
        lds_st<u2>(io, u2{t[0], t[1]}); lds_st<u2>(io + 2048, u2{t[2], t[3]});
    }
    bar();
    STAMP(7);
    u4 sv;
    {
        u4 v = lds_ld<u4>(LB_O + so);
        if (star) {
            u4 w;
#pragma unroll
            for (int s = 0; s < 2; s++) {
                const float ar = blo(v[s]), ai = bhi(v[s]), cr = blo(v[2 + s]), ci = bhi(v[2 + s]);
                w[s] = pk(ar + ci, ai - cr); w[2 + s] = pk(ar - ci, -ai - cr);
            }
            v = w;
        }
        asm volatile("s_waitcnt vmcnt(0)" : "+v"(rdy) :: "memory");
        if (!__all(rdy == tag)) wait_flag(rws, fy, tag, true);
        if (u2i < NB2 - 1 || (tid & 7) == 0)
            __builtin_amdgcn_raw_buffer_store_b128(v, rws, gq, 0, 16);
        sv = v;
    }
    STAMP(8);
    if (!rows) { STAMP_OUT(0); return; }

    u4 gre = lds_ld<u4>(tl + (F_GI + 2 * tau) * 1024), gim = lds_ld<u4>(tl + (F_GI + 2 * tau + 1) * 1024);
    u4 bre = lds_ld<u4>(tl + F_BRE * 1024), bim = lds_ld<u4>(tl + F_BIM * 1024);
    u4 ai0 = lds_ld<u4>(tl + F_AI * 1024), ai1 = lds_ld<u4>(tl + (F_AI + 1) * 1024);
    int eadr[4];
#pragma unroll
    for (int i = 0; i < 4; i++) {
        const int r = 4 * g + i;
        eadr[i] = LB_E2 + (mod9(4 * (c16 + 18 - r)) * E2V + r * E2U + 2 * wv) * 4;
        pin(eadr[i]);
    }
    int ao = LB_E2 + (wv * E2V + c16 * E2U + 8 * g) * 4;
    int yo = LB_Y + (c16 >> 2) * OP + 2 * (144 * (((c16 & 3) - wv) & 3) + 16 * wv + 4 * g);
    int yl = LB_Y + tid * 16 + R * (OP - 1152);
    u32 oo = 2 * xoff - (tid - R * 72) * 16 + 2 * DM;
    pin(gre); pin(gim); pin(bre); pin(bim); pin(ai0); pin(ai1); pin(ao); pin(gy); pin(yo); pin(yl); pin(oo);
    asm volatile("" :: "v"(sv));
    STAMP(9);
    {
        u4 v, w;
        if (wv < 3) { load_set<true>(rws, gy, true, v, tid <= 128, w); if (tid <= 128) lds_st<u4>(lin + 1056 * 16, w); }
        else load_set<false>(rws, gy, true, v, false, w);
        lds_st<u4>(lin, v);
    }
    bar();
    if (id == 0 && tid == 0) __builtin_amdgcn_raw_buffer_store_b32(tag, rws, WS_EPOCH, 0, 16);
    STAMP(10);
    {
        PRIOB(2);
        u4 d[2];
        u2 p[4];
#pragma unroll
        for (int i = 0; i < 4; i++) p[i] = lds_ld<u2>(sadr[i]);
        f4 re[2], im[2];
#pragma unroll
        for (int s = 0; s < 2; s++) { const u4 a = {p[0][s], p[1][s], p[2][s], p[3][s]}; re[s] = mfma(a, gre, Z); im[s] = mfma(a, gim, Z); }
        PRIOB(1);
#pragma unroll
        for (int s = 0; s < 2; s++) { const u4 t = pack4(re[s], im[s]); re[s] = mfma(t, bre, Z); im[s] = mfma(t, bim, Z); }
        PRIOB(0);
#pragma unroll
        for (int s = 0; s < 2; s++) d[s] = pack4(re[s], im[s]);
        if (c16 < 9) {
#pragma unroll
            for (int i = 0; i < 4; i++) lds_st<u2>(eadr[i], u2{d[0][i], d[1][i]});
        }
    }
    STAMP(15);
    bar();
    if (wv < 9) {
        const u4 p0 = lds_ld<u4>(ao), p1 = lds_ld<u4>(ao + 16);
#pragma unroll
        for (int s = 0; s < 2; s++) {
            const u4 a = {p0[s], p0[2 + s], p1[s], p1[2 + s]};
            const f4 y0 = mfma(a, ai0, Z), y1 = mfma(a, ai1, Z);
            lds_st<u2>(yo + s * (8 * OP), u2{pk(y0[0], y0[1]), pk(y0[2], y0[3])});
            lds_st<u2>(yo + s * (8 * OP) + 4 * OP, u2{pk(y1[0], y1[1]), pk(y1[2], y1[3])});
        }
    }
    bar();
    *(GAS u4*)((char*)out + oo) = lds_ld<u4>(yl);
    if (wv < 2) {
        const int r2 = tid < 56 ? 14 : 15, c2 = tid + 1024 - 72 * r2;
        *(GAS u4*)((char*)out + (u32)((s00 + 16 + 256 * (r2 - 8)) * (4 * DM) + 2 * DM + c2 * 16)) = lds_ld<u4>(LB_Y + r2 * OP + c2 * 16);
    }
    STAMP(11);
    STAMP_OUT(0);
}

DI f2 cexpa(float ar2, float air, float p) {
    const float e = __builtin_amdgcn_exp2f(ar2 * p), t = __builtin_amdgcn_fractf(air * p);
    return f2{e * __builtin_amdgcn_cosf(t), e * __builtin_amdgcn_sinf(t)};
}
DI u4 ld_k(int o) {
    const u2 a = *(const volatile LAS u2*)(u32)o, b = *(const volatile LAS u2*)(u32)(o + 8);
    return u4{a[0], a[1], b[0], b[1]};
}
DI void a_rows_out(int yb, int ystr, char* o) {
    u2 y[6];
#pragma unroll
    for (int c = 0; c < 6; c++) y[c] = lds_ld<u2>(yb + c * ystr);
#pragma unroll
    for (int r = 0; r < 4; r++) {
        u3 v;
#pragma unroll
        for (int c = 0; c < 3; c++) v[c] = __builtin_amdgcn_perm(y[2 * c + 1][r >> 1], y[2 * c][r >> 1], (r & 1) ? 0x07060302u : 0x05040100u);
        *(u3*)(o + r * (4 * DM)) = v;
    }
}
template <int R> DI void stage(u4 v) {
    asm volatile("ds_write_addtid_b32 %0 offset:%4\n\tds_write_addtid_b32 %1 offset:%5\n\tds_write_addtid_b32 %2 offset:%6\n\tds_write_addtid_b32 %3 offset:%7"
                 :: "v"(v[0]), "v"(v[1]), "v"(v[2]), "v"(v[3]), "i"(288 * R), "i"(288 * R + 288), "i"(288 * R + 576), "i"(288 * R + 864) : "memory");
}
#define CM_MUL(d, a, b) "v_pk_mul_f32 " d ", " a ", " b " op_sel_hi:[0,1]\n\t"
#define CM_FMA(d, a, b) "v_pk_fma_f32 " d ", " a ", " b ", " d " op_sel:[1,1,0] op_sel_hi:[1,0,1] neg_lo:[1,0,0]\n\t"
struct C4 { f2 a, b, c, d; };
DI C4 cmul4(f2 a0, f2 b0, f2 a1, f2 b1, f2 a2, f2 b2, f2 a3, f2 b3) {
    C4 r;
    asm("s_nop 0\n\t" CM_MUL("%0", "%4", "%5") CM_MUL("%1", "%6", "%7") CM_MUL("%2", "%8", "%9") CM_MUL("%3", "%10", "%11")
        CM_FMA("%0", "%4", "%5") CM_FMA("%1", "%6", "%7") CM_FMA("%2", "%8", "%9") CM_FMA("%3", "%10", "%11") "s_nop 0"
        : "=&v"(r.a), "=&v"(r.b), "=&v"(r.c), "=&v"(r.d) : "v"(a0), "v"(b0), "v"(a1), "v"(b1), "v"(a2), "v"(b2), "v"(a3), "v"(b3));
    return r;
}
DI C4 cmul4(const C4& a, f2 b) { return cmul4(a.a, b, a.b, b, a.c, b, a.d, b); }
DI u4 pk4(const C4& v) { return u4{pk(v.a.x, v.a.y), pk(v.b.x, v.b.y), pk(v.c.x, v.c.y), pk(v.d.x, v.d.y)}; }

DI void sig(int f) {
    asm volatile("s_waitcnt lgkmcnt(0)" ::: "memory");
    if ((threadIdx.x & 63) == 0) __hip_atomic_fetch_add((LAS u32*)(u32)(LA_F + 4 * f), 1u, __ATOMIC_RELAXED, __HIP_MEMORY_SCOPE_WORKGROUP);
}
template <int SLEEP = 0> DI void await(int f, u32 n) {
    while ((u32)__builtin_amdgcn_readfirstlane(*(const volatile LAS u32*)(u32)(LA_F + 4 * f)) < n) if (SLEEP) __builtin_amdgcn_s_sleep(SLEEP);
    asm volatile("" ::: "memory");
}
DI float exp_bf(float x) {
    const float t = x * 1.44269504f, r = __builtin_fmaf(x, 1.92596299e-8f, __builtin_fmaf(x, 1.44269504f, -t));
    const float e = __builtin_amdgcn_exp2f(t);
    return __builtin_fmaf(e * 0.693147181f, r, e);
}

struct APar { u32 l, a, r, i, t, d; };
DI void head_a(const u16* __restrict__ u, APar pr, u16* __restrict__ out, char* ws) {
    const int tid = threadIdx.x, lane = tid & 63, c16 = lane & 15, g = lane >> 4;
    const int wv = __builtin_amdgcn_readfirstlane(tid >> 6);
    STAMP_DECL;
    const int ai_ = blockIdx.x - A0;
    const int d0 = 6 * ((ai_ & 7) * 12 + (ai_ >> 3));
    const f4 Z = {0.f, 0.f, 0.f, 0.f};

    if (wv >= 12) {
        __builtin_amdgcn_s_setprio(3);
        const int t = tid - 768;
        if (t == 0) lds_st<u2>(LA_F, u2{0u, 0u});
        if (t < 180) lds_st<u4>(LA_U + (t / 30) * 4608 + (t % 30) * 16, u4{0u, 0u, 0u, 0u});
        bar();
        glds16((const char*)&TAB + F_END * 1024, t * 16, LA_S + (wv - 12) * 1024);
        if (wv < 14) glds16((const char*)&TAB + (F_END + 4) * 1024, t * 16, LA_S + (wv - 8) * 1024);
        u3 ua[4], ub[4];
#pragma unroll
        for (int r = 0; r < 4; r++) ua[r] = *(const u3*)(u + (4 * t + r) * DM + d0);
        asm volatile("s_waitcnt vmcnt(4)\n\ts_barrier" ::: "memory");
#pragma unroll
        for (int c = 0; c < 6; c++) {
            const u32 sel = (c & 1) ? 0x07060302u : 0x05040100u;
            lds_st<u2>(LA_U + c * 4608 + 480 + t * 8, u2{__builtin_amdgcn_perm(ua[1][c >> 1], ua[0][c >> 1], sel), __builtin_amdgcn_perm(ua[3][c >> 1], ua[2][c >> 1], sel)});
        }
        asm volatile("" ::: "memory");
#pragma unroll
        for (int r = 0; r < 4; r++) ub[r] = *(const u3*)(u + (1024 + 4 * t + r) * DM + d0);
        STAMP(7);
        bar();
#pragma unroll
        for (int c = 0; c < 6; c++) {
            const u32 sel = (c & 1) ? 0x07060302u : 0x05040100u;
            lds_st<u2>(LA_U + c * 4608 + 480 + 2048 + t * 8, u2{__builtin_amdgcn_perm(ub[1][c >> 1], ub[0][c >> 1], sel), __builtin_amdgcn_perm(ub[3][c >> 1], ub[2][c >> 1], sel)});
        }
        sig(FU2);
        STAMP(8);
        const int tile = wv - 12, yl = ((lane >> 2) + 16 * (lane & 3)) * 8;
        char* o = (char*)(out + 4 * t * (2 * DM) + d0);
        bar();
        bar();
        STAMP(11);
        a_rows_out(LA_G + ((tile >> 1) ^ 1) * 4608 + (tile & 1) * 1024 + yl, 9216, o);
        STAMP(9);
        bar();
        bar();
        STAMP(12);
        a_rows_out(LA_G + ((tile >> 1) ^ 1) * 4608 + (tile & 1) * 1024 + 2048 + yl, 9216, o + 1024 * (4 * DM));
        STAMP(10);
        STAMP_OUT(768);
        return;
    }
    const int ch = wv >> 1, hf = wv & 1;
    {
        __builtin_amdgcn_s_barrier();
        asm volatile("s_waitcnt vmcnt(0)" : "+v"(pr.l), "+v"(pr.a), "+v"(pr.r), "+v"(pr.i), "+v"(pr.t), "+v"(pr.d) :: "memory");
        PRIO(3);
        const float dt = rbf(exp_bf(blo(pr.t)));
        const float ar2 = -rbf(exp_bf(blo(pr.l))) * dt * 1.44269504f, air = blo(pr.a) * dt * 0.159154943f;
        const f2 C = {2.f * dt * blo(pr.r), 2.f * dt * blo(pr.i)};
        STAMP(1);
        const f2 z1 = cexpa(ar2, air, 1.f), z8c = cexpa(ar2, -air, 8.f), z128 = cexpa(ar2, air, 128.f);
        const int gr = LA_G + wv * 4608 + c16 * 288 + g * 16, q = LA_Q + wv * 2576;
        asm volatile("s_mov_b32 m0, %0\n\ts_nop 0" :: "s"(LA_G + wv * 4608) : "memory");
#define SB __builtin_amdgcn_sched_barrier(0)
#define STAGE16(v) do { stage<0>(v[0]); stage<4>(v[1]); stage<8>(v[2]); stage<12>(v[3]); } while (0)
#define READ4(f) do { _Pragma("unroll") for (int s = 0; s < 4; s++) f[s] = lds_ld<u4>(gr + 64 * s); SB; } while (0)
        u4 v[4], bfr[4], afr[4];
        const C4 s1 = cmul4(z8c, z8c, z1, z1, z128, z128, C, z1);
        const C4 s2 = cmul4(s1.a, s1.a, z8c, s1.a, s1.c, s1.c, C, s1.b);
        {
            C4 w = {f2{1.f, 0.f}, z8c, s1.a, s2.b};
            v[0] = pk4(w);
#pragma unroll
            for (int i = 1; i < 4; i++) { w = cmul4(w, s2.a); v[i] = pk4(w); }
        }
        STAGE16(v); READ4(bfr);
        PRIO(2);
        const C4 s3 = cmul4(s1.d, s1.b, s1.b, s1.b, s2.c, s2.c, s2.c, z128);
        const C4 e0 = {C, s1.d, s2.d, s3.a}, e1 = cmul4(e0, s3.b);
        v[0] = pk4(e0); v[1] = pk4(e1); v[2] = pk4(cmul4(e0, z128)); v[3] = pk4(cmul4(e1, z128));
        STAGE16(v); READ4(afr);
        PRIO(1);
        {
            const C4 f = cmul4(C4{e0.a, e0.c, e1.a, e1.c}, s1.c), h = cmul4(e0.a, s2.c, e1.a, s2.c, e0.a, s3.d, e1.a, s3.d);
            v[0] = pk4(f); v[1] = pk4(cmul4(f, z128)); v[2] = pk4(h); v[3] = pk4(cmul4(h, s1.c));
        }
        SB;
        f4 acc = Z;
#pragma unroll
        for (int s = 0; s < 4; s++) acc = mfma(afr[s], bfr[s], acc);
        STAGE16(v); READ4(afr);
        PRIO(0);
        lds_st<f4>(q + (4 * (g & 1) + 128 * (g >> 1) + 8 * c16) * 4, acc);
        {
            const f2 f0 = cmul(C, s3.c), f1 = cmul(f0, z128);
            const C4 f = cmul4(f0, s1.c, f1, s1.c, f0, s3.c, f0, s3.c), h = cmul4(C4{f0, f1, f.a, f.b}, s2.c);
            v[0] = pk4(C4{f0, f1, f.a, f.b}); v[1] = pk4(h); v[2][0] = pk(f.c.x, f.c.y);
        }
        SB;
        acc = Z;
#pragma unroll
        for (int s = 0; s < 4; s++) acc = mfma(afr[s], bfr[s], acc);
        stage<0>(v[0]); stage<4>(v[1]);
        asm volatile("ds_write_addtid_b32 %0 offset:%1" :: "v"(v[2][0]), "i"(288 * 8) : "memory");
        READ4(afr);
        if (g < 2) lds_st<f4>(q + (256 + 64 * g + 4 * c16) * 4, acc);
        else {
            const int o = q + (384 + 64 * (g - 2) + 2 * c16) * 4;
            lds_st<f2>(o, f2{acc[0], acc[1]}); lds_st<f2>(o + 128, f2{acc[2], acc[3]});
        }
        acc = Z;
#pragma unroll
        for (int s = 0; s < 4; s++) acc = mfma(afr[s], bfr[s], acc);
        if (g < 2) {
#pragma unroll
            for (int i = 0; i < 4; i++) lds_st<float>(q + (512 + 64 * g + 16 * i + c16) * 4, acc[i]);
        } else if (lane < 34) lds_st<float>(q + (lane == 32 ? 640 : 643) * 4, lane == 32 ? acc[0] : 0.f);
    }
    STAMP(2);
    asm volatile("s_waitcnt lgkmcnt(0)" ::: "memory");
    bar();
    STAMP(9);

    const int k0 = LA_K + ch * KC;
    {
        const LAS float* qa = (const LAS float*)(u32)(LA_Q + ch * 5152);
        const LAS float* qb = qa + 644;
        u2 w[4];
        int y;
        if (hf == 0) {
            const u32 ix = lds_ld<u32>(LA_S + 1024 + lane * 16);
            f4 k = *(const LAS f4*)(qa + 4 * lane) + *(const LAS f4*)(qb + 4 * lane);
            f4 p = *(const LAS f4*)(qa + 4 * lane - 4) + *(const LAS f4*)(qb + 4 * lane - 4);
            const f2 s = *(const LAS f2*)(qa + 512 - 2 * lane) + *(const LAS f2*)(qb + 512 - 2 * lane);
            const float e0 = qa[(ix & 0xffff) >> 2] + qb[(ix & 0xffff) >> 2], e1 = qa[ix >> 18] + qb[ix >> 18];
            lds_st<u32>(k0 + KQ + (32 + lane) * 4, pk(s.x, lane ? s.y : 0.f));
            if (lane == 0) lds_st<u32>(k0 + KX, pk(s.x, s.y));
            lds_st<u32>(k0 + KQ + (lane + (lane & 32) * 2) * 4, 0u);
            lds_st<u32>(k0 + g * KI + c16 * 4, pk(e0, e1));
            if (lane == 0) { k[0] += blo(pr.d); p = Z; }
            const u32 c = pk(k[1], k[0]), d = pk(k[0], p[3]);
            w[0] = u2{pk(k[3], k[2]), c}; w[1] = u2{pk(k[2], k[1]), d}; w[2] = u2{c, pk(p[3], p[2])}; w[3] = u2{d, pk(p[2], p[1])};
            y = k0 + (270 - 2 * lane) * 4;
        } else {
            const int j = 256 + 2 * lane;
            const float q0 = qa[j] + qb[j], q1 = qa[j + 1] + qb[j + 1], q2 = qa[j + 2] + qb[j + 2];
            const float p1 = qa[j - 1] + qb[j - 1], p2 = qa[j - 2] + qb[j - 2], p3 = qa[j - 3] + qb[j - 3];
            const u32 c = pk(q0, q0), d = pk(q0, p1), e = pk(p1, p2), f = lane ? c : d;
            w[0] = u2{pk(q2, q1), c}; w[1] = u2{pk(q1, q0), f}; w[2] = u2{c, lane ? d : e}; w[3] = u2{f, lane ? e : pk(p2, p3)};
            y = k0 + (142 - 2 * lane) * 4;
            if (lane < 48) lds_st<u32>(k0 + (lane < 32 ? (lane >> 3) * KI + (272 + (lane & 7)) * 4 : KP + (32 + lane) * 4), 0u);
            const f2 s = *(const LAS f2*)(qa + 640 - 2 * lane) + *(const LAS f2*)(qb + 640 - 2 * lane);
            lds_st<u32>(k0 + KP + lane * 4, pk(s.x, lane ? s.y : 0.f));
        }
#pragma unroll
        for (int i = 0; i < 4; i++) lds_st<u2>(y + i * KI, w[i]);
    }
    const int x0 = 2031 - c16 + 8 * g;
    int ka = k0 + (x0 & 3) * KI + (x0 >> 2) * 8 - 3008 - 256 * hf - 192 - 512;
    int ubs = LA_U + ch * 4608 + 480 + (16 * c16 + 8 * g - 16) * 2 - 256 * hf - 192;
    pin(ka); pin(ubs);
    const int lt = LA_S + lane * 16;
    const u4 bnd5 = lds_ld<u4>(lt + 4096), bnd3 = lds_ld<u4>(lt + 5120);
    int uv = LA_U + ch * 4608 + 480 + (8 * c16 + 8 * g - 24) * 2 + 256 * hf;
    pin(uv);
#define VSUM(n) ({ const u4 a_ = lds_ld<u4>(uv + 512 * (n)); pack4(mfma(a_, bnd5, Z), mfma(a_, bnd3, Z)); })
    const u4 bnd9 = lds_ld<u4>(lt + 2048), bnd7 = lds_ld<u4>(lt + 3072);
    STAMP(13);
    bar();
    STAMP(3);
    PRIO(3);
    u4 vs[4], ws0, ws1, ws2;
    {
        const int uw = LA_U + ch * 4608 + 480 + (16 * c16 + 8 * g - 28) * 2;
#define WSUM(I) ({ const u4 a_ = ld_k(uw + 512 * (I)); pack4(mfma(a_, bnd9, Z), mfma(a_, bnd7, Z)); })
#pragma unroll
        for (int n = 0; n < 4; n++) vs[n] = VSUM(n);
        ws0 = WSUM(hf);
        if (hf) { ws1 = WSUM(2); ws2 = WSUM(3); }
    }
#define KFR(rho, p) ld_k(ka + 192 - 64 * (rho) + 512 - 512 * (p))
#define UFR(rho, q) lds_ld<u4>(ubs + 192 - 64 * (rho) + 512 * (q))
    f4 acc[4];
#pragma unroll
    for (int i = 0; i < 4; i++) acc[i] = Z;
    u4 gv[3];
    {
        u4 A[2][2], B[2][4];
        PRIO(3);
#pragma unroll
        for (int p = 0; p < 4; p++) B[0][p] = UFR(0, p);
        A[0][0] = KFR(0, 0); A[0][1] = KFR(0, 1);
#pragma unroll
        for (int k = 0; k < 3; k++) gv[k] = ld_k(k0 + KQ + (94 + 16 * hf - 32 * k - 2 * c16 + 4 * g) * 4);
        if (!hf) { const u4 t = ld_k(ka - 320); acc[2] = mfma(t, B[0][0], acc[2]); acc[3] = mfma(t, B[0][1], acc[3]); }
        acc[2] = mfma(vs[0], gv[0], acc[2]); acc[3] = mfma(vs[0], gv[1], acc[3]); acc[3] = mfma(vs[1], gv[0], acc[3]);
#pragma unroll
        for (int rho = 0; rho < 4; rho++)
#pragma unroll
            for (int p = 0; p < 2; p++) {
                SB;
                if (p == 0) { if (rho == 1) PRIO(2); else if (rho == 2) PRIO(1); else if (rho == 3) PRIO(0); }
                if (rho < 3) { B[(rho + 1) & 1][2 * p] = UFR(rho + 1, 2 * p); B[(rho + 1) & 1][2 * p + 1] = UFR(rho + 1, 2 * p + 1); A[(rho + 1) & 1][p] = KFR(rho + 1, p); }
#pragma unroll
                for (int q = 0; p + q < 4; q++) acc[p + q] = mfma(A[rho & 1][p], B[rho & 1][q], acc[p + q]);
            }
        SB;
    }
#pragma unroll
    for (int i = 0; i < 4; i++) pin(acc[i]);
    STAMP(4);
#define EXCHANGE(off)                                                                         \
    {                                                                                         \
        const int mine = LA_G + wv * 4608 + (off), other = LA_G + (wv ^ 1) * 4608 + (off);     \
        if (hf) { lds_st<f4>(mine + lane * 16, acc[0]); lds_st<f4>(mine + 1024 + lane * 16, acc[1]); acc[0] = acc[2]; acc[1] = acc[3]; } \
        else { lds_st<f4>(mine + lane * 16, acc[2]); lds_st<f4>(mine + 1024 + lane * 16, acc[3]); } \
        bar();                                                                                \
        const f4 y0 = lds_ld<f4>(other + lane * 16) + acc[0], y1 = lds_ld<f4>(other + 1024 + lane * 16) + acc[1]; \
        lds_st<u2>(other + lane * 8, u2{pk(y0[0], y0[1]), pk(y0[2], y0[3])});                 \
        lds_st<u2>(other + 1024 + lane * 8, u2{pk(y1[0], y1[1]), pk(y1[2], y1[3])});          \
    }
    EXCHANGE(0);
    {
        const int po = k0 + KP + (15 - c16 + 4 * g) * 4;
#define PFR(d) u4{lds_ld<u32>(po + 64 * (7 - (d))), lds_ld<u32>(po + 64 * (7 - (d)) + 4), lds_ld<u32>(po + 64 * (7 - (d)) + 8), lds_ld<u32>(po + 64 * (7 - (d)) + 12)}
        const u4 g4 = PFR(4), g5 = PFR(5), g6 = PFR(6);
        bar();
        STAMP(14);
        PRIO(3);
        acc[0] = mfma(vs[0], gv[2], Z); acc[1] = mfma(vs[1], gv[2], Z); acc[2] = mfma(vs[2], gv[2], Z); acc[3] = mfma(vs[3], gv[2], Z);
        acc[0] = mfma(vs[1], gv[1], acc[0]); acc[1] = mfma(vs[2], gv[1], acc[1]); acc[2] = mfma(vs[3], gv[1], acc[2]);
        acc[0] = mfma(vs[2], gv[0], acc[0]); acc[1] = mfma(vs[3], gv[0], acc[1]);
        if (hf) {
            acc[3] = mfma(ws0, g6, acc[3]); acc[2] = mfma(ws0, g5, acc[2]); acc[1] = mfma(ws0, g4, acc[1]);
            acc[3] = mfma(ws1, g5, acc[3]); acc[2] = mfma(ws1, g4, acc[2]); acc[3] = mfma(ws2, g4, acc[3]);
        } else {
            const u4 g7 = PFR(7), sel = lds_ld<u4>(lt);
            const u32 r = lds_ld<u32>(k0 + KX);
            acc[3] = mfma(ws0, g7, acc[3]); acc[2] = mfma(ws0, g6, acc[2]); acc[0] = mfma(ws0, g4, acc[0]); acc[1] = mfma(ws0, g5, acc[1]);
            const u4 t = {__builtin_amdgcn_perm(r, r, sel[0]), __builtin_amdgcn_perm(r, r, sel[1]), __builtin_amdgcn_perm(r, r, sel[2]), __builtin_amdgcn_perm(r, r, sel[3])};
#pragma unroll
            for (int q = 0; q < 4; q++) acc[q] = mfma(t, UFR(0, q), acc[q]);
        }
#pragma unroll
        for (int i = 0; i < 4; i++) pin(acc[i]);
        await<1>(FU2, 4);
        u4 A[2][2], B[2][5];
#pragma unroll
        for (int p = 0; p < 5; p++) B[0][p] = UFR(0, 3 + p);
        A[0][0] = KFR(0, 0); A[0][1] = KFR(0, 1);
        vs[0] = VSUM(4); vs[1] = VSUM(5);
        if (!hf) {
            const u4 t = ld_k(ka - 320);
            acc[0] = mfma(t, UFR(0, 2), acc[0]);
#pragma unroll
            for (int q = 1; q < 4; q++) acc[q] = mfma(t, B[0][q - 1], acc[q]);
        }
        acc[2] = mfma(vs[0], gv[0], acc[2]); acc[3] = mfma(vs[0], gv[1], acc[3]); acc[3] = mfma(vs[1], gv[0], acc[3]);
#pragma unroll
        for (int rho = 0; rho < 4; rho++)
#pragma unroll
            for (int p = 0; p < 2; p++) {
                SB;
                if (p == 0) { if (rho == 1) PRIO(2); else if (rho == 2) PRIO(1); else if (rho == 3) PRIO(0); }
                if (rho < 3) {
#pragma unroll
                    for (int q = 3 * p; q < 3 + 2 * p; q++) B[(rho + 1) & 1][q] = UFR(rho + 1, 3 + q);
                    A[(rho + 1) & 1][p] = KFR(rho + 1, p);
                }
#pragma unroll
                for (int j = 0; j < 4; j++) acc[j] = mfma(A[rho & 1][p], B[rho & 1][j + 1 - p], acc[j]);
            }
        SB;
    }
#pragma unroll
    for (int i = 0; i < 4; i++) pin(acc[i]);
    STAMP(5);
    EXCHANGE(2048);
    bar();
    STAMP(6);
    STAMP_OUT(0);
}

__global__ void __launch_bounds__(NT) __attribute__((aligned(4096))) fused_kernel(const u16* __restrict__ u, const u16* __restrict__ lar, const u16* __restrict__ aimg,
                                                   const u16* __restrict__ cre, const u16* __restrict__ cim, const u16* __restrict__ ldt,
                                                   const u16* __restrict__ dsk, const u16* __restrict__ x, const u16* __restrict__ wr,
                                                   const u16* __restrict__ wi, u16* __restrict__ out, char* ws) {
    const char* entry;
    asm volatile("s_getpc_b64 %0" : "=s"(entry));
    APar pr;
    if (blockIdx.x >= A0 && blockIdx.x < H0 && __builtin_amdgcn_readfirstlane(threadIdx.x) < 768) {
        const int a = blockIdx.x - A0, d = 6 * ((a & 7) * 12 + (a >> 3)) + __builtin_amdgcn_readfirstlane(threadIdx.x >> 7);
        asm volatile("global_load_ushort %0, %6, %8\n\tglobal_load_ushort %1, %6, %9\n\tglobal_load_ushort %2, %6, %10\n\t"
                     "global_load_ushort %3, %6, %11\n\tglobal_load_ushort %4, %7, %12\n\tglobal_load_ushort %5, %7, %13"
                     : "=&v"(pr.l), "=&v"(pr.a), "=&v"(pr.r), "=&v"(pr.i), "=&v"(pr.t), "=&v"(pr.d)
                     : "v"((d * NS + (threadIdx.x & 127)) * 2), "v"(d * 2), "s"(lar), "s"(aimg), "s"(cre), "s"(cim), "s"(ldt), "s"(dsk) : "memory");
    }
    if (__builtin_expect(blockIdx.x < NB2, 1)) head_b(entry, x, wr, wi, out, ws);
    else if (blockIdx.x >= A0 && blockIdx.x < H0) head_a(u, pr, out, ws);
    else warm_code(entry);
}

extern "C" void kernel_launch(void* const* d_in, const int* in_sizes, int n_in, void* d_out, int out_size, void* d_ws,
                              size_t ws_size, hipStream_t stream) {
    (void)hipFuncSetAttribute((const void*)fused_kernel, hipFuncAttributeMaxDynamicSharedMemorySize, LDS_BYTES);
    fused_kernel<<<GRID, NT, LDS_BYTES, stream>>>((const u16*)d_in[0], (const u16*)d_in[1], (const u16*)d_in[2], (const u16*)d_in[3],
                                                  (const u16*)d_in[4], (const u16*)d_in[5], (const u16*)d_in[6], (const u16*)d_in[7],
                                                  (const u16*)d_in[8], (const u16*)d_in[9], (u16*)d_out, (char*)d_ws);
}
```

```cpp
#define POLL_SLEEP 6
#include <hip/hip_runtime.h>

typedef unsigned short u16;
typedef unsigned int u32;
typedef float f2 __attribute__((ext_vector_type(2)));
typedef float f4 __attribute__((ext_vector_type(4)));
typedef u32 u2 __attribute__((ext_vector_type(2)));
typedef u32 u4 __attribute__((ext_vector_type(4)));
typedef int i8v __attribute__((ext_vector_type(8)));
typedef short s4 __attribute__((ext_vector_type(4)));
typedef short s8 __attribute__((ext_vector_type(8)));
typedef __bf16 bf2 __attribute__((ext_vector_type(2)));
#define DI __device__ __forceinline__
#define LAS __attribute__((address_space(3)))
#define GAS __attribute__((address_space(1)))
#define CAS __attribute__((address_space(4)))
#pragma clang diagnostic ignored "-Wint-to-pointer-cast"
struct u3 { u32 v[3]; DI u32& operator[](int i) { return v[i]; } DI const u32& operator[](int i) const { return v[i]; } };

constexpr int SEQ = 2048, DM = 576, NS = 128, DF = 289;
constexpr int NB1 = 128;
constexpr int NB2 = 145;
constexpr int A0 = 152;
constexpr int NA = 96;
constexpr int H0 = A0 + NA;
constexpr int GRID = H0 + 8;
constexpr int NT = 1024;

constexpr u32 WS_EPOCH = 0, WS_F1 = 4096, WS_FY = WS_F1 + 8 * 16 * 128, WS_X1 = 1u << 20, WS_Y1 = 4u << 20, WS_USED = 8u << 20;
#define STAMP(i)
#define STAMP_DECL
#define STAMP_OUT(T)

constexpr int F_SA = 0, F_EO = 4;
constexpr int F_ARE = 9, F_AIM = 10, F_BRE = 11, F_G = 12;
constexpr int F_F16 = 16, F_M1C = 17, F_GI = 19, F_AI = 23;
constexpr int F_VAR = 25;
constexpr int F_END = F_VAR + 30;
constexpr int ET = 452, ER = 28, E2V = 580, E2U = 36, OP = 1168;
constexpr int LB_X = 0, LB_I = 0, LB_Y = 0;
constexpr int LB_E = 16 * OP;
constexpr int LB_O = LB_E, LB_E2 = LB_E;
constexpr int LB_S = LB_E + 16 * ET * 4;
constexpr int LB_W = LB_S + 1200 * 16;
constexpr int LB_T = LB_W + 2 * 256 * 36;
constexpr int LB_G = LB_T + (F_VAR + 12) * 1024;
constexpr int LB_END = LB_G + 16;
struct CosTab { double c[2048]; double c9[9], s9[9]; };
constexpr double tay_cos(double x) { double t = 1, s = 1; for (int k = 1; k <= 14; k++) { t *= -x * x / ((2 * k - 1) * (2 * k)); s += t; } return s; }
constexpr double tay_sin(double x) { double t = x, s = x; for (int k = 1; k <= 14; k++) { t *= -x * x / ((2 * k + 1) * (2 * k)); s += t; } return s; }
constexpr CosTab make_cos() {
    CosTab t = {};
    const double PI2 = 6.283185307179586476925;
    for (int k = 0; k <= 512; k++) {
        const double v = k == 512 ? 0.0 : (k <= 256 ? tay_cos(PI2 * k / 2048) : tay_sin(PI2 * (512 - k) / 2048));
        t.c[k] = v; t.c[(2048 - k) & 2047] = v; t.c[1024 - k] = -v; t.c[(1024 + k) & 2047] = -v;
    }
    t.c[512] = 0.0; t.c[1536] = 0.0;
    for (int k = 0; k < 9; k++) { const double a = PI2 * (k <= 4 ? k : k - 9) / 9; t.c9[k] = tay_cos(a); t.s9[k] = tay_sin(a); }
    return t;
}
constexpr CosTab CT = make_cos();
constexpr double cs(int n) { return CT.c[n & 2047]; }
constexpr double sn(int n) { return CT.c[(n - 512) & 2047]; }
constexpr u32 bfr(double x) { const u32 b = __builtin_bit_cast(u32, (float)x); return (b + 0x7fffu + ((b >> 16) & 1u)) >> 16; }
constexpr u32 pkc(double lo, double hi) { return bfr(lo) | (bfr(hi) << 16); }
constexpr u32 f32c(double x) { return __builtin_bit_cast(u32, (float)x); }
template <int N> struct Frags { u32 w[N * 256]; };
constexpr u32 w_re(double amp, int n) { return pkc(amp * cs(n), amp * sn(n)); }
constexpr Frags<F_VAR> make_t0() {
    Frags<F_VAR> t = {};
    const double R2 = 1.4142135623730951;
    for (int l = 0; l < 64; l++)
        for (int i = 0; i < 4; i++) {
            const int c = l & 15, g = l >> 4, r = 4 * g + i, o = l * 4 + i;
            {
                const int b = c >> 2, tq = c & 3;
                const double amp = b ? 1.0 : R2;
                const int n0 = 512 * tq * g + (b ? 256 * b * (2 * i) : -256), n1 = 512 * tq * g + (b ? 256 * b * (2 * i + 1) : 256);
                t.w[F_ARE * 256 + o] = pkc(amp * cs(n0), amp * cs(n1));
                t.w[F_AIM * 256 + o] = pkc(-amp * sn(n0), -amp * sn(n1));
            }
            for (int w = 0; w < 9; w++) {
                const int sh = 16 * (i & 1), oo = w * 128 + l * 2 + (i >> 1);
                t.w[F_EO * 256 + oo] |= (u32)((c * ET + r * ER + 2 * ((7 * w + r) % 9)) * 4) << sh;
                if (w < 8) {
                    const int k = (9 * (w & 3) + 36 * r + 64 * (c % 9)) % DM, slot = w < 4 ? (k <= 288 ? 2 * k : 2 * (DM - k) + 1) : k;
                    t.w[F_SA * 256 + oo] |= (u32)(slot * 8 + (slot >> 6) * 16) << sh;
                }
            }
            if (r < 9) { const int k = (r * c) % 9; t.w[F_BRE * 256 + o] = pkc(CT.c9[k], CT.s9[k]); }
            for (int q = 0; q < 4; q++) {
                t.w[(F_G + q) * 256 + o] = w_re(1.0, 32 * r * (q + 4 * c));
                t.w[(F_GI + q) * 256 + o] = w_re(1.0, 32 * c * (q + 4 * r));
            }
            t.w[F_F16 * 256 + o] = w_re(1.0, 128 * r * c);
            for (int h = 0; h < 2; h++) {
                const int j = 4 * h + (c >> 2), n = 512 * i * (c & 3) + (g ? 256 * g * j : ((j & 1) ? 256 : -256));
                t.w[(F_AI + h) * 256 + o] = w_re(g ? 2.0 : R2, n);
                const int m = 16 * r * (4 * h + 8 * c) - 512 * h;
                t.w[(F_M1C + h) * 256 + o] = pkc(0.5 * cs(m), -0.5 * sn(m));
            }
        }
    return t;
}
constexpr Frags<30> make_t1() {
    Frags<30> t = {};
    for (int v = 0; v < 5; v++)
        for (int l = 0; l < 64; l++)
            for (int i = 0; i < 4; i++) {
                const int be = v < 3 ? v + 1 : 4 * (v - 3), c = l & 15, r = 4 * (l >> 4) + i, o = v * 1536 + l * 4 + i;
                t.w[o] = w_re(v < 3 ? 1.0 : 0.5, 16 * r * (be + 8 * c) + (v == 4 ? 512 : 0));
                t.w[o + 256] = w_re(1.0, 16 * c * (be + 8 * r));
                t.w[o + 512] = f32c(cs(r * (be + 8 * c))); t.w[o + 768] = f32c(-sn(r * (be + 8 * c)));
                t.w[o + 1024] = f32c(cs(c * (be + 8 * r))); t.w[o + 1280] = f32c(-sn(c * (be + 8 * r)));
            }
    return t;
}
constexpr int qidx(int l) {
    if (l <= 256) return l;
    const int sh = l < 512 ? 1 : (l < 1024 ? 2 : 3), h = 1 << (sh - 1), m = l >> sh, r = l & ((1 << sh) - 1);
    return 128 * sh + m + ((r > h || (r == h && (m & 1))) ? 1 : 0);
}
constexpr Frags<6> make_t2() {
    Frags<6> t = {};
    for (int l = 0; l < 64; l++) {
        const int c = l & 15, g = l >> 4, e = 543 - 2 * c - g;
        t.w[256 + l * 4] = (u32)((e < 518 ? qidx(e) : 643) * 4) | ((u32)((e - 1 < 518 ? qidx(e - 1) : 643) * 4) << 16);
        for (int i = 0; i < 4; i++)
            for (int h = 0; h < 2; h++) {
                const int lag = 1024 + 16 + c - 8 * g - 2 * i - h, x = 8 * g + 2 * i + h;
                t.w[l * 4 + i] |= (lag < 1027 || lag > 1035 ? 0x0c0cu : (lag < 1029 ? 0x0100u : 0x0302u)) << (16 * h);
                if (x >= c + 8 && x <= c + 16) t.w[2 * 256 + l * 4 + i] |= 0x3f80u << (16 * h);
                if (x >= c + 1 && x <= c + 7) t.w[3 * 256 + l * 4 + i] |= 0x3f80u << (16 * h);
                if (x >= c + 6 && x <= c + 10) t.w[4 * 256 + l * 4 + i] |= 0x3f80u << (16 * h);
                if (x >= c + 3 && x <= c + 5) t.w[5 * 256 + l * 4 + i] |= 0x3f80u << (16 * h);
            }
    }
    return t;
}
struct Tables { Frags<F_VAR> a; Frags<30> b; Frags<6> c; };
constexpr Frags<6> T2 = make_t2();
constexpr Frags<F_VAR> T0 = make_t0();
constexpr Frags<30> T1 = make_t1();
static __device__ const __attribute__((aligned(128))) Tables TAB = {T0, T1, T2};
constexpr int LA_G = 0;
constexpr int LA_K = LA_G + 12 * 4608;
constexpr int KI = 1344, KQ = 4 * KI, KP = KQ + 512, KX = KP + 320, KC = KX + 16;
constexpr int LA_U = LA_K + 6 * KC;
constexpr int LA_Q = LA_U + 6 * 4608;
constexpr int LA_F = LA_Q + 12 * 2576;
constexpr int LA_S = LA_F + 16;
constexpr int LA_END = LA_S + 6144;
constexpr int FU2 = 0;
constexpr int LDS_BYTES = (LA_END > LB_END ? LA_END : LB_END);
static_assert(LDS_BYTES <= 160 * 1024 && LDS_BYTES > 80 * 1024, "one workgroup per CU");

DI u32 pk(float a, float b) { f2 v = {a, b}; return __builtin_bit_cast(u32, __builtin_convertvector(v, bf2)); }
DI float blo(u32 v) { return __uint_as_float(v << 16); }
DI float bhi(u32 v) { return __uint_as_float(v & 0xffff0000u); }
DI float bf2f(u16 v) { return __uint_as_float((u32)v << 16); }
DI float rbf(float x) { return blo(pk(x, x)); }
DI f2 cmul(f2 a, f2 b) { f2 t = a.xx * b; f2 s = {-b.y, b.x}; return __builtin_elementwise_fma(a.yy, s, t); }
DI f4 mfma(u4 a, u4 b, f4 c) { return __builtin_amdgcn_mfma_f32_16x16x32_bf16(__builtin_bit_cast(s8, a), __builtin_bit_cast(s8, b), c, 0, 0, 0); }
DI u4 pack4(f4 re, f4 im) { u4 r = {pk(re[0], im[0]), pk(re[1], im[1]), pk(re[2], im[2]), pk(re[3], im[3])}; return r; }
DI u4 twid(f4 re, f4 im, f4 wr, f4 wi, f4 wn) { return pack4(__builtin_elementwise_fma(re, wr, im * wn), __builtin_elementwise_fma(re, wi, im * wr)); }
DI void split(u2 a, u2 b, u2 c, u2 d, u4 (&o)[2]) {
    u2 w, x, y, z;
    asm("v_pk_mov_b32 %0, %4, %5 op_sel:[0,0]\n\tv_pk_mov_b32 %1, %6, %7 op_sel:[0,0]\n\t"
        "v_pk_mov_b32 %2, %4, %5 op_sel:[1,1]\n\tv_pk_mov_b32 %3, %6, %7 op_sel:[1,1]\n\ts_nop 1"
        : "=&v"(w), "=&v"(x), "=&v"(y), "=&v"(z) : "v"(a), "v"(b), "v"(c), "v"(d));
    o[0] = u4{w[0], w[1], x[0], x[1]}; o[1] = u4{y[0], y[1], z[0], z[1]};
}
DI u4 rot90(u4 a) { u4 r; for (int i = 0; i < 4; i++) r[i] = __builtin_amdgcn_alignbit(a[i], a[i], 16) ^ 0x8000u; return r; }
DI int mod9(int x) { return x - 9 * ((x * 57) >> 9); }
template <class T> DI void pin(T& v) { asm volatile("" : "+v"(v)); }

template <class T> DI T lds_ld(int off) { return *(const LAS T*)(u32)off; }
template <class T> DI void lds_st(int off, T v) { *(LAS T*)(u32)off = v; }
DI void bar() { __syncthreads(); }
#define PRIO(n) __builtin_amdgcn_s_setprio(n)
#define PRIOB(n)
DI void glds16(const void* g, u32 off, int lds) {
    u32 keep;
    asm volatile("s_mov_b32 %0, m0\n\ts_mov_b32 m0, %3\n\ts_nop 4\n\tglobal_load_lds_dwordx4 %1, %2\n\ts_mov_b32 m0, %0" : "=&s"(keep) : "v"(off), "s"(g), "s"(lds) : "memory");
}

DI void publish_tag(u32 epoch) {
    u32 t;
    asm volatile("v_add_u32 %0, 0x9E3779B1, %1\n\tds_write_b32 %2, %0\n\ts_waitcnt lgkmcnt(0)" : "=&v"(t) : "v"(epoch), "v"(LB_G) : "memory");
}
DI void wait_flag(__amdgpu_buffer_rsrc_t r, u32 off, u32 tag, bool on) {
    if (on) {
        const unsigned long long t0 = __builtin_amdgcn_s_memrealtime();
        while (!__all(__builtin_amdgcn_raw_buffer_load_b32(r, off, 0, 16) == tag)) {
            if (__builtin_amdgcn_s_memrealtime() - t0 > 5000000ull) break;
            __builtin_amdgcn_s_sleep(POLL_SLEEP);
            asm volatile("" ::: "memory");
        }
    }
    asm volatile("" ::: "memory");
}

constexpr u32 POISON = 0xFFFFFFFFu;
DI bool unset(u4 v) { return max(max(v[0], v[1]), max(v[2], v[3])) == POISON; }
#define DPOLL_SLEEP 1
template <bool TWO> DI void load_set(__amdgpu_buffer_rsrc_t r, u32 off, bool on, u4& a, bool on2, u4& b) {
    const unsigned long long t0 = __builtin_amdgcn_s_memrealtime();
    bool ma = on, mb = TWO && on2;
    for (;;) {
        if (ma) a = __builtin_amdgcn_raw_buffer_load_b128(r, off, 0, 16);
        if (TWO && mb) b = __builtin_amdgcn_raw_buffer_load_b128(r, off, 16384, 16);
        ma = ma && unset(a);
        if (TWO) mb = mb && unset(b);
        if (!__any(ma || mb) || __builtin_amdgcn_s_memrealtime() - t0 > 5000000ull) break;
        __builtin_amdgcn_s_sleep(DPOLL_SLEEP);
        asm volatile("" ::: "memory");
    }
}
DI int unit_w0(int u) {
    const int q = min((u * 57) >> 11, 3), kg = u - 36 * q, b = (q + 1) & 3;
    return u >= 108 ? min(16 * kg, 562) : (kg >= 18 ? (2048 - b) * (2 * DF) + 1122 - 32 * kg : b * (2 * DF) + 32 * kg);
}
constexpr int CODE_LINES = 120;
DI void warm_code(const char* entry) {
    if (threadIdx.x < CODE_LINES) { const u32 t = *(const GAS u32*)(entry + threadIdx.x * 128); asm volatile("" :: "v"(t)); }
}

DI void head_b(const char* entry, const u16* __restrict__ x, const u16* __restrict__ wr, const u16* __restrict__ wi,
               u16* __restrict__ out, char* ws) {
    const int tid = threadIdx.x, lane = tid & 63, c16 = lane & 15, g = lane >> 4;
    const int wv = __builtin_amdgcn_readfirstlane(tid >> 6);
    const int id = blockIdx.x;
    STAMP_DECL;
    const bool rows = id < NB1;
    const int s00 = 32 * (id >> 4) + (id & 15);
    const char* tab = (const char*)&TAB;
    const int R = (tid * 911) >> 16;
    const u32 xoff = (s00 + 16 * (R >> 3) + 256 * (R & 7)) * (2 * DM) + (tid - R * 72) * 16;
    asm volatile("" :: "s"(x), "s"(wr), "s"(wi), "s"(out), "s"(ws), "v"(xoff));
    const __amdgpu_buffer_rsrc_t rws = __builtin_amdgcn_make_buffer_rsrc(ws, 0, WS_USED, 0x00020000);
    STAMP(13);
    int gy = WS_Y1 + id * (NB2 * 128) + tid * 16;
    u32 epoch = 0;
    if (wv == 0) asm volatile("global_load_dword %0, %0, %1 sc1" : "+v"(epoch) : "s"(ws) : "memory");
    if (rows) {
        glds16(x, xoff, LB_X + wv * 1024);
        if (wv < 2) glds16(x, (s00 + 16 + 1536) * (2 * DM) + (tid < 56 ? (tid + 16) * 16 : 256 * 2 * DM + (tid - 56) * 16), LB_X + 16384 + wv * 1024);
        else glds16(tab, tid * 16 - 2048, LB_T + (wv - 2) * 1024);
        if (wv >= 14) glds16(tab, tid * 16, LB_T + wv * 1024);
    }
    const int u2i = (id & 7) ? (id & 7) * 18 + 1 + (id >> 3) : (id == 136 ? 0 : (id >> 3) + (id < 136 ? 1 : 0));
    const bool star = u2i >= 108;
    const int q36 = min((u2i * 57) >> 11, 3), kg = u2i - 36 * q36, bq = (q36 + 1) & 3;
    const bool mir = !star && kg >= 18;
    const int w0 = unit_w0(u2i);
    if (wv < 3) glds16(tab, F_F16 * 1024 + tid * 16, LB_T + (F_F16 + wv) * 1024);
    else if (wv < (star ? 15 : 9)) glds16(tab, (F_VAR + 6 * q36 - 3) * 1024 + tid * 16, LB_T + (F_VAR + wv - 3) * 1024);
    __builtin_amdgcn_s_barrier();
    STAMP(14);
    const u32 woff = w0 + ((tid >> 1) & 255) * (mir ? -16 * DF : 16 * DF) + (tid & 1) * (star ? 8 * DF : 16);
    u4 wra, wrb;
    if (wv >= 8 && !rows) asm volatile("global_load_dwordx4 %0, %2, %3\n\tglobal_load_dwordx4 %1, %2, %4" : "=&v"(wra), "=&v"(wrb) : "v"(woff), "s"(wr), "s"(wi) : "memory");
#define W_TO_LDS do { const int o = LB_W + ((tid >> 1) & 255) * 36 + (tid & 1) * 16; \
        asm volatile("s_waitcnt vmcnt(0)\n\tds_write2_b32 %0, %2, %3 offset1:1\n\tds_write2_b32 %0, %4, %5 offset0:2 offset1:3\n\t" \
                     "ds_write2_b32 %1, %6, %7 offset1:1\n\tds_write2_b32 %1, %8, %9 offset0:2 offset1:3\n\ts_waitcnt lgkmcnt(0)" \
                     :: "v"(o), "v"(o + 9216), "v"(wra[0]), "v"(wra[1]), "v"(wra[2]), "v"(wra[3]), "v"(wrb[0]), "v"(wrb[1]), "v"(wrb[2]), "v"(wrb[3]) : "memory"); } while (0)
    const f4 Z = {0.f, 0.f, 0.f, 0.f};
    const int tau = wv & 3, bw = wv >> 2;
    int tl = LB_T + lane * 16;
    int lin = LB_S + (tid + (tid >> 5)) * 16;
    pin(tl); pin(lin);
    int sadr[4];

    u32 tag;
    u4 sv0 = {}, sv1 = {}, sv2 = {};
    if (rows) {
        if (tid < 256) {
            const int o = LB_E + 72 + (tid & 15) * (ER * 4) + (tid >> 4) * (ET * 4);
            lds_st<u2>(o, u2{0u, 0u}); lds_st<u4>(o + 8, u4{0u, 0u, 0u, 0u});
        }
        int xo = LB_X + (c16 >> 2) * 1152 + 2 * (144 * ((g - wv) & 3) + 16 * wv + 4 * (c16 & 3));
        int bo = LB_E + (wv * ET + c16 * ER + min(8 * g, 16)) * 4;
        int go = WS_X1 + (tid >> 3) * 16384 + id * 128 + (tid & 7) * 16;
        int ta = LB_T + lane * 8 + F_SA * 1024 + (tau + (bw ? 4 : 0)) * 512, tb = LB_T + lane * 8 + F_EO * 1024 + wv * 512;
        pin(xo); pin(bo); pin(go); pin(ta); pin(tb);
        STAMP(1);
        asm volatile("s_cmp_lt_u32 %0, 8\n\ts_cbranch_scc0 .Lw%=\n\ts_waitcnt vmcnt(0)\n.Lw%=:\n\ts_waitcnt vmcnt(0)" :: "s"(wv) : "memory", "scc");
        if (wv == 0) publish_tag(epoch);
        bar();
        STAMP(2);
        if (wv >= 9) asm volatile("global_load_dwordx4 %0, %2, %3\n\tglobal_load_dwordx4 %1, %2, %4" : "=&v"(wra), "=&v"(wrb) : "v"(woff), "s"(wr), "s"(wi) : "memory");
        if (wv < 9) {
            u4 d[2];
#pragma unroll
            for (int s = 0; s < 2; s++) {
                const s4 lo = __builtin_amdgcn_ds_read_tr16_b64_v4i16((LAS s4*)(u32)(xo + s * 9216));
                const s4 hi = __builtin_amdgcn_ds_read_tr16_b64_v4i16((LAS s4*)(u32)(xo + s * 9216 + 4608));
                d[s] = __builtin_bit_cast(u4, __builtin_shufflevector(lo, hi, 0, 1, 2, 3, 4, 5, 6, 7));
            }
            const u4 are = lds_ld<u4>(tl + F_ARE * 1024), aim = lds_ld<u4>(tl + F_AIM * 1024);
            const u2 eo = lds_ld<u2>(tb);
#pragma unroll
            for (int s = 0; s < 2; s++) d[s] = pack4(mfma(d[s], are, Z), mfma(d[s], aim, Z));
#pragma unroll
            for (int i = 0; i < 4; i++) lds_st<u2>(LB_E + ((i & 1) ? eo[i >> 1] >> 16 : eo[i >> 1] & 0xffff), u2{d[0][i], d[1][i]});
        }
        if (wv == 8) asm volatile("global_load_dwordx4 %0, %2, %3\n\tglobal_load_dwordx4 %1, %2, %4" : "=&v"(wra), "=&v"(wrb) : "v"(woff), "s"(wr), "s"(wi) : "memory");
        u4 bre = lds_ld<u4>(tl + F_BRE * 1024), gre = lds_ld<u4>(tl + (F_G + tau) * 1024);
        u4 bim = rot90(bre), gim = rot90(gre);
        {
            const u2 sa = lds_ld<u2>(ta);
            const int base = LB_S + ((bw + 3) & 3) * (DM * 8 + 144);
#pragma unroll
            for (int i = 0; i < 4; i++) { sadr[i] = base + ((i & 1) ? sa[i >> 1] >> 16 : sa[i >> 1] & 0xffff); pin(sadr[i]); }
        }
        tag = lds_ld<u32>(LB_G);
        pin(bre); pin(bim); pin(gre); pin(gim); pin(tag);
        u4 d[2];
        bar();
        {
            PRIOB(2);
            const u4 p0 = lds_ld<u4>(bo), p1 = lds_ld<u4>(bo + 16);
            f4 re[2], im[2];
            split(p0.xy, p0.zw, p1.xy, p1.zw, d);
#pragma unroll
            for (int s = 0; s < 2; s++) { re[s] = mfma(d[s], bre, Z); im[s] = mfma(d[s], bim, Z); }
            PRIOB(1);
#pragma unroll
            for (int s = 0; s < 2; s++) { const u4 t = pack4(re[s], im[s]); re[s] = mfma(gre, t, Z); im[s] = mfma(gim, t, Z); }
            PRIOB(0);
#pragma unroll
            for (int s = 0; s < 2; s++) d[s] = pack4(re[s], im[s]);
            if (c16 < 9) {
#pragma unroll
                for (int i = 0; i < 4; i++) lds_st<u2>(sadr[i], u2{d[0][i], d[1][i]});
                if (wv == 0 && c16 == 0 && !(g & 1)) lds_st<u2>(sadr[0] + 8, u2{d[0][0], d[1][0]});
            }
        }
        bar();
        STAMP(3);
        if (wv < 8) {
            sv0 = lds_ld<u4>(lin); sv1 = lds_ld<u4>(lin + 528 * 16); sv2 = lds_ld<u4>(lin + (wv < 3 ? 1056 * 16 : 0));
            pin(sv2);
            __builtin_amdgcn_raw_buffer_store_b128(sv0, rws, go, 0, 16);
            __builtin_amdgcn_raw_buffer_store_b128(sv1, rws, go, 64 * 16384, 16);
            if (tid <= 128) __builtin_amdgcn_raw_buffer_store_b128(sv2, rws, go, 128 * 16384, 16);
        } else {
            W_TO_LDS;
        }
    } else {
        if (wv < 8 && (kg == 0 || kg == (star ? 36 : 18))) {
            const int m = tid & 255;
            lds_st<u16>(LB_W + tid * 36 + 32, ((wv >> 2) ? wi : wr)[((mir ? bq + 8 * m : 2048 - bq - 8 * m) & 2047) * DF + (kg ? 288 : 0)]);
        }
        warm_code(entry);
        if (wv >= 8) W_TO_LDS;
        asm volatile("s_waitcnt vmcnt(0)" ::: "memory");
        if (wv == 0) publish_tag(epoch);
        bar();
        tag = lds_ld<u32>(LB_G);
    }

    const int typ = star ? (wv & 1) : 0;
    const int tv = tl + (F_VAR + 6 * typ) * 1024;
    u4 fre = lds_ld<u4>(tl + F_F16 * 1024), m1r = lds_ld<u4>(tv), m4r = lds_ld<u4>(tv + 1024), mcr = lds_ld<u4>(tl + (F_M1C + typ) * 1024);
    f4 twr = lds_ld<f4>(tv + 2048), twi = lds_ld<f4>(tv + 3072), tur = lds_ld<f4>(tv + 4096), tui = lds_ld<f4>(tv + 5120);
    u4 fim = rot90(fre), m1i = rot90(m1r), m4i = rot90(m4r), mci = rot90(mcr) ^ 0x80008000u;
    f4 twn = -twi, tun = -tui;
    pin(fre); pin(fim); pin(m1r); pin(m1i); pin(m4r); pin(m4i); pin(twr); pin(twi); pin(tur); pin(tui); pin(mcr); pin(mci); pin(twn); pin(tun);
    if (rows) {
        if (wv >= 8) {
            const u4 P = {POISON, POISON, POISON, POISON};
            __builtin_amdgcn_raw_buffer_store_b128(P, rws, gy - 8192, 0, 16);
            __builtin_amdgcn_raw_buffer_store_b128(P, rws, gy, 0, 16);
            if (tid <= 640) __builtin_amdgcn_raw_buffer_store_b128(P, rws, gy, 8192, 16);
        }
        asm volatile("s_waitcnt vmcnt(0)" :: "v"(sv0), "v"(sv1), "v"(sv2) : "memory");
        if (lane == 0) __builtin_amdgcn_raw_buffer_store_b32(tag, rws, WS_F1 + (wv * 16 + (id >> 3)) * 128 + (id & 7) * 4, 0, 16);
        STAMP(4);
        if (wv >= 10) glds16(tab, (F_GI - 10) * 1024 + tid * 16, LB_T + (F_GI + wv - 10) * 1024);
        bar();
    }
    STAMP(12);
    f4 wfr, wfi;
    {
        const int kcol = star ? 8 * kg + (wv >> 1) : 16 * kg + wv;
        const int cc = star ? 8 * typ + (u2i < NB2 - 1 ? (wv >> 1) : 7) : (mir ? 15 - wv : wv);
        const int wo = LB_W + (c16 + 64 * g) * 36 + cc * 2;
#pragma unroll
        for (int i = 0; i < 4; i++) {
            wfr[i] = bf2f(lds_ld<u16>(wo + i * 576));
            wfi[i] = bf2f(lds_ld<u16>(wo + i * 576 + 9216));
        }
        float sc = 1.f / (float)(SEQ * DM);
        if (kcol == 0 || kcol == 288) {
            sc *= 0.5f;
#pragma unroll
            for (int i = 0; i < 4; i++) {
                const int m = c16 + 64 * g + 16 * i;
                const int o = LB_W + (typ ? (255 - m) * 36 + cc * 2 : m * 36 + 32);
                wfr[i] += bf2f(lds_ld<u16>(o));
                wfi[i] -= bf2f(lds_ld<u16>(o + 9216));
            }
        }
        wfr *= sc; wfi *= (mir ? -sc : sc);
    }
    f4 wnr = -wfr, wni = -wfi;
    int io = (32 * g + c16) * 128 + (((wv >> 1) ^ (c16 >> 1)) * 16);
    int so = (tid >> 3) * 128 + (((tid & 7) ^ ((tid >> 4) & 7)) * 16);
    int gi = WS_X1 + u2i * 16384 + tid * 16, gq = WS_Y1 + (tid >> 3) * (NB2 * 128) + u2i * 128 + (tid & 7) * 16;
    int i1 = LB_I + io + (star ? 0 : (wv & 1) * 8);
    io += LB_O + (wv & 1) * 8;
    pin(wfr); pin(wnr); pin(wni); pin(io); pin(so); pin(gi); pin(gq); pin(i1);
    u32 fy = WS_FY + (((u2i >> 3) & 7) * 16 + wv) * 128 + (lane & 7) * 4;
    pin(fy);
    wait_flag(rws, WS_F1 + (((u2i >> 3) & 7) * 16 + wv) * 128 + lane * 4, tag, lane < 8);
    STAMP(5);
    lds_st<u4>(LB_I + so, __builtin_amdgcn_raw_buffer_load_b128(rws, gi, 0, 16));
    bar();
    STAMP(6);
    u32 rdy;
    {
        const int o1 = i1;
        PRIOB(3);
        const u2 a0 = lds_ld<u2>(o1), a1 = lds_ld<u2>(o1 + 2048);
        const u4 a = {a0[0], a0[1], a1[0], a1[1]};
        f4 re, im;
        if (star) {
            const u2 b0 = lds_ld<u2>(o1 + 8), b1 = lds_ld<u2>(o1 + 8 + 2048);
            const u4 b = {b0[0], b0[1], b1[0], b1[1]};
            re = mfma(b, mcr, mfma(a, m1r, Z)); im = mfma(b, mci, mfma(a, m1i, Z));
        } else { re = mfma(a, m1r, Z); im = mfma(a, m1i, Z); }
        u4 t = twid(re, im, twr, twi, twn);
        re = mfma(fre, t, Z); im = mfma(fim, t, Z);
        PRIOB(2);
        __builtin_amdgcn_sched_barrier(0);
        asm volatile("buffer_load_dword %0, %1, %2, 0 offen sc1" : "=&v"(rdy) : "v"(fy), "s"(rws), "v"(t) : "memory");
        __builtin_amdgcn_sched_barrier(0);
        t = pack4(__builtin_elementwise_fma(re, wfr, im * wni), __builtin_elementwise_fma(re, wni, im * wnr));
        re = mfma(t, fre, Z); im = mfma(t, fim, Z);
        PRIOB(1);
        t = twid(re, im, tur, tui, tun);
        re = mfma(m4r, t, Z); im = mfma(m4i, t, Z);
        PRIOB(0);
        t = pack4(re, im);
        lds_st<u2>(io, u2{t[0], t[1]}); lds_st<u2>(io + 2048, u2{t[2], t[3]});
    }
    bar();
    STAMP(7);
    u4 sv;
    {
        const u4 v = lds_ld<u4>(LB_O + so);
        asm volatile("s_waitcnt vmcnt(0)" : "+v"(rdy) :: "memory");
        if (!__all(rdy == tag)) wait_flag(rws, fy, tag, true);
        if (u2i < NB2 - 1 || (tid & 7) == 0)
            __builtin_amdgcn_raw_buffer_store_b128(v, rws, gq, 0, 16);
        sv = v;
    }
    STAMP(8);
    if (!rows) { STAMP_OUT(0); return; }

    u4 gre = lds_ld<u4>(tl + (F_GI + tau) * 1024), gim = rot90(gre);
    u4 bre = lds_ld<u4>(tl + F_BRE * 1024), bim = rot90(bre);
    u4 ai0 = lds_ld<u4>(tl + F_AI * 1024), ai1 = lds_ld<u4>(tl + (F_AI + 1) * 1024);
    u4 gne = gre ^ 0x80008000u, cj;
#pragma unroll
    for (int i = 0; i < 4; i++) { cj[i] = (u32)(sadr[i] & 8) << 28; if (wv < 4) sadr[i] &= ~8; pin(sadr[i]); }
    pin(gne); pin(cj);
    int eadr[4];
#pragma unroll
    for (int i = 0; i < 4; i++) {
        const int r = 4 * g + i;
        eadr[i] = LB_E2 + (mod9(4 * (c16 + 18 - r)) * E2V + r * E2U + 2 * wv) * 4;
        pin(eadr[i]);
    }
    int ao = LB_E2 + (wv * E2V + c16 * E2U + 8 * g) * 4;
    int yo = LB_Y + (c16 >> 2) * OP + 2 * (144 * (((c16 & 3) - wv) & 3) + 16 * wv + 4 * g);
    int yl = LB_Y + tid * 16 + R * (OP - 1152);
    u32 oo = 2 * xoff - (tid - R * 72) * 16 + 2 * DM;
    const int r2 = tid < 56 ? 14 : 15, c2 = tid + 1024 - 72 * r2;
    int yl2 = LB_Y + r2 * OP + c2 * 16;
    u32 oo2 = (s00 + 16 + 256 * (r2 - 8)) * (4 * DM) + 2 * DM + c2 * 16;
    pin(gre); pin(gim); pin(bre); pin(bim); pin(ai0); pin(ai1); pin(ao); pin(gy); pin(yo); pin(yl); pin(oo); pin(yl2); pin(oo2);
    asm volatile("" :: "v"(sv));
    STAMP(9);
    {
        u4 v, w;
        if (wv < 3) { load_set<true>(rws, gy, true, v, tid <= 128, w); if (tid <= 128) lds_st<u4>(lin + 1056 * 16, w); }
        else load_set<false>(rws, gy, true, v, false, w);
        lds_st<u4>(lin, v);
    }
    bar();
    if (id == 0 && tid == 0) __builtin_amdgcn_raw_buffer_store_b32(tag, rws, WS_EPOCH, 0, 16);
    STAMP(10);
    {
        PRIOB(2);
        u4 d[2];
        f4 re[2], im[2];
        if (wv < 4) {
            u4 q[4], c[2];
#pragma unroll
            for (int i = 0; i < 4; i++) q[i] = lds_ld<u4>(sadr[i]) ^ cj[i];
            split(q[0].xy, q[1].xy, q[2].xy, q[3].xy, d);
            split(q[0].zw, q[1].zw, q[2].zw, q[3].zw, c);
#pragma unroll
            for (int s = 0; s < 2; s++) { re[s] = mfma(c[s], gim, mfma(d[s], gre, Z)); im[s] = mfma(c[s], gne, mfma(d[s], gim, Z)); }
        } else {
            u2 p[4];
#pragma unroll
            for (int i = 0; i < 4; i++) p[i] = lds_ld<u2>(sadr[i]);
            split(p[0], p[1], p[2], p[3], d);
#pragma unroll
            for (int s = 0; s < 2; s++) { re[s] = mfma(d[s], gre, Z); im[s] = mfma(d[s], gim, Z); }
        }
        PRIOB(1);
#pragma unroll
        for (int s = 0; s < 2; s++) { const u4 t = pack4(re[s], im[s]); re[s] = mfma(t, bre, Z); im[s] = mfma(t, bim, Z); }
        PRIOB(0);
#pragma unroll
        for (int s = 0; s < 2; s++) d[s] = pack4(re[s], im[s]);
        if (c16 < 9) {
#pragma unroll
            for (int i = 0; i < 4; i++) lds_st<u2>(eadr[i], u2{d[0][i], d[1][i]});
        }
    }
    STAMP(15);
    bar();
    if (wv < 9) {
        const u4 p0 = lds_ld<u4>(ao), p1 = lds_ld<u4>(ao + 16);
        u4 a[2];
        split(p0.xy, p0.zw, p1.xy, p1.zw, a);
#pragma unroll
        for (int s = 0; s < 2; s++) {
            const f4 y0 = mfma(a[s], ai0, Z), y1 = mfma(a[s], ai1, Z);
            lds_st<u2>(yo + s * (8 * OP), u2{pk(y0[0], y0[1]), pk(y0[2], y0[3])});
            lds_st<u2>(yo + s * (8 * OP) + 4 * OP, u2{pk(y1[0], y1[1]), pk(y1[2], y1[3])});
        }
    }
    bar();
    if (wv < 2) {
        const u4 v = lds_ld<u4>(yl), w = lds_ld<u4>(yl2);
        *(GAS u4*)((char*)out + oo) = v;
        *(GAS u4*)((char*)out + oo2) = w;
    } else *(GAS u4*)((char*)out + oo) = lds_ld<u4>(yl);
    STAMP(11);
    STAMP_OUT(0);
}

DI f2 cexpa(float ar2, float air, float p) {
    const float e = __builtin_amdgcn_exp2f(ar2 * p), t = __builtin_amdgcn_fractf(air * p);
    return f2{e * __builtin_amdgcn_cosf(t), e * __builtin_amdgcn_sinf(t)};
}
DI u4 ld_k(int o) {
    const u2 a = *(const volatile LAS u2*)(u32)o, b = *(const volatile LAS u2*)(u32)(o + 8);
    return u4{a[0], a[1], b[0], b[1]};
}
DI void a_rows_out(int yb, int ystr, char* o) {
    u2 y[6];
#pragma unroll
    for (int c = 0; c < 6; c++) y[c] = lds_ld<u2>(yb + c * ystr);
#pragma unroll
    for (int r = 0; r < 4; r++) {
        u3 v;
#pragma unroll
        for (int c = 0; c < 3; c++) v[c] = __builtin_amdgcn_perm(y[2 * c + 1][r >> 1], y[2 * c][r >> 1], (r & 1) ? 0x07060302u : 0x05040100u);
        *(u3*)(o + r * (4 * DM)) = v;
    }
}
template <int R> DI void stage(u4 v) {
    asm volatile("ds_write_addtid_b32 %0 offset:%4\n\tds_write_addtid_b32 %1 offset:%5\n\tds_write_addtid_b32 %2 offset:%6\n\tds_write_addtid_b32 %3 offset:%7"
                 :: "v"(v[0]), "v"(v[1]), "v"(v[2]), "v"(v[3]), "i"(288 * R), "i"(288 * R + 288), "i"(288 * R + 576), "i"(288 * R + 864) : "memory");
}
#define CM_MUL(d, a, b) "v_pk_mul_f32 " d ", " a ", " b " op_sel_hi:[0,1]\n\t"
#define CM_FMA(d, a, b) "v_pk_fma_f32 " d ", " a ", " b ", " d " op_sel:[1,1,0] op_sel_hi:[1,0,1] neg_lo:[1,0,0]\n\t"
struct C4 { f2 a, b, c, d; };
DI C4 cmul4(f2 a0, f2 b0, f2 a1, f2 b1, f2 a2, f2 b2, f2 a3, f2 b3) {
    C4 r;
    asm("s_nop 0\n\t" CM_MUL("%0", "%4", "%5") CM_MUL("%1", "%6", "%7") CM_MUL("%2", "%8", "%9") CM_MUL("%3", "%10", "%11")
        CM_FMA("%0", "%4", "%5") CM_FMA("%1", "%6", "%7") CM_FMA("%2", "%8", "%9") CM_FMA("%3", "%10", "%11") "s_nop 0"
        : "=&v"(r.a), "=&v"(r.b), "=&v"(r.c), "=&v"(r.d) : "v"(a0), "v"(b0), "v"(a1), "v"(b1), "v"(a2), "v"(b2), "v"(a3), "v"(b3));
    return r;
}
DI C4 cmul4(const C4& a, f2 b) { return cmul4(a.a, b, a.b, b, a.c, b, a.d, b); }
DI u4 pk4(const C4& v) { return u4{pk(v.a.x, v.a.y), pk(v.b.x, v.b.y), pk(v.c.x, v.c.y), pk(v.d.x, v.d.y)}; }

DI void sig(int f) {
    asm volatile("s_waitcnt lgkmcnt(0)" ::: "memory");
    if ((threadIdx.x & 63) == 0) __hip_atomic_fetch_add((LAS u32*)(u32)(LA_F + 4 * f), 1u, __ATOMIC_RELAXED, __HIP_MEMORY_SCOPE_WORKGROUP);
}
template <int SLEEP = 0> DI void await(int f, u32 n) {
    while ((u32)__builtin_amdgcn_readfirstlane(*(const volatile LAS u32*)(u32)(LA_F + 4 * f)) < n) if (SLEEP) __builtin_amdgcn_s_sleep(SLEEP);
    asm volatile("" ::: "memory");
}
DI float exp_bf(float x) {
    const float t = x * 1.44269504f, r = __builtin_fmaf(x, 1.92596299e-8f, __builtin_fmaf(x, 1.44269504f, -t));
    const float e = __builtin_amdgcn_exp2f(t);
    return __builtin_fmaf(e * 0.693147181f, r, e);
}

struct APar { u32 l, a, r, i, t, d; };
DI void head_a(const u16* __restrict__ u, APar pr, u16* __restrict__ out, char* ws) {
    const int tid = threadIdx.x, lane = tid & 63, c16 = lane & 15, g = lane >> 4;
    const int wv = __builtin_amdgcn_readfirstlane(tid >> 6);
    STAMP_DECL;
    const int ai_ = blockIdx.x - A0;
    const int d0 = 6 * ((ai_ & 7) * 12 + (ai_ >> 3));
    const f4 Z = {0.f, 0.f, 0.f, 0.f};

    if (wv >= 12) {
        __builtin_amdgcn_s_setprio(3);
        const int t = tid - 768;
        if (t == 0) lds_st<u2>(LA_F, u2{0u, 0u});
        if (t < 180) lds_st<u4>(LA_U + (t / 30) * 4608 + (t % 30) * 16, u4{0u, 0u, 0u, 0u});
        bar();
        glds16((const char*)&TAB + F_END * 1024, t * 16, LA_S + (wv - 12) * 1024);
        if (wv < 14) glds16((const char*)&TAB + (F_END + 4) * 1024, t * 16, LA_S + (wv - 8) * 1024);
        u3 ua[4], ub[4];
#pragma unroll
        for (int r = 0; r < 4; r++) ua[r] = *(const u3*)(u + (4 * t + r) * DM + d0);
        asm volatile("s_waitcnt vmcnt(4)\n\ts_barrier" ::: "memory");
#pragma unroll
        for (int c = 0; c < 6; c++) {
            const u32 sel = (c & 1) ? 0x07060302u : 0x05040100u;
            lds_st<u2>(LA_U + c * 4608 + 480 + t * 8, u2{__builtin_amdgcn_perm(ua[1][c >> 1], ua[0][c >> 1], sel), __builtin_amdgcn_perm(ua[3][c >> 1], ua[2][c >> 1], sel)});
        }
        asm volatile("" ::: "memory");
#pragma unroll
        for (int r = 0; r < 4; r++) ub[r] = *(const u3*)(u + (1024 + 4 * t + r) * DM + d0);
        STAMP(7);
        bar();
#pragma unroll
        for (int c = 0; c < 6; c++) {
            const u32 sel = (c & 1) ? 0x07060302u : 0x05040100u;
            lds_st<u2>(LA_U + c * 4608 + 480 + 2048 + t * 8, u2{__builtin_amdgcn_perm(ub[1][c >> 1], ub[0][c >> 1], sel), __builtin_amdgcn_perm(ub[3][c >> 1], ub[2][c >> 1], sel)});
        }
        sig(FU2);
        STAMP(8);
        const int tile = wv - 12, yl = ((lane >> 2) + 16 * (lane & 3)) * 8;
        char* o = (char*)(out + 4 * t * (2 * DM) + d0);
        bar();
        bar();
        STAMP(11);
        a_rows_out(LA_G + ((tile >> 1) ^ 1) * 4608 + (tile & 1) * 1024 + yl, 9216, o);
        STAMP(9);
        bar();
        bar();
        STAMP(12);
        a_rows_out(LA_G + ((tile >> 1) ^ 1) * 4608 + (tile & 1) * 1024 + 2048 + yl, 9216, o + 1024 * (4 * DM));
        STAMP(10);
        STAMP_OUT(768);
        return;
    }
    const int ch = wv >> 1, hf = wv & 1;
    {
        __builtin_amdgcn_s_barrier();
        asm volatile("s_waitcnt vmcnt(0)" : "+v"(pr.l), "+v"(pr.a), "+v"(pr.r), "+v"(pr.i), "+v"(pr.t), "+v"(pr.d) :: "memory");
        PRIO(3);
        const float dt = rbf(exp_bf(blo(pr.t)));
        const float ar2 = -rbf(exp_bf(blo(pr.l))) * dt * 1.44269504f, air = blo(pr.a) * dt * 0.159154943f;
        const f2 C = {2.f * dt * blo(pr.r), 2.f * dt * blo(pr.i)};
        STAMP(1);
        const f2 z1 = cexpa(ar2, air, 1.f), z8c = cexpa(ar2, -air, 8.f), z128 = cexpa(ar2, air, 128.f);
        const int gr = LA_G + wv * 4608 + c16 * 288 + g * 16, q = LA_Q + wv * 2576;
        asm volatile("s_mov_b32 m0, %0\n\ts_nop 0" :: "s"(LA_G + wv * 4608) : "memory");
#define SB __builtin_amdgcn_sched_barrier(0)
#define STAGE16(v) do { stage<0>(v[0]); stage<4>(v[1]); stage<8>(v[2]); stage<12>(v[3]); } while (0)
#define READ4(f) do { _Pragma("unroll") for (int s = 0; s < 4; s++) f[s] = lds_ld<u4>(gr + 64 * s); SB; } while (0)
        u4 v[4], bfr[4], afr[4];
        const C4 s1 = cmul4(z8c, z8c, z1, z1, z128, z128, C, z1);
        const C4 s2 = cmul4(s1.a, s1.a, z8c, s1.a, s1.c, s1.c, C, s1.b);
        {
            C4 w = {f2{1.f, 0.f}, z8c, s1.a, s2.b};
            v[0] = pk4(w);
#pragma unroll
            for (int i = 1; i < 4; i++) { w = cmul4(w, s2.a); v[i] = pk4(w); }
        }
        STAGE16(v); READ4(bfr);
        PRIO(2);
        const C4 s3 = cmul4(s1.d, s1.b, s1.b, s1.b, s2.c, s2.c, s2.c, z128);
        const C4 e0 = {C, s1.d, s2.d, s3.a}, e1 = cmul4(e0, s3.b);
        v[0] = pk4(e0); v[1] = pk4(e1); v[2] = pk4(cmul4(e0, z128)); v[3] = pk4(cmul4(e1, z128));
        STAGE16(v); READ4(afr);
        PRIO(1);
        {
            const C4 f = cmul4(C4{e0.a, e0.c, e1.a, e1.c}, s1.c), h = cmul4(e0.a, s2.c, e1.a, s2.c, e0.a, s3.d, e1.a, s3.d);
            v[0] = pk4(f); v[1] = pk4(cmul4(f, z128)); v[2] = pk4(h); v[3] = pk4(cmul4(h, s1.c));
        }
        SB;
        f4 acc = Z;
#pragma unroll
        for (int s = 0; s < 4; s++) acc = mfma(afr[s], bfr[s], acc);
        STAGE16(v); READ4(afr);
        PRIO(0);
        lds_st<f4>(q + (4 * (g & 1) + 128 * (g >> 1) + 8 * c16) * 4, acc);
        {
            const f2 f0 = cmul(C, s3.c), f1 = cmul(f0, z128);
            const C4 f = cmul4(f0, s1.c, f1, s1.c, f0, s3.c, f0, s3.c), h = cmul4(C4{f0, f1, f.a, f.b}, s2.c);
            v[0] = pk4(C4{f0, f1, f.a, f.b}); v[1] = pk4(h); v[2][0] = pk(f.c.x, f.c.y);
        }
        SB;
        acc = Z;
#pragma unroll
        for (int s = 0; s < 4; s++) acc = mfma(afr[s], bfr[s], acc);
        stage<0>(v[0]); stage<4>(v[1]);
        asm volatile("ds_write_addtid_b32 %0 offset:%1" :: "v"(v[2][0]), "i"(288 * 8) : "memory");
        READ4(afr);
        if (g < 2) lds_st<f4>(q + (256 + 64 * g + 4 * c16) * 4, acc);
        else {
            const int o = q + (384 + 64 * (g - 2) + 2 * c16) * 4;
            lds_st<f2>(o, f2{acc[0], acc[1]}); lds_st<f2>(o + 128, f2{acc[2], acc[3]});
        }
        acc = Z;
#pragma unroll
        for (int s = 0; s < 4; s++) acc = mfma(afr[s], bfr[s], acc);
        if (g < 2) {
#pragma unroll
            for (int i = 0; i < 4; i++) lds_st<float>(q + (512 + 64 * g + 16 * i + c16) * 4, acc[i]);
        } else if (lane < 34) lds_st<float>(q + (lane == 32 ? 640 : 643) * 4, lane == 32 ? acc[0] : 0.f);
    }
    STAMP(2);
    asm volatile("s_waitcnt lgkmcnt(0)" ::: "memory");
    bar();
    STAMP(9);

    const int k0 = LA_K + ch * KC;
    {
        const LAS float* qa = (const LAS float*)(u32)(LA_Q + ch * 5152);
        const LAS float* qb = qa + 644;
        u2 w[4];
        int y;
        if (hf == 0) {
            const u32 ix = lds_ld<u32>(LA_S + 1024 + lane * 16);
            f4 k = *(const LAS f4*)(qa + 4 * lane) + *(const LAS f4*)(qb + 4 * lane);
            f4 p = *(const LAS f4*)(qa + 4 * lane - 4) + *(const LAS f4*)(qb + 4 * lane - 4);
            const f2 s = *(const LAS f2*)(qa + 512 - 2 * lane) + *(const LAS f2*)(qb + 512 - 2 * lane);
            const float e0 = qa[(ix & 0xffff) >> 2] + qb[(ix & 0xffff) >> 2], e1 = qa[ix >> 18] + qb[ix >> 18];
            lds_st<u32>(k0 + KQ + (32 + lane) * 4, pk(s.x, lane ? s.y : 0.f));
            if (lane == 0) lds_st<u32>(k0 + KX, pk(s.x, s.y));
            lds_st<u32>(k0 + KQ + (lane + (lane & 32) * 2) * 4, 0u);
            lds_st<u32>(k0 + g * KI + c16 * 4, pk(e0, e1));
            if (lane == 0) { k[0] += blo(pr.d); p = Z; }
            const u32 c = pk(k[1], k[0]), d = pk(k[0], p[3]);
            w[0] = u2{pk(k[3], k[2]), c}; w[1] = u2{pk(k[2], k[1]), d}; w[2] = u2{c, pk(p[3], p[2])}; w[3] = u2{d, pk(p[2], p[1])};
            y = k0 + (270 - 2 * lane) * 4;
        } else {
            const int j = 256 + 2 * lane;
            const float q0 = qa[j] + qb[j], q1 = qa[j + 1] + qb[j + 1], q2 = qa[j + 2] + qb[j + 2];
            const float p1 = qa[j - 1] + qb[j - 1], p2 = qa[j - 2] + qb[j - 2], p3 = qa[j - 3] + qb[j - 3];
            const u32 c = pk(q0, q0), d = pk(q0, p1), e = pk(p1, p2), f = lane ? c : d;
            w[0] = u2{pk(q2, q1), c}; w[1] = u2{pk(q1, q0), f}; w[2] = u2{c, lane ? d : e}; w[3] = u2{f, lane ? e : pk(p2, p3)};
            y = k0 + (142 - 2 * lane) * 4;
            if (lane < 48) lds_st<u32>(k0 + (lane < 32 ? (lane >> 3) * KI + (272 + (lane & 7)) * 4 : KP + (32 + lane) * 4), 0u);
            const f2 s = *(const LAS f2*)(qa + 640 - 2 * lane) + *(const LAS f2*)(qb + 640 - 2 * lane);
            lds_st<u32>(k0 + KP + lane * 4, pk(s.x, lane ? s.y : 0.f));
        }
#pragma unroll
        for (int i = 0; i < 4; i++) lds_st<u2>(y + i * KI, w[i]);
    }
    const int x0 = 2031 - c16 + 8 * g;
    int ka = k0 + (x0 & 3) * KI + (x0 >> 2) * 8 - 3008 - 256 * hf - 192 - 512;
    int ubs = LA_U + ch * 4608 + 480 + (16 * c16 + 8 * g - 16) * 2 - 256 * hf - 192;
    pin(ka); pin(ubs);
    const int lt = LA_S + lane * 16;
    const u4 bnd5 = lds_ld<u4>(lt + 4096), bnd3 = lds_ld<u4>(lt + 5120);
    int uv = LA_U + ch * 4608 + 480 + (8 * c16 + 8 * g - 24) * 2 + 256 * hf;
    pin(uv);
#define VSUM(n) ({ const u4 a_ = lds_ld<u4>(uv + 512 * (n)); pack4(mfma(a_, bnd5, Z), mfma(a_, bnd3, Z)); })
    const u4 bnd9 = lds_ld<u4>(lt + 2048), bnd7 = lds_ld<u4>(lt + 3072);
    STAMP(13);
    bar();
    STAMP(3);
    PRIO(3);
    u4 vs[4], ws0, ws1, ws2;
    {
        const int uw = LA_U + ch * 4608 + 480 + (16 * c16 + 8 * g - 28) * 2;
#define WSUM(I) ({ const u4 a_ = ld_k(uw + 512 * (I)); pack4(mfma(a_, bnd9, Z), mfma(a_, bnd7, Z)); })
#pragma unroll
        for (int n = 0; n < 4; n++) vs[n] = VSUM(n);
        ws0 = WSUM(hf);
        if (hf) { ws1 = WSUM(2); ws2 = WSUM(3); }
    }
#define KFR(rho, p) ld_k(ka + 192 - 64 * (rho) + 512 - 512 * (p))
#define UFR(rho, q) lds_ld<u4>(ubs + 192 - 64 * (rho) + 512 * (q))
    f4 acc[4];
#pragma unroll
    for (int i = 0; i < 4; i++) acc[i] = Z;
    u4 gv[3];
    {
        u4 A[2][2], B[2][4];
        PRIO(3);
#pragma unroll
        for (int p = 0; p < 4; p++) B[0][p] = UFR(0, p);
        A[0][0] = KFR(0, 0); A[0][1] = KFR(0, 1);
#pragma unroll
        for (int k = 0; k < 3; k++) gv[k] = ld_k(k0 + KQ + (94 + 16 * hf - 32 * k - 2 * c16 + 4 * g) * 4);
        if (!hf) { const u4 t = ld_k(ka - 320); acc[2] = mfma(t, B[0][0], acc[2]); acc[3] = mfma(t, B[0][1], acc[3]); }
        acc[2] = mfma(vs[0], gv[0], acc[2]); acc[3] = mfma(vs[0], gv[1], acc[3]); acc[3] = mfma(vs[1], gv[0], acc[3]);
#pragma unroll
        for (int rho = 0; rho < 4; rho++)
#pragma unroll
            for (int p = 0; p < 2; p++) {
                SB;
                if (p == 0) { if (rho == 1) PRIO(2); else if (rho == 2) PRIO(1); else if (rho == 3) PRIO(0); }
                if (rho < 3) { B[(rho + 1) & 1][2 * p] = UFR(rho + 1, 2 * p); B[(rho + 1) & 1][2 * p + 1] = UFR(rho + 1, 2 * p + 1); A[(rho + 1) & 1][p] = KFR(rho + 1, p); }
#pragma unroll
                for (int q = 0; p + q < 4; q++) acc[p + q] = mfma(A[rho & 1][p], B[rho & 1][q], acc[p + q]);
            }
        SB;
    }
#pragma unroll
    for (int i = 0; i < 4; i++) pin(acc[i]);
    STAMP(4);
#define EXCHANGE(off)                                                                         \
    {                                                                                         \
        const int mine = LA_G + wv * 4608 + (off), other = LA_G + (wv ^ 1) * 4608 + (off);     \
        if (hf) { lds_st<f4>(mine + lane * 16, acc[0]); lds_st<f4>(mine + 1024 + lane * 16, acc[1]); acc[0] = acc[2]; acc[1] = acc[3]; } \
        else { lds_st<f4>(mine + lane * 16, acc[2]); lds_st<f4>(mine + 1024 + lane * 16, acc[3]); } \
        bar();                                                                                \
        const f4 y0 = lds_ld<f4>(other + lane * 16) + acc[0], y1 = lds_ld<f4>(other + 1024 + lane * 16) + acc[1]; \
        lds_st<u2>(other + lane * 8, u2{pk(y0[0], y0[1]), pk(y0[2], y0[3])});                 \
        lds_st<u2>(other + 1024 + lane * 8, u2{pk(y1[0], y1[1]), pk(y1[2], y1[3])});          \
    }
    EXCHANGE(0);
    {
        const int po = k0 + KP + (15 - c16 + 4 * g) * 4;
#define PFR(d) u4{lds_ld<u32>(po + 64 * (7 - (d))), lds_ld<u32>(po + 64 * (7 - (d)) + 4), lds_ld<u32>(po + 64 * (7 - (d)) + 8), lds_ld<u32>(po + 64 * (7 - (d)) + 12)}
        const u4 g4 = PFR(4), g5 = PFR(5), g6 = PFR(6);
        bar();
        STAMP(14);
        PRIO(3);
        acc[0] = mfma(vs[0], gv[2], Z); acc[1] = mfma(vs[1], gv[2], Z); acc[2] = mfma(vs[2], gv[2], Z); acc[3] = mfma(vs[3], gv[2], Z);
        acc[0] = mfma(vs[1], gv[1], acc[0]); acc[1] = mfma(vs[2], gv[1], acc[1]); acc[2] = mfma(vs[3], gv[1], acc[2]);
        acc[0] = mfma(vs[2], gv[0], acc[0]); acc[1] = mfma(vs[3], gv[0], acc[1]);
        if (hf) {
            acc[3] = mfma(ws0, g6, acc[3]); acc[2] = mfma(ws0, g5, acc[2]); acc[1] = mfma(ws0, g4, acc[1]);
            acc[3] = mfma(ws1, g5, acc[3]); acc[2] = mfma(ws1, g4, acc[2]); acc[3] = mfma(ws2, g4, acc[3]);
        } else {
            const u4 g7 = PFR(7), sel = lds_ld<u4>(lt);
            const u32 r = lds_ld<u32>(k0 + KX);
            acc[3] = mfma(ws0, g7, acc[3]); acc[2] = mfma(ws0, g6, acc[2]); acc[0] = mfma(ws0, g4, acc[0]); acc[1] = mfma(ws0, g5, acc[1]);
            const u4 t = {__builtin_amdgcn_perm(r, r, sel[0]), __builtin_amdgcn_perm(r, r, sel[1]), __builtin_amdgcn_perm(r, r, sel[2]), __builtin_amdgcn_perm(r, r, sel[3])};
#pragma unroll
            for (int q = 0; q < 4; q++) acc[q] = mfma(t, UFR(0, q), acc[q]);
        }
#pragma unroll
        for (int i = 0; i < 4; i++) pin(acc[i]);
        await<1>(FU2, 4);
        u4 A[2][2], B[2][5];
#pragma unroll
        for (int p = 0; p < 5; p++) B[0][p] = UFR(0, 3 + p);
        A[0][0] = KFR(0, 0); A[0][1] = KFR(0, 1);
        vs[0] = VSUM(4); vs[1] = VSUM(5);
        if (!hf) {
            const u4 t = ld_k(ka - 320);
            acc[0] = mfma(t, UFR(0, 2), acc[0]);
#pragma unroll
            for (int q = 1; q < 4; q++) acc[q] = mfma(t, B[0][q - 1], acc[q]);
        }
        acc[2] = mfma(vs[0], gv[0], acc[2]); acc[3] = mfma(vs[0], gv[1], acc[3]); acc[3] = mfma(vs[1], gv[0], acc[3]);
#pragma unroll
        for (int rho = 0; rho < 4; rho++)
#pragma unroll
            for (int p = 0; p < 2; p++) {
                SB;
                if (p == 0) { if (rho == 1) PRIO(2); else if (rho == 2) PRIO(1); else if (rho == 3) PRIO(0); }
                if (rho < 3) {
#pragma unroll
                    for (int q = 3 * p; q < 3 + 2 * p; q++) B[(rho + 1) & 1][q] = UFR(rho + 1, 3 + q);
                    A[(rho + 1) & 1][p] = KFR(rho + 1, p);
                }
#pragma unroll
                for (int j = 0; j < 4; j++) acc[j] = mfma(A[rho & 1][p], B[rho & 1][j + 1 - p], acc[j]);
            }
        SB;
    }
#pragma unroll
    for (int i = 0; i < 4; i++) pin(acc[i]);
    STAMP(5);
    EXCHANGE(2048);
    bar();
    STAMP(6);
    STAMP_OUT(0);
}

__global__ void __launch_bounds__(NT) __attribute__((aligned(4096))) fused_kernel(const u16* __restrict__ u, const u16* __restrict__ lar, const u16* __restrict__ aimg,
                                                   const u16* __restrict__ cre, const u16* __restrict__ cim, const u16* __restrict__ ldt,
                                                   const u16* __restrict__ dsk, const u16* __restrict__ x, const u16* __restrict__ wr,
                                                   const u16* __restrict__ wi, u16* __restrict__ out, char* ws) {
    const char* entry;
    asm volatile("s_getpc_b64 %0" : "=s"(entry));
    APar pr;
    if (blockIdx.x >= A0 && blockIdx.x < H0 && __builtin_amdgcn_readfirstlane(threadIdx.x) < 768) {
        const int a = blockIdx.x - A0, d = 6 * ((a & 7) * 12 + (a >> 3)) + __builtin_amdgcn_readfirstlane(threadIdx.x >> 7);
        asm volatile("global_load_ushort %0, %6, %8\n\tglobal_load_ushort %1, %6, %9\n\tglobal_load_ushort %2, %6, %10\n\t"
                     "global_load_ushort %3, %6, %11\n\tglobal_load_ushort %4, %7, %12\n\tglobal_load_ushort %5, %7, %13"
                     : "=&v"(pr.l), "=&v"(pr.a), "=&v"(pr.r), "=&v"(pr.i), "=&v"(pr.t), "=&v"(pr.d)
                     : "v"((d * NS + (threadIdx.x & 127)) * 2), "v"(d * 2), "s"(lar), "s"(aimg), "s"(cre), "s"(cim), "s"(ldt), "s"(dsk) : "memory");
    }
    if (__builtin_expect(blockIdx.x < NB2, 1)) head_b(entry, x, wr, wi, out, ws);
    else if (blockIdx.x >= A0 && blockIdx.x < H0) head_a(u, pr, out, ws);
    else warm_code(entry);
}

extern "C" void kernel_launch(void* const* d_in, const int* in_sizes, int n_in, void* d_out, int out_size, void* d_ws,
                              size_t ws_size, hipStream_t stream) {
    (void)hipFuncSetAttribute((const void*)fused_kernel, hipFuncAttributeMaxDynamicSharedMemorySize, LDS_BYTES);
    fused_kernel<<<GRID, NT, LDS_BYTES, stream>>>((const u16*)d_in[0], (const u16*)d_in[1], (const u16*)d_in[2], (const u16*)d_in[3],
                                                  (const u16*)d_in[4], (const u16*)d_in[5], (const u16*)d_in[6], (const u16*)d_in[7],
                                                  (const u16*)d_in[8], (const u16*)d_in[9], (u16*)d_out, (char*)d_ws);
}
```

```cpp
#define POLL_SLEEP 6
#include <hip/hip_runtime.h>

typedef unsigned short u16;
typedef unsigned int u32;
typedef float f2 __attribute__((ext_vector_type(2)));
typedef float f4 __attribute__((ext_vector_type(4)));
typedef u32 u2 __attribute__((ext_vector_type(2)));
typedef u32 u4 __attribute__((ext_vector_type(4)));
typedef int i8v __attribute__((ext_vector_type(8)));
typedef short s4 __attribute__((ext_vector_type(4)));
typedef short s8 __attribute__((ext_vector_type(8)));
typedef __bf16 bf2 __attribute__((ext_vector_type(2)));
#define DI __device__ __forceinline__
#define LAS __attribute__((address_space(3)))
#define GAS __attribute__((address_space(1)))
#define CAS __attribute__((address_space(4)))
#pragma clang diagnostic ignored "-Wint-to-pointer-cast"
struct u3 { u32 v[3]; DI u32& operator[](int i) { return v[i]; } DI const u32& operator[](int i) const { return v[i]; } };

constexpr int SEQ = 2048, DM = 576, NS = 128, DF = 289;
constexpr int NB1 = 128;
constexpr int NB2 = 145;
constexpr int A0 = 152;
constexpr int NA = 96;
constexpr int H0 = A0 + NA;
constexpr int GRID = H0 + 8;
constexpr int NT = 1024;

constexpr u32 WS_EPOCH = 0, WS_F1 = 4096, WS_FY = WS_F1 + 8 * 16 * 128, WS_X1 = 1u << 20, WS_Y1 = 4u << 20, WS_USED = 8u << 20;
#define STAMP(i)
#define STAMP_DECL
#define STAMP_OUT(T)

constexpr int F_SA = 0, F_EO = 4;
constexpr int F_ARE = 9, F_AIM = 10, F_BRE = 11, F_BIM = 12, F_G = 13;
constexpr int F_F16 = 21, F_GI = 23, F_AI = 31, F_M1C = 33;
constexpr int F_VAR = 37;
constexpr int F_END = F_VAR + 40;
constexpr int ET = 452, ER = 28, E2V = 580, E2U = 36, OP = 1168;
constexpr int LB_X = 0, LB_I = 0, LB_Y = 0;
constexpr int LB_E = 16 * OP;
constexpr int LB_O = LB_E, LB_E2 = LB_E;
constexpr int LB_S = LB_E + 16 * ET * 4;
constexpr int LB_W = LB_S + 1200 * 16;
constexpr int LB_T = LB_W + 2 * 256 * 36;
constexpr int LB_G = LB_T + (F_VAR + 16) * 1024;
constexpr int LB_END = LB_G + 16;
struct CosTab { double c[2048]; double c9[9], s9[9]; };
constexpr double tay_cos(double x) { double t = 1, s = 1; for (int k = 1; k <= 14; k++) { t *= -x * x / ((2 * k - 1) * (2 * k)); s += t; } return s; }
constexpr double tay_sin(double x) { double t = x, s = x; for (int k = 1; k <= 14; k++) { t *= -x * x / ((2 * k + 1) * (2 * k)); s += t; } return s; }
constexpr CosTab make_cos() {
    CosTab t = {};
    const double PI2 = 6.283185307179586476925;
    for (int k = 0; k <= 512; k++) {
        const double v = k == 512 ? 0.0 : (k <= 256 ? tay_cos(PI2 * k / 2048) : tay_sin(PI2 * (512 - k) / 2048));
        t.c[k] = v; t.c[(2048 - k) & 2047] = v; t.c[1024 - k] = -v; t.c[(1024 + k) & 2047] = -v;
    }
    t.c[512] = 0.0; t.c[1536] = 0.0;
    for (int k = 0; k < 9; k++) { const double a = PI2 * (k <= 4 ? k : k - 9) / 9; t.c9[k] = tay_cos(a); t.s9[k] = tay_sin(a); }
    return t;
}
constexpr CosTab CT = make_cos();
constexpr double cs(int n) { return CT.c[n & 2047]; }
constexpr double sn(int n) { return CT.c[(n - 512) & 2047]; }
constexpr u32 bfr(double x) { const u32 b = __builtin_bit_cast(u32, (float)x); return (b + 0x7fffu + ((b >> 16) & 1u)) >> 16; }
constexpr u32 pkc(double lo, double hi) { return bfr(lo) | (bfr(hi) << 16); }
constexpr u32 f32c(double x) { return __builtin_bit_cast(u32, (float)x); }
template <int N> struct Frags { u32 w[N * 256]; };
constexpr u32 w_re(double amp, int n) { return pkc(amp * cs(n), amp * sn(n)); }
constexpr u32 w_im(double amp, int n) { return pkc(-amp * sn(n), amp * cs(n)); }
constexpr Frags<F_VAR> make_t0() {
    Frags<F_VAR> t = {};
    const double R2 = 1.4142135623730951;
    for (int l = 0; l < 64; l++)
        for (int i = 0; i < 4; i++) {
            const int c = l & 15, g = l >> 4, r = 4 * g + i, o = l * 4 + i;
            {
                const int b = c >> 2, tq = c & 3;
                const double amp = b ? 1.0 : R2;
                const int n0 = 512 * tq * g + (b ? 256 * b * (2 * i) : -256), n1 = 512 * tq * g + (b ? 256 * b * (2 * i + 1) : 256);
                t.w[F_ARE * 256 + o] = pkc(amp * cs(n0), amp * cs(n1));
                t.w[F_AIM * 256 + o] = pkc(-amp * sn(n0), -amp * sn(n1));
            }
            for (int w = 0; w < 9; w++) {
                const int sh = 16 * (i & 1), oo = w * 128 + l * 2 + (i >> 1);
                t.w[F_EO * 256 + oo] |= (u32)((c * ET + r * ER + 2 * ((7 * w + r) % 9)) * 4) << sh;
                if (w < 8) {
                    const int k = (9 * (w & 3) + 36 * r + 64 * (c % 9)) % DM, slot = w < 4 ? (k <= 288 ? 2 * k : 2 * (DM - k) + 1) : k;
                    t.w[F_SA * 256 + oo] |= (u32)(slot * 8 + (slot >> 6) * 16) << sh;
                }
            }
            if (r < 9) { const int k = (r * c) % 9; t.w[F_BRE * 256 + o] = pkc(CT.c9[k], CT.s9[k]); t.w[F_BIM * 256 + o] = pkc(-CT.s9[k], CT.c9[k]); }
            for (int q = 0; q < 4; q++) {
                t.w[(F_G + 2 * q) * 256 + o] = w_re(1.0, 32 * r * (q + 4 * c)); t.w[(F_G + 2 * q + 1) * 256 + o] = w_im(1.0, 32 * r * (q + 4 * c));
                t.w[(F_GI + 2 * q) * 256 + o] = w_re(1.0, 32 * c * (q + 4 * r)); t.w[(F_GI + 2 * q + 1) * 256 + o] = w_im(1.0, 32 * c * (q + 4 * r));
            }
            t.w[F_F16 * 256 + o] = w_re(1.0, 128 * r * c); t.w[(F_F16 + 1) * 256 + o] = w_im(1.0, 128 * r * c);
            for (int h = 0; h < 2; h++) {
                const int j = 4 * h + (c >> 2), n = 512 * i * (c & 3) + (g ? 256 * g * j : ((j & 1) ? 256 : -256));
                t.w[(F_AI + h) * 256 + o] = w_re(g ? 2.0 : R2, n);
                const int m = 16 * r * (4 * h + 8 * c) - 512 * h;
                t.w[(F_M1C + 2 * h) * 256 + o] = pkc(0.5 * cs(m), -0.5 * sn(m));
                t.w[(F_M1C + 2 * h + 1) * 256 + o] = pkc(-0.5 * sn(m), -0.5 * cs(m));
            }
        }
    return t;
}
constexpr Frags<40> make_t1() {
    Frags<40> t = {};
    for (int v = 0; v < 5; v++)
        for (int l = 0; l < 64; l++)
            for (int i = 0; i < 4; i++) {
                const int be = v < 3 ? v + 1 : 4 * (v - 3), c = l & 15, r = 4 * (l >> 4) + i, o = v * 2048 + l * 4 + i;
                const double amp = v < 3 ? 1.0 : 0.5;
                const int n1 = 16 * r * (be + 8 * c) + (v == 4 ? 512 : 0), n4 = 16 * c * (be + 8 * r);
                t.w[o] = w_re(amp, n1); t.w[o + 256] = w_im(amp, n1);
                t.w[o + 512] = w_re(1.0, n4); t.w[o + 768] = w_im(1.0, n4);
                t.w[o + 1024] = f32c(cs(r * (be + 8 * c))); t.w[o + 1280] = f32c(-sn(r * (be + 8 * c)));
                t.w[o + 1536] = f32c(cs(c * (be + 8 * r))); t.w[o + 1792] = f32c(-sn(c * (be + 8 * r)));
            }
    return t;
}
constexpr int qidx(int l) {
    if (l <= 256) return l;
    const int sh = l < 512 ? 1 : (l < 1024 ? 2 : 3), h = 1 << (sh - 1), m = l >> sh, r = l & ((1 << sh) - 1);
    return 128 * sh + m + ((r > h || (r == h && (m & 1))) ? 1 : 0);
}
constexpr Frags<6> make_t2() {
    Frags<6> t = {};
    for (int l = 0; l < 64; l++) {
        const int c = l & 15, g = l >> 4, e = 543 - 2 * c - g;
        t.w[256 + l * 4] = (u32)((e < 518 ? qidx(e) : 643) * 4) | ((u32)((e - 1 < 518 ? qidx(e - 1) : 643) * 4) << 16);
        for (int i = 0; i < 4; i++)
            for (int h = 0; h < 2; h++) {
                const int lag = 1024 + 16 + c - 8 * g - 2 * i - h, x = 8 * g + 2 * i + h;
                t.w[l * 4 + i] |= (lag < 1027 || lag > 1035 ? 0x0c0cu : (lag < 1029 ? 0x0100u : 0x0302u)) << (16 * h);
                if (x >= c + 8 && x <= c + 16) t.w[2 * 256 + l * 4 + i] |= 0x3f80u << (16 * h);
                if (x >= c + 1 && x <= c + 7) t.w[3 * 256 + l * 4 + i] |= 0x3f80u << (16 * h);
                if (x >= c + 6 && x <= c + 10) t.w[4 * 256 + l * 4 + i] |= 0x3f80u << (16 * h);
                if (x >= c + 3 && x <= c + 5) t.w[5 * 256 + l * 4 + i] |= 0x3f80u << (16 * h);
            }
    }
    return t;
}
struct Tables { Frags<F_VAR> a; Frags<40> b; Frags<6> c; };
constexpr Frags<6> T2 = make_t2();
constexpr Frags<F_VAR> T0 = make_t0();
constexpr Frags<40> T1 = make_t1();
static __device__ const __attribute__((aligned(128))) Tables TAB = {T0, T1, T2};
constexpr int LA_G = 0;
constexpr int LA_K = LA_G + 12 * 4608;
constexpr int KI = 1344, KQ = 4 * KI, KP = KQ + 512, KX = KP + 320, KC = KX + 16;
constexpr int LA_U = LA_K + 6 * KC;
constexpr int LA_Q = LA_U + 6 * 4608;
constexpr int LA_F = LA_Q + 12 * 2576;
constexpr int LA_S = LA_F + 16;
constexpr int LA_END = LA_S + 6144;
constexpr int FU2 = 0;
constexpr int LDS_BYTES = (LA_END > LB_END ? LA_END : LB_END);
static_assert(LDS_BYTES <= 160 * 1024 && LDS_BYTES > 80 * 1024, "one workgroup per CU");

DI u32 pk(float a, float b) { f2 v = {a, b}; return __builtin_bit_cast(u32, __builtin_convertvector(v, bf2)); }
DI float blo(u32 v) { return __uint_as_float(v << 16); }
DI float bhi(u32 v) { return __uint_as_float(v & 0xffff0000u); }
DI float bf2f(u16 v) { return __uint_as_float((u32)v << 16); }
DI float rbf(float x) { return blo(pk(x, x)); }
DI f2 cmul(f2 a, f2 b) { f2 t = a.xx * b; f2 s = {-b.y, b.x}; return __builtin_elementwise_fma(a.yy, s, t); }
DI f4 mfma(u4 a, u4 b, f4 c) { return __builtin_amdgcn_mfma_f32_16x16x32_bf16(__builtin_bit_cast(s8, a), __builtin_bit_cast(s8, b), c, 0, 0, 0); }
DI u4 pack4(f4 re, f4 im) { u4 r = {pk(re[0], im[0]), pk(re[1], im[1]), pk(re[2], im[2]), pk(re[3], im[3])}; return r; }
DI u4 twid(f4 re, f4 im, f4 wr, f4 wi) { return pack4(__builtin_elementwise_fma(re, wr, -(im * wi)), __builtin_elementwise_fma(re, wi, im * wr)); }
DI int mod9(int x) { return x - 9 * ((x * 57) >> 9); }
template <class T> DI void pin(T& v) { asm volatile("" : "+v"(v)); }

template <class T> DI T lds_ld(int off) { return *(const LAS T*)(u32)off; }
template <class T> DI void lds_st(int off, T v) { *(LAS T*)(u32)off = v; }
DI void bar() { __syncthreads(); }
#define PRIO(n) __builtin_amdgcn_s_setprio(n)
#define PRIOB(n)
DI void glds16(const void* g, u32 off, int lds) {
    u32 keep;
    asm volatile("s_mov_b32 %0, m0\n\ts_mov_b32 m0, %3\n\ts_nop 4\n\tglobal_load_lds_dwordx4 %1, %2\n\ts_mov_b32 m0, %0" : "=&s"(keep) : "v"(off), "s"(g), "s"(lds) : "memory");
}

DI void publish_tag(u32 epoch) {
    u32 t;
    asm volatile("v_add_u32 %0, 0x9E3779B1, %1\n\tds_write_b32 %2, %0\n\ts_waitcnt lgkmcnt(0)" : "=&v"(t) : "v"(epoch), "v"(LB_G) : "memory");
}
DI void wait_flag(__amdgpu_buffer_rsrc_t r, u32 off, u32 tag, bool on) {
    if (on) {
        const unsigned long long t0 = __builtin_amdgcn_s_memrealtime();
        while (!__all(__builtin_amdgcn_raw_buffer_load_b32(r, off, 0, 16) == tag)) {
            if (__builtin_amdgcn_s_memrealtime() - t0 > 5000000ull) break;
            __builtin_amdgcn_s_sleep(POLL_SLEEP);
            asm volatile("" ::: "memory");
        }
    }
    asm volatile("" ::: "memory");
}

constexpr u32 POISON = 0xFFFFFFFFu;
DI bool unset(u4 v) { return max(max(v[0], v[1]), max(v[2], v[3])) == POISON; }
#define DPOLL_SLEEP 1
template <bool TWO> DI void load_set(__amdgpu_buffer_rsrc_t r, u32 off, bool on, u4& a, bool on2, u4& b) {
    const unsigned long long t0 = __builtin_amdgcn_s_memrealtime();
    bool ma = on, mb = TWO && on2;
    for (;;) {
        if (ma) a = __builtin_amdgcn_raw_buffer_load_b128(r, off, 0, 16);
        if (TWO && mb) b = __builtin_amdgcn_raw_buffer_load_b128(r, off, 16384, 16);
        ma = ma && unset(a);
        if (TWO) mb = mb && unset(b);
        if (!__any(ma || mb) || __builtin_amdgcn_s_memrealtime() - t0 > 5000000ull) break;
        __builtin_amdgcn_s_sleep(DPOLL_SLEEP);
        asm volatile("" ::: "memory");
    }
}
DI int unit_w0(int u) {
    const int q = min((u * 57) >> 11, 3), kg = u - 36 * q, b = (q + 1) & 3;
    return u >= 108 ? min(16 * kg, 562) : (kg >= 18 ? (2048 - b) * (2 * DF) + 1122 - 32 * kg : b * (2 * DF) + 32 * kg);
}
constexpr int CODE_LINES = 128;
DI void warm_code(const char* entry) {
    if (threadIdx.x < CODE_LINES) { const u32 t = *(const GAS u32*)(entry + threadIdx.x * 128); asm volatile("" :: "v"(t)); }
}

DI void head_b(const char* entry, const u16* __restrict__ x, const u16* __restrict__ wr, const u16* __restrict__ wi,
               u16* __restrict__ out, char* ws) {
    const int tid = threadIdx.x, lane = tid & 63, c16 = lane & 15, g = lane >> 4;
    const int wv = __builtin_amdgcn_readfirstlane(tid >> 6);
    const int id = blockIdx.x;
    STAMP_DECL;
    const bool rows = id < NB1;
    const int s00 = 32 * (id >> 4) + (id & 15);
    const char* tab = (const char*)&TAB;
    const int R = (tid * 911) >> 16;
    const u32 xoff = (s00 + 16 * (R >> 3) + 256 * (R & 7)) * (2 * DM) + (tid - R * 72) * 16;
    asm volatile("" :: "s"(x), "s"(wr), "s"(wi), "s"(out), "s"(ws), "v"(xoff));
    const __amdgpu_buffer_rsrc_t rws = __builtin_amdgcn_make_buffer_rsrc(ws, 0, WS_USED, 0x00020000);
    STAMP(13);
    int gy = WS_Y1 + id * (NB2 * 128) + tid * 16;
    u32 epoch = 0;
    if (wv == 0) asm volatile("global_load_dword %0, %0, %1 sc1" : "+v"(epoch) : "s"(ws) : "memory");
    if (rows) {
        glds16(x, xoff, LB_X + wv * 1024);
        if (wv < 2) glds16(x, (s00 + 16 + 1536) * (2 * DM) + (tid < 56 ? (tid + 16) * 16 : 256 * 2 * DM + (tid - 56) * 16), LB_X + 16384 + wv * 1024);
        else glds16(tab, tid * 16 - 2048, LB_T + (wv - 2) * 1024);
        if (wv >= 9) glds16(tab, tid * 16 + 5 * 1024, LB_T + (wv + 5) * 1024);
    }
    const int u2i = (id & 7) ? (id & 7) * 18 + 1 + (id >> 3) : (id == 136 ? 0 : (id >> 3) + (id < 136 ? 1 : 0));
    const bool star = u2i >= 108;
    const int q36 = min((u2i * 57) >> 11, 3), kg = u2i - 36 * q36, bq = (q36 + 1) & 3;
    const bool mir = !star && kg >= 18;
    const int w0 = unit_w0(u2i);
    __builtin_amdgcn_s_barrier();
    STAMP(14);
    const u32 woff = w0 + ((tid >> 1) & 255) * (mir ? -16 * DF : 16 * DF) + (tid & 1) * (star ? 8 * DF : 16);
    u4 wra, wrb;
    if (wv >= 8) asm volatile("global_load_dwordx4 %0, %2, %3\n\tglobal_load_dwordx4 %1, %2, %4" : "=&v"(wra), "=&v"(wrb) : "v"(woff), "s"(wr), "s"(wi) : "memory");
#define STAGE23 do { const int o = LB_W + ((tid >> 1) & 255) * 36 + (tid & 1) * 16, t16 = (tid - 512) * 16, lw = LB_T + (wv - 8) * 1024; \
        glds16(tab, F_F16 * 1024 + t16, lw + F_F16 * 1024); glds16(tab, (F_F16 + 8) * 1024 + t16, lw + (F_F16 + 8) * 1024); \
        glds16(tab, (F_VAR + 8 * q36) * 1024 + t16, lw + F_VAR * 1024); if (star) glds16(tab, (F_VAR + 32) * 1024 + t16, lw + (F_VAR + 8) * 1024); \
        asm volatile("s_waitcnt vmcnt(0)\n\tds_write2_b32 %0, %2, %3 offset1:1\n\tds_write2_b32 %0, %4, %5 offset0:2 offset1:3\n\t" \
                     "ds_write2_b32 %1, %6, %7 offset1:1\n\tds_write2_b32 %1, %8, %9 offset0:2 offset1:3\n\ts_waitcnt lgkmcnt(0)" \
                     :: "v"(o), "v"(o + 9216), "v"(wra[0]), "v"(wra[1]), "v"(wra[2]), "v"(wra[3]), "v"(wrb[0]), "v"(wrb[1]), "v"(wrb[2]), "v"(wrb[3]) : "memory"); } while (0)
    const f4 Z = {0.f, 0.f, 0.f, 0.f};
    const int tau = wv & 3, bw = wv >> 2;
    int tl = LB_T + lane * 16;
    int lin = LB_S + (tid + (tid >> 5)) * 16;
    pin(tl); pin(lin);
    int sadr[4];

    u32 tag;
    if (rows) {
        if (tid < 256) {
            const int o = LB_E + 72 + (tid & 15) * (ER * 4) + (tid >> 4) * (ET * 4);
            lds_st<u2>(o, u2{0u, 0u}); lds_st<u4>(o + 8, u4{0u, 0u, 0u, 0u});
        }
        int xo = LB_X + (c16 >> 2) * 1152 + 2 * (144 * ((g - wv) & 3) + 16 * wv + 4 * (c16 & 3));
        int bo = LB_E + (wv * ET + c16 * ER + min(8 * g, 16)) * 4;
        int go = WS_X1 + (tid >> 3) * 16384 + id * 128 + (tid & 7) * 16;
        int ta = LB_T + lane * 8;
        pin(xo); pin(bo); pin(go); pin(ta);
        STAMP(1);
        asm volatile("s_cmp_lt_u32 %0, 8\n\ts_cbranch_scc0 .Lw%=\n\ts_waitcnt vmcnt(0)\n.Lw%=:\n\ts_waitcnt vmcnt(2)" :: "s"(wv) : "memory", "scc");
        if (wv == 0) publish_tag(epoch);
        bar();
        STAMP(2);
        if (wv < 9) {
            u4 d[2];
#pragma unroll
            for (int s = 0; s < 2; s++) {
                const s4 lo = __builtin_amdgcn_ds_read_tr16_b64_v4i16((LAS s4*)(u32)(xo + s * 9216));
                const s4 hi = __builtin_amdgcn_ds_read_tr16_b64_v4i16((LAS s4*)(u32)(xo + s * 9216 + 4608));
                d[s] = __builtin_bit_cast(u4, __builtin_shufflevector(lo, hi, 0, 1, 2, 3, 4, 5, 6, 7));
            }
            const u4 are = lds_ld<u4>(tl + F_ARE * 1024), aim = lds_ld<u4>(tl + F_AIM * 1024);
            const u2 eo = lds_ld<u2>(ta + F_EO * 1024 + wv * 512);
#pragma unroll
            for (int s = 0; s < 2; s++) d[s] = pack4(mfma(d[s], are, Z), mfma(d[s], aim, Z));
#pragma unroll
            for (int i = 0; i < 4; i++) lds_st<u2>(LB_E + ((i & 1) ? eo[i >> 1] >> 16 : eo[i >> 1] & 0xffff), u2{d[0][i], d[1][i]});
        }
        u4 bre = lds_ld<u4>(tl + F_BRE * 1024), bim = lds_ld<u4>(tl + F_BIM * 1024);
        u4 gre = lds_ld<u4>(tl + (F_G + 2 * tau) * 1024), gim = lds_ld<u4>(tl + (F_G + 2 * tau + 1) * 1024);
        {
            const u2 sa = lds_ld<u2>(ta + F_SA * 1024 + (tau + (bw ? 4 : 0)) * 512);
            const int base = LB_S + ((bw + 3) & 3) * (DM * 8 + 144);
#pragma unroll
            for (int i = 0; i < 4; i++) { sadr[i] = base + ((i & 1) ? sa[i >> 1] >> 16 : sa[i >> 1] & 0xffff); pin(sadr[i]); }
        }
        tag = lds_ld<u32>(LB_G);
        pin(bre); pin(bim); pin(gre); pin(gim); pin(tag);
        bar();
        {
            PRIOB(2);
            const u4 p0 = lds_ld<u4>(bo), p1 = lds_ld<u4>(bo + 16);
            u4 d[2];
            f4 re[2], im[2];
#pragma unroll
            for (int s = 0; s < 2; s++) { const u4 a = {p0[s], p0[2 + s], p1[s], p1[2 + s]}; re[s] = mfma(a, bre, Z); im[s] = mfma(a, bim, Z); }
            PRIOB(1);
#pragma unroll
            for (int s = 0; s < 2; s++) { const u4 t = pack4(re[s], im[s]); re[s] = mfma(gre, t, Z); im[s] = mfma(gim, t, Z); }
            PRIOB(0);
#pragma unroll
            for (int s = 0; s < 2; s++) d[s] = pack4(re[s], im[s]);
            if (c16 < 9) {
#pragma unroll
                for (int i = 0; i < 4; i++) lds_st<u2>(sadr[i], u2{d[0][i], d[1][i]});
                if (wv == 0 && c16 == 0 && !(g & 1)) lds_st<u2>(sadr[0] + 8, u2{d[0][0], d[1][0]});
            }
        }
        bar();
        STAMP(3);
        if (wv < 8) {
            u4 v0 = lds_ld<u4>(lin), v1 = lds_ld<u4>(lin + 528 * 16), v2 = lds_ld<u4>(lin + (wv < 3 ? 1056 * 16 : 0));
            pin(v2);
            __builtin_amdgcn_raw_buffer_store_b128(v0, rws, go, 0, 16);
            __builtin_amdgcn_raw_buffer_store_b128(v1, rws, go, 64 * 16384, 16);
            if (tid <= 128) __builtin_amdgcn_raw_buffer_store_b128(v2, rws, go, 128 * 16384, 16);
            asm volatile("s_waitcnt vmcnt(0)" ::: "memory");
            if (lane == 0) __builtin_amdgcn_raw_buffer_store_b32(tag, rws, WS_F1 + (wv * 16 + (id >> 3)) * 128 + (id & 7) * 4, 0, 16);
            STAMP(4);
        } else {
            STAGE23;
            const u4 P = {POISON, POISON, POISON, POISON};
            __builtin_amdgcn_raw_buffer_store_b128(P, rws, gy - 8192, 0, 16);
            __builtin_amdgcn_raw_buffer_store_b128(P, rws, gy, 0, 16);
            if (tid <= 640) __builtin_amdgcn_raw_buffer_store_b128(P, rws, gy, 8192, 16);
        }
        bar();
    } else {
        if (wv < 8 && (kg == 0 || kg == (star ? 36 : 18))) {
            const int m = tid & 255;
            lds_st<u16>(LB_W + tid * 36 + 32, ((wv >> 2) ? wi : wr)[((mir ? bq + 8 * m : 2048 - bq - 8 * m) & 2047) * DF + (kg ? 288 : 0)]);
        }
        warm_code(entry);
        if (wv >= 8) STAGE23;
        asm volatile("s_waitcnt vmcnt(0)" ::: "memory");
        if (wv == 0) publish_tag(epoch);
        bar();
        tag = lds_ld<u32>(LB_G);
    }
    STAMP(12);

    const int typ = star ? (wv & 1) : 0;
    const int tv = tl + (F_VAR + 8 * typ) * 1024;
    u4 fre = lds_ld<u4>(tl + F_F16 * 1024), fim = lds_ld<u4>(tl + (F_F16 + 1) * 1024);
    u4 m1r = lds_ld<u4>(tv), m1i = lds_ld<u4>(tv + 1024), m4r = lds_ld<u4>(tv + 2048), m4i = lds_ld<u4>(tv + 3072);
    f4 twr = lds_ld<f4>(tv + 4096), twi = lds_ld<f4>(tv + 5120), tur = lds_ld<f4>(tv + 6144), tui = lds_ld<f4>(tv + 7168);
    u4 mcr = lds_ld<u4>(tl + (F_M1C + 2 * typ) * 1024), mci = lds_ld<u4>(tl + (F_M1C + 2 * typ + 1) * 1024);
    f4 wfr, wfi;
    {
        const int kcol = star ? 8 * kg + (wv >> 1) : 16 * kg + wv;
        const int cc = star ? 8 * typ + (u2i < NB2 - 1 ? (wv >> 1) : 7) : (mir ? 15 - wv : wv);
        const int wo = LB_W + (c16 + 64 * g) * 36 + cc * 2;
#pragma unroll
        for (int i = 0; i < 4; i++) {
            wfr[i] = bf2f(lds_ld<u16>(wo + i * 576));
            wfi[i] = bf2f(lds_ld<u16>(wo + i * 576 + 9216));
        }
        float sc = 1.f / (float)(SEQ * DM);
        if (kcol == 0 || kcol == 288) {
            sc *= 0.5f;
#pragma unroll
            for (int i = 0; i < 4; i++) {
                const int m = c16 + 64 * g + 16 * i;
                const int o = LB_W + (typ ? (255 - m) * 36 + cc * 2 : m * 36 + 32);
                wfr[i] += bf2f(lds_ld<u16>(o));
                wfi[i] -= bf2f(lds_ld<u16>(o + 9216));
            }
        }
        wfr *= sc; wfi *= (mir ? -sc : sc);
    }
    f4 wnr = -wfr, wni = -wfi;
    int io = (32 * g + c16) * 128 + (((wv >> 1) ^ (c16 >> 1)) * 16);
    int so = (tid >> 3) * 128 + (((tid & 7) ^ ((tid >> 4) & 7)) * 16);
    pin(fre); pin(fim); pin(m1r); pin(m1i); pin(m4r); pin(m4i); pin(twr); pin(twi); pin(tur); pin(tui); pin(mcr); pin(mci);
    int gi = WS_X1 + u2i * 16384 + tid * 16, gq = WS_Y1 + (tid >> 3) * (NB2 * 128) + u2i * 128 + (tid & 7) * 16;
    int i1 = LB_I + io + (star ? 0 : (wv & 1) * 8);
    io += LB_O + (wv & 1) * 8;
    pin(wfr); pin(wnr); pin(wni); pin(io); pin(so); pin(gi); pin(gq); pin(i1);
    u32 fy = WS_FY + (((u2i >> 3) & 7) * 16 + wv) * 128 + (lane & 7) * 4;
    pin(fy);
    if (rows && wv >= 8) {
        asm volatile("s_waitcnt vmcnt(0)" ::: "memory");
        if (lane == 0) __builtin_amdgcn_raw_buffer_store_b32(tag, rws, WS_FY + ((wv - 8) * 16 + (id >> 3)) * 128 + (id & 7) * 4, 0, 16);
    }
    wait_flag(rws, WS_F1 + (((u2i >> 3) & 7) * 16 + wv) * 128 + lane * 4, tag, lane < 8);
    STAMP(5);
    lds_st<u4>(LB_I + so, __builtin_amdgcn_raw_buffer_load_b128(rws, gi, 0, 16));
    bar();
    STAMP(6);
    u32 rdy;
    {
        const int o1 = i1;
        PRIOB(3);
        const u2 a0 = lds_ld<u2>(o1), a1 = lds_ld<u2>(o1 + 2048);
        const u4 a = {a0[0], a0[1], a1[0], a1[1]};
        f4 re = mfma(a, m1r, Z), im = mfma(a, m1i, Z);
        if (star) {
            const u2 b0 = lds_ld<u2>(o1 + 8), b1 = lds_ld<u2>(o1 + 8 + 2048);
            const u4 b = {b0[0], b0[1], b1[0], b1[1]};
            re = mfma(b, mcr, re); im = mfma(b, mci, im);
        }
        u4 t = twid(re, im, twr, twi);
        re = mfma(fre, t, Z); im = mfma(fim, t, Z);
        PRIOB(2);
        __builtin_amdgcn_sched_barrier(0);
        asm volatile("buffer_load_dword %0, %1, %2, 0 offen sc1" : "=&v"(rdy) : "v"(fy), "s"(rws), "v"(t) : "memory");
        __builtin_amdgcn_sched_barrier(0);
        t = pack4(__builtin_elementwise_fma(re, wfr, im * wni), __builtin_elementwise_fma(re, wni, im * wnr));
        re = mfma(t, fre, Z); im = mfma(t, fim, Z);
        PRIOB(1);
        t = twid(re, im, tur, tui);
        re = mfma(m4r, t, Z); im = mfma(m4i, t, Z);
        PRIOB(0);
        t = pack4(re, im);
        lds_st<u2>(io, u2{t[0], t[1]}); lds_st<u2>(io + 2048, u2{t[2], t[3]});
    }
    bar();
    STAMP(7);
    u4 sv;
    {
        u4 v = lds_ld<u4>(LB_O + so);
        if (star) {
            u4 w;
#pragma unroll
            for (int s = 0; s < 2; s++) {
                const float ar = blo(v[s]), ai = bhi(v[s]), cr = blo(v[2 + s]), ci = bhi(v[2 + s]);
                w[s] = pk(ar + ci, ai - cr); w[2 + s] = pk(ar - ci, -ai - cr);
            }
            v = w;
        }
        asm volatile("s_waitcnt vmcnt(0)" : "+v"(rdy) :: "memory");
        if (!__all(rdy == tag)) wait_flag(rws, fy, tag, true);
        if (u2i < NB2 - 1 || (tid & 7) == 0)
            __builtin_amdgcn_raw_buffer_store_b128(v, rws, gq, 0, 16);
        sv = v;
    }
    STAMP(8);
    if (!rows) { STAMP_OUT(0); return; }

    u4 gre = lds_ld<u4>(tl + (F_GI + 2 * tau) * 1024), gim = lds_ld<u4>(tl + (F_GI + 2 * tau + 1) * 1024);
    u4 bre = lds_ld<u4>(tl + F_BRE * 1024), bim = lds_ld<u4>(tl + F_BIM * 1024);
    u4 ai0 = lds_ld<u4>(tl + F_AI * 1024), ai1 = lds_ld<u4>(tl + (F_AI + 1) * 1024);
    int eadr[4];
#pragma unroll
    for (int i = 0; i < 4; i++) {
        const int r = 4 * g + i;
        eadr[i] = LB_E2 + (mod9(4 * (c16 + 18 - r)) * E2V + r * E2U + 2 * wv) * 4;
        pin(eadr[i]);
    }
    int ao = LB_E2 + (wv * E2V + c16 * E2U + 8 * g) * 4;
    int yo = LB_Y + (c16 >> 2) * OP + 2 * (144 * (((c16 & 3) - wv) & 3) + 16 * wv + 4 * g);
    int yl = LB_Y + tid * 16 + R * (OP - 1152);
    u32 oo = 2 * xoff - (tid - R * 72) * 16 + 2 * DM;
    pin(gre); pin(gim); pin(bre); pin(bim); pin(ai0); pin(ai1); pin(ao); pin(gy); pin(yo); pin(yl); pin(oo);
    asm volatile("" :: "v"(sv));
    STAMP(9);
    {
        u4 v, w;
        if (wv < 3) { load_set<true>(rws, gy, true, v, tid <= 128, w); if (tid <= 128) lds_st<u4>(lin + 1056 * 16, w); }
        else load_set<false>(rws, gy, true, v, false, w);
        lds_st<u4>(lin, v);
    }
    bar();
    if (id == 0 && tid == 0) __builtin_amdgcn_raw_buffer_store_b32(tag, rws, WS_EPOCH, 0, 16);
    STAMP(10);
    {
        PRIOB(2);
        u4 d[2];
        u2 p[4];
#pragma unroll
        for (int i = 0; i < 4; i++) p[i] = lds_ld<u2>(sadr[i]);
        f4 re[2], im[2];
#pragma unroll
        for (int s = 0; s < 2; s++) { const u4 a = {p[0][s], p[1][s], p[2][s], p[3][s]}; re[s] = mfma(a, gre, Z); im[s] = mfma(a, gim, Z); }
        PRIOB(1);
#pragma unroll
        for (int s = 0; s < 2; s++) { const u4 t = pack4(re[s], im[s]); re[s] = mfma(t, bre, Z); im[s] = mfma(t, bim, Z); }
        PRIOB(0);
#pragma unroll
        for (int s = 0; s < 2; s++) d[s] = pack4(re[s], im[s]);
        if (c16 < 9) {
#pragma unroll
            for (int i = 0; i < 4; i++) lds_st<u2>(eadr[i], u2{d[0][i], d[1][i]});
        }
    }
    STAMP(15);
    bar();
    if (wv < 9) {
        const u4 p0 = lds_ld<u4>(ao), p1 = lds_ld<u4>(ao + 16);
#pragma unroll
        for (int s = 0; s < 2; s++) {
            const u4 a = {p0[s], p0[2 + s], p1[s], p1[2 + s]};
            const f4 y0 = mfma(a, ai0, Z), y1 = mfma(a, ai1, Z);
            lds_st<u2>(yo + s * (8 * OP), u2{pk(y0[0], y0[1]), pk(y0[2], y0[3])});
            lds_st<u2>(yo + s * (8 * OP) + 4 * OP, u2{pk(y1[0], y1[1]), pk(y1[2], y1[3])});
        }
    }
    bar();
    *(GAS u4*)((char*)out + oo) = lds_ld<u4>(yl);
    if (wv < 2) {
        const int r2 = tid < 56 ? 14 : 15, c2 = tid + 1024 - 72 * r2;
        *(GAS u4*)((char*)out + (u32)((s00 + 16 + 256 * (r2 - 8)) * (4 * DM) + 2 * DM + c2 * 16)) = lds_ld<u4>(LB_Y + r2 * OP + c2 * 16);
    }
    STAMP(11);
    STAMP_OUT(0);
}

DI f2 cexpa(float ar2, float air, float p) {
    const float e = __builtin_amdgcn_exp2f(ar2 * p), t = __builtin_amdgcn_fractf(air * p);
    return f2{e * __builtin_amdgcn_cosf(t), e * __builtin_amdgcn_sinf(t)};
}
DI u4 ld_k(int o) {
    const u2 a = *(const volatile LAS u2*)(u32)o, b = *(const volatile LAS u2*)(u32)(o + 8);
    return u4{a[0], a[1], b[0], b[1]};
}
DI void a_rows_out(int yb, int ystr, char* o) {
    u2 y[6];
#pragma unroll
    for (int c = 0; c < 6; c++) y[c] = lds_ld<u2>(yb + c * ystr);
#pragma unroll
    for (int r = 0; r < 4; r++) {
        u3 v;
#pragma unroll
        for (int c = 0; c < 3; c++) v[c] = __builtin_amdgcn_perm(y[2 * c + 1][r >> 1], y[2 * c][r >> 1], (r & 1) ? 0x07060302u : 0x05040100u);
        *(u3*)(o + r * (4 * DM)) = v;
    }
}
template <int R> DI void stage(u4 v) {
    asm volatile("ds_write_addtid_b32 %0 offset:%4\n\tds_write_addtid_b32 %1 offset:%5\n\tds_write_addtid_b32 %2 offset:%6\n\tds_write_addtid_b32 %3 offset:%7"
                 :: "v"(v[0]), "v"(v[1]), "v"(v[2]), "v"(v[3]), "i"(288 * R), "i"(288 * R + 288), "i"(288 * R + 576), "i"(288 * R + 864) : "memory");
}
#define CM_MUL(d, a, b) "v_pk_mul_f32 " d ", " a ", " b " op_sel_hi:[0,1]\n\t"
#define CM_FMA(d, a, b) "v_pk_fma_f32 " d ", " a ", " b ", " d " op_sel:[1,1,0] op_sel_hi:[1,0,1] neg_lo:[1,0,0]\n\t"
struct C4 { f2 a, b, c, d; };
DI C4 cmul4(f2 a0, f2 b0, f2 a1, f2 b1, f2 a2, f2 b2, f2 a3, f2 b3) {
    C4 r;
    asm("s_nop 0\n\t" CM_MUL("%0", "%4", "%5") CM_MUL("%1", "%6", "%7") CM_MUL("%2", "%8", "%9") CM_MUL("%3", "%10", "%11")
        CM_FMA("%0", "%4", "%5") CM_FMA("%1", "%6", "%7") CM_FMA("%2", "%8", "%9") CM_FMA("%3", "%10", "%11") "s_nop 0"
        : "=&v"(r.a), "=&v"(r.b), "=&v"(r.c), "=&v"(r.d) : "v"(a0), "v"(b0), "v"(a1), "v"(b1), "v"(a2), "v"(b2), "v"(a3), "v"(b3));
    return r;
}
DI C4 cmul4(const C4& a, f2 b) { return cmul4(a.a, b, a.b, b, a.c, b, a.d, b); }
DI u4 pk4(const C4& v) { return u4{pk(v.a.x, v.a.y), pk(v.b.x, v.b.y), pk(v.c.x, v.c.y), pk(v.d.x, v.d.y)}; }

DI void sig(int f) {
    asm volatile("s_waitcnt lgkmcnt(0)" ::: "memory");
    if ((threadIdx.x & 63) == 0) __hip_atomic_fetch_add((LAS u32*)(u32)(LA_F + 4 * f), 1u, __ATOMIC_RELAXED, __HIP_MEMORY_SCOPE_WORKGROUP);
}
template <int SLEEP = 0> DI void await(int f, u32 n) {
    while ((u32)__builtin_amdgcn_readfirstlane(*(const volatile LAS u32*)(u32)(LA_F + 4 * f)) < n) if (SLEEP) __builtin_amdgcn_s_sleep(SLEEP);
    asm volatile("" ::: "memory");
}
DI float exp_bf(float x) {
    const float t = x * 1.44269504f, r = __builtin_fmaf(x, 1.92596299e-8f, __builtin_fmaf(x, 1.44269504f, -t));
    const float e = __builtin_amdgcn_exp2f(t);
    return __builtin_fmaf(e * 0.693147181f, r, e);
}

struct APar { u32 l, a, r, i, t, d; };
DI void head_a(const u16* __restrict__ u, APar pr, u16* __restrict__ out, char* ws) {
    const int tid = threadIdx.x, lane = tid & 63, c16 = lane & 15, g = lane >> 4;
    const int wv = __builtin_amdgcn_readfirstlane(tid >> 6);
    STAMP_DECL;
    const int ai_ = blockIdx.x - A0;
    const int d0 = 6 * ((ai_ & 7) * 12 + (ai_ >> 3));
    const f4 Z = {0.f, 0.f, 0.f, 0.f};

    if (wv >= 12) {
        __builtin_amdgcn_s_setprio(3);
        const int t = tid - 768;
        if (t == 0) lds_st<u2>(LA_F, u2{0u, 0u});
        if (t < 180) lds_st<u4>(LA_U + (t / 30) * 4608 + (t % 30) * 16, u4{0u, 0u, 0u, 0u});
        bar();
        glds16((const char*)&TAB + F_END * 1024, t * 16, LA_S + (wv - 12) * 1024);
        if (wv < 14) glds16((const char*)&TAB + (F_END + 4) * 1024, t * 16, LA_S + (wv - 8) * 1024);
        u3 ua[4], ub[4];
#pragma unroll
        for (int r = 0; r < 4; r++) ua[r] = *(const u3*)(u + (4 * t + r) * DM + d0);
        asm volatile("s_waitcnt vmcnt(4)\n\ts_barrier" ::: "memory");
#pragma unroll
        for (int c = 0; c < 6; c++) {
            const u32 sel = (c & 1) ? 0x07060302u : 0x05040100u;
            lds_st<u2>(LA_U + c * 4608 + 480 + t * 8, u2{__builtin_amdgcn_perm(ua[1][c >> 1], ua[0][c >> 1], sel), __builtin_amdgcn_perm(ua[3][c >> 1], ua[2][c >> 1], sel)});
        }
        asm volatile("" ::: "memory");
#pragma unroll
        for (int r = 0; r < 4; r++) ub[r] = *(const u3*)(u + (1024 + 4 * t + r) * DM + d0);
        STAMP(7);
        bar();
#pragma unroll
        for (int c = 0; c < 6; c++) {
            const u32 sel = (c & 1) ? 0x07060302u : 0x05040100u;
            lds_st<u2>(LA_U + c * 4608 + 480 + 2048 + t * 8, u2{__builtin_amdgcn_perm(ub[1][c >> 1], ub[0][c >> 1], sel), __builtin_amdgcn_perm(ub[3][c >> 1], ub[2][c >> 1], sel)});
        }
        sig(FU2);
        STAMP(8);
        const int tile = wv - 12, yl = ((lane >> 2) + 16 * (lane & 3)) * 8;
        char* o = (char*)(out + 4 * t * (2 * DM) + d0);
        bar();
        bar();
        STAMP(11);
        a_rows_out(LA_G + ((tile >> 1) ^ 1) * 4608 + (tile & 1) * 1024 + yl, 9216, o);
        STAMP(9);
        bar();
        bar();
        STAMP(12);
        a_rows_out(LA_G + ((tile >> 1) ^ 1) * 4608 + (tile & 1) * 1024 + 2048 + yl, 9216, o + 1024 * (4 * DM));
        STAMP(10);
        STAMP_OUT(768);
        return;
    }
    const int ch = wv >> 1, hf = wv & 1;
    {
        __builtin_amdgcn_s_barrier();
        asm volatile("s_waitcnt vmcnt(0)" : "+v"(pr.l), "+v"(pr.a), "+v"(pr.r), "+v"(pr.i), "+v"(pr.t), "+v"(pr.d) :: "memory");
        PRIO(3);
        const float dt = rbf(exp_bf(blo(pr.t)));
        const float ar2 = -rbf(exp_bf(blo(pr.l))) * dt * 1.44269504f, air = blo(pr.a) * dt * 0.159154943f;
        const f2 C = {2.f * dt * blo(pr.r), 2.f * dt * blo(pr.i)};
        STAMP(1);
        const f2 z1 = cexpa(ar2, air, 1.f), z8c = cexpa(ar2, -air, 8.f), z128 = cexpa(ar2, air, 128.f);
        const int gr = LA_G + wv * 4608 + c16 * 288 + g * 16, q = LA_Q + wv * 2576;
        asm volatile("s_mov_b32 m0, %0\n\ts_nop 0" :: "s"(LA_G + wv * 4608) : "memory");
#define SB __builtin_amdgcn_sched_barrier(0)
#define STAGE16(v) do { stage<0>(v[0]); stage<4>(v[1]); stage<8>(v[2]); stage<12>(v[3]); } while (0)
#define READ4(f) do { _Pragma("unroll") for (int s = 0; s < 4; s++) f[s] = lds_ld<u4>(gr + 64 * s); SB; } while (0)
        u4 v[4], bfr[4], afr[4];
        const C4 s1 = cmul4(z8c, z8c, z1, z1, z128, z128, C, z1);
        const C4 s2 = cmul4(s1.a, s1.a, z8c, s1.a, s1.c, s1.c, C, s1.b);
        {
            C4 w = {f2{1.f, 0.f}, z8c, s1.a, s2.b};
            v[0] = pk4(w);
#pragma unroll
            for (int i = 1; i < 4; i++) { w = cmul4(w, s2.a); v[i] = pk4(w); }
        }
        STAGE16(v); READ4(bfr);
        PRIO(2);
        const C4 s3 = cmul4(s1.d, s1.b, s1.b, s1.b, s2.c, s2.c, s2.c, z128);
        const C4 e0 = {C, s1.d, s2.d, s3.a}, e1 = cmul4(e0, s3.b);
        v[0] = pk4(e0); v[1] = pk4(e1); v[2] = pk4(cmul4(e0, z128)); v[3] = pk4(cmul4(e1, z128));
        STAGE16(v); READ4(afr);
        PRIO(1);
        {
            const C4 f = cmul4(C4{e0.a, e0.c, e1.a, e1.c}, s1.c), h = cmul4(e0.a, s2.c, e1.a, s2.c, e0.a, s3.d, e1.a, s3.d);
            v[0] = pk4(f); v[1] = pk4(cmul4(f, z128)); v[2] = pk4(h); v[3] = pk4(cmul4(h, s1.c));
        }
        SB;
        f4 acc = Z;
#pragma unroll
        for (int s = 0; s < 4; s++) acc = mfma(afr[s], bfr[s], acc);
        STAGE16(v); READ4(afr);
        PRIO(0);
        lds_st<f4>(q + (4 * (g & 1) + 128 * (g >> 1) + 8 * c16) * 4, acc);
        {
            const f2 f0 = cmul(C, s3.c), f1 = cmul(f0, z128);
            const C4 f = cmul4(f0, s1.c, f1, s1.c, f0, s3.c, f0, s3.c), h = cmul4(C4{f0, f1, f.a, f.b}, s2.c);
            v[0] = pk4(C4{f0, f1, f.a, f.b}); v[1] = pk4(h); v[2][0] = pk(f.c.x, f.c.y);
        }
        SB;
        acc = Z;
#pragma unroll
        for (int s = 0; s < 4; s++) acc = mfma(afr[s], bfr[s], acc);
        stage<0>(v[0]); stage<4>(v[1]);
        asm volatile("ds_write_addtid_b32 %0 offset:%1" :: "v"(v[2][0]), "i"(288 * 8) : "memory");
        READ4(afr);
        if (g < 2) lds_st<f4>(q + (256 + 64 * g + 4 * c16) * 4, acc);
        else {
            const int o = q + (384 + 64 * (g - 2) + 2 * c16) * 4;
            lds_st<f2>(o, f2{acc[0], acc[1]}); lds_st<f2>(o + 128, f2{acc[2], acc[3]});
        }
        acc = Z;
#pragma unroll
        for (int s = 0; s < 4; s++) acc = mfma(afr[s], bfr[s], acc);
        if (g < 2) {
#pragma unroll
            for (int i = 0; i < 4; i++) lds_st<float>(q + (512 + 64 * g + 16 * i + c16) * 4, acc[i]);
        } else if (lane < 34) lds_st<float>(q + (lane == 32 ? 640 : 643) * 4, lane == 32 ? acc[0] : 0.f);
    }
    STAMP(2);
    asm volatile("s_waitcnt lgkmcnt(0)" ::: "memory");
    bar();
    STAMP(9);

    const int k0 = LA_K + ch * KC;
    {
        const LAS float* qa = (const LAS float*)(u32)(LA_Q + ch * 5152);
        const LAS float* qb = qa + 644;
        u2 w[4];
        int y;
        if (hf == 0) {
            const u32 ix = lds_ld<u32>(LA_S + 1024 + lane * 16);
            f4 k = *(const LAS f4*)(qa + 4 * lane) + *(const LAS f4*)(qb + 4 * lane);
            f4 p = *(const LAS f4*)(qa + 4 * lane - 4) + *(const LAS f4*)(qb + 4 * lane - 4);
            const f2 s = *(const LAS f2*)(qa + 512 - 2 * lane) + *(const LAS f2*)(qb + 512 - 2 * lane);
            const float e0 = qa[(ix & 0xffff) >> 2] + qb[(ix & 0xffff) >> 2], e1 = qa[ix >> 18] + qb[ix >> 18];
            lds_st<u32>(k0 + KQ + (32 + lane) * 4, pk(s.x, lane ? s.y : 0.f));
            if (lane == 0) lds_st<u32>(k0 + KX, pk(s.x, s.y));
            lds_st<u32>(k0 + KQ + (lane + (lane & 32) * 2) * 4, 0u);
            lds_st<u32>(k0 + g * KI + c16 * 4, pk(e0, e1));
            if (lane == 0) { k[0] += blo(pr.d); p = Z; }
            const u32 c = pk(k[1], k[0]), d = pk(k[0], p[3]);
            w[0] = u2{pk(k[3], k[2]), c}; w[1] = u2{pk(k[2], k[1]), d}; w[2] = u2{c, pk(p[3], p[2])}; w[3] = u2{d, pk(p[2], p[1])};
            y = k0 + (270 - 2 * lane) * 4;
        } else {
            const int j = 256 + 2 * lane;
            const float q0 = qa[j] + qb[j], q1 = qa[j + 1] + qb[j + 1], q2 = qa[j + 2] + qb[j + 2];
            const float p1 = qa[j - 1] + qb[j - 1], p2 = qa[j - 2] + qb[j - 2], p3 = qa[j - 3] + qb[j - 3];
            const u32 c = pk(q0, q0), d = pk(q0, p1), e = pk(p1, p2), f = lane ? c : d;
            w[0] = u2{pk(q2, q1), c}; w[1] = u2{pk(q1, q0), f}; w[2] = u2{c, lane ? d : e}; w[3] = u2{f, lane ? e : pk(p2, p3)};
            y = k0 + (142 - 2 * lane) * 4;
            if (lane < 48) lds_st<u32>(k0 + (lane < 32 ? (lane >> 3) * KI + (272 + (lane & 7)) * 4 : KP + (32 + lane) * 4), 0u);
            const f2 s = *(const LAS f2*)(qa + 640 - 2 * lane) + *(const LAS f2*)(qb + 640 - 2 * lane);
            lds_st<u32>(k0 + KP + lane * 4, pk(s.x, lane ? s.y : 0.f));
        }
#pragma unroll
        for (int i = 0; i < 4; i++) lds_st<u2>(y + i * KI, w[i]);
    }
    const int x0 = 2031 - c16 + 8 * g;
    int ka = k0 + (x0 & 3) * KI + (x0 >> 2) * 8 - 3008 - 256 * hf - 192 - 512;
    int ubs = LA_U + ch * 4608 + 480 + (16 * c16 + 8 * g - 16) * 2 - 256 * hf - 192;
    pin(ka); pin(ubs);
    const int lt = LA_S + lane * 16;
    const u4 bnd5 = lds_ld<u4>(lt + 4096), bnd3 = lds_ld<u4>(lt + 5120);
    int uv = LA_U + ch * 4608 + 480 + (8 * c16 + 8 * g - 24) * 2 + 256 * hf;
    pin(uv);
#define VSUM(n) ({ const u4 a_ = lds_ld<u4>(uv + 512 * (n)); pack4(mfma(a_, bnd5, Z), mfma(a_, bnd3, Z)); })
    const u4 bnd9 = lds_ld<u4>(lt + 2048), bnd7 = lds_ld<u4>(lt + 3072);
    STAMP(13);
    bar();
    STAMP(3);
    PRIO(3);
    u4 vs[4], ws0, ws1, ws2;
    {
        const int uw = LA_U + ch * 4608 + 480 + (16 * c16 + 8 * g - 28) * 2;
#define WSUM(I) ({ const u4 a_ = ld_k(uw + 512 * (I)); pack4(mfma(a_, bnd9, Z), mfma(a_, bnd7, Z)); })
#pragma unroll
        for (int n = 0; n < 4; n++) vs[n] = VSUM(n);
        ws0 = WSUM(hf);
        if (hf) { ws1 = WSUM(2); ws2 = WSUM(3); }
    }
#define KFR(rho, p) ld_k(ka + 192 - 64 * (rho) + 512 - 512 * (p))
#define UFR(rho, q) lds_ld<u4>(ubs + 192 - 64 * (rho) + 512 * (q))
    f4 acc[4];
#pragma unroll
    for (int i = 0; i < 4; i++) acc[i] = Z;
    u4 gv[3];
    {
        u4 A[2][2], B[2][4];
        PRIO(3);
#pragma unroll
        for (int p = 0; p < 4; p++) B[0][p] = UFR(0, p);
        A[0][0] = KFR(0, 0); A[0][1] = KFR(0, 1);
#pragma unroll
        for (int k = 0; k < 3; k++) gv[k] = ld_k(k0 + KQ + (94 + 16 * hf - 32 * k - 2 * c16 + 4 * g) * 4);
        if (!hf) { const u4 t = ld_k(ka - 320); acc[2] = mfma(t, B[0][0], acc[2]); acc[3] = mfma(t, B[0][1], acc[3]); }
        acc[2] = mfma(vs[0], gv[0], acc[2]); acc[3] = mfma(vs[0], gv[1], acc[3]); acc[3] = mfma(vs[1], gv[0], acc[3]);
#pragma unroll
        for (int rho = 0; rho < 4; rho++)
#pragma unroll
            for (int p = 0; p < 2; p++) {
                SB;
                if (p == 0) { if (rho == 1) PRIO(2); else if (rho == 2) PRIO(1); else if (rho == 3) PRIO(0); }
                if (rho < 3) { B[(rho + 1) & 1][2 * p] = UFR(rho + 1, 2 * p); B[(rho + 1) & 1][2 * p + 1] = UFR(rho + 1, 2 * p + 1); A[(rho + 1) & 1][p] = KFR(rho + 1, p); }
#pragma unroll
                for (int q = 0; p + q < 4; q++) acc[p + q] = mfma(A[rho & 1][p], B[rho & 1][q], acc[p + q]);
            }
        SB;
    }
#pragma unroll
    for (int i = 0; i < 4; i++) pin(acc[i]);
    STAMP(4);
#define EXCHANGE(off)                                                                         \
    {                                                                                         \
        const int mine = LA_G + wv * 4608 + (off), other = LA_G + (wv ^ 1) * 4608 + (off);     \
        if (hf) { lds_st<f4>(mine + lane * 16, acc[0]); lds_st<f4>(mine + 1024 + lane * 16, acc[1]); acc[0] = acc[2]; acc[1] = acc[3]; } \
        else { lds_st<f4>(mine + lane * 16, acc[2]); lds_st<f4>(mine + 1024 + lane * 16, acc[3]); } \
        bar();                                                                                \
        const f4 y0 = lds_ld<f4>(other + lane * 16) + acc[0], y1 = lds_ld<f4>(other + 1024 + lane * 16) + acc[1]; \
        lds_st<u2>(other + lane * 8, u2{pk(y0[0], y0[1]), pk(y0[2], y0[3])});                 \
        lds_st<u2>(other + 1024 + lane * 8, u2{pk(y1[0], y1[1]), pk(y1[2], y1[3])});          \
    }
    EXCHANGE(0);
    {
        const int po = k0 + KP + (15 - c16 + 4 * g) * 4;
#define PFR(d) u4{lds_ld<u32>(po + 64 * (7 - (d))), lds_ld<u32>(po + 64 * (7 - (d)) + 4), lds_ld<u32>(po + 64 * (7 - (d)) + 8), lds_ld<u32>(po + 64 * (7 - (d)) + 12)}
        const u4 g4 = PFR(4), g5 = PFR(5), g6 = PFR(6);
        bar();
        STAMP(14);
        PRIO(3);
        acc[0] = mfma(vs[0], gv[2], Z); acc[1] = mfma(vs[1], gv[2], Z); acc[2] = mfma(vs[2], gv[2], Z); acc[3] = mfma(vs[3], gv[2], Z);
        acc[0] = mfma(vs[1], gv[1], acc[0]); acc[1] = mfma(vs[2], gv[1], acc[1]); acc[2] = mfma(vs[3], gv[1], acc[2]);
        acc[0] = mfma(vs[2], gv[0], acc[0]); acc[1] = mfma(vs[3], gv[0], acc[1]);
        if (hf) {
            acc[3] = mfma(ws0, g6, acc[3]); acc[2] = mfma(ws0, g5, acc[2]); acc[1] = mfma(ws0, g4, acc[1]);
            acc[3] = mfma(ws1, g5, acc[3]); acc[2] = mfma(ws1, g4, acc[2]); acc[3] = mfma(ws2, g4, acc[3]);
        } else {
            const u4 g7 = PFR(7), sel = lds_ld<u4>(lt);
            const u32 r = lds_ld<u32>(k0 + KX);
            acc[3] = mfma(ws0, g7, acc[3]); acc[2] = mfma(ws0, g6, acc[2]); acc[0] = mfma(ws0, g4, acc[0]); acc[1] = mfma(ws0, g5, acc[1]);
            const u4 t = {__builtin_amdgcn_perm(r, r, sel[0]), __builtin_amdgcn_perm(r, r, sel[1]), __builtin_amdgcn_perm(r, r, sel[2]), __builtin_amdgcn_perm(r, r, sel[3])};
#pragma unroll
            for (int q = 0; q < 4; q++) acc[q] = mfma(t, UFR(0, q), acc[q]);
        }
#pragma unroll
        for (int i = 0; i < 4; i++) pin(acc[i]);
        await<1>(FU2, 4);
        u4 A[2][2], B[2][5];
#pragma unroll
        for (int p = 0; p < 5; p++) B[0][p] = UFR(0, 3 + p);
        A[0][0] = KFR(0, 0); A[0][1] = KFR(0, 1);
        vs[0] = VSUM(4); vs[1] = VSUM(5);
        if (!hf) {
            const u4 t = ld_k(ka - 320);
            acc[0] = mfma(t, UFR(0, 2), acc[0]);
#pragma unroll
            for (int q = 1; q < 4; q++) acc[q] = mfma(t, B[0][q - 1], acc[q]);
        }
        acc[2] = mfma(vs[0], gv[0], acc[2]); acc[3] = mfma(vs[0], gv[1], acc[3]); acc[3] = mfma(vs[1], gv[0], acc[3]);
#pragma unroll
        for (int rho = 0; rho < 4; rho++)
#pragma unroll
            for (int p = 0; p < 2; p++) {
                SB;
                if (p == 0) { if (rho == 1) PRIO(2); else if (rho == 2) PRIO(1); else if (rho == 3) PRIO(0); }
                if (rho < 3) {
#pragma unroll
                    for (int q = 3 * p; q < 3 + 2 * p; q++) B[(rho + 1) & 1][q] = UFR(rho + 1, 3 + q);
                    A[(rho + 1) & 1][p] = KFR(rho + 1, p);
                }
#pragma unroll
                for (int j = 0; j < 4; j++) acc[j] = mfma(A[rho & 1][p], B[rho & 1][j + 1 - p], acc[j]);
            }
        SB;
    }
#pragma unroll
    for (int i = 0; i < 4; i++) pin(acc[i]);
    STAMP(5);
    EXCHANGE(2048);
    bar();
    STAMP(6);
    STAMP_OUT(0);
}

__global__ void __launch_bounds__(NT) __attribute__((aligned(4096))) fused_kernel(const u16* __restrict__ u, const u16* __restrict__ lar, const u16* __restrict__ aimg,
                                                   const u16* __restrict__ cre, const u16* __restrict__ cim, const u16* __restrict__ ldt,
                                                   const u16* __restrict__ dsk, const u16* __restrict__ x, const u16* __restrict__ wr,
                                                   const u16* __restrict__ wi, u16* __restrict__ out, char* ws) {
    const char* entry;
    asm volatile("s_getpc_b64 %0" : "=s"(entry));
    APar pr;
    if (blockIdx.x >= A0 && blockIdx.x < H0 && __builtin_amdgcn_readfirstlane(threadIdx.x) < 768) {
        const int a = blockIdx.x - A0, d = 6 * ((a & 7) * 12 + (a >> 3)) + __builtin_amdgcn_readfirstlane(threadIdx.x >> 7);
        asm volatile("global_load_ushort %0, %6, %8\n\tglobal_load_ushort %1, %6, %9\n\tglobal_load_ushort %2, %6, %10\n\t"
                     "global_load_ushort %3, %6, %11\n\tglobal_load_ushort %4, %7, %12\n\tglobal_load_ushort %5, %7, %13"
                     : "=&v"(pr.l), "=&v"(pr.a), "=&v"(pr.r), "=&v"(pr.i), "=&v"(pr.t), "=&v"(pr.d)
                     : "v"((d * NS + (threadIdx.x & 127)) * 2), "v"(d * 2), "s"(lar), "s"(aimg), "s"(cre), "s"(cim), "s"(ldt), "s"(dsk) : "memory");
    }
    if (__builtin_expect(blockIdx.x < NB2, 1)) head_b(entry, x, wr, wi, out, ws);
    else if (blockIdx.x >= A0 && blockIdx.x < H0) head_a(u, pr, out, ws);
    else warm_code(entry);
}

extern "C" void kernel_launch(void* const* d_in, const int* in_sizes, int n_in, void* d_out, int out_size, void* d_ws,
                              size_t ws_size, hipStream_t stream) {
    (void)hipFuncSetAttribute((const void*)fused_kernel, hipFuncAttributeMaxDynamicSharedMemorySize, LDS_BYTES);
    fused_kernel<<<GRID, NT, LDS_BYTES, stream>>>((const u16*)d_in[0], (const u16*)d_in[1], (const u16*)d_in[2], (const u16*)d_in[3],
                                                  (const u16*)d_in[4], (const u16*)d_in[5], (const u16*)d_in[6], (const u16*)d_in[7],
                                                  (const u16*)d_in[8], (const u16*)d_in[9], (u16*)d_out, (char*)d_ws);
}
```
